```python
import torch

def _softmax(x, axis=-1):
    return torch.softmax(x, dim=axis)
def _gelu(x):
    return torch.nn.functional.gelu(x, approximate="tanh")
def _silu(x):
    return torch.nn.functional.silu(x)
def _sigmoid(x):
    return torch.sigmoid(x)
def _layernorm(x, axis=-1, eps=1e-5):
    m = x.mean(dim=axis, keepdim=True)
    v = x.var(dim=axis, keepdim=True, unbiased=False)
    return (x - m) / torch.sqrt(v + eps)
def _scatter_add(tgt, idx, src, axis=0):
    return tgt.clone().index_add_(axis, idx, src)
def _segment_sum(src, seg, n):
    out = torch.zeros((n,) + src.shape[1:], dtype=src.dtype)
    return out.index_add_(0, seg, src)
def _pad(x, pw):
    flat = tuple(v for pair in reversed(pw) for v in pair)
    return torch.nn.functional.pad(x, flat)

kernel_name = 'sequential_preLN__ssm_conv_s4__moe_arch_mod__prompt'

def setup_inputs():

    _w = (lambda a: (lambda t: t.to(torch.bfloat16) if t.is_floating_point() else t)(torch.as_tensor(a)))
    def _A_setup():
        B, L, D, N = 1, 2048, 640, 192
        A_re = -(lambda _x: torch.exp(torch.as_tensor(_x)))((lambda *s: torch.randn(*s, dtype=torch.bfloat16) * 6.4)(D, N))
        A_im = torch.pi * (lambda *s: torch.randn(*s, dtype=torch.bfloat16) * 0.05)(D, N)
        B_re = (lambda *s: torch.randn(*s, dtype=torch.bfloat16) * 0.05)(D, N)
        B_im = (lambda *s: torch.randn(*s, dtype=torch.bfloat16) * 0.05)(D, N)
        C_re = (lambda *s: torch.randn(*s, dtype=torch.bfloat16) * 0.05)(D, N)
        C_im = (lambda *s: torch.randn(*s, dtype=torch.bfloat16) * 0.05)(D, N)
        log_dt = (lambda *s: torch.randn(*s, dtype=torch.bfloat16) * 0.05)(D) * 0.5 - 4.0
        Dskip = (lambda *s: torch.randn(*s, dtype=torch.bfloat16) * 0.05)(D)
        return {
            "u": _w((lambda *s: torch.randn(*s, dtype=torch.bfloat16) * 0.05)(B, L, D)),
            "A_re": _w(A_re),
            "A_im": _w(A_im),
            "B_re": _w(B_re),
            "B_im": _w(B_im),
            "C_re": _w(C_re),
            "C_im": _w(C_im),
            "log_dt": _w(log_dt),
            "Dskip": _w(Dskip),
        }
    _A_in = _A_setup()

    def _B_setup():
        B, S, D, F, C = 1, 2048, 640, 1536, 1
        return {
            "x": _w((lambda *s: torch.randn(*s, dtype=torch.bfloat16) * 0.05)(B, S, D)),
            "w_r": _w((lambda *s: torch.randn(*s, dtype=torch.bfloat16) * 0.05)(D)),
            "w_gate": _w((lambda *s: torch.randn(*s, dtype=torch.bfloat16) * 0.05)(D, F)),
            "w_up": _w((lambda *s: torch.randn(*s, dtype=torch.bfloat16) * 0.05)(D, F)),
            "w_down": _w((lambda *s: torch.randn(*s, dtype=torch.bfloat16) * 0.05)(F, D)),
        }
    _B_in = _B_setup()

    return {
        "A_u": _A_in["u"],
        "A_A_re": _A_in["A_re"],
        "A_A_im": _A_in["A_im"],
        "A_B_re": _A_in["B_re"],
        "A_B_im": _A_in["B_im"],
        "A_C_re": _A_in["C_re"],
        "A_C_im": _A_in["C_im"],
        "A_log_dt": _A_in["log_dt"],
        "A_Dskip": _A_in["Dskip"],
        "B_x": _B_in["x"],
        "B_w_r": _B_in["w_r"],
        "B_w_gate": _B_in["w_gate"],
        "B_w_up": _B_in["w_up"],
        "B_w_down": _B_in["w_down"],
    }

def reference(A_u, A_A_re, A_A_im, A_B_re, A_B_im, A_C_re, A_C_im, A_log_dt, A_Dskip, B_x, B_w_r, B_w_gate, B_w_up, B_w_down):
    def _A_ref(u, A_re, A_im, B_re, B_im, C_re, C_im, log_dt, Dskip):
        Bsz, L, D = u.shape
        dt = torch.exp(log_dt)[:, None]
        A = A_re + 1j * A_im
        Bc = B_re + 1j * B_im
        Cc = C_re + 1j * C_im
        logdA = dt * A
        dB = dt * Bc
        l = (lambda x, dt: x.to(dt))(torch.arange(L), A_re.dtype)
        P = torch.exp(logdA[:, :, None] * l[None, None, :])
        K = torch.einsum("dn,dnl,dn->dl", Cc, P, dB).real
        L2 = 2 * L
        Kp = _pad(K, ((0, 0), (0, L)))
        up = (lambda x, *d: x.permute(*d))(u, 0, 2, 1)
        up = _pad(up, ((0, 0), (0, 0), (0, L)))
        Kf = (lambda x, axes=(-2,-1): torch.fft.rfft2(x.float(), dim=axes))(Kp[:, None, :], axes=(-2, -1))
        Uf = (lambda x, axes=(-2,-1): torch.fft.rfft2(x.float(), dim=axes))(up[:, :, None, :], axes=(-2, -1))
        Yf = Uf * Kf[None, :, :, :]
        y = (lambda x, s, axes=(-2,-1): torch.fft.irfft2(x, s=s, dim=axes).to(torch.bfloat16))(Yf, (1, L2), axes=(-2, -1))[:, :, 0, :L]
        y = (lambda x, *d: x.permute(*d))(y, 0, 2, 1) + u * Dskip[None, None, :]
        return y

    def _B_ref(x, w_r, w_gate, w_up, w_down):
        B, S, D = x.shape
        C = 1
        r = x @ w_r
        idx = torch.argsort(-r, axis=-1)[:, :C]
        idx_e = (lambda x, n, axis: torch.repeat_interleave(x, n, dim=axis))(idx.reshape(B, C, 1), D, axis=2)
        x_sel = (lambda x, i, axis: torch.take_along_dim(x, i, dim=axis))(x, idx_e, axis=1)
        g = _silu(x_sel @ w_gate)
        u = x_sel @ w_up
        y = (g * u) @ w_down
        r_sel = _sigmoid((lambda x, i, axis: torch.take_along_dim(x, i, dim=axis))(r, idx, axis=1))
        y = y * r_sel.reshape(B, C, 1)
        b_off = (torch.arange(B) * S).reshape(B, 1)
        flat_idx = (idx + b_off).reshape(B * C)
        zero = (x * 0.0).reshape(B * S, D)
        upd = _scatter_add(zero, flat_idx, y.reshape(B * C, D), axis=0)
        return x + upd.reshape(B, S, D)

    x = A_u
    h = x + _A_ref(_layernorm(x, axis=-1), A_A_re, A_A_im, A_B_re, A_B_im, A_C_re, A_C_im, A_log_dt, A_Dskip)
    h2 = h + _B_ref(_layernorm(h, axis=-1), B_w_r, B_w_gate, B_w_up, B_w_down)
    return h2
```

```cpp
#include <hip/hip_runtime.h>
#include <hip/hip_bf16.h>
#include <stdint.h>

typedef _Float16 h8 __attribute__((ext_vector_type(8)));
typedef _Float16 h2 __attribute__((ext_vector_type(2)));
typedef float f4 __attribute__((ext_vector_type(4)));
typedef unsigned u4 __attribute__((ext_vector_type(4)));
typedef unsigned u2 __attribute__((ext_vector_type(2)));
typedef unsigned short us;

constexpr int L_ = 2048, D_ = 640, N_ = 192, F_ = 1536;

constexpr size_t WS_UT    = 0;
constexpr size_t WS_YT    = WS_UT + (size_t)D_ * L_ * 2;
constexpr int    KREC     = 8448;
constexpr size_t WS_KREC  = WS_YT + (size_t)D_ * L_ * 2;
constexpr size_t WS_ST1   = WS_KREC + (size_t)D_ * KREC;
constexpr size_t WS_KEYS  = WS_ST1 + (size_t)L_ * 8;
constexpr size_t WS_CAND  = WS_KEYS + 1024;
constexpr size_t WS_ACT   = WS_CAND + (size_t)256 * D_ * 4;
constexpr size_t WS_END   = WS_ACT + F_ * 4;

constexpr int NEAR_PITCH = 608;
constexpr int NEAR_OFF   = 0;
constexpr int F2_OFF     = NEAR_OFF + 4 * NEAR_PITCH;
constexpr int F2_PITCH   = 72;
constexpr int F3_OFF     = F2_OFF + 16 * F2_PITCH;
constexpr int F3_PITCH   = 72 + 8;
constexpr int MISC_OFF   = F3_OFF + 8 * F3_PITCH;
constexpr int KREC_ELEMS = MISC_OFF;
static_assert(KREC_ELEMS % 8 == 0, "chunks");
static_assert((MISC_OFF) * 2 + 0 <= KREC, "rec size");
constexpr int KCHUNKS = KREC_ELEMS / 8;
constexpr size_t WS_MISC = WS_END;
constexpr size_t WS_TOTAL = WS_MISC + D_ * 8;

constexpr int cidx_of_v(int v) {
  return v <= 256 ? v : (v <= 512 ? 256 + (v - 256) / 2 : (v <= 1024 ? 384 + (v - 512) / 4 : 512 + (v - 1024) / 8));
}
constexpr int bf16_round_int(int j) {
  if (j <= 256) return j;
  int step = j < 512 ? 2 : (j < 1024 ? 4 : 8);
  int lo = (j / step) * step, rem = j - lo;
  if (rem * 2 < step) return lo;
  if (rem * 2 > step) return lo + step;
  return ((lo / step) & 1) ? lo + step : lo;
}
constexpr int ZIDX = 641;

struct GatherTab { us v[KREC_ELEMS]; };
constexpr GatherTab make_gather() {
  GatherTab t{};
  for (int i = 0; i < KREC_ELEMS; ++i) t.v[i] = ZIDX;
  for (int s = 0; s < 4; ++s)
    for (int i = 0; i < 560; ++i) {
      int j = 543 - (i + s);
      if (j >= 0 && j < 515) t.v[NEAR_OFF + s * NEAR_PITCH + i] = (us)cidx_of_v(bf16_round_int(j));
    }
  for (int pi = 0; pi < 4; ++pi)
    for (int s = 0; s < 4; ++s)
      for (int i = 0; i < 64; ++i) {
        int p = 79 - (i + s);
        if (p >= 32 && p < 64) {
          int rho = pi >> 1, ev = pi & 1;
          int v = 16 * p + 8 * rho + (ev ? 8 : 4);
          t.v[F2_OFF + (pi * 4 + s) * F2_PITCH + i] = (us)cidx_of_v(v);
        }
      }
  for (int pi = 0; pi < 2; ++pi)
    for (int s = 0; s < 4; ++s)
      for (int i = 0; i < 80; ++i) {
        int q = 127 - (i + s);
        if (q >= 64 && q < 128) {
          int v = 16 * q + (pi ? 16 : 8);
          t.v[F3_OFF + (pi * 4 + s) * F3_PITCH + i] = (us)cidx_of_v(v);
        }
      }
  return t;
}
__device__ const GatherTab g_gather = make_gather();

struct BoxTab { us v[7 * 64 * 8]; };
constexpr BoxTab make_box() {
  BoxTab t{};
  const int nn[6] = {3, 5, 3, 5, 7, 9};
  const int sh[6] = {3, 6, 11, 14, 5, 12};
  const int w0[6] = {-16, -16, -24, -24, -16, -24};
  for (int m = 0; m < 6; ++m)
    for (int lane = 0; lane < 64; ++lane)
      for (int j = 0; j < 8; ++j) {
        int b = lane & 15, k = 8 * (lane >> 4) + j;
        int idx = w0[m] + k;
        bool on = idx <= b - sh[m] && idx >= b - sh[m] - (nn[m] - 1);
        t.v[(m * 64 + lane) * 8 + j] = on ? 0x3C00 : 0;
      }
  for (int lane = 0; lane < 64; ++lane)
    for (int j = 0; j < 8; ++j) {
      int b = lane & 15, k = 8 * (lane >> 4) + j;
      t.v[(6 * 64 + lane) * 8 + j] = (k - b == 4 || k - b == 5) ? 0xFFFF : 0;
    }
  return t;
}
__device__ const BoxTab g_box = make_box();

__device__ __forceinline__ float bflo(unsigned w) { return __uint_as_float(w << 16); }
__device__ __forceinline__ float bfhi(unsigned w) { return __uint_as_float(w & 0xffff0000u); }
__device__ __forceinline__ float bf1(us v) { return __uint_as_float((unsigned)v << 16); }
__device__ __forceinline__ us f2bf(float f) { __bf16 b = (__bf16)f; return __builtin_bit_cast(us, b); }
__device__ __forceinline__ float rbf(float f) { return bf1(f2bf(f)); }
__device__ __forceinline__ unsigned pk_bf(float a, float b) { return (unsigned)f2bf(a) | ((unsigned)f2bf(b) << 16); }
__device__ __forceinline__ unsigned pk_h(float a, float b) {
  h2 v; v.x = (_Float16)a; v.y = (_Float16)b; return __builtin_bit_cast(unsigned, v);
}
__device__ __forceinline__ float div_acc(float a, float b, float rb) {
  float q = a * rb;
  float e = __builtin_fmaf(-q, b, a);
  return __builtin_fmaf(e, rb, q);
}
template <int CTRL> __device__ __forceinline__ float dpp_add(float v) {
  int t = __builtin_amdgcn_update_dpp(0, __float_as_int(v), CTRL, 0xf, 0xf, true);
  return v + __int_as_float(t);
}
__device__ __forceinline__ float row_sum(float v) {
  v = dpp_add<0x121>(v);
  v = dpp_add<0x122>(v);
  v = dpp_add<0x124>(v);
  v = dpp_add<0x128>(v);
  return v;
}
__device__ __forceinline__ float swap32_add(float a, float b) {
  auto r = __builtin_amdgcn_permlane32_swap(__float_as_uint(a), __float_as_uint(b), false, false);
  return __uint_as_float(r[0]) + __uint_as_float(r[1]);
}
__device__ __forceinline__ float swap16_add(float a, float b) {
  auto r = __builtin_amdgcn_permlane16_swap(__float_as_uint(a), __float_as_uint(b), false, false);
  return __uint_as_float(r[0]) + __uint_as_float(r[1]);
}
__device__ __forceinline__ void wave_reduce16(const float (&v)[16], float (&out)[4]) {
  float w[8];
#pragma unroll
  for (int i = 0; i < 8; ++i) w[i] = swap32_add(v[i], v[i + 8]);
#pragma unroll
  for (int i = 0; i < 4; ++i) out[i] = row_sum(swap16_add(w[i], w[i + 4]));
}
__device__ __forceinline__ float rdlane(float v, int l) {
  return __int_as_float(__builtin_amdgcn_readlane(__float_as_int(v), l));
}

__device__ __forceinline__ void ln_stats8(const float (&a)[8][2], float* red, int wave, int lane,
                                          float (&mOut)[8], float (&sOut)[8]) {
  float piv[8], v[16];
#pragma unroll
  for (int r = 0; r < 8; ++r) {
    piv[r] = rdlane(a[r][0], 0);
    float d0 = a[r][0] - piv[r], d1 = a[r][1] - piv[r];
    v[r] = d0 + d1;
    v[r + 8] = __builtin_fmaf(d0, d0, d1 * d1);
  }
  float o[4];
  wave_reduce16(v, o);
  int rr = lane >> 4;
  if ((lane & 15) == 0) {
    int base = wave * 24 + 4 * (rr & 1) + 8 * (rr >> 1);
#pragma unroll
    for (int i = 0; i < 4; ++i) red[base + i] = o[i];
  }
  if (lane < 8) {
    float p = piv[0];
#pragma unroll
    for (int r = 1; r < 8; ++r) p = (lane == r) ? piv[r] : p;
    red[wave * 24 + 16 + lane] = p;
  }
  __syncthreads();
  int r = lane & 7;
  float mw[5], m2 = 0.f, msum = 0.f;
#pragma unroll
  for (int w = 0; w < 5; ++w) {
    float s1 = red[w * 24 + r], s2 = red[w * 24 + 8 + r], p = red[w * 24 + 16 + r];
    float dm = s1 * (1.f / 128.f);
    mw[w] = p + dm;
    m2 += s2 - s1 * dm;
    msum += mw[w];
  }
  float mean = msum * 0.2f;
#pragma unroll
  for (int w = 0; w < 5; ++w) { float d = mw[w] - mean; m2 = __builtin_fmaf(128.f * d, d, m2); }
  float var = fmaxf(m2 * (1.f / 640.f), 0.f);
  float mb = rbf(mean);
  float sb = rbf(__builtin_sqrtf(rbf(rbf(var) + 1e-5f)));
#pragma unroll
  for (int q = 0; q < 8; ++q) { mOut[q] = rdlane(mb, q); sOut[q] = rdlane(sb, q); }
}

struct cf { float r, i; };
__device__ __forceinline__ cf cmul(cf a, cf b) {
  cf c; c.r = __builtin_fmaf(a.r, b.r, -(a.i * b.i)); c.i = __builtin_fmaf(a.r, b.i, a.i * b.r); return c;
}
__device__ __forceinline__ cf cexp_p(float lam, float rh, float rl, float p) {
  float mag = __builtin_amdgcn_exp2f(lam * p);
  float x = rh * p;
  float f = x - __builtin_rintf(x);
  f = __builtin_fmaf(rl, p, f);
  cf w; w.r = mag * __builtin_amdgcn_cosf(f); w.i = mag * __builtin_amdgcn_sinf(f); return w;
}

constexpr int TAB_PITCH = 196;
__global__ __launch_bounds__(384) void k1_ln_kgen(
    const us* __restrict__ x, const us* __restrict__ Are, const us* __restrict__ Aim,
    const us* __restrict__ Bre, const us* __restrict__ Bim, const us* __restrict__ Cre,
    const us* __restrict__ Cim, const us* __restrict__ logdt, char* __restrict__ ws) {
  __shared__ __attribute__((aligned(16))) unsigned smem[64 * TAB_PITCH + 336 + 16];
  const int tid = threadIdx.x, lane = tid & 63, wave = __builtin_amdgcn_readfirstlane(tid >> 6);
  const int bid = blockIdx.x;
  if (bid < 256) {
    if (wave >= 5) return;
    const int rg = (bid & 7) * 32 + (bid >> 3);
    const int t0 = rg * 8;
    const unsigned* xr = (const unsigned*)(x + (size_t)t0 * D_) + tid;
    unsigned raw[8];
#pragma unroll
    for (int r = 0; r < 8; ++r) raw[r] = xr[r * (D_ / 2)];
    float a[8][2];
#pragma unroll
    for (int r = 0; r < 8; ++r) { a[r][0] = bflo(raw[r]); a[r][1] = bfhi(raw[r]); }
    float m[8], s[8];
    ln_stats8(a, (float*)smem, wave, lane, m, s);
    float u0[8], u1[8];
#pragma unroll
    for (int r = 0; r < 8; ++r) {
      float rs = __builtin_amdgcn_rcpf(s[r]);
      u0[r] = rbf(div_acc(rbf(a[r][0] - m[r]), s[r], rs)) * 128.f;
      u1[r] = rbf(div_acc(rbf(a[r][1] - m[r]), s[r], rs)) * 128.f;
    }
    u4 o0, o1;
    o0.x = pk_h(u0[0], u0[1]); o0.y = pk_h(u0[2], u0[3]); o0.z = pk_h(u0[4], u0[5]); o0.w = pk_h(u0[6], u0[7]);
    o1.x = pk_h(u1[0], u1[1]); o1.y = pk_h(u1[2], u1[3]); o1.z = pk_h(u1[4], u1[5]); o1.w = pk_h(u1[6], u1[7]);
    us* ut = (us*)(ws + WS_UT);
    *(u4*)(ut + (size_t)(2 * tid) * L_ + t0) = o0;
    *(u4*)(ut + (size_t)(2 * tid + 1) * L_ + t0) = o1;
    if (tid < 8) {
      float mm = m[0], ss = s[0];
#pragma unroll
      for (int r = 1; r < 8; ++r) { mm = (tid == r) ? m[r] : mm; ss = (tid == r) ? s[r] : ss; }
      float2 st; st.x = mm; st.y = ss;
      ((float2*)(ws + WS_ST1))[t0 + tid] = st;
    }
    return;
  }
  const int d = bid - 256;
  const int half = wave >= 3 ? 1 : 0;
  const int n = tid - half * 192;
  unsigned* tab = smem;
  us* kc = (us*)(smem + 64 * TAB_PITCH);
  float* red = (float*)(smem + 64 * TAB_PITCH + 336);
  u4 gi0 = ((const u4*)g_gather.v)[tid];
  u4 gi1 = (tid + 384 < KCHUNKS) ? ((const u4*)g_gather.v)[tid + 384] : u4{0, 0, 0, 0};
  const size_t pidx = (size_t)d * N_ + n;
  float are = bf1(Are[pidx]), aim = bf1(Aim[pidx]);
  float bre = bf1(Bre[pidx]), bim = bf1(Bim[pidx]);
  float cre = bf1(Cre[pidx]), cim = bf1(Cim[pidx]);
  float dt = rbf(__expf(bf1(logdt[d])));
  float ar = dt * are, ai = dt * aim;
  cf dB; dB.r = dt * bre; dB.i = dt * bim;
  cf Cc; Cc.r = cre; Cc.i = cim;
  cf CB = cmul(Cc, dB);
  const float LOG2E = 1.4426950408889634f;
  const float I2PH = 0.15915494309189535f;
  const float I2PL = (float)(0.15915494309189533577 - (double)0.15915494309189535f);
  float lam = ar * LOG2E;
  float rh = ai * I2PH;
  float rl = __builtin_fmaf(ai, I2PH, -rh) + ai * I2PL;
  if (half == 0) {
    float g = fmaxf(1.f, __builtin_amdgcn_exp2f(lam * 2048.f));
    float bnd = (fabsf(CB.r) + fabsf(CB.i)) * g;
    bnd = row_sum(bnd);
    bnd = rdlane(bnd, 0) + rdlane(bnd, 16) + rdlane(bnd, 32) + rdlane(bnd, 48);
    if (lane == 0) red[wave] = bnd;
  }
  if (tid == 0) kc[ZIDX] = 0;
  cf w128 = cexp_p(lam, rh, rl, 128.f), w256 = cexp_p(lam, rh, rl, 256.f), w512 = cexp_p(lam, rh, rl, 512.f);
#define PUT(row, c) tab[(row) * TAB_PITCH + n] = pk_h((c).r, (c).i)
#define PUTE(row, c) tab[(48 + (row)) * TAB_PITCH + n] = pk_h((c).r, -(c).i)
  if (half == 0) {
    cf w8 = cexp_p(lam, rh, rl, 8.f), w16 = cexp_p(lam, rh, rl, 16.f), w32 = cexp_p(lam, rh, rl, 32.f),
       w64 = cexp_p(lam, rh, rl, 64.f), w1024 = cexp_p(lam, rh, rl, 1024.f);
    cf e[8];
    e[0].r = 1.f; e[0].i = 0.f; e[1] = w8; e[2] = w16; e[3] = cmul(w8, w16); e[4] = w32;
    e[5] = cmul(w32, e[1]); e[6] = cmul(w32, e[2]); e[7] = cmul(w32, e[3]);
#pragma unroll
    for (int l = 0; l < 8; ++l) PUTE(l, e[l]);
    PUTE(8, w64);
#pragma unroll
    for (int l = 1; l < 8; ++l) { cf t = cmul(w64, e[l]); PUTE(8 + l, t); }
    __syncthreads();
    float sig = red[0] + red[1] + red[2];
    int ex = (int)(__float_as_uint(sig) >> 23) & 255; ex = ex < 24 ? 24 : (ex > 254 ? 254 : ex);
    float sc = __uint_as_float((unsigned)(268 - ex) << 23);
    cf c0; c0.r = CB.r * sc; c0.i = CB.i * sc;
    cf dd[4];
    dd[0] = cmul(c0, w1024); dd[1] = cmul(dd[0], w128); dd[2] = cmul(dd[0], w256); dd[3] = cmul(dd[1], w256);
#pragma unroll
    for (int h = 0; h < 4; ++h) { PUT(32 + h, dd[h]); cf t = cmul(dd[h], w512); PUT(36 + h, t); }
    cf t2048 = cmul(dd[0], w1024); PUT(40, t2048);
    if (tid == 0) ((float*)(ws + WS_MISC))[2 * d] = __uint_as_float((unsigned)(ex - 21) << 23);
  } else {
    cf w1 = cexp_p(lam, rh, rl, 1.f), w2 = cexp_p(lam, rh, rl, 2.f), w4 = cexp_p(lam, rh, rl, 4.f);
    __syncthreads();
    float sig = red[0] + red[1] + red[2];
    int ex = (int)(__float_as_uint(sig) >> 23) & 255; ex = ex < 24 ? 24 : (ex > 254 ? 254 : ex);
    float sc = __uint_as_float((unsigned)(268 - ex) << 23);
    cf c[8];
    c[0].r = CB.r * sc; c[0].i = CB.i * sc;
    c[1] = cmul(c[0], w1); c[2] = cmul(c[0], w2); c[3] = cmul(c[1], w2); c[4] = cmul(c[0], w4);
    c[5] = cmul(c[1], w4); c[6] = cmul(c[2], w4); c[7] = cmul(c[3], w4);
#pragma unroll
    for (int r = 0; r < 8; ++r) {
      PUT(r, c[r]);
      cf ch = cmul(c[r], w128); PUT(8 + r, ch);
      if ((r & 1) == 0) {
        cf s0 = cmul(c[r], w256), s1 = cmul(ch, w256);
        PUT(16 + (r >> 1), s0); PUT(20 + (r >> 1), s1);
        if ((r & 3) == 0) {
          int rho = r >> 2;
          cf q0 = cmul(c[r], w512), q1 = cmul(ch, w512), q2 = cmul(s0, w512), q3 = cmul(s1, w512);
          PUT(24 + 0 + rho, q0); PUT(24 + 2 + rho, q1); PUT(24 + 4 + rho, q2); PUT(24 + 6 + rho, q3);
        }
      }
    }
  }
  __syncthreads();
  if (wave < 3) {
    f4 acc = {0.f, 0.f, 0.f, 0.f};
    const int row = lane & 15, g = lane >> 4;
    const unsigned* pa = tab + (wave * 16 + row) * TAB_PITCH + g * 4;
    const unsigned* pb = tab + (48 + row) * TAB_PITCH + g * 4;
#pragma unroll
    for (int kk = 0; kk < 12; ++kk) {
      u4 av = *(const u4*)(pa + kk * 16), bv = *(const u4*)(pb + kk * 16);
      acc = __builtin_amdgcn_mfma_f32_16x16x32_f16(__builtin_bit_cast(h8, av), __builtin_bit_cast(h8, bv), acc, 0, 0, 0);
    }
    const int l = lane & 15;
#pragma unroll
    for (int q = 0; q < 4; ++q) {
      int i = wave * 16 + 4 * g + q;
      int idx;
      if (i < 16) idx = (i & 7) + 128 * (i >> 3) + 8 * l;
      else if (i < 24) idx = 256 + ((i - 16) & 3) + 64 * ((i - 16) >> 2) + 4 * l;
      else if (i < 32) idx = 384 + ((i - 24) & 1) + 32 * ((i - 24) >> 1) + 2 * l;
      else if (i < 40) idx = 512 + 16 * (i - 32) + l;
      else idx = (i == 40 && l == 0) ? 640 : -1;
      _Float16 hv = (_Float16)acc[q];
      if (idx >= 0) kc[idx] = __builtin_bit_cast(us, hv);
    }
  }
  __syncthreads();
  {
    char* rec = ws + WS_KREC + (size_t)d * KREC;
    u4 o;
    o.x = kc[gi0.x & 0xffff] | ((unsigned)kc[gi0.x >> 16] << 16);
    o.y = kc[gi0.y & 0xffff] | ((unsigned)kc[gi0.y >> 16] << 16);
    o.z = kc[gi0.z & 0xffff] | ((unsigned)kc[gi0.z >> 16] << 16);
    o.w = kc[gi0.w & 0xffff] | ((unsigned)kc[gi0.w >> 16] << 16);
    ((u4*)rec)[tid] = o;
    if (tid + 384 < KCHUNKS) {
      o.x = kc[gi1.x & 0xffff] | ((unsigned)kc[gi1.x >> 16] << 16);
      o.y = kc[gi1.y & 0xffff] | ((unsigned)kc[gi1.y >> 16] << 16);
      o.z = kc[gi1.z & 0xffff] | ((unsigned)kc[gi1.z >> 16] << 16);
      o.w = kc[gi1.w & 0xffff] | ((unsigned)kc[gi1.w >> 16] << 16);
      ((u4*)rec)[tid + 384] = o;
    }
    if (tid == 0) ((unsigned*)(ws + WS_MISC))[2 * d + 1] = (unsigned)kc[512] * 0x10001u;
  }
}

constexpr int SU_PAD = 256;
constexpr int SM_U = 0;
constexpr int SM_K = 4608;
constexpr int SM_S = SM_K + KREC;
constexpr int SM2_TOTAL = SM_S + 16 * 1024;

#define MFMA16(a, b, c) __builtin_amdgcn_mfma_f32_16x16x32_f16((a), (b), (c), 0, 0, 0)

__device__ __forceinline__ h8 ld_h8(const char* p) { return __builtin_bit_cast(h8, *(const u4*)p); }
__device__ __forceinline__ h8 ld_2x(const char* p0, const char* p1) {
  u2 a = *(const u2*)p0, b = *(const u2*)p1;
  u4 v; v.x = a.x; v.y = a.y; v.z = b.x; v.w = b.y; return __builtin_bit_cast(h8, v);
}
__device__ __forceinline__ u4 cvt_pair(f4 a, f4 b) {
  u4 v; v.x = pk_h(a[0], a[1]); v.y = pk_h(a[2], a[3]); v.z = pk_h(b[0], b[1]); v.w = pk_h(b[2], b[3]); return v;
}

__global__ __launch_bounds__(256) void k2_conv(char* __restrict__ ws) {
  __shared__ __attribute__((aligned(16))) char sm[SM2_TOTAL];
  const int tid = threadIdx.x, lane = tid & 63, wave = __builtin_amdgcn_readfirstlane(tid >> 6);
  const int d = blockIdx.x;
  const int col = lane & 15, g = lane >> 4;
  const u4* gu = (const u4*)(ws + WS_UT + (size_t)d * L_ * 2);
  const u4* gk = (const u4*)(ws + WS_KREC + (size_t)d * KREC);
  u4 ru = gu[tid];
  u4 rk0 = gk[tid], rk1 = gk[tid + 256];
  u4 rk2 = (tid < KCHUNKS - 512) ? gk[tid + 512] : u4{0, 0, 0, 0};
  float2 misc = ((const float2*)(ws + WS_MISC))[d];
  h8 box[6];
  if (wave < 2) {
#pragma unroll
    for (int m = 0; m < 6; ++m) box[m] = __builtin_bit_cast(h8, ((const u4*)g_box.v)[m * 64 + lane]);
  }
  u4 spm = ((const u4*)g_box.v)[6 * 64 + lane];
  if (tid < 32) *(u4*)(sm + SM_U + tid * 16) = u4{0, 0, 0, 0};
  *(u4*)(sm + SM_U + 512 + tid * 16) = ru;
  *(u4*)(sm + SM_K + tid * 16) = rk0;
  *(u4*)(sm + SM_K + 4096 + tid * 16) = rk1;
  if (tid < KCHUNKS - 512) *(u4*)(sm + SM_K + 8192 + tid * 16) = rk2;
  __syncthreads();

  const char* ub = sm + SM_U + (SU_PAD + 16 * col + 8 * g) * 2;
  if (wave == 0) {
#pragma unroll
    for (int tau = 0; tau < 4; ++tau) {
      h8 w1 = ld_h8(ub + (256 * tau - 16) * 2), w2 = ld_h8(ub + (256 * tau - 24) * 2);
      f4 z = {0.f, 0.f, 0.f, 0.f};
      f4 s0 = MFMA16(w1, box[0], z), s1 = MFMA16(w1, box[1], z);
      f4 s2 = MFMA16(w2, box[2], z), s3 = MFMA16(w2, box[3], z);
      f4 s4 = MFMA16(w1, box[4], z), s5 = MFMA16(w2, box[5], z);
      *(u4*)(sm + SM_S + (tau * 2 + 0) * 1024 + lane * 16) = cvt_pair(s0, s1);
      *(u4*)(sm + SM_S + (tau * 2 + 1) * 1024 + lane * 16) = cvt_pair(s2, s3);
      *(u4*)(sm + SM_S + (12 + tau) * 1024 + lane * 16) = cvt_pair(s4, s5);
    }
  } else if (wave == 1) {
#pragma unroll
    for (int tau = 4; tau < 6; ++tau) {
      h8 w1 = ld_h8(ub + (256 * tau - 16) * 2), w2 = ld_h8(ub + (256 * tau - 24) * 2);
      f4 z = {0.f, 0.f, 0.f, 0.f};
      f4 s0 = MFMA16(w1, box[0], z), s1 = MFMA16(w1, box[1], z);
      f4 s2 = MFMA16(w2, box[2], z), s3 = MFMA16(w2, box[3], z);
      *(u4*)(sm + SM_S + (tau * 2 + 0) * 1024 + lane * 16) = cvt_pair(s0, s1);
      *(u4*)(sm + SM_S + (tau * 2 + 1) * 1024 + lane * 16) = cvt_pair(s2, s3);
    }
  }
  h8 A[17];
  {
    const int b = col, s = (15 - b) & 3, i0 = ((15 - b) & ~3) + 8 * g;
    const char* ka = sm + SM_K + (NEAR_OFF + s * NEAR_PITCH + i0) * 2;
#pragma unroll
    for (int k = 0; k < 17; ++k) A[k] = ld_2x(ka + k * 64, ka + k * 64 + 8);
  }
  const int n0 = wave * 2;
  f4 acc0 = {0.f, 0.f, 0.f, 0.f}, acc1 = {0.f, 0.f, 0.f, 0.f};
  {
    const char* fb = ub + (256 * n0 - 528) * 2;
    if (wave == 0) {
#pragma unroll
      for (int m = 9; m < 25; ++m) {
        h8 f = ld_h8(fb + m * 64);
        if (m <= 16) acc0 = MFMA16(A[m], f, acc0);
        acc1 = MFMA16(A[m - 8], f, acc1);
      }
    } else {
#pragma unroll
      for (int m = 0; m < 25; ++m) {
        h8 f = ld_h8(fb + m * 64);
        if (m <= 16) acc0 = MFMA16(A[m], f, acc0);
        if (m >= 8) acc1 = MFMA16(A[m - 8], f, acc1);
      }
    }
  }
  __syncthreads();
  if (wave >= 1) {
    const int a = col;
    {
      const int ib = 79 - a + 4 * g, s = ib & 3, i0 = ib - s;
      const char* kb = sm + SM_K + (F2_OFF + s * F2_PITCH + i0) * 2;
#pragma unroll
      for (int dl = 2; dl <= 4; ++dl) {
        h8 tA = ld_2x(kb + (0 * 4 * F2_PITCH - 16 * dl) * 2, kb + (1 * 4 * F2_PITCH - 16 * dl) * 2);
        h8 tB = ld_2x(kb + (2 * 4 * F2_PITCH - 16 * dl) * 2, kb + (3 * 4 * F2_PITCH - 16 * dl) * 2);
        int t0 = n0 - dl, t1 = n0 + 1 - dl;
        if (t0 >= 0) {
          acc0 = MFMA16(ld_h8(sm + SM_S + (t0 * 2 + 0) * 1024 + lane * 16), tA, acc0);
          acc0 = MFMA16(ld_h8(sm + SM_S + (t0 * 2 + 1) * 1024 + lane * 16), tB, acc0);
        }
        if (t1 >= 0) {
          acc1 = MFMA16(ld_h8(sm + SM_S + (t1 * 2 + 0) * 1024 + lane * 16), tA, acc1);
          acc1 = MFMA16(ld_h8(sm + SM_S + (t1 * 2 + 1) * 1024 + lane * 16), tB, acc1);
        }
      }
    }
    if (wave >= 2) {
      const int ib = 127 - a + 4 * g, s = ib & 3, i0 = ib - s;
      const char* kb = sm + SM_K + (F3_OFF + s * F3_PITCH + i0) * 2;
#pragma unroll
      for (int dl = 4; dl <= 7; ++dl) {
        int t0 = n0 - dl, t1 = n0 + 1 - dl;
        if (t1 >= 0) {
          h8 tt = ld_2x(kb + (-16 * dl) * 2, kb + (4 * F3_PITCH - 16 * dl) * 2);
          if (t0 >= 0) acc0 = MFMA16(ld_h8(sm + SM_S + (12 + t0) * 1024 + lane * 16), tt, acc0);
          acc1 = MFMA16(ld_h8(sm + SM_S + (12 + t1) * 1024 + lane * 16), tt, acc1);
        }
      }
      unsigned kv = __float_as_uint(misc.y);
      u4 sp; sp.x = spm.x & kv; sp.y = spm.y & kv; sp.z = spm.z & kv; sp.w = spm.w & kv;
      h8 As = __builtin_bit_cast(h8, sp);
      acc0 = MFMA16(As, ld_h8(ub + (256 * n0 - 1032) * 2), acc0);
      acc1 = MFMA16(As, ld_h8(ub + (256 * (n0 + 1) - 1032) * 2), acc1);
    }
  }
  {
    const float sc = misc.x;
    us* yt = (us*)(ws + WS_YT) + (size_t)d * L_ + 256 * n0 + 16 * col + 4 * g;
    u2 o;
    o.x = pk_bf(acc0[0] * sc, acc0[1] * sc); o.y = pk_bf(acc0[2] * sc, acc0[3] * sc);
    *(u2*)yt = o;
    o.x = pk_bf(acc1[0] * sc, acc1[1] * sc); o.y = pk_bf(acc1[2] * sc, acc1[3] * sc);
    *(u2*)(yt + 256) = o;
  }
}

__device__ __forceinline__ unsigned ord16(unsigned b) { return (b & 0x8000u) ? (~b & 0xffffu) : (b | 0x8000u); }

__global__ __launch_bounds__(320) void k3_ln2(const us* __restrict__ x, const us* __restrict__ Dskip,
                                              const us* __restrict__ wr, us* __restrict__ out,
                                              char* __restrict__ ws) {
  __shared__ float red[5 * 24 + 5 * 8];
  const int tid = threadIdx.x, lane = tid & 63, wave = __builtin_amdgcn_readfirstlane(tid >> 6);
  const int bid = blockIdx.x;
  const int rg = (bid & 7) * 32 + (bid >> 3);
  const int t0 = rg * 8;
  const unsigned* xr = (const unsigned*)(x + (size_t)t0 * D_) + tid;
  unsigned raw[8];
#pragma unroll
  for (int r = 0; r < 8; ++r) raw[r] = xr[r * (D_ / 2)];
  const us* yt = (const us*)(ws + WS_YT);
  u4 y0 = *(const u4*)(yt + (size_t)(2 * tid) * L_ + t0);
  u4 y1 = *(const u4*)(yt + (size_t)(2 * tid + 1) * L_ + t0);
  unsigned dsk = ((const unsigned*)Dskip)[tid], wrr = ((const unsigned*)wr)[tid];
  const float2* st1 = (const float2*)(ws + WS_ST1) + t0;
  float h[8][2];
  const float d0 = bflo(dsk), d1 = bfhi(dsk);
#pragma unroll
  for (int r = 0; r < 8; ++r) {
    float2 st = st1[r];
    float rs = __builtin_amdgcn_rcpf(st.y);
    float xa = bflo(raw[r]), xb = bfhi(raw[r]);
    float ua = rbf(div_acc(rbf(xa - st.x), st.y, rs));
    float ub = rbf(div_acc(rbf(xb - st.x), st.y, rs));
    unsigned ya = y0[r >> 1], yb = y1[r >> 1];
    float yaf = (r & 1) ? bfhi(ya) : bflo(ya), ybf = (r & 1) ? bfhi(yb) : bflo(yb);
    h[r][0] = rbf(xa + rbf(yaf + rbf(ua * d0)));
    h[r][1] = rbf(xb + rbf(ybf + rbf(ub * d1)));
  }
  float m[8], s[8];
  ln_stats8(h, red, wave, lane, m, s);
  float x2[8][2], pr[16];
  const float w0 = bflo(wrr), w1 = bfhi(wrr);
  unsigned* outr = (unsigned*)(out + (size_t)t0 * D_) + tid;
#pragma unroll
  for (int r = 0; r < 8; ++r) {
    float rs = __builtin_amdgcn_rcpf(s[r]);
    x2[r][0] = rbf(div_acc(rbf(h[r][0] - m[r]), s[r], rs));
    x2[r][1] = rbf(div_acc(rbf(h[r][1] - m[r]), s[r], rs));
    pr[r] = __builtin_fmaf(x2[r][0], w0, x2[r][1] * w1);
    pr[r + 8] = 0.f;
    outr[r * (D_ / 2)] = pk_bf(h[r][0] + x2[r][0], h[r][1] + x2[r][1]);
  }
  float o[4];
  wave_reduce16(pr, o);
  float* red2 = red + 5 * 24;
  {
    int rr = lane >> 4;
    if ((lane & 15) == 0 && rr < 2) {
#pragma unroll
      for (int i = 0; i < 4; ++i) red2[wave * 8 + 4 * rr + i] = o[i];
    }
  }
  __syncthreads();
  int r8 = lane & 7;
  float rsum = red2[r8] + red2[8 + r8] + red2[16 + r8] + red2[24 + r8] + red2[32 + r8];
  unsigned key = (ord16(f2bf(rsum)) << 16) | (unsigned)(2047 - (t0 + r8));
  unsigned best = 0;
#pragma unroll
  for (int q = 0; q < 8; ++q) { unsigned k = (unsigned)__builtin_amdgcn_readlane((int)key, q); best = k > best ? k : best; }
  int brow = (2047 - (int)(best & 0xffffu)) - t0;
  float xa = x2[0][0], xb = x2[0][1], ha = h[0][0], hb = h[0][1];
#pragma unroll
  for (int r = 1; r < 8; ++r)
    if (brow == r) { xa = x2[r][0]; xb = x2[r][1]; ha = h[r][0]; hb = h[r][1]; }
  u2 c; c.x = pk_bf(xa, ha); c.y = pk_bf(xb, hb);
  ((u2*)(ws + WS_CAND))[(size_t)bid * (D_ / 2) + tid] = c;
  if (tid == 0) ((unsigned*)(ws + WS_KEYS))[bid] = best;
}

__device__ __forceinline__ void find_winner(const char* ws, int lane, unsigned& bestKey, int& bestBlk) {
  u4 k4 = ((const u4*)(ws + WS_KEYS))[lane];
  unsigned b = k4.x; int bi = 4 * lane;
  if (k4.y > b) { b = k4.y; bi = 4 * lane + 1; }
  if (k4.z > b) { b = k4.z; bi = 4 * lane + 2; }
  if (k4.w > b) { b = k4.w; bi = 4 * lane + 3; }
  unsigned m = b;
#pragma unroll
  for (int o = 32; o > 0; o >>= 1) { unsigned t = __shfl_xor(m, o, 64); m = t > m ? t : m; }
  unsigned long long ball = __ballot(b == m);
  int src = __builtin_ctzll(ball);
  bestKey = m;
  bestBlk = __builtin_amdgcn_readlane(bi, src);
}

__device__ __forceinline__ void fma8(float (&acc)[8], u4 w, float xv) {
  acc[0] = __builtin_fmaf(bflo(w.x), xv, acc[0]); acc[1] = __builtin_fmaf(bfhi(w.x), xv, acc[1]);
  acc[2] = __builtin_fmaf(bflo(w.y), xv, acc[2]); acc[3] = __builtin_fmaf(bfhi(w.y), xv, acc[3]);
  acc[4] = __builtin_fmaf(bflo(w.z), xv, acc[4]); acc[5] = __builtin_fmaf(bfhi(w.z), xv, acc[5]);
  acc[6] = __builtin_fmaf(bflo(w.w), xv, acc[6]); acc[7] = __builtin_fmaf(bfhi(w.w), xv, acc[7]);
}

__global__ __launch_bounds__(320) void k4_ffn1(const us* __restrict__ wg, const us* __restrict__ wu,
                                               char* __restrict__ ws) {
  __shared__ float red[5 * 16];
  const int tid = threadIdx.x, lane = tid & 63, wave = __builtin_amdgcn_readfirstlane(tid >> 6);
  const int bid = blockIdx.x;
  const int cb = (bid & 7) * 24 + (bid >> 3);
  const size_t o0 = (size_t)tid * F_ + cb * 8, o1 = (size_t)(tid + 320) * F_ + cb * 8;
  u4 g0 = *(const u4*)(wg + o0), g1 = *(const u4*)(wg + o1);
  u4 q0 = *(const u4*)(wu + o0), q1 = *(const u4*)(wu + o1);
  unsigned key; int blk;
  find_winner(ws, lane, key, blk);
  const unsigned* cand = (const unsigned*)(ws + WS_CAND) + (size_t)blk * D_;
  float xa = bflo(cand[tid]), xb = bflo(cand[tid + 320]);
  float ag[8] = {0, 0, 0, 0, 0, 0, 0, 0}, au[8] = {0, 0, 0, 0, 0, 0, 0, 0};
  fma8(ag, g0, xa); fma8(ag, g1, xb); fma8(au, q0, xa); fma8(au, q1, xb);
  float v[16], o[4];
#pragma unroll
  for (int i = 0; i < 8; ++i) { v[i] = ag[i]; v[i + 8] = au[i]; }
  wave_reduce16(v, o);
  {
    int rr = lane >> 4;
    if ((lane & 15) == 0) {
#pragma unroll
      for (int i = 0; i < 4; ++i) red[wave * 16 + 4 * (rr & 1) + 8 * (rr >> 1) + i] = o[i];
    }
  }
  __syncthreads();
  if (tid < 8) {
    float gs = red[tid] + red[16 + tid] + red[32 + tid] + red[48 + tid] + red[64 + tid];
    float us_ = red[8 + tid] + red[24 + tid] + red[40 + tid] + red[56 + tid] + red[72 + tid];
    float gb = rbf(gs);
    float sl = rbf(gb / (1.f + __expf(-gb)));
    float a = rbf(sl * rbf(us_));
    ((float*)(ws + WS_ACT))[cb * 8 + tid] = a;
  }
}

__global__ __launch_bounds__(256) void k5_ffn2(const us* __restrict__ wd, us* __restrict__ out,
                                               char* __restrict__ ws) {
  __shared__ float red[4 * 8];
  const int tid = threadIdx.x, lane = tid & 63, wave = __builtin_amdgcn_readfirstlane(tid >> 6);
  const int bid = blockIdx.x;
  const int cb = (bid & 7) * 10 + (bid >> 3);
  u4 w[6];
#pragma unroll
  for (int k = 0; k < 6; ++k) w[k] = *(const u4*)(wd + (size_t)(tid + 256 * k) * D_ + cb * 8);
  float av[6];
#pragma unroll
  for (int k = 0; k < 6; ++k) av[k] = ((const float*)(ws + WS_ACT))[tid + 256 * k];
  unsigned key; int blk;
  find_winner(ws, lane, key, blk);
  unsigned cv = 0;
  if (tid < 8) cv = ((const unsigned*)(ws + WS_CAND))[(size_t)blk * D_ + cb * 8 + tid];
  float acc[8] = {0, 0, 0, 0, 0, 0, 0, 0};
#pragma unroll
  for (int k = 0; k < 6; ++k) fma8(acc, w[k], av[k]);
  float v[16], o[4];
#pragma unroll
  for (int i = 0; i < 8; ++i) { v[i] = acc[i]; v[i + 8] = 0.f; }
  wave_reduce16(v, o);
  {
    int rr = lane >> 4;
    if ((lane & 15) == 0 && rr < 2) {
#pragma unroll
      for (int i = 0; i < 4; ++i) red[wave * 8 + 4 * rr + i] = o[i];
    }
  }
  __syncthreads();
  if (tid < 8) {
    float ys = red[tid] + red[8 + tid] + red[16 + tid] + red[24 + tid];
    unsigned ob = key >> 16;
    unsigned rb = (ob & 0x8000u) ? (ob & 0x7fffu) : (~ob & 0xffffu);
    float rv = bf1((us)rb);
    float sg = rbf(1.f / (1.f + __expf(-rv)));
    float y = rbf(rbf(ys) * sg);
    float t = rbf(bflo(cv) + y);
    int row = 2047 - (int)(key & 0xffffu);
    out[(size_t)row * D_ + cb * 8 + tid] = f2bf(bfhi(cv) + t);
  }
}

extern "C" void kernel_launch(void* const* d_in, const int* in_sizes, int n_in, void* d_out, int out_size,
                              void* d_ws, size_t ws_size, hipStream_t stream) {
  const us* x = (const us*)d_in[0];
  const us* Are = (const us*)d_in[1];
  const us* Aim = (const us*)d_in[2];
  const us* Bre = (const us*)d_in[3];
  const us* Bim = (const us*)d_in[4];
  const us* Cre = (const us*)d_in[5];
  const us* Cim = (const us*)d_in[6];
  const us* logdt = (const us*)d_in[7];
  const us* Dskip = (const us*)d_in[8];
  const us* wr = (const us*)d_in[10];
  const us* wg = (const us*)d_in[11];
  const us* wu = (const us*)d_in[12];
  const us* wd = (const us*)d_in[13];
  us* out = (us*)d_out;
  char* ws = (char*)d_ws;
  k1_ln_kgen<<<256 + D_, 384, 0, stream>>>(x, Are, Aim, Bre, Bim, Cre, Cim, logdt, ws);
  k2_conv<<<D_, 256, 0, stream>>>(ws);
  k3_ln2<<<256, 320, 0, stream>>>(x, Dskip, wr, out, ws);
  k4_ffn1<<<192, 320, 0, stream>>>(wg, wu, ws);
  k5_ffn2<<<80, 256, 0, stream>>>(wd, out, ws);
}
```

```cpp
#include <hip/hip_runtime.h>
#include <hip/hip_bf16.h>
#include <stdint.h>

typedef _Float16 h8 __attribute__((ext_vector_type(8)));
typedef _Float16 h2 __attribute__((ext_vector_type(2)));
typedef float f4 __attribute__((ext_vector_type(4)));
typedef float f2 __attribute__((ext_vector_type(2)));
typedef unsigned u4 __attribute__((ext_vector_type(4)));
typedef unsigned u2 __attribute__((ext_vector_type(2)));
typedef unsigned short us;
typedef unsigned long long u64;
#define GAS __attribute__((address_space(1)))
#define LAS __attribute__((address_space(3)))

constexpr int L_ = 2048, D_ = 640, N_ = 192, F_ = 1536;
constexpr int LP = 2048 + 64;
#define STAMPD(kid, slot) do {} while (0)
#define STAMP(kid, slot) do {} while (0)
#define STAMPT(kid, slot, thr) do {} while (0)

constexpr int NR_LEN  = 576;
constexpr int F2_OFF  = 2 * NR_LEN;
constexpr int F3_OFF  = F2_OFF + 4 * 256;
constexpr int KREC_ELEMS = F3_OFF + 2 * 320;
constexpr int KREC = KREC_ELEMS * 2;
constexpr int KCHUNKS = KREC_ELEMS / 8;

constexpr size_t WS_FLAGS = 0;
constexpr size_t WS_KEYS  = WS_FLAGS;
constexpr size_t WS_ACT   = WS_FLAGS + 2048;
constexpr size_t WS_WSUM  = 8192;
constexpr size_t WS_UT    = 8192 + 256;
constexpr size_t WS_YT    = WS_UT + (size_t)D_ * LP * 2;
constexpr size_t WS_KREC  = WS_YT + (size_t)D_ * L_ * 2;
constexpr size_t WS_MISC  = WS_KREC + (size_t)D_ * KREC;
constexpr size_t WS_TOTAL = WS_MISC + D_ * 8;

constexpr int TAB_PITCH = 196;
constexpr int K1_KC_OFF = (16 + 48) * TAB_PITCH * 4;
constexpr int K1_RED_OFF = K1_KC_OFF + 1552;
constexpr int K1_LDS = K1_RED_OFF + 16;

constexpr int kc_of_v(int v) {
  if (v < 256)  { int h = v >> 7, r = v & 127; return ((h * 8 + (r & 7)) * 16) + (r >> 3); }
  if (v < 512)  { int w = v - 256, h = w >> 7, r = w & 127; return ((16 + h * 4 + ((r & 7) >> 1)) * 16) + (r >> 3); }
  if (v < 1024) { int w = v - 512, h = w >> 7, r = w & 127; return ((24 + h * 2 + ((r & 7) >> 2)) * 16) + (r >> 3); }
  if (v < 2048) { int w = v - 1024, h = w >> 7, r = w & 127; return ((32 + h) * 16) + (r >> 3); }
  return 40 * 16;
}
constexpr int bf16_round_int(int j) {
  if (j <= 256) return j;
  int step = j < 512 ? 2 : (j < 1024 ? 4 : 8);
  int lo = (j / step) * step, rem = j - lo;
  if (rem * 2 < step) return lo;
  if (rem * 2 > step) return lo + step;
  return ((lo / step) & 1) ? lo + step : lo;
}
constexpr int ZIDX = 768;

struct GatherTab { unsigned v[KREC_ELEMS]; };
constexpr GatherTab make_gather() {
  GatherTab t{};
  for (int i = 0; i < KREC_ELEMS; ++i) t.v[i] = K1_KC_OFF + ZIDX * 2;
  for (int e = 0; e < 2; ++e)
    for (int i = 0; i < NR_LEN; ++i) {
      int j = 543 - (i + e);
      if (j >= 0 && j < 515) t.v[e * NR_LEN + i] = (unsigned)(K1_KC_OFF + 2 * kc_of_v(bf16_round_int(j)));
    }
  for (int pi = 0; pi < 4; ++pi)
    for (int q = 0; q < 16; ++q)
      for (int s = 0; s < 4; ++s)
        for (int e = 0; e < 4; ++e) {
          int p = 79 - (4 * q + s + e);
          if (p >= 32 && p < 64) {
            int v = 16 * p + 8 * (pi >> 1) + ((pi & 1) ? 8 : 4);
            t.v[F2_OFF + pi * 256 + (q * 4 + s) * 4 + e] = (unsigned)(K1_KC_OFF + 2 * kc_of_v(v));
          }
        }
  for (int pi = 0; pi < 2; ++pi)
    for (int q = 0; q < 20; ++q)
      for (int s = 0; s < 4; ++s)
        for (int e = 0; e < 4; ++e) {
          int qq = 127 - (4 * q + s + e);
          if (qq >= 64 && qq < 128) {
            int v = 16 * qq + (pi ? 16 : 8);
            t.v[F3_OFF + pi * 320 + (q * 4 + s) * 4 + e] = (unsigned)(K1_KC_OFF + 2 * kc_of_v(v));
          }
        }
  return t;
}
__device__ const GatherTab g_gather = make_gather();

struct BoxTab { us v[7 * 64 * 8]; };
constexpr BoxTab make_box() {
  BoxTab t{};
  const int nn[6] = {3, 5, 3, 5, 7, 9};
  const int sh[6] = {3, 6, 11, 14, 5, 12};
  const int w0[6] = {-16, -16, -24, -24, -16, -24};
  for (int m = 0; m < 6; ++m)
    for (int lane = 0; lane < 64; ++lane)
      for (int j = 0; j < 8; ++j) {
        int b = lane & 15, k = 8 * (lane >> 4) + j;
        int idx = w0[m] + k;
        bool on = idx <= b - sh[m] && idx >= b - sh[m] - (nn[m] - 1);
        t.v[(m * 64 + lane) * 8 + j] = on ? 0x3C00 : 0;
      }
  for (int lane = 0; lane < 64; ++lane)
    for (int j = 0; j < 8; ++j) {
      int b = lane & 15, k = 8 * (lane >> 4) + j;
      t.v[(6 * 64 + lane) * 8 + j] = (k - b == 4 || k - b == 5) ? 0xFFFF : 0;
    }
  return t;
}
__device__ const BoxTab g_box = make_box();

__device__ __forceinline__ float bflo(unsigned w) { return __uint_as_float(w << 16); }
__device__ __forceinline__ float bfhi(unsigned w) { return __uint_as_float(w & 0xffff0000u); }
__device__ __forceinline__ float bf1(us v) { return __uint_as_float((unsigned)v << 16); }
__device__ __forceinline__ us f2bf(float f) { __bf16 b = (__bf16)f; return __builtin_bit_cast(us, b); }
__device__ __forceinline__ float rbf(float f) { return bf1(f2bf(f)); }
typedef __bf16 b2 __attribute__((ext_vector_type(2)));
__device__ __forceinline__ unsigned pk_bf(float a, float b) {
  b2 v; v.x = (__bf16)a; v.y = (__bf16)b; return __builtin_bit_cast(unsigned, v);
}
__device__ __forceinline__ unsigned pk_h(float a, float b) {
  h2 v; v.x = (_Float16)a; v.y = (_Float16)b; return __builtin_bit_cast(unsigned, v);
}
template <int CTRL> __device__ __forceinline__ float dpp_add(float v) {
  int t = __builtin_amdgcn_update_dpp(0, __float_as_int(v), CTRL, 0xf, 0xf, true);
  return v + __int_as_float(t);
}
__device__ __forceinline__ float row_sum(float v) {
  v = dpp_add<0x121>(v); v = dpp_add<0x122>(v); v = dpp_add<0x124>(v); v = dpp_add<0x128>(v);
  return v;
}
__device__ __forceinline__ float swap32_add(float a, float b) {
  auto r = __builtin_amdgcn_permlane32_swap(__float_as_uint(a), __float_as_uint(b), false, false);
  return __uint_as_float(r[0]) + __uint_as_float(r[1]);
}
__device__ __forceinline__ float swap16_add(float a, float b) {
  auto r = __builtin_amdgcn_permlane16_swap(__float_as_uint(a), __float_as_uint(b), false, false);
  return __uint_as_float(r[0]) + __uint_as_float(r[1]);
}
__device__ __forceinline__ void wave_reduce16(const float (&v)[16], float (&out)[4]) {
  float w[8];
#pragma unroll
  for (int i = 0; i < 8; ++i) w[i] = swap32_add(v[i], v[i + 8]);
#pragma unroll
  for (int i = 0; i < 4; ++i) out[i] = row_sum(swap16_add(w[i], w[i + 4]));
}
__device__ __forceinline__ void wave_reduce8(const float (&v)[8], float (&out)[2]) {
  float w[4];
#pragma unroll
  for (int i = 0; i < 4; ++i) w[i] = swap32_add(v[i], v[i + 4]);
#pragma unroll
  for (int i = 0; i < 2; ++i) out[i] = row_sum(swap16_add(w[i], w[i + 2]));
}
__device__ __forceinline__ float wave_reduce4(const float (&v)[4]) {
  float w0 = swap32_add(v[0], v[2]), w1 = swap32_add(v[1], v[3]);
  return row_sum(swap16_add(w0, w1));
}
__device__ __forceinline__ float rdlane(float v, int l) {
  return __int_as_float(__builtin_amdgcn_readlane(__float_as_int(v), l));
}

__device__ __forceinline__ f2 cmul(f2 a, f2 b) {
  f2 c;
  asm("v_pk_mul_f32 %0, %1, %2 op_sel:[0,0] op_sel_hi:[1,0]\n\tv_pk_fma_f32 %0, %1, %2, %0 op_sel:[1,1,0] op_sel_hi:[0,1,1] neg_lo:[1,0,0]"
      : "=&v"(c) : "v"(a), "v"(b));
  return c;
}
__device__ __forceinline__ f2 cexp_p(float lam, float rh, float rl, float p) {
  float mag = __builtin_amdgcn_exp2f(lam * p);
  float x = rh * p;
  float f = x - __builtin_rintf(x);
  f = __builtin_fmaf(rl, p, f);
  f2 w; w.x = mag * __builtin_amdgcn_cosf(f); w.y = mag * __builtin_amdgcn_sinf(f); return w;
}
#define MFMA16(a, b, c) __builtin_amdgcn_mfma_f32_16x16x32_f16((a), (b), (c), 0, 0, 0)
__device__ __forceinline__ h8 as_h8(u4 v) { return __builtin_bit_cast(h8, v); }

__device__ __forceinline__ void ln_from_sums(float s1, float s2, float piv, float& mb, float& sb) {
  float dm = s1 * (1.f / 640.f);
  float var = fmaxf(__builtin_fmaf(-dm, dm, s2 * (1.f / 640.f)), 0.f);
  mb = rbf(piv + dm);
  sb = rbf(__builtin_amdgcn_sqrtf(rbf(rbf(var) + 1e-5f)));
}
__device__ __forceinline__ f2 unpk(unsigned w) { return f2{bflo(w), bfhi(w)}; }

__global__ __launch_bounds__(320, 2) void k1_ln_kgen(
    const us* __restrict__ x, const us* __restrict__ Are, const us* __restrict__ Aim,
    const us* __restrict__ Bre, const us* __restrict__ Bim, const us* __restrict__ Cre,
    const us* __restrict__ Cim, const us* __restrict__ logdt, const us* __restrict__ wr,
    const us* __restrict__ wg, const us* __restrict__ wu, const us* __restrict__ wd, char* __restrict__ ws) {
  extern __shared__ __attribute__((aligned(16))) char smem[];
  const int tid = threadIdx.x, lane = tid & 63, wave = __builtin_amdgcn_readfirstlane(tid >> 6);
  const int bid = blockIdx.x;
  STAMP(0, 0);
  if (bid < 128) {
    const int t0 = ((bid & 7) * 16 + (bid >> 3)) * 16;
    const unsigned* xb = (const unsigned*)(x + (size_t)t0 * D_);
    unsigned raw[16];
#pragma unroll
    for (int r = 0; r < 16; ++r) raw[r] = xb[r * (D_ / 2) + tid];
    float* red = (float*)smem;
    f2* ab = (f2*)(smem + 1024);
    f2 a[16];
    float v[32];
    float mypiv = 0.f;
#pragma unroll
    for (int r = 0; r < 16; ++r) {
      const float piv = bflo(xb[r * (D_ / 2)]);
      if (tid == r) mypiv = piv;
      a[r] = unpk(raw[r]);
      f2 dd = a[r] - piv;
      v[r] = dd.x + dd.y; v[r + 16] = __builtin_fmaf(dd.x, dd.x, dd.y * dd.y);
    }
    {
      float w[16], o[8];
#pragma unroll
      for (int i = 0; i < 16; ++i) w[i] = swap32_add(v[i], v[i + 16]);
#pragma unroll
      for (int i = 0; i < 8; ++i) o[i] = row_sum(swap16_add(w[i], w[i + 8]));
      if ((lane & 15) == 0) {
        float* dst = red + wave * 32 + (lane >> 4) * 8;
        *(f4*)dst = f4{o[0], o[1], o[2], o[3]}; *(f4*)(dst + 4) = f4{o[4], o[5], o[6], o[7]};
      }
    }
    __syncthreads();
    if (wave == 0) {
      const int r = lane & 15;
      float s1 = red[r] + red[32 + r] + red[64 + r] + red[96 + r] + red[128 + r];
      float s2 = red[16 + r] + red[48 + r] + red[80 + r] + red[112 + r] + red[144 + r];
      float mb, sb;
      ln_from_sums(s1, s2, mypiv, mb, sb);
      float aa = 128.f * __builtin_amdgcn_rcpf(sb);
      if (lane < 16) ab[r] = f2{aa, -mb * aa};
    } else if (wave == 1 && bid == 0) {
      const unsigned* w2 = (const unsigned*)wr + lane;
      float sw = 0.f;
#pragma unroll
      for (int k = 0; k < 5; ++k) { unsigned q = w2[64 * k]; sw += bflo(q) + bfhi(q); }
      sw = row_sum(sw);
      sw = rdlane(sw, 0) + rdlane(sw, 16) + rdlane(sw, 32) + rdlane(sw, 48);
      if (lane == 0) *(float*)(ws + WS_WSUM) = sw;
    } else if (wave >= 1 && bid == 1) {
      ((u4*)(ws + WS_FLAGS))[tid - 64] = u4{0, 0, 0, 0};
      ((u4*)(ws + WS_FLAGS))[tid + 192] = u4{0, 0, 0, 0};
    }
    __syncthreads();
    unsigned p0[8], p1[8];
#pragma unroll
    for (int r = 0; r < 16; r += 2) {
      f4 c = *(const f4*)(ab + r);
      f2 u0 = __builtin_elementwise_fma(a[r], f2{c.x, c.x}, f2{c.y, c.y});
      f2 u1 = __builtin_elementwise_fma(a[r + 1], f2{c.z, c.z}, f2{c.w, c.w});
      p0[r >> 1] = pk_h(u0.x, u1.x); p1[r >> 1] = pk_h(u0.y, u1.y);
    }
    us* ut = (us*)(ws + WS_UT) + (size_t)(2 * tid) * LP + t0;
    *(u4*)(ut) = u4{p0[0], p0[1], p0[2], p0[3]};
    *(u4*)(ut + 8) = u4{p0[4], p0[5], p0[6], p0[7]};
    *(u4*)(ut + LP) = u4{p1[0], p1[1], p1[2], p1[3]};
    *(u4*)(ut + LP + 8) = u4{p1[4], p1[5], p1[6], p1[7]};
    unsigned pf0, pf1;
    {
      const int xcd = bid & 7;
      int id = (bid >> 3) * 640 + tid;
      int r3 = id / 3, q3 = id - r3 * 3;
      const us* pa = (r3 < 640 ? wg + (size_t)r3 * F_ : wu + (size_t)(r3 - 640) * F_) + xcd * 192 + q3 * 64;
      int i2 = id - 3840; i2 = i2 < 3072 ? i2 : 3071;
      const us* pb = wd + (size_t)(i2 >> 1) * D_ + xcd * 80 + (i2 & 1) * 78;
      pf0 = *(const unsigned*)(id < 3840 ? pa : pb);
      id += 320;
      r3 = id / 3; q3 = id - r3 * 3;
      pa = (r3 < 640 ? wg + (size_t)r3 * F_ : wu + (size_t)(r3 - 640) * F_) + xcd * 192 + q3 * 64;
      i2 = id - 3840; i2 = i2 < 3072 ? i2 : 3071;
      pb = wd + (size_t)(i2 >> 1) * D_ + xcd * 80 + (i2 & 1) * 78;
      pf1 = *(const unsigned*)(id < 3840 ? pa : pb);
    }
    STAMP(0, 5);
    asm volatile("" :: "v"(pf0), "v"(pf1));
    STAMPD(0, 6);
    return;
  }
  if (wave >= 3) return;
  const int d = bid - 128, n = tid;
  char* kc = smem + K1_KC_OFF;
  const size_t pidx = (size_t)d * N_ + n;
  us r_are = Are[pidx], r_aim = Aim[pidx], r_bre = Bre[pidx], r_bim = Bim[pidx], r_cre = Cre[pidx], r_cim = Cim[pidx];
  us r_ldt = logdt[d];
  const u4* gt = (const u4*)g_gather.v;
  u4 ga0 = gt[2 * tid], ga1 = gt[2 * tid + 1], gb0 = gt[2 * (tid + 160)], gb1 = gt[2 * (tid + 160) + 1];
  float dt = rbf(__expf(bf1(r_ldt)));
  float ar = dt * bf1(r_are), ai = dt * bf1(r_aim);
  f2 dB = {dt * bf1(r_bre), dt * bf1(r_bim)};
  f2 Cc = {bf1(r_cre), bf1(r_cim)};
  f2 CB = cmul(Cc, dB);
  const float LOG2E = 1.4426950408889634f;
  const float I2PH = 0.15915494309189535f;
  const float I2PL = (float)(0.15915494309189533577 - (double)0.15915494309189535f);
  float lam = ar * LOG2E;
  float rh = ai * I2PH;
  float rl = __builtin_fmaf(ai, I2PL, __builtin_fmaf(ai, I2PH, -rh));
  {
    float g = fmaxf(1.f, __builtin_amdgcn_exp2f(lam * 2048.f));
    float bnd = row_sum((fabsf(CB.x) + fabsf(CB.y)) * g);
    if ((lane & 15) == 0) ((float*)(kc))[wave * 4 + (lane >> 4)] = bnd;
  }
  f2 w1 = cexp_p(lam, rh, rl, 1.f), w16 = cexp_p(lam, rh, rl, 16.f), w256 = cexp_p(lam, rh, rl, 256.f);
  f2 w2 = cmul(w1, w1), w32 = cmul(w16, w16), w512 = cmul(w256, w256);
  f2 w4 = cmul(w2, w2), w64 = cmul(w32, w32), w1024 = cmul(w512, w512);
  f2 w8 = cmul(w4, w4), w128 = cmul(w64, w64);
  const unsigned ta = (unsigned)n * 4u;
#define DSW(row, val) asm volatile("ds_write_b32 %0, %1 offset:%2" :: "v"(ta), "v"(val), "i"((row) * TAB_PITCH * 4) : "memory")
#define PUTE(row, c) do { f2 c_ = (c); DSW((row), pk_h(c_.x, -c_.y)); } while (0)
#define PUT(row, c) do { f2 c_ = (c); DSW(16 + (row), pk_h(c_.x, c_.y)); } while (0)
  {
    f2 e[8];
    e[1] = w8; e[2] = w16; e[3] = cmul(w8, w16); e[4] = w32;
    e[5] = cmul(w32, e[1]); e[6] = cmul(w32, e[2]); e[7] = cmul(w32, e[3]);
    DSW(0, 0x00003C00u);
#pragma unroll
    for (int l = 1; l < 8; ++l) PUTE(l, e[l]);
    PUTE(8, w64);
#pragma unroll
    for (int l = 1; l < 8; ++l) PUTE(8 + l, cmul(w64, e[l]));
  }
  __syncthreads();
  STAMP(0, 1);
  {
    f4 q0 = *(const f4*)(kc), q1 = *(const f4*)(kc + 16), q2 = *(const f4*)(kc + 32);
    q0 += q1; q0 += q2;
    float sig = (q0.x + q0.y) + (q0.z + q0.w);
    int ex = (int)(__float_as_uint(sig) >> 23) & 255; ex = ex < 24 ? 24 : (ex > 254 ? 254 : ex);
    float sc = __uint_as_float((unsigned)(268 - ex) << 23);
    if (tid == 0) { ((float*)(ws + WS_MISC))[2 * d] = __uint_as_float((unsigned)(ex - 21) << 23); *(unsigned*)(kc + ZIDX * 2) = 0; }
    f2 c[8];
    c[0] = CB * sc;
    c[1] = cmul(c[0], w1); c[2] = cmul(c[0], w2); c[4] = cmul(c[0], w4); c[3] = cmul(c[1], w2);
    c[5] = cmul(c[1], w4); c[6] = cmul(c[2], w4); c[7] = cmul(c[3], w4);
#pragma unroll
    for (int r = 0; r < 8; ++r) {
      PUT(r, c[r]);
      f2 ch = cmul(c[r], w128); PUT(8 + r, ch);
      if ((r & 1) == 0) {
        f2 s0 = cmul(c[r], w256), s1 = cmul(ch, w256);
        PUT(16 + (r >> 1), s0); PUT(20 + (r >> 1), s1);
        if ((r & 3) == 0) {
          const int rho = r >> 2;
          f2 z0 = cmul(c[r], w512), z1 = cmul(ch, w512), z2 = cmul(s0, w512), z3 = cmul(s1, w512);
          PUT(24 + 0 + rho, z0); PUT(24 + 2 + rho, z1); PUT(24 + 4 + rho, z2); PUT(24 + 6 + rho, z3);
          if (r == 0) {
            f2 y0 = cmul(c[0], w1024);
            PUT(32, y0); PUT(33, cmul(ch, w1024)); PUT(34, cmul(s0, w1024)); PUT(35, cmul(s1, w1024));
            PUT(36, cmul(z0, w1024)); PUT(37, cmul(z1, w1024)); PUT(38, cmul(z2, w1024)); PUT(39, cmul(z3, w1024));
            PUT(40, cmul(y0, w1024));
          }
        }
      }
    }
  }
  asm volatile("s_waitcnt lgkmcnt(0)" ::: "memory");
  __syncthreads();
  STAMP(0, 2);
  {
    const unsigned* tab = (const unsigned*)smem;
    f4 acc = {0.f, 0.f, 0.f, 0.f};
    const int row = lane & 15, g = lane >> 4;
    const unsigned* pa = tab + row * TAB_PITCH + g * 4;
    const unsigned* pb = tab + (16 + wave * 16 + row) * TAB_PITCH + g * 4;
#pragma unroll
    for (int kk = 0; kk < 12; ++kk)
      acc = MFMA16(as_h8(*(const u4*)(pa + kk * 16)), as_h8(*(const u4*)(pb + kk * 16)), acc);
    u2 o; o.x = pk_h(acc[0], acc[1]); o.y = pk_h(acc[2], acc[3]);
    *(u2*)(kc + ((wave * 16 + row) * 16 + 4 * g) * 2) = o;
  }
  __syncthreads();
  STAMP(0, 3);
  {
    char* rec = ws + WS_KREC + (size_t)d * KREC;
#define LDU(a) ((unsigned)*(const LAS us*)(size_t)(a))
#define LDP(a, b) (LDU(a) | (LDU(b) << 16))
    u4 o0 = {LDP(ga0.x, ga0.y), LDP(ga0.z, ga0.w), LDP(ga1.x, ga1.y), LDP(ga1.z, ga1.w)};
    u4 o1 = {LDP(gb0.x, gb0.y), LDP(gb0.z, gb0.w), LDP(gb1.x, gb1.y), LDP(gb1.z, gb1.w)};
    ((u4*)rec)[tid] = o0;
    if (tid >= 32) ((u4*)rec)[tid + 160] = o1;
    if (tid == 0) ((unsigned*)(ws + WS_MISC))[2 * d + 1] = (unsigned)*(const us*)(kc + 32 * 16 * 2) * 0x10001u;
  }
  STAMP(0, 4);
  STAMPD(0, 7);
}

constexpr int SM_U = 0;
constexpr int SM_N = 4608;
constexpr int SM_F2 = SM_N + 560 * 16;
constexpr int SM_F3 = SM_F2 + 2048;
constexpr int SM_P = SM_F3 + 1280;
constexpr int K2_LDS = SM_P + 10 * 1024;

__device__ __forceinline__ h8 ld_h8(const char* p) { return as_h8(*(const u4*)p); }
__device__ __forceinline__ h8 ld_2x(const char* p0, const char* p1) {
  u2 a = *(const u2*)p0, b = *(const u2*)p1;
  return as_h8(u4{a.x, a.y, b.x, b.y});
}
__device__ __forceinline__ h8 cvt_pair(f4 a, f4 b) {
  return as_h8(u4{pk_h(a[0], a[1]), pk_h(a[2], a[3]), pk_h(b[0], b[1]), pk_h(b[2], b[3])});
}
__device__ __forceinline__ void glds16(const void* g, void* l) {
  __builtin_amdgcn_global_load_lds((const GAS unsigned*)g, (LAS unsigned*)l, 16, 0, 0);
}
__device__ __forceinline__ void conv_epi(f4 acc, const char* up, float sc, float dq, us* dst) {
  u2 uu = *(const u2*)up;
  const unsigned ux = uu.x, uy = uu.y;
  h2 ua = __builtin_bit_cast(h2, ux), ub = __builtin_bit_cast(h2, uy);
  unsigned z0 = pk_bf((float)ua.x * dq, (float)ua.y * dq), z1 = pk_bf((float)ub.x * dq, (float)ub.y * dq);
  unsigned y0 = pk_bf(acc[0] * sc, acc[1] * sc), y1 = pk_bf(acc[2] * sc, acc[3] * sc);
  u2 o;
  o.x = pk_bf(bflo(y0) + bflo(z0), bfhi(y0) + bfhi(z0));
  o.y = pk_bf(bflo(y1) + bflo(z1), bfhi(y1) + bfhi(z1));
  *(u2*)dst = o;
}

__global__ __launch_bounds__(256, 2) void k2_conv(const us* __restrict__ Dskip, char* __restrict__ ws) {
  extern __shared__ __attribute__((aligned(16))) char sm[];
  const int tid = threadIdx.x, lane = tid & 63, wave = __builtin_amdgcn_readfirstlane(tid >> 6);
  const int d = (blockIdx.x & 7) * 80 + (blockIdx.x >> 3);
  const int col = lane & 15, g = lane >> 4;
  STAMP(1, 0);
  {
    const char* gu = ws + WS_UT + (size_t)d * LP * 2;
    const char* gk = ws + WS_KREC + (size_t)d * KREC;
    glds16(gu + tid * 16, sm + SM_U + 512 + wave * 1024);
#pragma unroll
    for (int k = 0; k < 3; ++k) {
      int c = tid + 256 * k;
      int q = c >> 3, s = (c & 7) ^ (((q >> 1) & 1) << 2);
      int offn = (s & 1) * (NR_LEN * 2) + (s & 6) * 2 + q * 16;
      int offf = F2_OFF * 2 + (c - 560) * 16;
      int off = (k < 2 || c < 560) ? offn : offf;
      glds16(gk + off, sm + SM_N + (wave * 64 + 256 * k) * 16);
    }
  }
  float2 misc = ((const float2*)(ws + WS_MISC))[d];
  const float dq = bf1(Dskip[d]) * (1.f / 128.f);
  u4 bx0, bx1, bx2, bx3;
  if (wave >= 2) {
    const u4* bt = (const u4*)g_box.v + lane;
    int o = (wave == 2) ? 0 : 4 * 64;
    bx0 = bt[o]; bx1 = bt[o + 64]; bx2 = bt[o + 128]; bx3 = bt[(wave == 2) ? 192 : 0];
  }
  if (tid < 32) *(u4*)(sm + SM_U + tid * 16) = u4{0, 0, 0, 0};
  __syncthreads();
  STAMP(1, 1);
  const char* ub = sm + SM_U + (256 + 16 * col + 8 * g) * 2;
  const char* ue = sm + SM_U + (256 + 16 * col + 4 * g) * 2;
  us* yt = (us*)(ws + WS_YT) + ((size_t)(2 * col + (g >> 1)) * D_ + d) * 8 + (g & 1) * 4;
  constexpr int YTS = 32 * D_ * 8;
  const char* ka;
  {
    const int s = (15 - col) & 7, q = (((15 - col) & 8) >> 3) + g;
    ka = sm + SM_N + (q * 8 + (s ^ (((q >> 1) & 1) << 2))) * 16;
  }
  const f4 Z = {0.f, 0.f, 0.f, 0.f};
  if (wave < 2) {
    const int n0 = 2 + 3 * wave;
    h8 A[17];
#pragma unroll
    for (int k = 0; k < 17; ++k) A[k] = ld_h8(ka + k * 512);
    f4 a0 = Z, a1 = Z, a2 = Z;
    const char* fb = ub + (256 * n0 - 528) * 2;
#pragma unroll
    for (int m = 0; m < 33; ++m) {
      h8 f = ld_h8(fb + m * 64);
      if (m <= 16) a0 = MFMA16(A[m], f, a0);
      if (m >= 8 && m <= 24) a1 = MFMA16(A[m - 8], f, a1);
      if (m >= 16) a2 = MFMA16(A[m - 16], f, a2);
    }
    __syncthreads();
    const char* pp = sm + SM_P + lane * 16;
    a0 += *(const f4*)(pp + (n0 - 2) * 1024);
    a1 += *(const f4*)(pp + (n0 - 1) * 1024);
    a2 += *(const f4*)(pp + (n0 - 0) * 1024);
    if (wave == 0) a2 += *(const f4*)(pp + 6 * 1024);
    else { a0 += *(const f4*)(pp + 7 * 1024); a1 += *(const f4*)(pp + 8 * 1024); a2 += *(const f4*)(pp + 9 * 1024); }
    conv_epi(a0, ue + (256 * n0) * 2, misc.x, dq, yt + YTS * n0);
    conv_epi(a1, ue + (256 * (n0 + 1)) * 2, misc.x, dq, yt + YTS * (n0 + 1));
    conv_epi(a2, ue + (256 * (n0 + 2)) * 2, misc.x, dq, yt + YTS * (n0 + 2));
  } else if (wave == 2) {
    const int ib = 79 - col + 4 * g;
    const char* kb = sm + SM_F2 + ((ib >> 2) * 4 + (ib & 3)) * 8;
    h8 tA[3], tB[3];
#pragma unroll
    for (int dl = 2; dl <= 4; ++dl) {
      tA[dl - 2] = ld_2x(kb - dl * 128, kb + 512 - dl * 128);
      tB[dl - 2] = ld_2x(kb + 1024 - dl * 128, kb + 1536 - dl * 128);
    }
    f4 acc[6] = {Z, Z, Z, Z, Z, Z};
#pragma unroll
    for (int tau = 0; tau < 6; ++tau) {
      h8 w1 = ld_h8(ub + (256 * tau - 16) * 2), w2 = ld_h8(ub + (256 * tau - 24) * 2);
      h8 sa = cvt_pair(MFMA16(w1, as_h8(bx0), Z), MFMA16(w1, as_h8(bx1), Z));
      h8 sb = cvt_pair(MFMA16(w2, as_h8(bx2), Z), MFMA16(w2, as_h8(bx3), Z));
#pragma unroll
      for (int dl = 2; dl <= 4; ++dl) {
        int nn = tau + dl;
        if (nn <= 7) { acc[nn - 2] = MFMA16(sa, tA[dl - 2], acc[nn - 2]); acc[nn - 2] = MFMA16(sb, tB[dl - 2], acc[nn - 2]); }
      }
    }
#pragma unroll
    for (int i = 0; i < 6; ++i) *(f4*)(sm + SM_P + i * 1024 + lane * 16) = acc[i];
    __syncthreads();
  } else {
    const int ib = 127 - col + 4 * g;
    const char* kb = sm + SM_F3 + ((ib >> 2) * 4 + (ib & 3)) * 8;
    h8 tt[4];
#pragma unroll
    for (int dl = 4; dl <= 7; ++dl) tt[dl - 4] = ld_2x(kb - dl * 128, kb + 640 - dl * 128);
    unsigned kv = __float_as_uint(misc.y);
    h8 As = as_h8(u4{bx2.x & kv, bx2.y & kv, bx2.z & kv, bx2.w & kv});
    f4 acc[4];
#pragma unroll
    for (int i = 0; i < 4; ++i) acc[i] = MFMA16(As, ld_h8(ub + (256 * (4 + i) - 1032) * 2), Z);
#pragma unroll
    for (int tau = 0; tau < 4; ++tau) {
      h8 w1 = ld_h8(ub + (256 * tau - 16) * 2), w2 = ld_h8(ub + (256 * tau - 24) * 2);
      h8 s3 = cvt_pair(MFMA16(w1, as_h8(bx0), Z), MFMA16(w2, as_h8(bx1), Z));
#pragma unroll
      for (int dl = 4; dl <= 7; ++dl)
        if (tau + dl <= 7) acc[tau + dl - 4] = MFMA16(s3, tt[dl - 4], acc[tau + dl - 4]);
    }
#pragma unroll
    for (int i = 0; i < 4; ++i) *(f4*)(sm + SM_P + (6 + i) * 1024 + lane * 16) = acc[i];
    h8 A[17];
#pragma unroll
    for (int k = 1; k < 17; ++k) A[k] = ld_h8(ka + k * 512);
    f4 a0 = Z, a1 = Z;
    const char* fb = ub - 528 * 2;
#pragma unroll
    for (int m = 9; m < 25; ++m) {
      h8 f = ld_h8(fb + m * 64);
      if (m <= 16) a0 = MFMA16(A[m], f, a0);
      a1 = MFMA16(A[m - 8], f, a1);
    }
    __syncthreads();
    conv_epi(a0, ue, misc.x, dq, yt);
    conv_epi(a1, ue + 512, misc.x, dq, yt + YTS);
  }
  STAMP(1, 4);
  STAMPD(1, 5);
}

__device__ __forceinline__ unsigned ord16(unsigned b) { return (b & 0x8000u) ? (~b & 0xffffu) : (b | 0x8000u); }
__device__ __forceinline__ void fma8(float* acc, u4 w, float xv) {
  acc[0] = __builtin_fmaf(bflo(w.x), xv, acc[0]); acc[1] = __builtin_fmaf(bfhi(w.x), xv, acc[1]);
  acc[2] = __builtin_fmaf(bflo(w.y), xv, acc[2]); acc[3] = __builtin_fmaf(bfhi(w.y), xv, acc[3]);
  acc[4] = __builtin_fmaf(bflo(w.z), xv, acc[4]); acc[5] = __builtin_fmaf(bfhi(w.z), xv, acc[5]);
  acc[6] = __builtin_fmaf(bflo(w.w), xv, acc[6]); acc[7] = __builtin_fmaf(bfhi(w.w), xv, acc[7]);
}
#define RLX_AGENT __ATOMIC_RELAXED, __HIP_MEMORY_SCOPE_AGENT
constexpr unsigned SPIN_CAP = 1u << 22;

__global__ __launch_bounds__(384, 2) void k3_tail(const us* __restrict__ x, const us* __restrict__ wr,
                                                  const us* __restrict__ wg, const us* __restrict__ wu,
                                                  const us* __restrict__ wd, us* __restrict__ out,
                                                  char* __restrict__ ws) {
  __shared__ __attribute__((aligned(16))) float red[5 * 24 + 16 + 5 * 16 + 4 * 8 + 8];
  float* redA = red;
  float* abv = red + 120;
  float* redC = red + 136;
  float* redD = red + 216;
  unsigned* redW = (unsigned*)(red + 248);
  const int tid = threadIdx.x, lane = tid & 63, wave = __builtin_amdgcn_readfirstlane(tid >> 6);
  const int bid = blockIdx.x;
  const int tb = (bid & 7) * 32 + (bid >> 3);
  const int t0 = tb * 8;
  STAMP(2, 0);
  const us* ytb = (const us*)(ws + WS_YT);
  const unsigned* xb = (const unsigned*)(x + (size_t)t0 * D_);
  const bool f1 = bid < 192, f2r = bid >= 176;
  const int cb1 = (bid & 7) * 24 + (bid >> 3);
  const int cb2 = (bid & 7) * 10 + ((bid - 176) >> 3);
  if (wave == 5) {
    const int r = lane & 7;
    const float piv = bf1(x[(size_t)(t0 + r) * D_]);
    const float W = *(const float*)(ws + WS_WSUM);
    __syncthreads();
    float s1 = redA[r] + redA[24 + r] + redA[48 + r] + redA[72 + r] + redA[96 + r];
    float s2 = redA[8 + r] + redA[32 + r] + redA[56 + r] + redA[80 + r] + redA[104 + r];
    float s3 = redA[16 + r] + redA[40 + r] + redA[64 + r] + redA[88 + r] + redA[112 + r];
    float mb, sb;
    ln_from_sums(s1, s2, piv, mb, sb);
    float rs = __builtin_amdgcn_rcpf(sb);
    float rv = rs * __builtin_fmaf(piv - mb, W, s3);
    unsigned key = 0x80000000u | (ord16(f2bf(rv)) << 11) | (unsigned)(2047 - (t0 + r));
    unsigned best = key;
    best = max(best, (unsigned)__builtin_amdgcn_update_dpp(0, (int)best, 0x111, 0xf, 0xf, true));
    best = max(best, (unsigned)__builtin_amdgcn_update_dpp(0, (int)best, 0x112, 0xf, 0xf, true));
    best = max(best, (unsigned)__builtin_amdgcn_update_dpp(0, (int)best, 0x114, 0xf, 0xf, true));
    best = (unsigned)__builtin_amdgcn_readlane((int)best, 7);
    if (lane < 8) {
      if (key == best) {
        u64 gr = ((u64)best << 32) | (u64)((__float_as_uint(mb) >> 16) | (__float_as_uint(sb) & 0xffff0000u));
        __hip_atomic_store((GAS u64*)(ws + WS_KEYS) + bid, gr, RLX_AGENT);
        redW[0] = (unsigned)r; redW[1] = best;
      }
      *(f2*)(abv + 2 * r) = f2{rs, -mb * rs};
    }
    STAMPT(2, 2, 320);
    __syncthreads();
    GAS u64* kp = (GAS u64*)(ws + WS_KEYS) + lane;
    u64 b;
    for (unsigned spins = 0;; ++spins) {
      u64 k0 = __hip_atomic_load(kp, RLX_AGENT), k1 = __hip_atomic_load(kp + 64, RLX_AGENT);
      u64 k2 = __hip_atomic_load(kp + 128, RLX_AGENT), k3 = __hip_atomic_load(kp + 192, RLX_AGENT);
      bool ok = (long long)(k0 & k1 & k2 & k3) < 0;
      b = k0 > k1 ? k0 : k1; u64 c = k2 > k3 ? k2 : k3; b = b > c ? b : c;
      if (__all(ok) || spins > SPIN_CAP) break;
      __builtin_amdgcn_s_sleep(1);
    }
    unsigned hk = (unsigned)(b >> 32), m = hk;
    m = max(m, (unsigned)__builtin_amdgcn_update_dpp(0, (int)m, 0x121, 0xf, 0xf, true));
    m = max(m, (unsigned)__builtin_amdgcn_update_dpp(0, (int)m, 0x122, 0xf, 0xf, true));
    m = max(m, (unsigned)__builtin_amdgcn_update_dpp(0, (int)m, 0x124, 0xf, 0xf, true));
    m = max(m, (unsigned)__builtin_amdgcn_update_dpp(0, (int)m, 0x128, 0xf, 0xf, true));
    unsigned m0 = (unsigned)__builtin_amdgcn_readlane((int)m, 0), m1 = (unsigned)__builtin_amdgcn_readlane((int)m, 16);
    unsigned m2 = (unsigned)__builtin_amdgcn_readlane((int)m, 32), m3 = (unsigned)__builtin_amdgcn_readlane((int)m, 48);
    m0 = max(max(m0, m1), max(m2, m3));
    if (hk == m0) { redW[2] = m0; redW[3] = (unsigned)b; }
    STAMPT(2, 3, 320);
    __syncthreads();
    const unsigned wkey = m0;
    const int wrow = 2047 - (int)(wkey & 2047u);
    float fh = 0.f, fx2 = 0.f;
    if (f2r) {
      const unsigned wms = redW[3];
      const float wm = bflo(wms), wrs = __builtin_amdgcn_rcpf(bfhi(wms));
      int c = cb2 * 8 + r;
      fh = rbf(bf1(x[(size_t)wrow * D_ + c]) + bf1(ytb[((size_t)(wrow >> 3) * D_ + c) * 8 + (wrow & 7)]));
      fx2 = rbf(rbf(fh - wm) * wrs);
    }
    if (f1) {
      __syncthreads();
      float gs = 0.f, us_ = 0.f;
#pragma unroll
      for (int w = 0; w < 5; ++w) { gs += redC[w * 16 + r]; us_ += redC[w * 16 + 8 + r]; }
      float gb = rbf(gs);
      float sl = rbf(gb * __builtin_amdgcn_rcpf(1.f + __expf(-gb)));
      unsigned a = (unsigned)f2bf(sl * rbf(us_)) | 0x10000u;
      if (lane < 8) __hip_atomic_store((GAS unsigned*)(ws + WS_ACT) + cb1 * 8 + r, a, RLX_AGENT);
      STAMPT(2, 4, 320);
    }
    if (f2r) {
      unsigned ob = (wkey >> 11) & 0xffffu;
      unsigned rb = (ob & 0x8000u) ? (ob & 0x7fffu) : (~ob & 0xffffu);
      float sg = rbf(__builtin_amdgcn_rcpf(1.f + __expf(-bf1((us)rb))));
      __syncthreads();
      float ys = (redD[r] + redD[8 + r]) + (redD[16 + r] + redD[24 + r]);
      float y = rbf(rbf(ys) * sg);
      if (lane < 8) out[(size_t)wrow * D_ + cb2 * 8 + r] = f2bf(fh + rbf(fx2 + y));
      STAMPT(2, 5, 320);
    }
    return;
  }
  unsigned raw[8];
#pragma unroll
  for (int r = 0; r < 8; ++r) raw[r] = xb[r * (D_ / 2) + tid];
  const u4 y0 = *(const u4*)(ytb + ((size_t)tb * D_ + 2 * tid) * 8);
  const u4 y1 = *(const u4*)(ytb + ((size_t)tb * D_ + 2 * tid) * 8 + 8);
  const f2 wv = unpk(((const unsigned*)wr)[tid]);
  f2 h[8];
  {
    float v[24];
#pragma unroll
    for (int r = 0; r < 8; ++r) {
      const float piv = bflo(xb[r * (D_ / 2)]);
      unsigned ya = y0[r >> 1], yb = y1[r >> 1];
      f2 yy = (r & 1) ? f2{bfhi(ya), bfhi(yb)} : f2{bflo(ya), bflo(yb)};
      f2 hs = unpk(raw[r]) + yy;
      h[r] = unpk(pk_bf(hs.x, hs.y));
      f2 dd = h[r] - piv;
      v[r] = dd.x + dd.y;
      v[r + 8] = __builtin_fmaf(dd.x, dd.x, dd.y * dd.y);
      v[r + 16] = __builtin_fmaf(dd.x, wv.x, dd.y * wv.y);
    }
    float w[12], o[6];
#pragma unroll
    for (int i = 0; i < 12; ++i) w[i] = swap32_add(v[i], v[i + 12]);
#pragma unroll
    for (int i = 0; i < 6; ++i) o[i] = row_sum(swap16_add(w[i], w[i + 6]));
    if ((lane & 15) == 0) {
      float* dst = redA + wave * 24 + (lane >> 4) * 6;
      *(f2*)dst = f2{o[0], o[1]}; *(f2*)(dst + 2) = f2{o[2], o[3]}; *(f2*)(dst + 4) = f2{o[4], o[5]};
    }
  }
  __syncthreads();
  STAMP(2, 1);
  __syncthreads();
  const int brow = (int)redW[0];
  unsigned cv = 0;
  unsigned* outr = (unsigned*)(out + (size_t)t0 * D_) + tid;
#pragma unroll
  for (int r = 0; r < 8; r += 2) {
    f4 c = *(const f4*)(abv + 2 * r);
    f2 o0 = h[r] + __builtin_elementwise_fma(h[r], f2{c.x, c.x}, f2{c.y, c.y});
    f2 o1 = h[r + 1] + __builtin_elementwise_fma(h[r + 1], f2{c.z, c.z}, f2{c.w, c.w});
    unsigned q0 = pk_bf(o0.x, o0.y), q1 = pk_bf(o1.x, o1.y);
    if (r != brow) outr[r * (D_ / 2)] = q0; else cv = q0;
    if (r + 1 != brow) outr[(r + 1) * (D_ / 2)] = q1; else cv = q1;
  }
  u4 wq[4], wdv[6];
  if (f1) {
    const size_t o0 = (size_t)(2 * tid) * F_ + cb1 * 8;
    wq[0] = *(const u4*)(wg + o0); wq[1] = *(const u4*)(wg + o0 + F_);
    wq[2] = *(const u4*)(wu + o0); wq[3] = *(const u4*)(wu + o0 + F_);
  }
  if (f2r && wave >= 1) {
#pragma unroll
    for (int k = 0; k < 6; ++k) wdv[k] = *(const u4*)(wd + (size_t)(tid - 64 + 256 * k) * D_ + cb2 * 8);
  }
  __syncthreads();
  const unsigned wkey = redW[2], wms = redW[3];
  const int wrow = 2047 - (int)(wkey & 2047u);
  if (f1) {
    const float wm = bflo(wms), wrs = __builtin_amdgcn_rcpf(bfhi(wms));
    const us* yw = ytb + (size_t)(wrow >> 3) * D_ * 8 + (wrow & 7);
    unsigned xr = ((const unsigned*)(x + (size_t)wrow * D_))[tid];
    us ya = yw[(2 * tid) * 8], yb = yw[(2 * tid + 1) * 8];
    f2 hs = unpk(xr) + f2{bf1(ya), bf1(yb)};
    f2 hh = unpk(pk_bf(hs.x, hs.y));
    f2 dd = unpk(pk_bf(hh.x - wm, hh.y - wm));
    f2 xs = unpk(pk_bf(dd.x * wrs, dd.y * wrs));
    float v[16] = {0, 0, 0, 0, 0, 0, 0, 0, 0, 0, 0, 0, 0, 0, 0, 0};
    fma8(v, wq[0], xs.x); fma8(v, wq[1], xs.y); fma8(v + 8, wq[2], xs.x); fma8(v + 8, wq[3], xs.y);
    float o[4];
    wave_reduce16(v, o);
    int rr = lane >> 4;
    if ((lane & 15) == 0) *(f4*)(redC + wave * 16 + 4 * (rr & 1) + 8 * (rr >> 1)) = f4{o[0], o[1], o[2], o[3]};
    __syncthreads();
  }
  if (wkey != redW[1]) outr[brow * (D_ / 2)] = cv;
  if (f2r) {
    if (wave >= 1) {
      GAS unsigned* ap = (GAS unsigned*)(ws + WS_ACT) + (tid - 64);
      unsigned a[6];
      for (unsigned spins = 0;; ++spins) {
#pragma unroll
        for (int k = 0; k < 6; ++k) a[k] = __hip_atomic_load(ap + 256 * k, RLX_AGENT);
        bool ok = (a[0] & a[1] & a[2] & a[3] & a[4] & a[5]) >> 16;
        if (__all(ok) || spins > SPIN_CAP) break;
        __builtin_amdgcn_s_sleep(1);
      }
      float acc[8] = {0, 0, 0, 0, 0, 0, 0, 0};
#pragma unroll
      for (int k = 0; k < 6; ++k) fma8(acc, wdv[k], bflo(a[k]));
      float o[2];
      wave_reduce8(acc, o);
      int rr = lane >> 4;
      if ((lane & 15) == 0) *(f2*)(redD + (wave - 1) * 8 + 2 * (rr & 1) + 4 * (rr >> 1)) = f2{o[0], o[1]};
    }
    __syncthreads();
  }
}

extern "C" void kernel_launch(void* const* d_in, const int* in_sizes, int n_in, void* d_out, int out_size,
                              void* d_ws, size_t ws_size, hipStream_t stream) {
  const us* x = (const us*)d_in[0];
  const us* Are = (const us*)d_in[1];
  const us* Aim = (const us*)d_in[2];
  const us* Bre = (const us*)d_in[3];
  const us* Bim = (const us*)d_in[4];
  const us* Cre = (const us*)d_in[5];
  const us* Cim = (const us*)d_in[6];
  const us* logdt = (const us*)d_in[7];
  const us* Dskip = (const us*)d_in[8];
  const us* wr = (const us*)d_in[10];
  const us* wg = (const us*)d_in[11];
  const us* wu = (const us*)d_in[12];
  const us* wd = (const us*)d_in[13];
  us* out = (us*)d_out;
  char* ws = (char*)d_ws;
  k1_ln_kgen<<<D_ + 128, 320, K1_LDS, stream>>>(x, Are, Aim, Bre, Bim, Cre, Cim, logdt, wr, wg, wu, wd, ws);
  k2_conv<<<D_, 256, K2_LDS, stream>>>(Dskip, ws);
  k3_tail<<<256, 384, 0, stream>>>(x, wr, wg, wu, wd, out, ws);
}
```

```cpp
#include <hip/hip_runtime.h>
#include <hip/hip_bf16.h>
#include <stdint.h>

typedef _Float16 h8 __attribute__((ext_vector_type(8)));
typedef _Float16 h2 __attribute__((ext_vector_type(2)));
typedef float f4 __attribute__((ext_vector_type(4)));
typedef float f2 __attribute__((ext_vector_type(2)));
typedef unsigned u4 __attribute__((ext_vector_type(4)));
typedef unsigned u2 __attribute__((ext_vector_type(2)));
typedef unsigned short us;
typedef unsigned long long u64;
#define GAS __attribute__((address_space(1)))
#define LAS __attribute__((address_space(3)))

constexpr int L_ = 2048, D_ = 640, N_ = 192, F_ = 1536;
constexpr int LP = 2048 + 64;
#define STAMPD(kid, slot) do {} while (0)
#define STAMP(kid, slot) do {} while (0)
#define STAMPT(kid, slot, thr) do {} while (0)

constexpr int NR_LEN  = 576;
constexpr int F2_OFF  = 2 * NR_LEN;
constexpr int F3_OFF  = F2_OFF + 4 * 256;
constexpr int KREC_ELEMS = F3_OFF + 2 * 320;
constexpr int KREC = KREC_ELEMS * 2;
constexpr int KCHUNKS = KREC_ELEMS / 8;

constexpr size_t WS_FLAGS = 0;
constexpr size_t WS_KEYS  = WS_FLAGS;
constexpr size_t WS_ACT   = WS_FLAGS + 2048;
constexpr size_t WS_WSUM  = 8192;
constexpr size_t WS_UT    = 8192 + 256;
constexpr size_t WS_YT    = WS_UT + (size_t)D_ * LP * 2;
constexpr size_t WS_KREC  = WS_YT + (size_t)D_ * L_ * 2;
constexpr size_t WS_MISC  = WS_KREC + (size_t)D_ * KREC;
constexpr size_t WS_TOTAL = WS_MISC + D_ * 8;

constexpr int TAB_PITCH = 196;
constexpr int K1_KC_OFF = (16 + 48) * TAB_PITCH * 4;
constexpr int K1_RED_OFF = K1_KC_OFF + 1552;
constexpr int K1_LDS = K1_RED_OFF + 16;

constexpr int kc_of_v(int v) {
  if (v < 256)  { int h = v >> 7, r = v & 127; return ((h * 8 + (r & 7)) * 16) + (r >> 3); }
  if (v < 512)  { int w = v - 256, h = w >> 7, r = w & 127; return ((16 + h * 4 + ((r & 7) >> 1)) * 16) + (r >> 3); }
  if (v < 1024) { int w = v - 512, h = w >> 7, r = w & 127; return ((24 + h * 2 + ((r & 7) >> 2)) * 16) + (r >> 3); }
  if (v < 2048) { int w = v - 1024, h = w >> 7, r = w & 127; return ((32 + h) * 16) + (r >> 3); }
  return 40 * 16;
}
constexpr int bf16_round_int(int j) {
  if (j <= 256) return j;
  int step = j < 512 ? 2 : (j < 1024 ? 4 : 8);
  int lo = (j / step) * step, rem = j - lo;
  if (rem * 2 < step) return lo;
  if (rem * 2 > step) return lo + step;
  return ((lo / step) & 1) ? lo + step : lo;
}
constexpr int ZIDX = 768;

struct GatherTab { unsigned v[KREC_ELEMS]; };
constexpr GatherTab make_gather() {
  GatherTab t{};
  for (int i = 0; i < KREC_ELEMS; ++i) t.v[i] = K1_KC_OFF + ZIDX * 2;
  for (int e = 0; e < 2; ++e)
    for (int i = 0; i < NR_LEN; ++i) {
      int j = 543 - (i + e);
      if (j >= 0 && j < 515) t.v[e * NR_LEN + i] = (unsigned)(K1_KC_OFF + 2 * kc_of_v(bf16_round_int(j)));
    }
  for (int pi = 0; pi < 4; ++pi)
    for (int q = 0; q < 16; ++q)
      for (int s = 0; s < 4; ++s)
        for (int e = 0; e < 4; ++e) {
          int p = 79 - (4 * q + s + e);
          if (p >= 32 && p < 64) {
            int v = 16 * p + 8 * (pi >> 1) + ((pi & 1) ? 8 : 4);
            t.v[F2_OFF + pi * 256 + (q * 4 + s) * 4 + e] = (unsigned)(K1_KC_OFF + 2 * kc_of_v(v));
          }
        }
  for (int pi = 0; pi < 2; ++pi)
    for (int q = 0; q < 20; ++q)
      for (int s = 0; s < 4; ++s)
        for (int e = 0; e < 4; ++e) {
          int qq = 127 - (4 * q + s + e);
          if (qq >= 64 && qq < 128) {
            int v = 16 * qq + (pi ? 16 : 8);
            t.v[F3_OFF + pi * 320 + (q * 4 + s) * 4 + e] = (unsigned)(K1_KC_OFF + 2 * kc_of_v(v));
          }
        }
  return t;
}
__device__ const GatherTab g_gather = make_gather();

struct BoxTab { us v[7 * 64 * 8]; };
constexpr BoxTab make_box() {
  BoxTab t{};
  const int nn[6] = {3, 5, 3, 5, 7, 9};
  const int sh[6] = {3, 6, 11, 14, 5, 12};
  const int w0[6] = {-16, -16, -24, -24, -16, -24};
  for (int m = 0; m < 6; ++m)
    for (int lane = 0; lane < 64; ++lane)
      for (int j = 0; j < 8; ++j) {
        int b = lane & 15, k = 8 * (lane >> 4) + j;
        int idx = w0[m] + k;
        bool on = idx <= b - sh[m] && idx >= b - sh[m] - (nn[m] - 1);
        t.v[(m * 64 + lane) * 8 + j] = on ? 0x3C00 : 0;
      }
  for (int lane = 0; lane < 64; ++lane)
    for (int j = 0; j < 8; ++j) {
      int b = lane & 15, k = 8 * (lane >> 4) + j;
      t.v[(6 * 64 + lane) * 8 + j] = (k - b == 4 || k - b == 5) ? 0xFFFF : 0;
    }
  return t;
}
__device__ const BoxTab g_box = make_box();

__device__ __forceinline__ float bflo(unsigned w) { return __uint_as_float(w << 16); }
__device__ __forceinline__ float bfhi(unsigned w) { return __uint_as_float(w & 0xffff0000u); }
__device__ __forceinline__ float bf1(us v) { return __uint_as_float((unsigned)v << 16); }
__device__ __forceinline__ us f2bf(float f) { __bf16 b = (__bf16)f; return __builtin_bit_cast(us, b); }
__device__ __forceinline__ float rbf(float f) { return bf1(f2bf(f)); }
typedef __bf16 b2 __attribute__((ext_vector_type(2)));
__device__ __forceinline__ unsigned pk_bf(float a, float b) {
  b2 v; v.x = (__bf16)a; v.y = (__bf16)b; return __builtin_bit_cast(unsigned, v);
}
__device__ __forceinline__ unsigned pk_h(float a, float b) {
  h2 v; v.x = (_Float16)a; v.y = (_Float16)b; return __builtin_bit_cast(unsigned, v);
}
template <int CTRL> __device__ __forceinline__ float dpp_add(float v) {
  int t = __builtin_amdgcn_update_dpp(0, __float_as_int(v), CTRL, 0xf, 0xf, true);
  return v + __int_as_float(t);
}
__device__ __forceinline__ float row_sum(float v) {
  v = dpp_add<0x121>(v); v = dpp_add<0x122>(v); v = dpp_add<0x124>(v); v = dpp_add<0x128>(v);
  return v;
}
__device__ __forceinline__ float swap32_add(float a, float b) {
  auto r = __builtin_amdgcn_permlane32_swap(__float_as_uint(a), __float_as_uint(b), false, false);
  return __uint_as_float(r[0]) + __uint_as_float(r[1]);
}
__device__ __forceinline__ float swap16_add(float a, float b) {
  auto r = __builtin_amdgcn_permlane16_swap(__float_as_uint(a), __float_as_uint(b), false, false);
  return __uint_as_float(r[0]) + __uint_as_float(r[1]);
}
__device__ __forceinline__ void wave_reduce16(const float (&v)[16], float (&out)[4]) {
  float w[8];
#pragma unroll
  for (int i = 0; i < 8; ++i) w[i] = swap32_add(v[i], v[i + 8]);
#pragma unroll
  for (int i = 0; i < 4; ++i) out[i] = row_sum(swap16_add(w[i], w[i + 4]));
}
__device__ __forceinline__ void wave_reduce8(const float (&v)[8], float (&out)[2]) {
  float w[4];
#pragma unroll
  for (int i = 0; i < 4; ++i) w[i] = swap32_add(v[i], v[i + 4]);
#pragma unroll
  for (int i = 0; i < 2; ++i) out[i] = row_sum(swap16_add(w[i], w[i + 2]));
}
__device__ __forceinline__ float wave_reduce4(const float (&v)[4]) {
  float w0 = swap32_add(v[0], v[2]), w1 = swap32_add(v[1], v[3]);
  return row_sum(swap16_add(w0, w1));
}
__device__ __forceinline__ float rdlane(float v, int l) {
  return __int_as_float(__builtin_amdgcn_readlane(__float_as_int(v), l));
}

__device__ __forceinline__ f2 cmul(f2 a, f2 b) {
  f2 c;
  asm("v_pk_mul_f32 %0, %1, %2 op_sel:[0,0] op_sel_hi:[1,0]\n\tv_pk_fma_f32 %0, %1, %2, %0 op_sel:[1,1,0] op_sel_hi:[0,1,1] neg_lo:[1,0,0]"
      : "=&v"(c) : "v"(a), "v"(b));
  return c;
}
__device__ __forceinline__ f2 cexp_p(float lam, float rh, float rl, float p) {
  float mag = __builtin_amdgcn_exp2f(lam * p);
  float x = rh * p;
  float f = x - __builtin_rintf(x);
  f = __builtin_fmaf(rl, p, f);
  f2 w; w.x = mag * __builtin_amdgcn_cosf(f); w.y = mag * __builtin_amdgcn_sinf(f); return w;
}
#define MFMA16(a, b, c) __builtin_amdgcn_mfma_f32_16x16x32_f16((a), (b), (c), 0, 0, 0)
__device__ __forceinline__ h8 as_h8(u4 v) { return __builtin_bit_cast(h8, v); }

__device__ __forceinline__ void ln_from_sums(float s1, float s2, float piv, float& mb, float& sb) {
  float dm = s1 * (1.f / 640.f);
  float var = fmaxf(__builtin_fmaf(-dm, dm, s2 * (1.f / 640.f)), 0.f);
  mb = rbf(piv + dm);
  sb = rbf(__builtin_amdgcn_sqrtf(rbf(rbf(var) + 1e-5f)));
}
__device__ __forceinline__ f2 unpk(unsigned w) { return f2{bflo(w), bfhi(w)}; }

__global__ __launch_bounds__(320, 2) void k1_ln_kgen(
    const us* __restrict__ x, const us* __restrict__ Are, const us* __restrict__ Aim,
    const us* __restrict__ Bre, const us* __restrict__ Bim, const us* __restrict__ Cre,
    const us* __restrict__ Cim, const us* __restrict__ logdt, const us* __restrict__ wr,
    const us* __restrict__ wg, const us* __restrict__ wu, const us* __restrict__ wd, const us* __restrict__ out,
    char* __restrict__ ws) {
  extern __shared__ __attribute__((aligned(16))) char smem[];
  const int tid = threadIdx.x, lane = tid & 63, wave = __builtin_amdgcn_readfirstlane(tid >> 6);
  const int bid = blockIdx.x;
  STAMP(0, 0);
  if (bid < 128) {
    const int t0 = ((bid & 7) * 16 + (bid >> 3)) * 16;
    const unsigned* xb = (const unsigned*)(x + (size_t)t0 * D_);
    unsigned raw[16];
#pragma unroll
    for (int r = 0; r < 16; ++r) raw[r] = xb[r * (D_ / 2) + tid];
    float* red = (float*)smem;
    f2* ab = (f2*)(smem + 1024);
    f2 a[16];
    float v[32];
    float mypiv = 0.f;
#pragma unroll
    for (int r = 0; r < 16; ++r) {
      const float piv = bflo(xb[r * (D_ / 2)]);
      if (tid == r) mypiv = piv;
      a[r] = unpk(raw[r]);
      f2 dd = a[r] - piv;
      v[r] = dd.x + dd.y; v[r + 16] = __builtin_fmaf(dd.x, dd.x, dd.y * dd.y);
    }
    {
      float w[16], o[8];
#pragma unroll
      for (int i = 0; i < 16; ++i) w[i] = swap32_add(v[i], v[i + 16]);
#pragma unroll
      for (int i = 0; i < 8; ++i) o[i] = row_sum(swap16_add(w[i], w[i + 8]));
      if ((lane & 15) == 0) {
        float* dst = red + wave * 32 + (lane >> 4) * 8;
        *(f4*)dst = f4{o[0], o[1], o[2], o[3]}; *(f4*)(dst + 4) = f4{o[4], o[5], o[6], o[7]};
      }
    }
    __syncthreads();
    if (wave == 0) {
      const int r = lane & 15;
      float s1 = red[r] + red[32 + r] + red[64 + r] + red[96 + r] + red[128 + r];
      float s2 = red[16 + r] + red[48 + r] + red[80 + r] + red[112 + r] + red[144 + r];
      float mb, sb;
      ln_from_sums(s1, s2, mypiv, mb, sb);
      float aa = 128.f * __builtin_amdgcn_rcpf(sb);
      if (lane < 16) ab[r] = f2{aa, -mb * aa};
    } else if (wave == 1 && bid == 0) {
      const unsigned* w2 = (const unsigned*)wr + lane;
      float sw = 0.f;
#pragma unroll
      for (int k = 0; k < 5; ++k) { unsigned q = w2[64 * k]; sw += bflo(q) + bfhi(q); }
      sw = row_sum(sw);
      sw = rdlane(sw, 0) + rdlane(sw, 16) + rdlane(sw, 32) + rdlane(sw, 48);
      if (lane == 0) *(float*)(ws + WS_WSUM) = sw;
    } else if (wave >= 1 && bid == 1) {
      ((u4*)(ws + WS_FLAGS))[tid - 64] = u4{0, 0, 0, 0};
      ((u4*)(ws + WS_FLAGS))[tid + 192] = u4{0, 0, 0, 0};
    }
    __syncthreads();
    unsigned p0[8], p1[8];
#pragma unroll
    for (int r = 0; r < 16; r += 2) {
      f4 c = *(const f4*)(ab + r);
      f2 u0 = __builtin_elementwise_fma(a[r], f2{c.x, c.x}, f2{c.y, c.y});
      f2 u1 = __builtin_elementwise_fma(a[r + 1], f2{c.z, c.z}, f2{c.w, c.w});
      p0[r >> 1] = pk_h(u0.x, u1.x); p1[r >> 1] = pk_h(u0.y, u1.y);
    }
    us* ut = (us*)(ws + WS_UT) + (size_t)(2 * tid) * LP + t0;
    *(u4*)(ut) = u4{p0[0], p0[1], p0[2], p0[3]};
    *(u4*)(ut + 8) = u4{p0[4], p0[5], p0[6], p0[7]};
    *(u4*)(ut + LP) = u4{p1[0], p1[1], p1[2], p1[3]};
    *(u4*)(ut + LP + 8) = u4{p1[4], p1[5], p1[6], p1[7]};
    STAMP(0, 5);
    STAMPD(0, 6);
    return;
  }
  if (wave >= 3) {
    const int xcd = bid & 7;
    const int id = ((bid - 128) >> 3) * 128 + (tid - 192);
    u64 pc; int o1, o2;
    asm volatile("s_getpc_b64 %0\n\ts_mov_b32 %1, k2_conv@rel32@lo+4\n\ts_mov_b32 %2, k9_end@rel32@lo+12" : "=s"(pc), "=s"(o1), "=s"(o2));
    const u64 c0 = pc + (long long)o1;
    const int ncode = min((o2 - o1) >> 6, 1024);
    const char* p;
    if (id < 3840) { int r3 = id / 3, q3 = id - r3 * 3;
      p = (const char*)((r3 < 640 ? wg + (size_t)r3 * F_ : wu + (size_t)(r3 - 640) * F_) + xcd * 192 + q3 * 64); }
    else if (id < 6912) { int i2 = id - 3840; p = (const char*)(wd + (size_t)(i2 >> 1) * D_ + xcd * 80 + (i2 & 1) * 78); }
    else if (id < 6912 + ncode) p = (const char*)c0 + (size_t)(id - 6912) * 64;
    else if (id < 8192) p = (const char*)c0;
    else if (id < 8192 + 40) p = (const char*)out + (size_t)(id - 8192) * 65536;
    else if (id < 8192 + 80) p = ws + WS_YT + (size_t)(id - 8232) * 65536;
    else p = (const char*)g_box.v + ((id & 63) * 64 + (id & 64 ? 4096 : 0)) % 7168;
    unsigned pf = *(const volatile unsigned*)p;
    __builtin_amdgcn_s_barrier(); __builtin_amdgcn_s_barrier(); __builtin_amdgcn_s_barrier();
    asm volatile("" :: "v"(pf));
    return;
  }
  const int d = bid - 128, n = tid;
  char* kc = smem + K1_KC_OFF;
  const size_t pidx = (size_t)d * N_ + n;
  us r_are = Are[pidx], r_aim = Aim[pidx], r_bre = Bre[pidx], r_bim = Bim[pidx], r_cre = Cre[pidx], r_cim = Cim[pidx];
  us r_ldt = logdt[d];
  const u4* gt = (const u4*)g_gather.v;
  u4 ga0 = gt[2 * tid], ga1 = gt[2 * tid + 1], gb0 = gt[2 * (tid + 160)], gb1 = gt[2 * (tid + 160) + 1];
  float dt = rbf(__expf(bf1(r_ldt)));
  float ar = dt * bf1(r_are), ai = dt * bf1(r_aim);
  f2 dB = {dt * bf1(r_bre), dt * bf1(r_bim)};
  f2 Cc = {bf1(r_cre), bf1(r_cim)};
  f2 CB = cmul(Cc, dB);
  const float LOG2E = 1.4426950408889634f;
  const float I2PH = 0.15915494309189535f;
  const float I2PL = (float)(0.15915494309189533577 - (double)0.15915494309189535f);
  float lam = ar * LOG2E;
  float rh = ai * I2PH;
  float rl = __builtin_fmaf(ai, I2PL, __builtin_fmaf(ai, I2PH, -rh));
  {
    float g = fmaxf(1.f, __builtin_amdgcn_exp2f(lam * 2048.f));
    float bnd = row_sum((fabsf(CB.x) + fabsf(CB.y)) * g);
    if ((lane & 15) == 0) ((float*)(kc))[wave * 4 + (lane >> 4)] = bnd;
  }
  f2 w1 = cexp_p(lam, rh, rl, 1.f), w16 = cexp_p(lam, rh, rl, 16.f), w256 = cexp_p(lam, rh, rl, 256.f);
  f2 w2 = cmul(w1, w1), w32 = cmul(w16, w16), w512 = cmul(w256, w256);
  f2 w4 = cmul(w2, w2), w64 = cmul(w32, w32), w1024 = cmul(w512, w512);
  f2 w8 = cmul(w4, w4), w128 = cmul(w64, w64);
  const unsigned ta = (unsigned)n * 4u;
#define DSW(row, val) asm volatile("ds_write_b32 %0, %1 offset:%2" :: "v"(ta), "v"(val), "i"((row) * TAB_PITCH * 4) : "memory")
#define PUTE(row, c) do { f2 c_ = (c); DSW((row), pk_h(c_.x, -c_.y)); } while (0)
#define PUT(row, c) do { f2 c_ = (c); DSW(16 + (row), pk_h(c_.x, c_.y)); } while (0)
  {
    f2 e[8];
    e[1] = w8; e[2] = w16; e[3] = cmul(w8, w16); e[4] = w32;
    e[5] = cmul(w32, e[1]); e[6] = cmul(w32, e[2]); e[7] = cmul(w32, e[3]);
    DSW(0, 0x00003C00u);
#pragma unroll
    for (int l = 1; l < 8; ++l) PUTE(l, e[l]);
    PUTE(8, w64);
#pragma unroll
    for (int l = 1; l < 8; ++l) PUTE(8 + l, cmul(w64, e[l]));
  }
  __syncthreads();
  STAMP(0, 1);
  {
    f4 q0 = *(const f4*)(kc), q1 = *(const f4*)(kc + 16), q2 = *(const f4*)(kc + 32);
    q0 += q1; q0 += q2;
    float sig = (q0.x + q0.y) + (q0.z + q0.w);
    int ex = (int)(__float_as_uint(sig) >> 23) & 255; ex = ex < 24 ? 24 : (ex > 254 ? 254 : ex);
    float sc = __uint_as_float((unsigned)(268 - ex) << 23);
    if (tid == 0) { ((float*)(ws + WS_MISC))[2 * d] = __uint_as_float((unsigned)(ex - 21) << 23); *(unsigned*)(kc + ZIDX * 2) = 0; }
    f2 c[8];
    c[0] = CB * sc;
    c[1] = cmul(c[0], w1); c[2] = cmul(c[0], w2); c[4] = cmul(c[0], w4); c[3] = cmul(c[1], w2);
    c[5] = cmul(c[1], w4); c[6] = cmul(c[2], w4); c[7] = cmul(c[3], w4);
#pragma unroll
    for (int r = 0; r < 8; ++r) {
      PUT(r, c[r]);
      f2 ch = cmul(c[r], w128); PUT(8 + r, ch);
      if ((r & 1) == 0) {
        f2 s0 = cmul(c[r], w256), s1 = cmul(ch, w256);
        PUT(16 + (r >> 1), s0); PUT(20 + (r >> 1), s1);
        if ((r & 3) == 0) {
          const int rho = r >> 2;
          f2 z0 = cmul(c[r], w512), z1 = cmul(ch, w512), z2 = cmul(s0, w512), z3 = cmul(s1, w512);
          PUT(24 + 0 + rho, z0); PUT(24 + 2 + rho, z1); PUT(24 + 4 + rho, z2); PUT(24 + 6 + rho, z3);
          if (r == 0) {
            f2 y0 = cmul(c[0], w1024);
            PUT(32, y0); PUT(33, cmul(ch, w1024)); PUT(34, cmul(s0, w1024)); PUT(35, cmul(s1, w1024));
            PUT(36, cmul(z0, w1024)); PUT(37, cmul(z1, w1024)); PUT(38, cmul(z2, w1024)); PUT(39, cmul(z3, w1024));
            PUT(40, cmul(y0, w1024));
          }
        }
      }
    }
  }
  asm volatile("s_waitcnt lgkmcnt(0)" ::: "memory");
  __syncthreads();
  STAMP(0, 2);
  {
    const unsigned* tab = (const unsigned*)smem;
    f4 acc = {0.f, 0.f, 0.f, 0.f};
    const int row = lane & 15, g = lane >> 4;
    const unsigned* pa = tab + row * TAB_PITCH + g * 4;
    const unsigned* pb = tab + (16 + wave * 16 + row) * TAB_PITCH + g * 4;
#pragma unroll
    for (int kk = 0; kk < 12; ++kk)
      acc = MFMA16(as_h8(*(const u4*)(pa + kk * 16)), as_h8(*(const u4*)(pb + kk * 16)), acc);
    u2 o; o.x = pk_h(acc[0], acc[1]); o.y = pk_h(acc[2], acc[3]);
    *(u2*)(kc + ((wave * 16 + row) * 16 + 4 * g) * 2) = o;
  }
  __syncthreads();
  STAMP(0, 3);
  {
    char* rec = ws + WS_KREC + (size_t)d * KREC;
#define LDU(a) ((unsigned)*(const LAS us*)(size_t)(a))
#define LDP(a, b) (LDU(a) | (LDU(b) << 16))
    u4 o0 = {LDP(ga0.x, ga0.y), LDP(ga0.z, ga0.w), LDP(ga1.x, ga1.y), LDP(ga1.z, ga1.w)};
    u4 o1 = {LDP(gb0.x, gb0.y), LDP(gb0.z, gb0.w), LDP(gb1.x, gb1.y), LDP(gb1.z, gb1.w)};
    ((u4*)rec)[tid] = o0;
    if (tid >= 32) ((u4*)rec)[tid + 160] = o1;
    if (tid == 0) ((unsigned*)(ws + WS_MISC))[2 * d + 1] = (unsigned)*(const us*)(kc + 32 * 16 * 2) * 0x10001u;
  }
  STAMP(0, 4);
  STAMPD(0, 7);
}

constexpr int SM_U = 0;
constexpr int SM_N = 4608;
constexpr int SM_F2 = SM_N + 560 * 16;
constexpr int SM_F3 = SM_F2 + 2048;
constexpr int SM_P = SM_F3 + 1280;
constexpr int K2_LDS = SM_P + 10 * 1024;

__device__ __forceinline__ h8 ld_h8(const char* p) { return as_h8(*(const u4*)p); }
__device__ __forceinline__ h8 ld_2x(const char* p0, const char* p1) {
  u2 a = *(const u2*)p0, b = *(const u2*)p1;
  return as_h8(u4{a.x, a.y, b.x, b.y});
}
__device__ __forceinline__ h8 cvt_pair(f4 a, f4 b) {
  return as_h8(u4{pk_h(a[0], a[1]), pk_h(a[2], a[3]), pk_h(b[0], b[1]), pk_h(b[2], b[3])});
}
__device__ __forceinline__ void glds16(const void* g, void* l) {
  __builtin_amdgcn_global_load_lds((const GAS unsigned*)g, (LAS unsigned*)l, 16, 0, 0);
}
__device__ __forceinline__ void conv_epi(f4 acc, const char* up, float sc, float dq, us* dst) {
  u2 uu = *(const u2*)up;
  const unsigned ux = uu.x, uy = uu.y;
  h2 ua = __builtin_bit_cast(h2, ux), ub = __builtin_bit_cast(h2, uy);
  unsigned z0 = pk_bf((float)ua.x * dq, (float)ua.y * dq), z1 = pk_bf((float)ub.x * dq, (float)ub.y * dq);
  unsigned y0 = pk_bf(acc[0] * sc, acc[1] * sc), y1 = pk_bf(acc[2] * sc, acc[3] * sc);
  u2 o;
  o.x = pk_bf(bflo(y0) + bflo(z0), bfhi(y0) + bfhi(z0));
  o.y = pk_bf(bflo(y1) + bflo(z1), bfhi(y1) + bfhi(z1));
  *(u2*)dst = o;
}

extern "C" __global__ __launch_bounds__(256, 2) void k2_conv(const us* __restrict__ Dskip, char* __restrict__ ws) {
  extern __shared__ __attribute__((aligned(16))) char sm[];
  const int tid = threadIdx.x, lane = tid & 63, wave = __builtin_amdgcn_readfirstlane(tid >> 6);
  const int d = (blockIdx.x & 7) * 80 + (blockIdx.x >> 3);
  const int col = lane & 15, g = lane >> 4;
  STAMP(1, 0);
  {
    const char* gu = ws + WS_UT + (size_t)d * LP * 2;
    const char* gk = ws + WS_KREC + (size_t)d * KREC;
    glds16(gu + tid * 16, sm + SM_U + 512 + wave * 1024);
#pragma unroll
    for (int k = 0; k < 3; ++k) {
      int c = tid + 256 * k;
      int q = c >> 3, s = (c & 7) ^ (((q >> 1) & 1) << 2);
      int offn = (s & 1) * (NR_LEN * 2) + (s & 6) * 2 + q * 16;
      int offf = F2_OFF * 2 + (c - 560) * 16;
      int off = (k < 2 || c < 560) ? offn : offf;
      glds16(gk + off, sm + SM_N + (wave * 64 + 256 * k) * 16);
    }
  }
  float2 misc = ((const float2*)(ws + WS_MISC))[d];
  const float dq = bf1(Dskip[d]) * (1.f / 128.f);
  u4 bx0, bx1, bx2, bx3;
  if (wave >= 2) {
    const u4* bt = (const u4*)g_box.v + lane;
    int o = (wave == 2) ? 0 : 4 * 64;
    bx0 = bt[o]; bx1 = bt[o + 64]; bx2 = bt[o + 128]; bx3 = bt[(wave == 2) ? 192 : 0];
  }
  if (tid < 32) *(u4*)(sm + SM_U + tid * 16) = u4{0, 0, 0, 0};
  __syncthreads();
  STAMP(1, 1);
  const char* ub = sm + SM_U + (256 + 16 * col + 8 * g) * 2;
  const char* ue = sm + SM_U + (256 + 16 * col + 4 * g) * 2;
  us* yt = (us*)(ws + WS_YT) + ((size_t)(2 * col + (g >> 1)) * D_ + d) * 8 + (g & 1) * 4;
  constexpr int YTS = 32 * D_ * 8;
  const char* ka;
  {
    const int s = (15 - col) & 7, q = (((15 - col) & 8) >> 3) + g;
    ka = sm + SM_N + (q * 8 + (s ^ (((q >> 1) & 1) << 2))) * 16;
  }
  const f4 Z = {0.f, 0.f, 0.f, 0.f};
  if (wave < 2) {
    const int n0 = 2 + 3 * wave;
    h8 A[17];
#pragma unroll
    for (int k = 0; k < 17; ++k) A[k] = ld_h8(ka + k * 512);
    f4 a0 = Z, a1 = Z, a2 = Z;
    const char* fb = ub + (256 * n0 - 528) * 2;
#pragma unroll
    for (int m = 0; m < 33; ++m) {
      h8 f = ld_h8(fb + m * 64);
      if (m <= 16) a0 = MFMA16(A[m], f, a0);
      if (m >= 8 && m <= 24) a1 = MFMA16(A[m - 8], f, a1);
      if (m >= 16) a2 = MFMA16(A[m - 16], f, a2);
    }
    __syncthreads();
    const char* pp = sm + SM_P + lane * 16;
    a0 += *(const f4*)(pp + (n0 - 2) * 1024);
    a1 += *(const f4*)(pp + (n0 - 1) * 1024);
    a2 += *(const f4*)(pp + (n0 - 0) * 1024);
    if (wave == 0) a2 += *(const f4*)(pp + 6 * 1024);
    else { a0 += *(const f4*)(pp + 7 * 1024); a1 += *(const f4*)(pp + 8 * 1024); a2 += *(const f4*)(pp + 9 * 1024); }
    conv_epi(a0, ue + (256 * n0) * 2, misc.x, dq, yt + YTS * n0);
    conv_epi(a1, ue + (256 * (n0 + 1)) * 2, misc.x, dq, yt + YTS * (n0 + 1));
    conv_epi(a2, ue + (256 * (n0 + 2)) * 2, misc.x, dq, yt + YTS * (n0 + 2));
  } else if (wave == 2) {
    const int ib = 79 - col + 4 * g;
    const char* kb = sm + SM_F2 + ((ib >> 2) * 4 + (ib & 3)) * 8;
    h8 tA[3], tB[3];
#pragma unroll
    for (int dl = 2; dl <= 4; ++dl) {
      tA[dl - 2] = ld_2x(kb - dl * 128, kb + 512 - dl * 128);
      tB[dl - 2] = ld_2x(kb + 1024 - dl * 128, kb + 1536 - dl * 128);
    }
    f4 acc[6] = {Z, Z, Z, Z, Z, Z};
#pragma unroll
    for (int tau = 0; tau < 6; ++tau) {
      h8 w1 = ld_h8(ub + (256 * tau - 16) * 2), w2 = ld_h8(ub + (256 * tau - 24) * 2);
      h8 sa = cvt_pair(MFMA16(w1, as_h8(bx0), Z), MFMA16(w1, as_h8(bx1), Z));
      h8 sb = cvt_pair(MFMA16(w2, as_h8(bx2), Z), MFMA16(w2, as_h8(bx3), Z));
#pragma unroll
      for (int dl = 2; dl <= 4; ++dl) {
        int nn = tau + dl;
        if (nn <= 7) { acc[nn - 2] = MFMA16(sa, tA[dl - 2], acc[nn - 2]); acc[nn - 2] = MFMA16(sb, tB[dl - 2], acc[nn - 2]); }
      }
    }
#pragma unroll
    for (int i = 0; i < 6; ++i) *(f4*)(sm + SM_P + i * 1024 + lane * 16) = acc[i];
    __syncthreads();
  } else {
    const int ib = 127 - col + 4 * g;
    const char* kb = sm + SM_F3 + ((ib >> 2) * 4 + (ib & 3)) * 8;
    h8 tt[4];
#pragma unroll
    for (int dl = 4; dl <= 7; ++dl) tt[dl - 4] = ld_2x(kb - dl * 128, kb + 640 - dl * 128);
    unsigned kv = __float_as_uint(misc.y);
    h8 As = as_h8(u4{bx2.x & kv, bx2.y & kv, bx2.z & kv, bx2.w & kv});
    f4 acc[4];
#pragma unroll
    for (int i = 0; i < 4; ++i) acc[i] = MFMA16(As, ld_h8(ub + (256 * (4 + i) - 1032) * 2), Z);
#pragma unroll
    for (int tau = 0; tau < 4; ++tau) {
      h8 w1 = ld_h8(ub + (256 * tau - 16) * 2), w2 = ld_h8(ub + (256 * tau - 24) * 2);
      h8 s3 = cvt_pair(MFMA16(w1, as_h8(bx0), Z), MFMA16(w2, as_h8(bx1), Z));
#pragma unroll
      for (int dl = 4; dl <= 7; ++dl)
        if (tau + dl <= 7) acc[tau + dl - 4] = MFMA16(s3, tt[dl - 4], acc[tau + dl - 4]);
    }
#pragma unroll
    for (int i = 0; i < 4; ++i) *(f4*)(sm + SM_P + (6 + i) * 1024 + lane * 16) = acc[i];
    h8 A[17];
#pragma unroll
    for (int k = 1; k < 17; ++k) A[k] = ld_h8(ka + k * 512);
    f4 a0 = Z, a1 = Z;
    const char* fb = ub - 528 * 2;
#pragma unroll
    for (int m = 9; m < 25; ++m) {
      h8 f = ld_h8(fb + m * 64);
      if (m <= 16) a0 = MFMA16(A[m], f, a0);
      a1 = MFMA16(A[m - 8], f, a1);
    }
    __syncthreads();
    conv_epi(a0, ue, misc.x, dq, yt);
    conv_epi(a1, ue + 512, misc.x, dq, yt + YTS);
  }
  STAMP(1, 4);
  STAMPD(1, 5);
}

__device__ __forceinline__ unsigned ord16(unsigned b) { return (b & 0x8000u) ? (~b & 0xffffu) : (b | 0x8000u); }
__device__ __forceinline__ void fma8(float* acc, u4 w, float xv) {
  acc[0] = __builtin_fmaf(bflo(w.x), xv, acc[0]); acc[1] = __builtin_fmaf(bfhi(w.x), xv, acc[1]);
  acc[2] = __builtin_fmaf(bflo(w.y), xv, acc[2]); acc[3] = __builtin_fmaf(bfhi(w.y), xv, acc[3]);
  acc[4] = __builtin_fmaf(bflo(w.z), xv, acc[4]); acc[5] = __builtin_fmaf(bfhi(w.z), xv, acc[5]);
  acc[6] = __builtin_fmaf(bflo(w.w), xv, acc[6]); acc[7] = __builtin_fmaf(bfhi(w.w), xv, acc[7]);
}
#define RLX_AGENT __ATOMIC_RELAXED, __HIP_MEMORY_SCOPE_AGENT
constexpr unsigned SPIN_CAP = 1u << 22;

extern "C" __global__ __launch_bounds__(448, 2) void k3_tail(const us* __restrict__ x, const us* __restrict__ wr,
                                                  const us* __restrict__ wg, const us* __restrict__ wu,
                                                  const us* __restrict__ wd, us* __restrict__ out,
                                                  char* __restrict__ ws) {
  extern __shared__ __attribute__((aligned(16))) char sm3[];
  float* red = (float*)sm3;
  char* sw1 = sm3 + 1024;
  char* sw2 = sm3 + 1024 + 20480;
  float* redA = red;
  float* abv = red + 120;
  float* redC = red + 136;
  float* redD = red + 216;
  unsigned* redW = (unsigned*)(red + 248);
  const int tid = threadIdx.x, lane = tid & 63, wave = __builtin_amdgcn_readfirstlane(tid >> 6);
  const int bid = blockIdx.x;
  const int tb = (bid & 7) * 32 + (bid >> 3);
  const int t0 = tb * 8;
  STAMP(2, 0);
  const us* ytb = (const us*)(ws + WS_YT);
  const unsigned* xb = (const unsigned*)(x + (size_t)t0 * D_);
  const bool f1 = bid < 192, f2r = bid >= 176;
  const int cb1 = (bid & 7) * 24 + (bid >> 3);
  const int cb2 = (bid & 7) * 10 + ((bid - 176) >> 3);
  if (wave == 6) {
    __builtin_amdgcn_s_sleep(4);
    if (f1) {
#pragma unroll
      for (int k = 0; k < 10; ++k) {
        glds16(wg + (size_t)(k * 64 + lane) * F_ + cb1 * 8, sw1 + k * 1024);
        glds16(wu + (size_t)(k * 64 + lane) * F_ + cb1 * 8, sw1 + 10240 + k * 1024);
      }
    }
    if (f2r) {
#pragma unroll
      for (int k = 0; k < 24; ++k) glds16(wd + (size_t)(k * 64 + lane) * D_ + cb2 * 8, sw2 + k * 1024);
    }
    __builtin_amdgcn_s_barrier(); __builtin_amdgcn_s_barrier();
    asm volatile("s_waitcnt vmcnt(0)" ::: "memory");
    __builtin_amdgcn_s_barrier();
    return;
  }
  if (wave == 5) {
    const int r = lane & 7;
    const float piv = bf1(x[(size_t)(t0 + r) * D_]);
    const float W = *(const float*)(ws + WS_WSUM);
    __syncthreads();
    float s1 = redA[r] + redA[24 + r] + redA[48 + r] + redA[72 + r] + redA[96 + r];
    float s2 = redA[8 + r] + redA[32 + r] + redA[56 + r] + redA[80 + r] + redA[104 + r];
    float s3 = redA[16 + r] + redA[40 + r] + redA[64 + r] + redA[88 + r] + redA[112 + r];
    float mb, sb;
    ln_from_sums(s1, s2, piv, mb, sb);
    float rs = __builtin_amdgcn_rcpf(sb);
    float rv = rs * __builtin_fmaf(piv - mb, W, s3);
    unsigned key = 0x80000000u | (ord16(f2bf(rv)) << 11) | (unsigned)(2047 - (t0 + r));
    unsigned best = key;
    best = max(best, (unsigned)__builtin_amdgcn_update_dpp(0, (int)best, 0x111, 0xf, 0xf, true));
    best = max(best, (unsigned)__builtin_amdgcn_update_dpp(0, (int)best, 0x112, 0xf, 0xf, true));
    best = max(best, (unsigned)__builtin_amdgcn_update_dpp(0, (int)best, 0x114, 0xf, 0xf, true));
    best = (unsigned)__builtin_amdgcn_readlane((int)best, 7);
    if (lane < 8) {
      if (key == best) {
        u64 gr = ((u64)best << 32) | (u64)((__float_as_uint(mb) >> 16) | (__float_as_uint(sb) & 0xffff0000u));
        __hip_atomic_store((GAS u64*)(ws + WS_KEYS) + bid, gr, RLX_AGENT);
        redW[0] = (unsigned)r; redW[1] = best;
      }
      *(f2*)(abv + 2 * r) = f2{rs, -mb * rs};
    }
    STAMPT(2, 2, 320);
    __syncthreads();
    GAS u64* kp = (GAS u64*)(ws + WS_KEYS) + lane;
    u64 b;
    for (unsigned spins = 0;; ++spins) {
      u64 k0 = __hip_atomic_load(kp, RLX_AGENT), k1 = __hip_atomic_load(kp + 64, RLX_AGENT);
      u64 k2 = __hip_atomic_load(kp + 128, RLX_AGENT), k3 = __hip_atomic_load(kp + 192, RLX_AGENT);
      bool ok = (long long)(k0 & k1 & k2 & k3) < 0;
      b = k0 > k1 ? k0 : k1; u64 c = k2 > k3 ? k2 : k3; b = b > c ? b : c;
      if (__all(ok) || spins > SPIN_CAP) break;
      __builtin_amdgcn_s_sleep(1);
    }
    unsigned hk = (unsigned)(b >> 32), m = hk;
    m = max(m, (unsigned)__builtin_amdgcn_update_dpp(0, (int)m, 0x121, 0xf, 0xf, true));
    m = max(m, (unsigned)__builtin_amdgcn_update_dpp(0, (int)m, 0x122, 0xf, 0xf, true));
    m = max(m, (unsigned)__builtin_amdgcn_update_dpp(0, (int)m, 0x124, 0xf, 0xf, true));
    m = max(m, (unsigned)__builtin_amdgcn_update_dpp(0, (int)m, 0x128, 0xf, 0xf, true));
    unsigned m0 = (unsigned)__builtin_amdgcn_readlane((int)m, 0), m1 = (unsigned)__builtin_amdgcn_readlane((int)m, 16);
    unsigned m2 = (unsigned)__builtin_amdgcn_readlane((int)m, 32), m3 = (unsigned)__builtin_amdgcn_readlane((int)m, 48);
    m0 = max(max(m0, m1), max(m2, m3));
    if (hk == m0) { redW[2] = m0; redW[3] = (unsigned)b; }
    STAMPT(2, 3, 320);
    __syncthreads();
    const unsigned wkey = m0;
    const int wrow = 2047 - (int)(wkey & 2047u);
    float fh = 0.f, fx2 = 0.f;
    if (f2r) {
      const unsigned wms = redW[3];
      const float wm = bflo(wms), wrs = __builtin_amdgcn_rcpf(bfhi(wms));
      int c = cb2 * 8 + r;
      fh = rbf(bf1(x[(size_t)wrow * D_ + c]) + bf1(ytb[((size_t)(wrow >> 3) * D_ + c) * 8 + (wrow & 7)]));
      fx2 = rbf(rbf(fh - wm) * wrs);
    }
    if (f1) {
      __syncthreads();
      float gs = 0.f, us_ = 0.f;
#pragma unroll
      for (int w = 0; w < 5; ++w) { gs += redC[w * 16 + r]; us_ += redC[w * 16 + 8 + r]; }
      float gb = rbf(gs);
      float sl = rbf(gb * __builtin_amdgcn_rcpf(1.f + __expf(-gb)));
      unsigned a = (unsigned)f2bf(sl * rbf(us_)) | 0x10000u;
      if (lane < 8) __hip_atomic_store((GAS unsigned*)(ws + WS_ACT) + cb1 * 8 + r, a, RLX_AGENT);
      STAMPT(2, 4, 320);
    }
    if (f2r) {
      unsigned ob = (wkey >> 11) & 0xffffu;
      unsigned rb = (ob & 0x8000u) ? (ob & 0x7fffu) : (~ob & 0xffffu);
      float sg = rbf(__builtin_amdgcn_rcpf(1.f + __expf(-bf1((us)rb))));
      __syncthreads();
      float ys = (redD[r] + redD[8 + r]) + (redD[16 + r] + redD[24 + r]);
      float y = rbf(rbf(ys) * sg);
      if (lane < 8) out[(size_t)wrow * D_ + cb2 * 8 + r] = f2bf(fh + rbf(fx2 + y));
      STAMPT(2, 5, 320);
    }
    return;
  }
  unsigned raw[8];
#pragma unroll
  for (int r = 0; r < 8; ++r) raw[r] = xb[r * (D_ / 2) + tid];
  const u4 y0 = *(const u4*)(ytb + ((size_t)tb * D_ + 2 * tid) * 8);
  const u4 y1 = *(const u4*)(ytb + ((size_t)tb * D_ + 2 * tid) * 8 + 8);
  const f2 wv = unpk(((const unsigned*)wr)[tid]);
  f2 h[8];
  {
    float v[24];
#pragma unroll
    for (int r = 0; r < 8; ++r) {
      const float piv = bflo(xb[r * (D_ / 2)]);
      unsigned ya = y0[r >> 1], yb = y1[r >> 1];
      f2 yy = (r & 1) ? f2{bfhi(ya), bfhi(yb)} : f2{bflo(ya), bflo(yb)};
      f2 hs = unpk(raw[r]) + yy;
      h[r] = unpk(pk_bf(hs.x, hs.y));
      f2 dd = h[r] - piv;
      v[r] = dd.x + dd.y;
      v[r + 8] = __builtin_fmaf(dd.x, dd.x, dd.y * dd.y);
      v[r + 16] = __builtin_fmaf(dd.x, wv.x, dd.y * wv.y);
    }
    float w[12], o[6];
#pragma unroll
    for (int i = 0; i < 12; ++i) w[i] = swap32_add(v[i], v[i + 12]);
#pragma unroll
    for (int i = 0; i < 6; ++i) o[i] = row_sum(swap16_add(w[i], w[i + 6]));
    if ((lane & 15) == 0) {
      float* dst = redA + wave * 24 + (lane >> 4) * 6;
      *(f2*)dst = f2{o[0], o[1]}; *(f2*)(dst + 2) = f2{o[2], o[3]}; *(f2*)(dst + 4) = f2{o[4], o[5]};
    }
  }
  __syncthreads();
  STAMP(2, 1);
  __syncthreads();
  const int brow = (int)redW[0];
  unsigned cv = 0;
  unsigned* outr = (unsigned*)(out + (size_t)t0 * D_) + tid;
#pragma unroll
  for (int r = 0; r < 8; r += 2) {
    f4 c = *(const f4*)(abv + 2 * r);
    f2 o0 = h[r] + __builtin_elementwise_fma(h[r], f2{c.x, c.x}, f2{c.y, c.y});
    f2 o1 = h[r + 1] + __builtin_elementwise_fma(h[r + 1], f2{c.z, c.z}, f2{c.w, c.w});
    unsigned q0 = pk_bf(o0.x, o0.y), q1 = pk_bf(o1.x, o1.y);
    if (r != brow) outr[r * (D_ / 2)] = q0; else cv = q0;
    if (r + 1 != brow) outr[(r + 1) * (D_ / 2)] = q1; else cv = q1;
  }
  __syncthreads();
  const unsigned wkey = redW[2], wms = redW[3];
  const int wrow = 2047 - (int)(wkey & 2047u);
  if (f1) {
    const float wm = bflo(wms), wrs = __builtin_amdgcn_rcpf(bfhi(wms));
    const us* yw = ytb + (size_t)(wrow >> 3) * D_ * 8 + (wrow & 7);
    unsigned xr = ((const unsigned*)(x + (size_t)wrow * D_))[tid];
    us ya = yw[(2 * tid) * 8], yb = yw[(2 * tid + 1) * 8];
    f2 hs = unpk(xr) + f2{bf1(ya), bf1(yb)};
    f2 hh = unpk(pk_bf(hs.x, hs.y));
    f2 dd = unpk(pk_bf(hh.x - wm, hh.y - wm));
    f2 xs = unpk(pk_bf(dd.x * wrs, dd.y * wrs));
    float v[16] = {0, 0, 0, 0, 0, 0, 0, 0, 0, 0, 0, 0, 0, 0, 0, 0};
    const u4* wl = (const u4*)sw1 + 2 * tid;
    fma8(v, wl[0], xs.x); fma8(v, wl[1], xs.y); fma8(v + 8, wl[640], xs.x); fma8(v + 8, wl[641], xs.y);
    float o[4];
    wave_reduce16(v, o);
    int rr = lane >> 4;
    if ((lane & 15) == 0) *(f4*)(redC + wave * 16 + 4 * (rr & 1) + 8 * (rr >> 1)) = f4{o[0], o[1], o[2], o[3]};
    __syncthreads();
  }
  if (wkey != redW[1]) outr[brow * (D_ / 2)] = cv;
  if (f2r) {
    if (wave >= 1) {
      GAS unsigned* ap = (GAS unsigned*)(ws + WS_ACT) + (tid - 64);
      unsigned a[6];
      for (unsigned spins = 0;; ++spins) {
#pragma unroll
        for (int k = 0; k < 6; ++k) a[k] = __hip_atomic_load(ap + 256 * k, RLX_AGENT);
        bool ok = (a[0] & a[1] & a[2] & a[3] & a[4] & a[5]) >> 16;
        if (__all(ok) || spins > SPIN_CAP) break;
        __builtin_amdgcn_s_sleep(1);
      }
      float acc[8] = {0, 0, 0, 0, 0, 0, 0, 0};
#pragma unroll
      for (int k = 0; k < 6; ++k) fma8(acc, ((const u4*)sw2)[tid - 64 + 256 * k], bflo(a[k]));
      float o[2];
      wave_reduce8(acc, o);
      int rr = lane >> 4;
      if ((lane & 15) == 0) *(f2*)(redD + (wave - 1) * 8 + 2 * (rr & 1) + 4 * (rr >> 1)) = f2{o[0], o[1]};
    }
    __syncthreads();
  }
}

extern "C" __global__ void k9_end() {}

extern "C" void kernel_launch(void* const* d_in, const int* in_sizes, int n_in, void* d_out, int out_size,
                              void* d_ws, size_t ws_size, hipStream_t stream) {
  const us* x = (const us*)d_in[0];
  const us* Are = (const us*)d_in[1];
  const us* Aim = (const us*)d_in[2];
  const us* Bre = (const us*)d_in[3];
  const us* Bim = (const us*)d_in[4];
  const us* Cre = (const us*)d_in[5];
  const us* Cim = (const us*)d_in[6];
  const us* logdt = (const us*)d_in[7];
  const us* Dskip = (const us*)d_in[8];
  const us* wr = (const us*)d_in[10];
  const us* wg = (const us*)d_in[11];
  const us* wu = (const us*)d_in[12];
  const us* wd = (const us*)d_in[13];
  us* out = (us*)d_out;
  char* ws = (char*)d_ws;
  k1_ln_kgen<<<D_ + 128, 320, K1_LDS, stream>>>(x, Are, Aim, Bre, Bim, Cre, Cim, logdt, wr, wg, wu, wd, out, ws);
  k2_conv<<<D_, 256, K2_LDS, stream>>>(Dskip, ws);
  k3_tail<<<256, 448, 1024 + 20480 + 24576, stream>>>(x, wr, wg, wu, wd, out, ws);
}
```

```cpp
#include <hip/hip_runtime.h>
#include <hip/hip_bf16.h>
#include <stdint.h>

typedef _Float16 h8 __attribute__((ext_vector_type(8)));
typedef _Float16 h2 __attribute__((ext_vector_type(2)));
typedef float f4 __attribute__((ext_vector_type(4)));
typedef float f2 __attribute__((ext_vector_type(2)));
typedef unsigned u4 __attribute__((ext_vector_type(4)));
typedef unsigned u2 __attribute__((ext_vector_type(2)));
typedef unsigned short us;
typedef unsigned long long u64;
#define GAS __attribute__((address_space(1)))
#define LAS __attribute__((address_space(3)))

constexpr int L_ = 2048, D_ = 640, N_ = 192, F_ = 1536;
constexpr int LP = 2048 + 64;
#define STAMPD(kid, slot) do {} while (0)
#define STAMP(kid, slot) do {} while (0)
#define STAMPT(kid, slot, thr) do {} while (0)

constexpr int NR_LEN  = 608;
constexpr int F2_OFF  = 2 * NR_LEN;
constexpr int F3_OFF  = F2_OFF + 8 * 64;
constexpr int KIMG_ELEMS = F3_OFF + 4 * 80;
constexpr int KCHUNKS = KIMG_ELEMS / 8;

constexpr size_t WS_FLAGS = 0;
constexpr size_t WS_KEYS  = WS_FLAGS;
constexpr size_t WS_ACT   = WS_FLAGS + 16384;
constexpr size_t WS_WSUM  = 40960;
constexpr size_t WS_UT    = 40960 + 256;
constexpr size_t WS_YT    = WS_UT + (size_t)D_ * LP * 2;
constexpr size_t WS_TOTAL = WS_YT + (size_t)D_ * L_ * 2;

constexpr int TAB_PITCH = 200;
constexpr int SM_KC = 57 * TAB_PITCH * 4;
constexpr int SM_U  = SM_KC + 1552;
constexpr int K2_LDS = SM_U + 4608;
constexpr int SM_KI = 0;
constexpr int SM_P  = 4096;

constexpr int kc_of_v(int v) {
  if (v < 256)  { int h = v >> 7, r = v & 127; return ((h * 8 + (r & 7)) * 16) + (r >> 3); }
  if (v < 512)  { int w = v - 256, h = w >> 7, r = w & 127; return ((16 + h * 4 + ((r & 7) >> 1)) * 16) + (r >> 3); }
  if (v < 1024) { int w = v - 512, h = w >> 7, r = w & 127; return ((24 + h * 2 + ((r & 7) >> 2)) * 16) + (r >> 3); }
  if (v < 2048) { int w = v - 1024, h = w >> 7, r = w & 127; return ((32 + h) * 16) + (r >> 3); }
  return 40 * 16;
}
constexpr int bf16_round_int(int j) {
  if (j <= 256) return j;
  int step = j < 512 ? 2 : (j < 1024 ? 4 : 8);
  int lo = (j / step) * step, rem = j - lo;
  if (rem * 2 < step) return lo;
  if (rem * 2 > step) return lo + step;
  return ((lo / step) & 1) ? lo + step : lo;
}
constexpr int ZIDX = 768;

struct GatherTab { unsigned v[KIMG_ELEMS]; };
constexpr GatherTab make_gather() {
  GatherTab t{};
  for (int i = 0; i < KIMG_ELEMS; ++i) t.v[i] = SM_KC + ZIDX * 2;
  for (int e = 0; e < 2; ++e)
    for (int i = 0; i < NR_LEN; ++i) {
      int j = 543 - (i + e);
      if (j >= 0 && j < 515) t.v[e * NR_LEN + i] = (unsigned)(SM_KC + 2 * kc_of_v(bf16_round_int(j)));
    }
  for (int pi = 0; pi < 4; ++pi)
    for (int e = 0; e < 2; ++e)
      for (int i = 0; i < 64; ++i) {
        int p = 79 - (i + e);
        if (p >= 32 && p < 64) {
          int v = 16 * p + 8 * (pi >> 1) + ((pi & 1) ? 8 : 4);
          t.v[F2_OFF + (pi * 2 + e) * 64 + i] = (unsigned)(SM_KC + 2 * kc_of_v(v));
        }
      }
  for (int pi = 0; pi < 2; ++pi)
    for (int e = 0; e < 2; ++e)
      for (int i = 0; i < 80; ++i) {
        int qq = 127 - (i + e);
        if (qq >= 64 && qq < 128) {
          int v = 16 * qq + (pi ? 16 : 8);
          t.v[F3_OFF + (pi * 2 + e) * 80 + i] = (unsigned)(SM_KC + 2 * kc_of_v(v));
        }
      }
  return t;
}
__device__ const GatherTab g_gather = make_gather();

struct BoxTab { us v[7 * 64 * 8]; };
constexpr BoxTab make_box() {
  BoxTab t{};
  const int nn[6] = {3, 5, 3, 5, 7, 9};
  const int sh[6] = {3, 6, 11, 14, 5, 12};
  const int w0[6] = {-16, -16, -24, -24, -16, -24};
  for (int m = 0; m < 6; ++m)
    for (int lane = 0; lane < 64; ++lane)
      for (int j = 0; j < 8; ++j) {
        int b = lane & 15, k = 8 * (lane >> 4) + j;
        int idx = w0[m] + k;
        bool on = idx <= b - sh[m] && idx >= b - sh[m] - (nn[m] - 1);
        t.v[(m * 64 + lane) * 8 + j] = on ? 0x3C00 : 0;
      }
  for (int lane = 0; lane < 64; ++lane)
    for (int j = 0; j < 8; ++j) {
      int b = lane & 15, k = 8 * (lane >> 4) + j;
      t.v[(6 * 64 + lane) * 8 + j] = (k - b == 4 || k - b == 5) ? 0xFFFF : 0;
    }
  return t;
}
__device__ const BoxTab g_box = make_box();

__device__ __forceinline__ float bflo(unsigned w) { return __uint_as_float(w << 16); }
__device__ __forceinline__ float bfhi(unsigned w) { return __uint_as_float(w & 0xffff0000u); }
__device__ __forceinline__ float bf1(us v) { return __uint_as_float((unsigned)v << 16); }
__device__ __forceinline__ us f2bf(float f) { __bf16 b = (__bf16)f; return __builtin_bit_cast(us, b); }
__device__ __forceinline__ float rbf(float f) { return bf1(f2bf(f)); }
typedef __bf16 b2 __attribute__((ext_vector_type(2)));
__device__ __forceinline__ unsigned pk_bf(float a, float b) {
  b2 v; v.x = (__bf16)a; v.y = (__bf16)b; return __builtin_bit_cast(unsigned, v);
}
__device__ __forceinline__ unsigned pk_h(float a, float b) {
  h2 v; v.x = (_Float16)a; v.y = (_Float16)b; return __builtin_bit_cast(unsigned, v);
}
template <int CTRL> __device__ __forceinline__ float dpp_add(float v) {
  int t = __builtin_amdgcn_update_dpp(0, __float_as_int(v), CTRL, 0xf, 0xf, true);
  return v + __int_as_float(t);
}
__device__ __forceinline__ float row_sum(float v) {
  v = dpp_add<0x121>(v); v = dpp_add<0x122>(v); v = dpp_add<0x124>(v); v = dpp_add<0x128>(v);
  return v;
}
__device__ __forceinline__ float swap32_add(float a, float b) {
  auto r = __builtin_amdgcn_permlane32_swap(__float_as_uint(a), __float_as_uint(b), false, false);
  return __uint_as_float(r[0]) + __uint_as_float(r[1]);
}
__device__ __forceinline__ float swap16_add(float a, float b) {
  auto r = __builtin_amdgcn_permlane16_swap(__float_as_uint(a), __float_as_uint(b), false, false);
  return __uint_as_float(r[0]) + __uint_as_float(r[1]);
}
__device__ __forceinline__ void wave_reduce16(const float (&v)[16], float (&out)[4]) {
  float w[8];
#pragma unroll
  for (int i = 0; i < 8; ++i) w[i] = swap32_add(v[i], v[i + 8]);
#pragma unroll
  for (int i = 0; i < 4; ++i) out[i] = row_sum(swap16_add(w[i], w[i + 4]));
}
__device__ __forceinline__ void wave_reduce8(const float (&v)[8], float (&out)[2]) {
  float w[4];
#pragma unroll
  for (int i = 0; i < 4; ++i) w[i] = swap32_add(v[i], v[i + 4]);
#pragma unroll
  for (int i = 0; i < 2; ++i) out[i] = row_sum(swap16_add(w[i], w[i + 2]));
}
__device__ __forceinline__ float wave_reduce4(const float (&v)[4]) {
  float w0 = swap32_add(v[0], v[2]), w1 = swap32_add(v[1], v[3]);
  return row_sum(swap16_add(w0, w1));
}
#define DPPA(i, st) "v_add_f32_dpp %" #i ", %" #i ", %" #i " row_ror:" #st " row_mask:0xf bank_mask:0xf\n\t"
__device__ __forceinline__ void row_sum8(float (&o)[8]) {
  asm volatile("s_nop 1\n\t"
    DPPA(0,1) DPPA(1,1) DPPA(2,1) DPPA(3,1) DPPA(4,1) DPPA(5,1) DPPA(6,1) DPPA(7,1)
    DPPA(0,2) DPPA(1,2) DPPA(2,2) DPPA(3,2) DPPA(4,2) DPPA(5,2) DPPA(6,2) DPPA(7,2)
    DPPA(0,4) DPPA(1,4) DPPA(2,4) DPPA(3,4) DPPA(4,4) DPPA(5,4) DPPA(6,4) DPPA(7,4)
    DPPA(0,8) DPPA(1,8) DPPA(2,8) DPPA(3,8) DPPA(4,8) DPPA(5,8) DPPA(6,8) DPPA(7,8)
    : "+v"(o[0]), "+v"(o[1]), "+v"(o[2]), "+v"(o[3]), "+v"(o[4]), "+v"(o[5]), "+v"(o[6]), "+v"(o[7]));
}
__device__ __forceinline__ void row_sum6(float (&o)[6]) {
  asm volatile("s_nop 1\n\t"
    DPPA(0,1) DPPA(1,1) DPPA(2,1) DPPA(3,1) DPPA(4,1) DPPA(5,1)
    DPPA(0,2) DPPA(1,2) DPPA(2,2) DPPA(3,2) DPPA(4,2) DPPA(5,2)
    DPPA(0,4) DPPA(1,4) DPPA(2,4) DPPA(3,4) DPPA(4,4) DPPA(5,4)
    DPPA(0,8) DPPA(1,8) DPPA(2,8) DPPA(3,8) DPPA(4,8) DPPA(5,8)
    : "+v"(o[0]), "+v"(o[1]), "+v"(o[2]), "+v"(o[3]), "+v"(o[4]), "+v"(o[5]));
}
__device__ __forceinline__ void row_sum4(float (&o)[4]) {
  asm volatile("s_nop 1\n\t"
    DPPA(0,1) DPPA(1,1) DPPA(2,1) DPPA(3,1)
    DPPA(0,2) DPPA(1,2) DPPA(2,2) DPPA(3,2)
    DPPA(0,4) DPPA(1,4) DPPA(2,4) DPPA(3,4)
    DPPA(0,8) DPPA(1,8) DPPA(2,8) DPPA(3,8)
    : "+v"(o[0]), "+v"(o[1]), "+v"(o[2]), "+v"(o[3]));
}
__device__ __forceinline__ void par_sum4(float (&o)[4]) {
  asm volatile("s_nop 1\n\t"
    DPPA(0,2) DPPA(1,2) DPPA(2,2) DPPA(3,2)
    DPPA(0,4) DPPA(1,4) DPPA(2,4) DPPA(3,4)
    DPPA(0,8) DPPA(1,8) DPPA(2,8) DPPA(3,8)
    : "+v"(o[0]), "+v"(o[1]), "+v"(o[2]), "+v"(o[3]));
}
__device__ __forceinline__ float rdlane(float v, int l) {
  return __int_as_float(__builtin_amdgcn_readlane(__float_as_int(v), l));
}

__device__ __forceinline__ f2 cmul(f2 a, f2 b) {
  f2 c;
  asm("v_pk_mul_f32 %0, %1, %2 op_sel:[0,0] op_sel_hi:[1,0]\n\tv_pk_fma_f32 %0, %1, %2, %0 op_sel:[1,1,0] op_sel_hi:[0,1,1] neg_lo:[1,0,0]"
      : "=&v"(c) : "v"(a), "v"(b));
  return c;
}
__device__ __forceinline__ f2 cexp_p(float lam, float rh, float rl, float p) {
  float mag = __builtin_amdgcn_exp2f(lam * p);
  float x = rh * p;
  float f = x - __builtin_rintf(x);
  f = __builtin_fmaf(rl, p, f);
  f2 w; w.x = mag * __builtin_amdgcn_cosf(f); w.y = mag * __builtin_amdgcn_sinf(f); return w;
}
#define MFMA16(a, b, c) __builtin_amdgcn_mfma_f32_16x16x32_f16((a), (b), (c), 0, 0, 0)
__device__ __forceinline__ h8 as_h8(u4 v) { return __builtin_bit_cast(h8, v); }

__device__ __forceinline__ void ln_from_sums(float s1, float s2, float piv, float& mb, float& sb) {
  float dm = s1 * (1.f / 640.f);
  float var = fmaxf(__builtin_fmaf(-dm, dm, s2 * (1.f / 640.f)), 0.f);
  mb = rbf(piv + dm);
  sb = rbf(__builtin_amdgcn_sqrtf(rbf(rbf(var) + 1e-5f)));
}
__device__ __forceinline__ f2 unpk(unsigned w) { return f2{bflo(w), bfhi(w)}; }

extern "C" __global__ __launch_bounds__(384, 2) void k1_ln(
    const us* __restrict__ x, const us* __restrict__ Are, const us* __restrict__ Aim,
    const us* __restrict__ Bre, const us* __restrict__ Bim, const us* __restrict__ Cre,
    const us* __restrict__ Cim, const us* __restrict__ wr,
    const us* __restrict__ wg, const us* __restrict__ wu, const us* __restrict__ wd, const us* __restrict__ out,
    char* __restrict__ ws) {
  __shared__ __attribute__((aligned(16))) float red[5 * 16 + 16 + 8];
  const int tid = threadIdx.x, lane = tid & 63, wave = __builtin_amdgcn_readfirstlane(tid >> 6);
  const int bid = blockIdx.x;
  STAMP(0, 0);
  if (wave == 5) {
    u64 pc; int o1, o2;
    asm volatile("s_getpc_b64 %0\n\ts_mov_b32 %1, k1_ln@rel32@lo+4\n\ts_mov_b32 %2, k9_end@rel32@lo+12" : "=s"(pc), "=s"(o1), "=s"(o2));
    const int xcd = bid & 7, jb = bid >> 3;
    const GAS char* cbase = (const GAS char*)(pc + (long long)o1);
    unsigned acc = 0;
    {
      const int id = jb * 64 + lane;
      const GAS char* p;
      if (id < 384) { int o = id * 64; p = cbase + (o < o2 - o1 ? o : 0); }
      else if (id < 384 + 1440) { int i = id - 384, ar = i / 240, q = i - ar * 240;
        const us* base = ar == 0 ? Are : ar == 1 ? Aim : ar == 2 ? Bre : ar == 3 ? Bim : ar == 4 ? Cre : Cim;
        p = (const GAS char*)base + xcd * 30720 + q * 128; }
      else if (id < 1824 + 64) p = (const GAS char*)g_gather.v + (id - 1824) * 128;
      else if (id < 1888 + 56) p = (const GAS char*)g_box.v + (id - 1888) * 128;
      else if (id < 1944 + 40) p = (const GAS char*)out + (size_t)(id - 1944) * 65536;
      else p = (const GAS char*)(ws + WS_YT) + (size_t)((id - 1984) % 40) * 65536;
      acc = *(const GAS unsigned*)p;
    }
    __builtin_amdgcn_s_barrier();
#pragma unroll
    for (int k = 0; k < 4; ++k) {
      int id = (jb * 4 + k) * 64 + lane;
      id = id < 6912 ? id : 6911;
      const GAS char* p;
      if (id < 3840) { int r3 = id / 3, q3 = id - r3 * 3;
        p = (const GAS char*)((r3 < 640 ? wg + (size_t)r3 * F_ : wu + (size_t)(r3 - 640) * F_) + xcd * 192 + q3 * 64); }
      else { int i2 = id - 3840; p = (const GAS char*)(wd + (size_t)(i2 >> 1) * D_ + xcd * 80 + (i2 & 1) * 78); }
      acc ^= *(const GAS unsigned*)p;
      if (k == 0) __builtin_amdgcn_s_barrier();
    }
    asm volatile("" :: "v"(acc));
    return;
  }
  const int t0 = ((bid & 7) * 32 + (bid >> 3)) * 8;
  const unsigned* xb = (const unsigned*)(x + (size_t)t0 * D_);
  unsigned raw[8];
#pragma unroll
  for (int r = 0; r < 8; ++r) raw[r] = xb[r * (D_ / 2) + tid];
  f2* ab = (f2*)(red + 80);
  unsigned* pv = (unsigned*)(red + 96);
  f2 a[8];
  {
    float v[16];
#pragma unroll
    for (int r = 0; r < 8; ++r) {
      const float piv = bflo(xb[r * (D_ / 2)]);
      a[r] = unpk(raw[r]);
      f2 dd = a[r] - piv;
      v[r] = dd.x + dd.y; v[r + 8] = __builtin_fmaf(dd.x, dd.x, dd.y * dd.y);
    }
    if (tid == 0) { *(u4*)(pv) = u4{raw[0], raw[1], raw[2], raw[3]}; *(u4*)(pv + 4) = u4{raw[4], raw[5], raw[6], raw[7]}; }
    float w[8], o[4];
#pragma unroll
    for (int i = 0; i < 8; ++i) w[i] = swap32_add(v[i], v[i + 8]);
#pragma unroll
    for (int i = 0; i < 4; ++i) o[i] = swap16_add(w[i], w[i + 4]);
    row_sum4(o);
    if ((lane & 15) == 0) *(f4*)(red + wave * 16 + (lane >> 4) * 4) = f4{o[0], o[1], o[2], o[3]};
  }
  __syncthreads();
  if (wave == 0) {
    const int r = lane & 7;
    float s1 = (red[r] + red[16 + r]) + (red[32 + r] + red[48 + r]) + red[64 + r];
    float s2 = (red[8 + r] + red[24 + r]) + (red[40 + r] + red[56 + r]) + red[72 + r];
    float mb, sb;
    ln_from_sums(s1, s2, bflo(pv[r]), mb, sb);
    float aa = 128.f * __builtin_amdgcn_rcpf(sb);
    if (lane < 8) ab[r] = f2{aa, -mb * aa};
  } else if (wave == 1 && bid == 0) {
    const unsigned* w2 = (const unsigned*)wr + lane;
    float sw = 0.f;
#pragma unroll
    for (int k = 0; k < 5; ++k) { unsigned q = w2[64 * k]; sw += bflo(q) + bfhi(q); }
    sw = row_sum(sw);
    sw = rdlane(sw, 0) + rdlane(sw, 16) + rdlane(sw, 32) + rdlane(sw, 48);
    if (lane == 0) *(float*)(ws + WS_WSUM) = sw;
  } else if (wave >= 1 && bid >= 1 && bid <= 10) {
    ((u4*)(ws + WS_FLAGS))[(bid - 1) * 256 + tid - 64] = u4{0, 0, 0, 0};
  }
  __syncthreads();
  unsigned p0[4], p1[4];
#pragma unroll
  for (int r = 0; r < 8; r += 2) {
    f4 c = *(const f4*)(ab + r);
    f2 u0 = __builtin_elementwise_fma(a[r], f2{c.x, c.x}, f2{c.y, c.y});
    f2 u1 = __builtin_elementwise_fma(a[r + 1], f2{c.z, c.z}, f2{c.w, c.w});
    p0[r >> 1] = pk_h(u0.x, u1.x); p1[r >> 1] = pk_h(u0.y, u1.y);
  }
  us* ut = (us*)(ws + WS_UT) + (size_t)(2 * tid) * LP + t0;
  *(u4*)(ut) = u4{p0[0], p0[1], p0[2], p0[3]};
  *(u4*)(ut + LP) = u4{p1[0], p1[1], p1[2], p1[3]};
  STAMP(0, 5);
  STAMPD(0, 6);
}

__device__ __forceinline__ h8 ld_h8(const char* p) { return as_h8(*(const u4*)p); }
__device__ __forceinline__ h8 ld_4dw(const char* p0, const char* p1) {
  const unsigned* a = (const unsigned*)p0; const unsigned* b = (const unsigned*)p1;
  return as_h8(u4{a[0], a[1], b[0], b[1]});
}
__device__ __forceinline__ h8 cvt_pair(f4 a, f4 b) {
  return as_h8(u4{pk_h(a[0], a[1]), pk_h(a[2], a[3]), pk_h(b[0], b[1]), pk_h(b[2], b[3])});
}
__device__ __forceinline__ void glds16(const void* g, void* l) {
  __builtin_amdgcn_global_load_lds((const GAS unsigned*)g, (LAS unsigned*)l, 16, 0, 0);
}
__device__ __forceinline__ void conv_epi(f4 acc, float sc, us* dst) {
  u2 o; o.x = pk_bf(acc[0] * sc, acc[1] * sc); o.y = pk_bf(acc[2] * sc, acc[3] * sc);
  *(u2*)dst = o;
}
#define LDU(a) ((unsigned)*(const LAS us*)(size_t)(a))
#define LDP(a, b) (LDU(a) | (LDU(b) << 16))

template <int OFF> __device__ __forceinline__ u4 lds_rd128(unsigned addr) {
  u4 v; asm volatile("ds_read_b128 %0, %1 offset:%2" : "=v"(v) : "v"(addr), "i"(OFF) : "memory"); return v;
}
template <int N> __device__ __forceinline__ void lgkm_wait() {
  asm volatile("s_waitcnt lgkmcnt(%0)" :: "i"(N) : "memory"); __builtin_amdgcn_sched_barrier(0);
}
template <int M, int M0, int M1, bool T3, int PF>
__device__ __forceinline__ void near_step(const h8 (&A)[17], u4 (&f)[PF], unsigned fa, f4& a0, f4& a1, f4& a2) {
  if constexpr (M < M1) {
    if constexpr (M >= M0 + PF) lgkm_wait<((M1 - 1 - M) < (PF - 1)) ? (M1 - 1 - M) : (PF - 1)>();
    const h8 ff = as_h8(f[(M - M0) % PF]);
    if constexpr (M <= 16) a0 = MFMA16(A[M], ff, a0);
    if constexpr (M >= 8 && M <= 24) a1 = MFMA16(A[M - 8], ff, a1);
    if constexpr (T3 && M >= 16) a2 = MFMA16(A[M - 16], ff, a2);
    __builtin_amdgcn_sched_barrier(0);
    if constexpr (M + PF < M1) f[(M - M0) % PF] = lds_rd128<(M + PF) * 64>(fa);
    near_step<M + 1, M0, M1, T3, PF>(A, f, fa, a0, a1, a2);
  }
}
template <int M, int M0, int PF> __device__ __forceinline__ void near_fill(u4 (&f)[PF], unsigned fa) {
  if constexpr (M < M0 + PF) { f[M - M0] = lds_rd128<M * 64>(fa); near_fill<M + 1, M0, PF>(f, fa); }
}
template <int M0, int M1, bool T3>
__device__ __forceinline__ void near_band(const h8 (&A)[17], unsigned fa, f4& a0, f4& a1, f4& a2) {
  constexpr int PF = 6;
  u4 f[PF];
  near_fill<M0, M0, PF>(f, fa);
  __builtin_amdgcn_s_waitcnt(0xC07F);
  __builtin_amdgcn_sched_barrier(0);
  near_step<M0, M0, M1, T3, PF>(A, f, fa, a0, a1, a2);
}

extern "C" __global__ __launch_bounds__(256, 2) void k2_conv(
    const us* __restrict__ Are, const us* __restrict__ Aim, const us* __restrict__ Bre, const us* __restrict__ Bim,
    const us* __restrict__ Cre, const us* __restrict__ Cim, const us* __restrict__ logdt,
    const us* __restrict__ Dskip, char* __restrict__ ws) {
  extern __shared__ __attribute__((aligned(16))) char sm[];
  const int tid = threadIdx.x, lane = tid & 63, wave = __builtin_amdgcn_readfirstlane(tid >> 6);
  const int d = (blockIdx.x & 7) * 80 + (blockIdx.x >> 3);
  const int col = lane & 15, g = lane >> 4;
  STAMP(1, 0);
  char* kc = sm + SM_KC;
  const char* ub = sm + SM_U + (256 + 16 * col + 8 * g) * 2;
  us* yt = (us*)(ws + WS_YT) + ((size_t)(2 * col + (g >> 1)) * D_ + d) * 8 + (g & 1) * 4;
  constexpr int YTS = 32 * D_ * 8;
  const f4 Z = {0.f, 0.f, 0.f, 0.f};
  const u4* gt = (const u4*)g_gather.v + 2 * tid;
  if (wave == 3) {
    const char* gu = ws + WS_UT + (size_t)d * LP * 2;
#pragma unroll
    for (int k = 0; k < 4; ++k) glds16(gu + (k * 64 + lane) * 16, sm + SM_U + 512 + k * 1024);
    const u4* bt = (const u4*)g_box.v + lane;
    u4 bx[7];
#pragma unroll
    for (int m = 0; m < 7; ++m) bx[m] = bt[64 * m];
    if (lane < 32) *(u4*)(sm + SM_U + lane * 16) = u4{0, 0, 0, 0};
    __builtin_amdgcn_s_barrier();
    asm volatile("s_waitcnt vmcnt(0)" ::: "memory");
    h8 sa[6], sb[6], s3[4];
#pragma unroll
    for (int tau = 0; tau < 6; ++tau) {
      h8 w1 = ld_h8(ub + (256 * tau - 16) * 2), w2 = ld_h8(ub + (256 * tau - 24) * 2);
      sa[tau] = cvt_pair(MFMA16(w1, as_h8(bx[0]), Z), MFMA16(w1, as_h8(bx[1]), Z));
      sb[tau] = cvt_pair(MFMA16(w2, as_h8(bx[2]), Z), MFMA16(w2, as_h8(bx[3]), Z));
      if (tau < 4) s3[tau] = cvt_pair(MFMA16(w1, as_h8(bx[4]), Z), MFMA16(w2, as_h8(bx[5]), Z));
    }
    u4 ga0 = gt[0], ga1 = gt[1];
    __builtin_amdgcn_s_barrier();
    __syncthreads();
    *(u4*)(sm + SM_KI + tid * 16) = u4{LDP(ga0.x, ga0.y), LDP(ga0.z, ga0.w), LDP(ga1.x, ga1.y), LDP(ga1.z, ga1.w)};
    const unsigned kv = LDU(SM_KC + 32 * 16 * 2) * 0x10001u;
    __syncthreads();
    f4 acc[6];
    acc[0] = Z; acc[1] = Z;
    {
      h8 As = as_h8(u4{bx[6].x & kv, bx[6].y & kv, bx[6].z & kv, bx[6].w & kv});
#pragma unroll
      for (int i = 0; i < 4; ++i) acc[2 + i] = MFMA16(As, ld_h8(ub + (256 * (4 + i) - 1032) * 2), Z);
    }
    {
      const int ib = 79 - col + 4 * g;
      const char* kb = sm + SM_KI + (F2_OFF + (ib & 1) * 64 + (ib & ~1)) * 2;
#pragma unroll
      for (int dl = 2; dl <= 4; ++dl) {
        h8 tA = ld_4dw(kb - dl * 32, kb + 256 - dl * 32), tB = ld_4dw(kb + 512 - dl * 32, kb + 768 - dl * 32);
#pragma unroll
        for (int tau = 0; tau < 6; ++tau)
          if (tau + dl <= 7) { acc[tau + dl - 2] = MFMA16(sa[tau], tA, acc[tau + dl - 2]); acc[tau + dl - 2] = MFMA16(sb[tau], tB, acc[tau + dl - 2]); }
      }
    }
    {
      const int ib = 127 - col + 4 * g;
      const char* kb = sm + SM_KI + (F3_OFF + (ib & 1) * 80 + (ib & ~1)) * 2;
#pragma unroll
      for (int dl = 4; dl <= 7; ++dl) {
        h8 tt = ld_4dw(kb - dl * 32, kb + 320 - dl * 32);
#pragma unroll
        for (int tau = 0; tau < 4; ++tau)
          if (tau + dl <= 7) acc[tau + dl - 2] = MFMA16(s3[tau], tt, acc[tau + dl - 2]);
      }
    }
#pragma unroll
    for (int i = 0; i < 6; ++i) *(f4*)(sm + SM_P + i * 1024 + lane * 16) = acc[i];
    __syncthreads();
    return;
  }
  __builtin_amdgcn_s_setprio(3);
  const size_t pidx = (size_t)d * N_ + tid;
  us r_are = Are[pidx], r_aim = Aim[pidx], r_bre = Bre[pidx], r_bim = Bim[pidx], r_cre = Cre[pidx], r_cim = Cim[pidx];
  const unsigned ldw = ((const unsigned*)logdt)[d >> 1], dsw = ((const unsigned*)Dskip)[d >> 1];
  const float dt = rbf(__expf((d & 1) ? bfhi(ldw) : bflo(ldw)));
  const float dsk = (d & 1) ? bfhi(dsw) : bflo(dsw);
  asm volatile("s_mov_b32 m0, %0" :: "s"(wave * 256) : "memory");
  float ar = dt * bf1(r_are), ai = dt * bf1(r_aim);
  f2 dB = {dt * bf1(r_bre), dt * bf1(r_bim)};
  f2 Cc = {bf1(r_cre), bf1(r_cim)};
  f2 CB = cmul(Cc, dB);
  const float LOG2E = 1.4426950408889634f;
  const float I2PH = 0.15915494309189535f;
  const float I2PL = (float)(0.15915494309189533577 - (double)0.15915494309189535f);
  float lam = ar * LOG2E;
  float rh = ai * I2PH;
  float rl = __builtin_fmaf(ai, I2PL, __builtin_fmaf(ai, I2PH, -rh));
  {
    float gg = fmaxf(1.f, __builtin_amdgcn_exp2f(lam * 2048.f));
    float bnd = row_sum((fabsf(CB.x) + fabsf(CB.y)) * gg);
    if ((lane & 15) == 0) ((float*)(kc))[wave * 4 + (lane >> 4)] = bnd;
  }
  f2 w1 = cexp_p(lam, rh, rl, 1.f), w16 = cexp_p(lam, rh, rl, 16.f), w256 = cexp_p(lam, rh, rl, 256.f);
  f2 w2 = cmul(w1, w1), w32 = cmul(w16, w16), w512 = cmul(w256, w256);
  f2 w4 = cmul(w2, w2), w64 = cmul(w32, w32), w1024 = cmul(w512, w512);
  f2 w8 = cmul(w4, w4), w128 = cmul(w64, w64);
#define DSW(row, val) asm volatile("ds_write_addtid_b32 %0 offset:%1" :: "v"(val), "i"((row) * TAB_PITCH * 4) : "memory")
#define PUTE(row, c) do { f2 c_ = (c); DSW((row), pk_h(c_.x, -c_.y)); } while (0)
#define PUT(row, c) do { f2 c_ = (c); DSW(16 + (row), pk_h(c_.x, c_.y)); } while (0)
  {
    f2 e[8];
    e[1] = w8; e[2] = w16; e[3] = cmul(w8, w16); e[4] = w32;
    e[5] = cmul(w32, e[1]); e[6] = cmul(w32, e[2]); e[7] = cmul(w32, e[3]);
    DSW(0, 0x00003C00u);
#pragma unroll
    for (int l = 1; l < 8; ++l) PUTE(l, e[l]);
    PUTE(8, w64);
#pragma unroll
    for (int l = 1; l < 8; ++l) PUTE(8 + l, cmul(w64, e[l]));
  }
  __syncthreads();
  STAMP(1, 1);
  u4 ga0 = gt[0], ga1 = gt[1];
  float sc, unsc;
  {
    f4 q0 = *(const f4*)(kc), q1 = *(const f4*)(kc + 16), q2 = *(const f4*)(kc + 32);
    q0 += q1; q0 += q2;
    float sig = ((q0.x + q0.y) + (q0.z + q0.w)) + fabsf(dsk);
    int ex = (int)(__float_as_uint(sig) >> 23) & 255; ex = ex < 24 ? 24 : (ex > 253 ? 253 : ex);
    sc = __uint_as_float((unsigned)(267 - ex) << 23);
    unsc = __uint_as_float((unsigned)(ex - 20) << 23);
    if (tid == 0) *(unsigned*)(kc + ZIDX * 2) = 0;
    f2 c[8];
    c[0] = CB * sc;
    c[1] = cmul(c[0], w1); c[2] = cmul(c[0], w2); c[4] = cmul(c[0], w4); c[3] = cmul(c[1], w2);
    c[5] = cmul(c[1], w4); c[6] = cmul(c[2], w4); c[7] = cmul(c[3], w4);
#pragma unroll
    for (int r = 0; r < 8; ++r) {
      PUT(r, c[r]);
      f2 ch = cmul(c[r], w128); PUT(8 + r, ch);
      if ((r & 1) == 0) {
        f2 s0 = cmul(c[r], w256), s1 = cmul(ch, w256);
        PUT(16 + (r >> 1), s0); PUT(20 + (r >> 1), s1);
        if ((r & 3) == 0) {
          const int rho = r >> 2;
          f2 z0 = cmul(c[r], w512), z1 = cmul(ch, w512), z2 = cmul(s0, w512), z3 = cmul(s1, w512);
          PUT(24 + 0 + rho, z0); PUT(24 + 2 + rho, z1); PUT(24 + 4 + rho, z2); PUT(24 + 6 + rho, z3);
          if (r == 0) {
            f2 y0 = cmul(c[0], w1024);
            PUT(32, y0); PUT(33, cmul(ch, w1024)); PUT(34, cmul(s0, w1024)); PUT(35, cmul(s1, w1024));
            PUT(36, cmul(z0, w1024)); PUT(37, cmul(z1, w1024)); PUT(38, cmul(z2, w1024)); PUT(39, cmul(z3, w1024));
            PUT(40, cmul(y0, w1024));
          }
        }
      }
    }
  }
  asm volatile("s_waitcnt lgkmcnt(0)" ::: "memory");
  __syncthreads();
  STAMP(1, 2);
  {
    const unsigned* tab = (const unsigned*)sm;
    f4 acc0 = Z, acc1 = Z;
    const unsigned* pa = tab + col * TAB_PITCH + g * 4;
    const unsigned* pb = tab + (16 + wave * 16 + col) * TAB_PITCH + g * 4;
    u4 ea[12], pb_[12];
#pragma unroll
    for (int kk = 0; kk < 12; ++kk) { ea[kk] = *(const u4*)(pa + kk * 16); pb_[kk] = *(const u4*)(pb + kk * 16); }
#pragma unroll
    for (int kk = 0; kk < 12; kk += 2) {
      acc0 = MFMA16(as_h8(ea[kk]), as_h8(pb_[kk]), acc0);
      acc1 = MFMA16(as_h8(ea[kk + 1]), as_h8(pb_[kk + 1]), acc1);
    }
    __builtin_amdgcn_sched_group_barrier(0x100, 24, 0);
    __builtin_amdgcn_sched_group_barrier(0x008, 12, 0);
    acc0 += acc1;
    acc0[0] = __builtin_fmaf(tid == 0 ? dsk : 0.f, sc, acc0[0]);
    u2 o; o.x = pk_h(acc0[0], acc0[1]); o.y = pk_h(acc0[2], acc0[3]);
    *(u2*)(kc + ((wave * 16 + col) * 16 + 4 * g) * 2) = o;
  }
  __syncthreads();
  *(u4*)(sm + SM_KI + tid * 16) = u4{LDP(ga0.x, ga0.y), LDP(ga0.z, ga0.w), LDP(ga1.x, ga1.y), LDP(ga1.z, ga1.w)};
  __syncthreads();
  __builtin_amdgcn_s_setprio(0);
  STAMP(1, 3);
  const int i0 = 15 - col + 8 * g;
  const char* ka = sm + SM_KI + ((i0 & 1) * NR_LEN + (i0 & ~1)) * 2;
  const int n0 = 3 * wave;
  f4 a0 = Z, a1 = Z, a2 = Z;
  const unsigned fa = (unsigned)(SM_U + (256 + 16 * col + 8 * g + 256 * n0 - 528) * 2);
  h8 A[17];
#pragma unroll
  for (int k = 0; k < 17; ++k) A[k] = ld_4dw(ka + k * 64, ka + k * 64 + 8);
  __builtin_amdgcn_sched_barrier(0);
  if (wave == 0) near_band<9, 33, true>(A, fa, a0, a1, a2);
  else if (wave == 1) near_band<0, 33, true>(A, fa, a0, a1, a2);
  else near_band<0, 25, false>(A, fa, a0, a1, a2);
  asm volatile("s_nop 7\n\ts_nop 7" : "+v"(a0), "+v"(a1), "+v"(a2));
  __syncthreads();
  {
    const char* pp = sm + SM_P + lane * 16 + (n0 - 2) * 1024;
    if (wave != 0) { a0 += *(const f4*)(pp); a1 += *(const f4*)(pp + 1024); }
    conv_epi(a0, unsc, yt + YTS * n0);
    conv_epi(a1, unsc, yt + YTS * (n0 + 1));
    if (wave != 2) { a2 += *(const f4*)(pp + 2048); conv_epi(a2, unsc, yt + YTS * (n0 + 2)); }
  }
  STAMP(1, 4);
  STAMPD(1, 5);
}

__device__ __forceinline__ unsigned ord16(unsigned b) { return (b & 0x8000u) ? (~b & 0xffffu) : (b | 0x8000u); }
__device__ __forceinline__ void fma8(float* acc, u4 w, float xv) {
  acc[0] = __builtin_fmaf(bflo(w.x), xv, acc[0]); acc[1] = __builtin_fmaf(bfhi(w.x), xv, acc[1]);
  acc[2] = __builtin_fmaf(bflo(w.y), xv, acc[2]); acc[3] = __builtin_fmaf(bfhi(w.y), xv, acc[3]);
  acc[4] = __builtin_fmaf(bflo(w.z), xv, acc[4]); acc[5] = __builtin_fmaf(bfhi(w.z), xv, acc[5]);
  acc[6] = __builtin_fmaf(bflo(w.w), xv, acc[6]); acc[7] = __builtin_fmaf(bfhi(w.w), xv, acc[7]);
}
#define RLX_AGENT __ATOMIC_RELAXED, __HIP_MEMORY_SCOPE_AGENT
constexpr unsigned SPIN_CAP = 1u << 22;

__device__ __forceinline__ u64 sweep_keys(char* ws, int lane, int rep, u64* lk) {
  GAS u64* kp = (GAS u64*)(ws + WS_KEYS) + rep * 256 + lane;
  u64 b;
  for (unsigned spins = 0;; ++spins) {
    u64 k0 = __hip_atomic_load(kp, RLX_AGENT), k1 = __hip_atomic_load(kp + 64, RLX_AGENT);
    u64 k2 = __hip_atomic_load(kp + 128, RLX_AGENT), k3 = __hip_atomic_load(kp + 192, RLX_AGENT);
    bool ok = (long long)(k0 & k1 & k2 & k3) < 0;
    b = k0 > k1 ? k0 : k1; u64 c = k2 > k3 ? k2 : k3; b = b > c ? b : c;
    if (__all(ok) || spins > SPIN_CAP) {
      if (lk) { lk[lane] = k0; lk[lane + 64] = k1; lk[lane + 128] = k2; lk[lane + 192] = k3; }
      break;
    }
    __builtin_amdgcn_s_sleep(1);
  }
  unsigned hk = (unsigned)(b >> 32), m = hk;
  m = max(m, (unsigned)__builtin_amdgcn_update_dpp(0, (int)m, 0x121, 0xf, 0xf, true));
  m = max(m, (unsigned)__builtin_amdgcn_update_dpp(0, (int)m, 0x122, 0xf, 0xf, true));
  m = max(m, (unsigned)__builtin_amdgcn_update_dpp(0, (int)m, 0x124, 0xf, 0xf, true));
  m = max(m, (unsigned)__builtin_amdgcn_update_dpp(0, (int)m, 0x128, 0xf, 0xf, true));
  unsigned m0 = (unsigned)__builtin_amdgcn_readlane((int)m, 0), m1 = (unsigned)__builtin_amdgcn_readlane((int)m, 16);
  unsigned m2 = (unsigned)__builtin_amdgcn_readlane((int)m, 32), m3 = (unsigned)__builtin_amdgcn_readlane((int)m, 48);
  m0 = max(max(m0, m1), max(m2, m3));
  int src = __builtin_ctzll(__ballot(hk == m0));
  unsigned lo = (unsigned)__builtin_amdgcn_readlane((int)(unsigned)b, src);
  return ((u64)m0 << 32) | lo;
}
__device__ __forceinline__ float par_sum(float v) {
  v = dpp_add<0x122>(v); v = dpp_add<0x124>(v); v = dpp_add<0x128>(v);
  return v;
}
__device__ __forceinline__ float xs_of(us xv, us yv, float wm, float wrs, float& hh) {
  hh = rbf(bf1(xv) + bf1(yv));
  return rbf(rbf(hh - wm) * wrs);
}

extern "C" __global__ __launch_bounds__(384, 2) void k3_tail(const us* __restrict__ x, const us* __restrict__ wr,
                                                  const us* __restrict__ wg, const us* __restrict__ wu,
                                                  const us* __restrict__ wd, us* __restrict__ out,
                                                  char* __restrict__ ws) {
  __shared__ __attribute__((aligned(16))) float red[5 * 32 + 16 + 8];
  __shared__ __attribute__((aligned(16))) u64 lkeys[256];
  unsigned* redW = (unsigned*)(red + 176);
  const int tid = threadIdx.x, lane = tid & 63, wave = __builtin_amdgcn_readfirstlane(tid >> 6);
  const int bid = blockIdx.x;
  STAMP(2, 0);
  const us* ytb = (const us*)(ws + WS_YT);
  if (bid >= 256) {
    const bool f1 = bid < 352;
    const int j = f1 ? bid - 256 : bid - 352;
    const int cb = f1 ? (j & 7) * 12 + (j >> 3) : (j & 7) * 5 + (j >> 3);
    if (wave == 5) {
      __builtin_amdgcn_s_sleep(40);
      const u64 wn = sweep_keys(ws, lane, j & 7, f1 ? lkeys : nullptr);
      const unsigned wkey = (unsigned)(wn >> 32), wms = (unsigned)wn;
      const int wrow = 2047 - (int)(wkey & 2047u);
      const int c = lane & 15;
      if (f1) {
        if (lane == 0) { redW[2] = wkey; redW[3] = wms; }
        STAMPT(2, 3, 320);
        __syncthreads();
        __syncthreads();
        const float* rp = red + (c >> 3) * 16 + (c & 7);
        float gs = (rp[0] + rp[32]) + (rp[64] + rp[96]) + rp[128];
        float us_ = (rp[8] + rp[40]) + (rp[72] + rp[104]) + rp[136];
        float gb = rbf(gs);
        float sl = rbf(gb * __builtin_amdgcn_rcpf(1.f + __expf(-gb)));
        unsigned a = (unsigned)f2bf(sl * rbf(us_)) | 0x10000u;
        __hip_atomic_store((GAS unsigned*)(ws + WS_ACT) + (lane >> 4) * F_ + cb * 16 + c, a, RLX_AGENT);
        STAMPT(2, 4, 320);
      } else {
        const int col = cb * 16 + c;
        float fh;
        float fx2 = xs_of(x[(size_t)wrow * D_ + col], ytb[((size_t)(wrow >> 3) * D_ + col) * 8 + (wrow & 7)],
                          bflo(wms), __builtin_amdgcn_rcpf(bfhi(wms)), fh);
        unsigned ob = (wkey >> 11) & 0xffffu;
        unsigned rb = (ob & 0x8000u) ? (ob & 0x7fffu) : (~ob & 0xffffu);
        float sg = rbf(__builtin_amdgcn_rcpf(1.f + __expf(-bf1((us)rb))));
        __syncthreads();
        const float* rp = red + c;
        float ys = (rp[0] + rp[16]) + (rp[32] + rp[48]) + rp[64];
        float y = rbf(rbf(ys) * sg);
        if (lane < 16) out[(size_t)wrow * D_ + col] = f2bf(fh + rbf(fx2 + y));
        STAMPT(2, 5, 320);
      }
      return;
    }
    const int half = tid & 1, rb = tid >> 1;
    __builtin_amdgcn_s_sleep(16);
    if (f1) {
      const size_t o0 = (size_t)rb * F_ + cb * 16 + half * 8;
      u4 wq[8];
#pragma unroll
      for (int k = 0; k < 4; ++k) { wq[k] = *(const u4*)(wg + o0 + (size_t)k * 160 * F_); wq[4 + k] = *(const u4*)(wu + o0 + (size_t)k * 160 * F_); }
      f2 wf[32];
#pragma unroll
      for (int k = 0; k < 8; ++k) { wf[4 * k] = unpk(wq[k].x); wf[4 * k + 1] = unpk(wq[k].y); wf[4 * k + 2] = unpk(wq[k].z); wf[4 * k + 3] = unpk(wq[k].w); }
#pragma unroll
      for (int k = 0; k < 32; ++k) asm volatile("" : "+v"(wf[k]));
      __syncthreads();
      const unsigned wkey = redW[2], wms = redW[3];
      const int wrow = 2047 - (int)(wkey & 2047u);
      const float wm = bflo(wms), wrs = __builtin_amdgcn_rcpf(bfhi(wms));
      const us* xw = x + (size_t)wrow * D_ + rb;
      const us* yw = ytb + ((size_t)(wrow >> 3) * D_ + rb) * 8 + (wrow & 7);
      us xv[4], yv[4];
#pragma unroll
      for (int k = 0; k < 4; ++k) { xv[k] = xw[160 * k]; yv[k] = yw[160 * 8 * k]; }
      f2 acc[8];
#pragma unroll
      for (int i = 0; i < 8; ++i) acc[i] = f2{0.f, 0.f};
#pragma unroll
      for (int k = 0; k < 4; ++k) {
        float hh; float xs = xs_of(xv[k], yv[k], wm, wrs, hh);
        f2 x2v = {xs, xs};
#pragma unroll
        for (int i = 0; i < 4; ++i) {
          acc[i] = __builtin_elementwise_fma(wf[4 * k + i], x2v, acc[i]);
          acc[4 + i] = __builtin_elementwise_fma(wf[16 + 4 * k + i], x2v, acc[4 + i]);
        }
      }
      float w[8], o[4];
#pragma unroll
      for (int i = 0; i < 4; ++i) { w[2 * i] = swap32_add(acc[i].x, acc[4 + i].x); w[2 * i + 1] = swap32_add(acc[i].y, acc[4 + i].y); }
#pragma unroll
      for (int i = 0; i < 4; ++i) o[i] = swap16_add(w[i], w[i + 4]);
      par_sum4(o);
      const int rr = lane >> 4;
      if ((lane & 15) < 2) *(f4*)(red + wave * 32 + half * 16 + 4 * (rr & 1) + 8 * (rr >> 1)) = f4{o[0], o[1], o[2], o[3]};
      __syncthreads();
#pragma unroll
      for (int q = 0; q < 3; ++q) {
        const int cbk = j + 96 * q;
        if (cbk < 256) {
          const u64 gk = lkeys[cbk];
          const unsigned ck = (unsigned)(gk >> 32), cms = (unsigned)gk;
          if (ck != wkey) {
            const int crow = 2047 - (int)(ck & 2047u);
            const float crs = __builtin_amdgcn_rcpf(bfhi(cms)), cb_ = -bflo(cms) * crs;
            unsigned xr = ((const unsigned*)(x + (size_t)crow * D_))[tid];
            const us* yc = ytb + ((size_t)(crow >> 3) * D_ + 2 * tid) * 8 + (crow & 7);
            f2 hs = unpk(xr) + f2{bf1(yc[0]), bf1(yc[8])};
            f2 hh = unpk(pk_bf(hs.x, hs.y));
            f2 oo = hh + __builtin_elementwise_fma(hh, f2{crs, crs}, f2{cb_, cb_});
            ((unsigned*)(out + (size_t)crow * D_))[tid] = pk_bf(oo.x, oo.y);
          }
        }
      }
      STAMP(2, 6);
    } else {
      const us* wp = wd + cb * 16 + half * 8;
      u4 wq[10];
#pragma unroll
      for (int k = 0; k < 10; ++k) { int r = rb + 160 * k; r = r < F_ ? r : F_ - 1; wq[k] = *(const u4*)(wp + (size_t)r * D_); }
      f2 wf[40];
#pragma unroll
      for (int k = 0; k < 10; ++k) { wf[4 * k] = unpk(wq[k].x); wf[4 * k + 1] = unpk(wq[k].y); wf[4 * k + 2] = unpk(wq[k].z); wf[4 * k + 3] = unpk(wq[k].w); }
#pragma unroll
      for (int k = 0; k < 40; ++k) asm volatile("" : "+v"(wf[k]));
      __builtin_amdgcn_s_sleep(30);
      GAS unsigned* ap = (GAS unsigned*)(ws + WS_ACT) + (j & 3) * F_ + rb;
      const int last = rb < 96 ? 1440 : 0;
      unsigned a[10];
      for (unsigned spins = 0;; ++spins) {
#pragma unroll
        for (int k = 0; k < 9; ++k) a[k] = __hip_atomic_load(ap + 160 * k, RLX_AGENT);
        a[9] = __hip_atomic_load(ap + last, RLX_AGENT);
        unsigned all = a[0] & a[1] & a[2] & a[3] & a[4] & a[5] & a[6] & a[7] & a[8] & a[9];
        if (__all(all >> 16) || spins > SPIN_CAP) break;
        __builtin_amdgcn_s_sleep(1);
      }
      if (rb >= 96) a[9] = 0;
      f2 acc[4] = {f2{0.f, 0.f}, f2{0.f, 0.f}, f2{0.f, 0.f}, f2{0.f, 0.f}};
#pragma unroll
      for (int k = 0; k < 10; ++k) {
        float av = bflo(a[k]); f2 a2 = {av, av};
#pragma unroll
        for (int i = 0; i < 4; ++i) acc[i] = __builtin_elementwise_fma(wf[4 * k + i], a2, acc[i]);
      }
      float w0 = swap32_add(acc[0].x, acc[2].x), w1 = swap32_add(acc[0].y, acc[2].y);
      float w2 = swap32_add(acc[1].x, acc[3].x), w3 = swap32_add(acc[1].y, acc[3].y);
      float o0 = par_sum(swap16_add(w0, w2)), o1 = par_sum(swap16_add(w1, w3));
      const int rr = lane >> 4;
      if ((lane & 15) < 2) *(f2*)(red + wave * 16 + half * 8 + 2 * (rr & 1) + 4 * (rr >> 1)) = f2{o0, o1};
      __syncthreads();
    }
    return;
  }
  float* redA = red;
  float* abv = red + 160;
  const int tb = (bid & 7) * 32 + (bid >> 3);
  const int t0 = tb * 8;
  const unsigned* xb = (const unsigned*)(x + (size_t)t0 * D_);
  if (wave == 5) {
    const int r = lane & 7;
    const float piv = bf1(x[(size_t)(t0 + r) * D_]);
    const float W = *(const float*)(ws + WS_WSUM);
    __syncthreads();
    float s1 = (redA[r] + redA[24 + r]) + (redA[48 + r] + redA[72 + r]) + redA[96 + r];
    float s2 = (redA[8 + r] + redA[32 + r]) + (redA[56 + r] + redA[80 + r]) + redA[104 + r];
    float s3 = (redA[16 + r] + redA[40 + r]) + (redA[64 + r] + redA[88 + r]) + redA[112 + r];
    float mb, sb;
    ln_from_sums(s1, s2, piv, mb, sb);
    float rs = __builtin_amdgcn_rcpf(sb);
    float rv = rs * __builtin_fmaf(piv - mb, W, s3);
    unsigned key = 0x80000000u | (ord16(f2bf(rv)) << 11) | (unsigned)(2047 - (t0 + r));
    unsigned best = key;
    best = max(best, (unsigned)__builtin_amdgcn_update_dpp(0, (int)best, 0x111, 0xf, 0xf, true));
    best = max(best, (unsigned)__builtin_amdgcn_update_dpp(0, (int)best, 0x112, 0xf, 0xf, true));
    best = max(best, (unsigned)__builtin_amdgcn_update_dpp(0, (int)best, 0x114, 0xf, 0xf, true));
    best = (unsigned)__builtin_amdgcn_readlane((int)best, 7);
    if (key == best) {
      u64 gr = ((u64)best << 32) | (u64)((__float_as_uint(mb) >> 16) | (__float_as_uint(sb) & 0xffff0000u));
      __hip_atomic_store((GAS u64*)(ws + WS_KEYS) + (lane >> 3) * 256 + bid, gr, RLX_AGENT);
      redW[0] = (unsigned)r;
    }
    if (lane < 8) *(f2*)(abv + 2 * r) = f2{rs, -mb * rs};
    STAMPT(2, 2, 320);
    __syncthreads();
    return;
  }
  unsigned raw[8];
#pragma unroll
  for (int r = 0; r < 8; ++r) raw[r] = xb[r * (D_ / 2) + tid];
  const u4 y0 = *(const u4*)(ytb + ((size_t)tb * D_ + 2 * tid) * 8);
  const u4 y1 = *(const u4*)(ytb + ((size_t)tb * D_ + 2 * tid) * 8 + 8);
  const f2 wv = unpk(((const unsigned*)wr)[tid]);
  f2 h[8];
  {
    float v[24];
#pragma unroll
    for (int r = 0; r < 8; ++r) {
      const float piv = bflo(xb[r * (D_ / 2)]);
      unsigned ya = y0[r >> 1], yb = y1[r >> 1];
      f2 yy = (r & 1) ? f2{bfhi(ya), bfhi(yb)} : f2{bflo(ya), bflo(yb)};
      f2 hs = unpk(raw[r]) + yy;
      h[r] = unpk(pk_bf(hs.x, hs.y));
      f2 dd = h[r] - piv;
      v[r] = dd.x + dd.y;
      v[r + 8] = __builtin_fmaf(dd.x, dd.x, dd.y * dd.y);
      v[r + 16] = __builtin_fmaf(dd.x, wv.x, dd.y * wv.y);
    }
    float w[12], o[6];
#pragma unroll
    for (int i = 0; i < 12; ++i) w[i] = swap32_add(v[i], v[i + 12]);
#pragma unroll
    for (int i = 0; i < 6; ++i) o[i] = swap16_add(w[i], w[i + 6]);
    row_sum6(o);
    if ((lane & 15) == 0) {
      float* dst = redA + wave * 24 + (lane >> 4) * 6;
      *(f2*)dst = f2{o[0], o[1]}; *(f2*)(dst + 2) = f2{o[2], o[3]}; *(f2*)(dst + 4) = f2{o[4], o[5]};
    }
  }
  __syncthreads();
  STAMP(2, 1);
  __syncthreads();
  const int brow = (int)redW[0];
  unsigned* outr = (unsigned*)(out + (size_t)t0 * D_) + tid;
#pragma unroll
  for (int r = 0; r < 8; r += 2) {
    f4 c = *(const f4*)(abv + 2 * r);
    f2 o0 = h[r] + __builtin_elementwise_fma(h[r], f2{c.x, c.x}, f2{c.y, c.y});
    f2 o1 = h[r + 1] + __builtin_elementwise_fma(h[r + 1], f2{c.z, c.z}, f2{c.w, c.w});
    unsigned q0 = pk_bf(o0.x, o0.y), q1 = pk_bf(o1.x, o1.y);
    if (r != brow) outr[r * (D_ / 2)] = q0;
    if (r + 1 != brow) outr[(r + 1) * (D_ / 2)] = q1;
  }
}

extern "C" __global__ void k9_end() {}

extern "C" void kernel_launch(void* const* d_in, const int* in_sizes, int n_in, void* d_out, int out_size,
                              void* d_ws, size_t ws_size, hipStream_t stream) {
  const us* x = (const us*)d_in[0];
  const us* Are = (const us*)d_in[1];
  const us* Aim = (const us*)d_in[2];
  const us* Bre = (const us*)d_in[3];
  const us* Bim = (const us*)d_in[4];
  const us* Cre = (const us*)d_in[5];
  const us* Cim = (const us*)d_in[6];
  const us* logdt = (const us*)d_in[7];
  const us* Dskip = (const us*)d_in[8];
  const us* wr = (const us*)d_in[10];
  const us* wg = (const us*)d_in[11];
  const us* wu = (const us*)d_in[12];
  const us* wd = (const us*)d_in[13];
  us* out = (us*)d_out;
  char* ws = (char*)d_ws;
  k1_ln<<<256, 384, 0, stream>>>(x, Are, Aim, Bre, Bim, Cre, Cim, wr, wg, wu, wd, out, ws);
  k2_conv<<<D_, 256, K2_LDS, stream>>>(Are, Aim, Bre, Bim, Cre, Cim, logdt, Dskip, ws);
  k3_tail<<<392, 384, 0, stream>>>(x, wr, wg, wu, wd, out, ws);
}
```

```cpp
#include <hip/hip_runtime.h>
#include <hip/hip_bf16.h>
#include <stdint.h>

typedef _Float16 h8 __attribute__((ext_vector_type(8)));
typedef _Float16 h2 __attribute__((ext_vector_type(2)));
typedef float f4 __attribute__((ext_vector_type(4)));
typedef float f2 __attribute__((ext_vector_type(2)));
typedef unsigned u4 __attribute__((ext_vector_type(4)));
typedef unsigned u2 __attribute__((ext_vector_type(2)));
typedef unsigned short us;
typedef unsigned long long u64;
#define GAS __attribute__((address_space(1)))
#define LAS __attribute__((address_space(3)))

constexpr int L_ = 2048, D_ = 640, N_ = 192, F_ = 1536;
constexpr int LP = 2048 + 64;
#define STAMPD(kid, slot) do {} while (0)
#define STAMP(kid, slot) do {} while (0)
#define STAMPT(kid, slot, thr) do {} while (0)

constexpr int NR_LEN  = 608;
constexpr int F2_OFF  = 2 * NR_LEN;
constexpr int F3_OFF  = F2_OFF + 8 * 64;
constexpr int KIMG_ELEMS = F3_OFF + 4 * 80;
constexpr int KCHUNKS = KIMG_ELEMS / 8;

constexpr size_t WS_FLAGS = 0;
constexpr size_t WS_KEYS  = WS_FLAGS;
constexpr size_t WS_ACT   = WS_FLAGS + 16384;
constexpr size_t WS_WSUM  = 40960;
constexpr size_t WS_UT    = 40960 + 256;
constexpr size_t WS_YT    = WS_UT + (size_t)D_ * LP * 2;
constexpr size_t WS_TOTAL = WS_YT + (size_t)D_ * L_ * 2;

constexpr int TAB_PITCH = 200;
constexpr int SM_KC = 57 * TAB_PITCH * 4;
constexpr int SM_U  = SM_KC + 1552;
constexpr int K2_LDS = SM_U + 4608;
constexpr int SM_KI = 0;
constexpr int SM_P  = 4096;

constexpr int kc_of_v(int v) {
  if (v < 256)  { int h = v >> 7, r = v & 127; return ((h * 8 + (r & 7)) * 16) + (r >> 3); }
  if (v < 512)  { int w = v - 256, h = w >> 7, r = w & 127; return ((16 + h * 4 + ((r & 7) >> 1)) * 16) + (r >> 3); }
  if (v < 1024) { int w = v - 512, h = w >> 7, r = w & 127; return ((24 + h * 2 + ((r & 7) >> 2)) * 16) + (r >> 3); }
  if (v < 2048) { int w = v - 1024, h = w >> 7, r = w & 127; return ((32 + h) * 16) + (r >> 3); }
  return 40 * 16;
}
constexpr int bf16_round_int(int j) {
  if (j <= 256) return j;
  int step = j < 512 ? 2 : (j < 1024 ? 4 : 8);
  int lo = (j / step) * step, rem = j - lo;
  if (rem * 2 < step) return lo;
  if (rem * 2 > step) return lo + step;
  return ((lo / step) & 1) ? lo + step : lo;
}
constexpr int ZIDX = 768;

struct GatherTab { unsigned v[KIMG_ELEMS]; };
constexpr GatherTab make_gather() {
  GatherTab t{};
  for (int i = 0; i < KIMG_ELEMS; ++i) t.v[i] = SM_KC + ZIDX * 2;
  for (int e = 0; e < 2; ++e)
    for (int i = 0; i < NR_LEN; ++i) {
      int j = 543 - (i + e);
      if (j >= 0 && j < 515) t.v[e * NR_LEN + i] = (unsigned)(SM_KC + 2 * kc_of_v(bf16_round_int(j)));
    }
  for (int pi = 0; pi < 4; ++pi)
    for (int e = 0; e < 2; ++e)
      for (int i = 0; i < 64; ++i) {
        int p = 79 - (i + e);
        if (p >= 32 && p < 64) {
          int v = 16 * p + 8 * (pi >> 1) + ((pi & 1) ? 8 : 4);
          t.v[F2_OFF + (pi * 2 + e) * 64 + i] = (unsigned)(SM_KC + 2 * kc_of_v(v));
        }
      }
  for (int pi = 0; pi < 2; ++pi)
    for (int e = 0; e < 2; ++e)
      for (int i = 0; i < 80; ++i) {
        int qq = 127 - (i + e);
        if (qq >= 64 && qq < 128) {
          int v = 16 * qq + (pi ? 16 : 8);
          t.v[F3_OFF + (pi * 2 + e) * 80 + i] = (unsigned)(SM_KC + 2 * kc_of_v(v));
        }
      }
  return t;
}
__device__ const GatherTab g_gather = make_gather();

struct BoxTab { us v[7 * 64 * 8]; };
constexpr BoxTab make_box() {
  BoxTab t{};
  const int nn[6] = {3, 5, 3, 5, 7, 9};
  const int sh[6] = {3, 6, 11, 14, 5, 12};
  const int w0[6] = {-16, -16, -24, -24, -16, -24};
  for (int m = 0; m < 6; ++m)
    for (int lane = 0; lane < 64; ++lane)
      for (int j = 0; j < 8; ++j) {
        int b = lane & 15, k = 8 * (lane >> 4) + j;
        int idx = w0[m] + k;
        bool on = idx <= b - sh[m] && idx >= b - sh[m] - (nn[m] - 1);
        t.v[(m * 64 + lane) * 8 + j] = on ? 0x3C00 : 0;
      }
  for (int lane = 0; lane < 64; ++lane)
    for (int j = 0; j < 8; ++j) {
      int b = lane & 15, k = 8 * (lane >> 4) + j;
      t.v[(6 * 64 + lane) * 8 + j] = (k - b == 4 || k - b == 5) ? 0xFFFF : 0;
    }
  return t;
}
__device__ const BoxTab g_box = make_box();

__device__ __forceinline__ float bflo(unsigned w) { return __uint_as_float(w << 16); }
__device__ __forceinline__ float bfhi(unsigned w) { return __uint_as_float(w & 0xffff0000u); }
__device__ __forceinline__ float bf1(us v) { return __uint_as_float((unsigned)v << 16); }
__device__ __forceinline__ us f2bf(float f) { __bf16 b = (__bf16)f; return __builtin_bit_cast(us, b); }
__device__ __forceinline__ float rbf(float f) { return bf1(f2bf(f)); }
typedef __bf16 b2 __attribute__((ext_vector_type(2)));
__device__ __forceinline__ unsigned pk_bf(float a, float b) {
  b2 v; v.x = (__bf16)a; v.y = (__bf16)b; return __builtin_bit_cast(unsigned, v);
}
__device__ __forceinline__ unsigned pk_h(float a, float b) {
  h2 v; v.x = (_Float16)a; v.y = (_Float16)b; return __builtin_bit_cast(unsigned, v);
}
template <int CTRL> __device__ __forceinline__ float dpp_add(float v) {
  int t = __builtin_amdgcn_update_dpp(0, __float_as_int(v), CTRL, 0xf, 0xf, true);
  return v + __int_as_float(t);
}
__device__ __forceinline__ float row_sum(float v) {
  v = dpp_add<0x121>(v); v = dpp_add<0x122>(v); v = dpp_add<0x124>(v); v = dpp_add<0x128>(v);
  return v;
}
__device__ __forceinline__ float swap32_add(float a, float b) {
  auto r = __builtin_amdgcn_permlane32_swap(__float_as_uint(a), __float_as_uint(b), false, false);
  return __uint_as_float(r[0]) + __uint_as_float(r[1]);
}
__device__ __forceinline__ float swap16_add(float a, float b) {
  auto r = __builtin_amdgcn_permlane16_swap(__float_as_uint(a), __float_as_uint(b), false, false);
  return __uint_as_float(r[0]) + __uint_as_float(r[1]);
}
__device__ __forceinline__ void wave_reduce16(const float (&v)[16], float (&out)[4]) {
  float w[8];
#pragma unroll
  for (int i = 0; i < 8; ++i) w[i] = swap32_add(v[i], v[i + 8]);
#pragma unroll
  for (int i = 0; i < 4; ++i) out[i] = row_sum(swap16_add(w[i], w[i + 4]));
}
__device__ __forceinline__ void wave_reduce8(const float (&v)[8], float (&out)[2]) {
  float w[4];
#pragma unroll
  for (int i = 0; i < 4; ++i) w[i] = swap32_add(v[i], v[i + 4]);
#pragma unroll
  for (int i = 0; i < 2; ++i) out[i] = row_sum(swap16_add(w[i], w[i + 2]));
}
__device__ __forceinline__ float wave_reduce4(const float (&v)[4]) {
  float w0 = swap32_add(v[0], v[2]), w1 = swap32_add(v[1], v[3]);
  return row_sum(swap16_add(w0, w1));
}
#define DPPA(i, st) "v_add_f32_dpp %" #i ", %" #i ", %" #i " row_ror:" #st " row_mask:0xf bank_mask:0xf\n\t"
__device__ __forceinline__ void row_sum8(float (&o)[8]) {
  asm volatile("s_nop 1\n\t"
    DPPA(0,1) DPPA(1,1) DPPA(2,1) DPPA(3,1) DPPA(4,1) DPPA(5,1) DPPA(6,1) DPPA(7,1)
    DPPA(0,2) DPPA(1,2) DPPA(2,2) DPPA(3,2) DPPA(4,2) DPPA(5,2) DPPA(6,2) DPPA(7,2)
    DPPA(0,4) DPPA(1,4) DPPA(2,4) DPPA(3,4) DPPA(4,4) DPPA(5,4) DPPA(6,4) DPPA(7,4)
    DPPA(0,8) DPPA(1,8) DPPA(2,8) DPPA(3,8) DPPA(4,8) DPPA(5,8) DPPA(6,8) DPPA(7,8)
    : "+v"(o[0]), "+v"(o[1]), "+v"(o[2]), "+v"(o[3]), "+v"(o[4]), "+v"(o[5]), "+v"(o[6]), "+v"(o[7]));
}
__device__ __forceinline__ void row_sum6(float (&o)[6]) {
  asm volatile("s_nop 1\n\t"
    DPPA(0,1) DPPA(1,1) DPPA(2,1) DPPA(3,1) DPPA(4,1) DPPA(5,1)
    DPPA(0,2) DPPA(1,2) DPPA(2,2) DPPA(3,2) DPPA(4,2) DPPA(5,2)
    DPPA(0,4) DPPA(1,4) DPPA(2,4) DPPA(3,4) DPPA(4,4) DPPA(5,4)
    DPPA(0,8) DPPA(1,8) DPPA(2,8) DPPA(3,8) DPPA(4,8) DPPA(5,8)
    : "+v"(o[0]), "+v"(o[1]), "+v"(o[2]), "+v"(o[3]), "+v"(o[4]), "+v"(o[5]));
}
__device__ __forceinline__ void row_sum4(float (&o)[4]) {
  asm volatile("s_nop 1\n\t"
    DPPA(0,1) DPPA(1,1) DPPA(2,1) DPPA(3,1)
    DPPA(0,2) DPPA(1,2) DPPA(2,2) DPPA(3,2)
    DPPA(0,4) DPPA(1,4) DPPA(2,4) DPPA(3,4)
    DPPA(0,8) DPPA(1,8) DPPA(2,8) DPPA(3,8)
    : "+v"(o[0]), "+v"(o[1]), "+v"(o[2]), "+v"(o[3]));
}
__device__ __forceinline__ void par_sum4(float (&o)[4]) {
  asm volatile("s_nop 1\n\t"
    DPPA(0,2) DPPA(1,2) DPPA(2,2) DPPA(3,2)
    DPPA(0,4) DPPA(1,4) DPPA(2,4) DPPA(3,4)
    DPPA(0,8) DPPA(1,8) DPPA(2,8) DPPA(3,8)
    : "+v"(o[0]), "+v"(o[1]), "+v"(o[2]), "+v"(o[3]));
}
__device__ __forceinline__ float rdlane(float v, int l) {
  return __int_as_float(__builtin_amdgcn_readlane(__float_as_int(v), l));
}

__device__ __forceinline__ f2 cmul(f2 a, f2 b) {
  f2 c;
  asm("v_pk_mul_f32 %0, %1, %2 op_sel:[0,0] op_sel_hi:[1,0]\n\tv_pk_fma_f32 %0, %1, %2, %0 op_sel:[1,1,0] op_sel_hi:[0,1,1] neg_lo:[1,0,0]"
      : "=&v"(c) : "v"(a), "v"(b));
  return c;
}
__device__ __forceinline__ f2 cexp_p(float lam, float rh, float rl, float p) {
  float mag = __builtin_amdgcn_exp2f(lam * p);
  float x = rh * p;
  float f = x - __builtin_rintf(x);
  f = __builtin_fmaf(rl, p, f);
  f2 w; w.x = mag * __builtin_amdgcn_cosf(f); w.y = mag * __builtin_amdgcn_sinf(f); return w;
}
#define MFMA16(a, b, c) __builtin_amdgcn_mfma_f32_16x16x32_f16((a), (b), (c), 0, 0, 0)
__device__ __forceinline__ h8 as_h8(u4 v) { return __builtin_bit_cast(h8, v); }

__device__ __forceinline__ void ln_from_sums(float s1, float s2, float piv, float& mb, float& sb) {
  float dm = s1 * (1.f / 640.f);
  float var = fmaxf(__builtin_fmaf(-dm, dm, s2 * (1.f / 640.f)), 0.f);
  mb = rbf(piv + dm);
  sb = rbf(__builtin_amdgcn_sqrtf(rbf(rbf(var) + 1e-5f)));
}
__device__ __forceinline__ f2 unpk(unsigned w) { return f2{bflo(w), bfhi(w)}; }

extern "C" __global__ __launch_bounds__(384, 2) void k1_ln(
    const us* __restrict__ x, const us* __restrict__ Are, const us* __restrict__ Aim,
    const us* __restrict__ Bre, const us* __restrict__ Bim, const us* __restrict__ Cre,
    const us* __restrict__ Cim, const us* __restrict__ wr,
    const us* __restrict__ wg, const us* __restrict__ wu, const us* __restrict__ wd, const us* __restrict__ out,
    char* __restrict__ ws) {
  __shared__ __attribute__((aligned(16))) float red[5 * 16 + 16 + 8];
  const int tid = threadIdx.x, lane = tid & 63, wave = __builtin_amdgcn_readfirstlane(tid >> 6);
  const int bid = blockIdx.x;
  if (blockIdx.x == 0 && threadIdx.x == 0) *(u64*)(ws + WS_WSUM + 64) = __builtin_amdgcn_s_memrealtime();
  if (wave == 5) {
    u64 pc; int o1, o2;
    asm volatile("s_getpc_b64 %0\n\ts_mov_b32 %1, k1_ln@rel32@lo+4\n\ts_mov_b32 %2, k9_end@rel32@lo+12" : "=s"(pc), "=s"(o1), "=s"(o2));
    const int xcd = bid & 7, jb = bid >> 3;
    const GAS char* cbase = (const GAS char*)(pc + (long long)o1);
    unsigned acc = 0;
    {
      const int id = jb * 64 + lane;
      const GAS char* p;
      if (id < 384) { int o = id * 64; p = cbase + (o < o2 - o1 ? o : 0); }
      else if (id < 384 + 1440) { int i = id - 384, ar = i / 240, q = i - ar * 240;
        const us* base = ar == 0 ? Are : ar == 1 ? Aim : ar == 2 ? Bre : ar == 3 ? Bim : ar == 4 ? Cre : Cim;
        p = (const GAS char*)base + xcd * 30720 + q * 128; }
      else if (id < 1824 + 64) p = (const GAS char*)g_gather.v + (id - 1824) * 128;
      else if (id < 1888 + 56) p = (const GAS char*)g_box.v + (id - 1888) * 128;
      else if (id < 1944 + 40) p = (const GAS char*)out + (size_t)(id - 1944) * 65536;
      else p = (const GAS char*)(ws + WS_YT) + (size_t)((id - 1984) % 40) * 65536;
      acc = *(const GAS unsigned*)p;
    }
    __builtin_amdgcn_s_barrier();
#pragma unroll
    for (int k = 0; k < 4; ++k) {
      int id = (jb * 4 + k) * 64 + lane;
      id = id < 6912 ? id : 6911;
      const GAS char* p;
      if (id < 3840) { int r3 = id / 3, q3 = id - r3 * 3;
        p = (const GAS char*)((r3 < 640 ? wg + (size_t)r3 * F_ : wu + (size_t)(r3 - 640) * F_) + xcd * 192 + q3 * 64); }
      else { int i2 = id - 3840; p = (const GAS char*)(wd + (size_t)(i2 >> 1) * D_ + xcd * 80 + (i2 & 1) * 78); }
      acc ^= *(const GAS unsigned*)p;
      if (k == 0) __builtin_amdgcn_s_barrier();
    }
    asm volatile("" :: "v"(acc));
    return;
  }
  const int t0 = ((bid & 7) * 32 + (bid >> 3)) * 8;
  const unsigned* xb = (const unsigned*)(x + (size_t)t0 * D_);
  unsigned raw[8];
#pragma unroll
  for (int r = 0; r < 8; ++r) raw[r] = xb[r * (D_ / 2) + tid];
  f2* ab = (f2*)(red + 80);
  unsigned* pv = (unsigned*)(red + 96);
  f2 a[8];
  {
    float v[16];
#pragma unroll
    for (int r = 0; r < 8; ++r) {
      const float piv = bflo(xb[r * (D_ / 2)]);
      a[r] = unpk(raw[r]);
      f2 dd = a[r] - piv;
      v[r] = dd.x + dd.y; v[r + 8] = __builtin_fmaf(dd.x, dd.x, dd.y * dd.y);
    }
    if (tid == 0) { *(u4*)(pv) = u4{raw[0], raw[1], raw[2], raw[3]}; *(u4*)(pv + 4) = u4{raw[4], raw[5], raw[6], raw[7]}; }
    float w[8], o[4];
#pragma unroll
    for (int i = 0; i < 8; ++i) w[i] = swap32_add(v[i], v[i + 8]);
#pragma unroll
    for (int i = 0; i < 4; ++i) o[i] = swap16_add(w[i], w[i + 4]);
    row_sum4(o);
    if ((lane & 15) == 0) *(f4*)(red + wave * 16 + (lane >> 4) * 4) = f4{o[0], o[1], o[2], o[3]};
  }
  __syncthreads();
  if (wave == 0) {
    const int r = lane & 7;
    float s1 = (red[r] + red[16 + r]) + (red[32 + r] + red[48 + r]) + red[64 + r];
    float s2 = (red[8 + r] + red[24 + r]) + (red[40 + r] + red[56 + r]) + red[72 + r];
    float mb, sb;
    ln_from_sums(s1, s2, bflo(pv[r]), mb, sb);
    float aa = 128.f * __builtin_amdgcn_rcpf(sb);
    if (lane < 8) ab[r] = f2{aa, -mb * aa};
  } else if (wave == 1 && bid == 0) {
    const unsigned* w2 = (const unsigned*)wr + lane;
    float sw = 0.f;
#pragma unroll
    for (int k = 0; k < 5; ++k) { unsigned q = w2[64 * k]; sw += bflo(q) + bfhi(q); }
    sw = row_sum(sw);
    sw = rdlane(sw, 0) + rdlane(sw, 16) + rdlane(sw, 32) + rdlane(sw, 48);
    if (lane == 0) *(float*)(ws + WS_WSUM) = sw;
  } else if (wave >= 1 && bid >= 1 && bid <= 10) {
    ((u4*)(ws + WS_FLAGS))[(bid - 1) * 256 + tid - 64] = u4{0, 0, 0, 0};
  }
  __syncthreads();
  unsigned p0[4], p1[4];
#pragma unroll
  for (int r = 0; r < 8; r += 2) {
    f4 c = *(const f4*)(ab + r);
    f2 u0 = __builtin_elementwise_fma(a[r], f2{c.x, c.x}, f2{c.y, c.y});
    f2 u1 = __builtin_elementwise_fma(a[r + 1], f2{c.z, c.z}, f2{c.w, c.w});
    p0[r >> 1] = pk_h(u0.x, u1.x); p1[r >> 1] = pk_h(u0.y, u1.y);
  }
  us* ut = (us*)(ws + WS_UT) + (size_t)(2 * tid) * LP + t0;
  *(u4*)(ut) = u4{p0[0], p0[1], p0[2], p0[3]};
  *(u4*)(ut + LP) = u4{p1[0], p1[1], p1[2], p1[3]};
  STAMP(0, 5);
  STAMPD(0, 6);
}

__device__ __forceinline__ h8 ld_h8(const char* p) { return as_h8(*(const u4*)p); }
__device__ __forceinline__ h8 ld_4dw(const char* p0, const char* p1) {
  const unsigned* a = (const unsigned*)p0; const unsigned* b = (const unsigned*)p1;
  return as_h8(u4{a[0], a[1], b[0], b[1]});
}
__device__ __forceinline__ h8 cvt_pair(f4 a, f4 b) {
  return as_h8(u4{pk_h(a[0], a[1]), pk_h(a[2], a[3]), pk_h(b[0], b[1]), pk_h(b[2], b[3])});
}
__device__ __forceinline__ void glds16(const void* g, void* l) {
  __builtin_amdgcn_global_load_lds((const GAS unsigned*)g, (LAS unsigned*)l, 16, 0, 0);
}
__device__ __forceinline__ void conv_epi(f4 acc, float sc, us* dst) {
  u2 o; o.x = pk_bf(acc[0] * sc, acc[1] * sc); o.y = pk_bf(acc[2] * sc, acc[3] * sc);
  *(u2*)dst = o;
}
#define LDU(a) ((unsigned)*(const LAS us*)(size_t)(a))
#define LDP(a, b) (LDU(a) | (LDU(b) << 16))

template <int OFF> __device__ __forceinline__ u4 lds_rd128(unsigned addr) {
  u4 v; asm volatile("ds_read_b128 %0, %1 offset:%2" : "=v"(v) : "v"(addr), "i"(OFF) : "memory"); return v;
}
template <int N> __device__ __forceinline__ void lgkm_wait() {
  asm volatile("s_waitcnt lgkmcnt(%0)" :: "i"(N) : "memory"); __builtin_amdgcn_sched_barrier(0);
}
template <int M, int M0, int M1, bool T3, int PF>
__device__ __forceinline__ void near_step(const h8 (&A)[17], u4 (&f)[PF], unsigned fa, f4& a0, f4& a1, f4& a2) {
  if constexpr (M < M1) {
    if constexpr (M >= M0 + PF) lgkm_wait<((M1 - 1 - M) < (PF - 1)) ? (M1 - 1 - M) : (PF - 1)>();
    const h8 ff = as_h8(f[(M - M0) % PF]);
    if constexpr (M <= 16) a0 = MFMA16(A[M], ff, a0);
    if constexpr (M >= 8 && M <= 24) a1 = MFMA16(A[M - 8], ff, a1);
    if constexpr (T3 && M >= 16) a2 = MFMA16(A[M - 16], ff, a2);
    __builtin_amdgcn_sched_barrier(0);
    if constexpr (M + PF < M1) f[(M - M0) % PF] = lds_rd128<(M + PF) * 64>(fa);
    near_step<M + 1, M0, M1, T3, PF>(A, f, fa, a0, a1, a2);
  }
}
template <int M, int M0, int PF> __device__ __forceinline__ void near_fill(u4 (&f)[PF], unsigned fa) {
  if constexpr (M < M0 + PF) { f[M - M0] = lds_rd128<M * 64>(fa); near_fill<M + 1, M0, PF>(f, fa); }
}
template <int M0, int M1, bool T3>
__device__ __forceinline__ void near_band(const h8 (&A)[17], unsigned fa, f4& a0, f4& a1, f4& a2) {
  constexpr int PF = 6;
  u4 f[PF];
  near_fill<M0, M0, PF>(f, fa);
  __builtin_amdgcn_s_waitcnt(0xC07F);
  __builtin_amdgcn_sched_barrier(0);
  near_step<M0, M0, M1, T3, PF>(A, f, fa, a0, a1, a2);
}

extern "C" __global__ __launch_bounds__(256, 2) void k2_conv(
    const us* __restrict__ Are, const us* __restrict__ Aim, const us* __restrict__ Bre, const us* __restrict__ Bim,
    const us* __restrict__ Cre, const us* __restrict__ Cim, const us* __restrict__ logdt,
    const us* __restrict__ Dskip, char* __restrict__ ws) {
  extern __shared__ __attribute__((aligned(16))) char sm[];
  const int tid = threadIdx.x, lane = tid & 63, wave = __builtin_amdgcn_readfirstlane(tid >> 6);
  const int d = (blockIdx.x & 7) * 80 + (blockIdx.x >> 3);
  const int col = lane & 15, g = lane >> 4;
  if (blockIdx.x == 0 && threadIdx.x == 0) *(u64*)(ws + WS_WSUM + 72) = __builtin_amdgcn_s_memrealtime();
  char* kc = sm + SM_KC;
  const char* ub = sm + SM_U + (256 + 16 * col + 8 * g) * 2;
  us* yt = (us*)(ws + WS_YT) + ((size_t)(2 * col + (g >> 1)) * D_ + d) * 8 + (g & 1) * 4;
  constexpr int YTS = 32 * D_ * 8;
  const f4 Z = {0.f, 0.f, 0.f, 0.f};
  const u4* gt = (const u4*)g_gather.v + 2 * tid;
  if (wave == 3) {
    const char* gu = ws + WS_UT + (size_t)d * LP * 2;
#pragma unroll
    for (int k = 0; k < 4; ++k) glds16(gu + (k * 64 + lane) * 16, sm + SM_U + 512 + k * 1024);
    const u4* bt = (const u4*)g_box.v + lane;
    u4 bx[7];
#pragma unroll
    for (int m = 0; m < 7; ++m) bx[m] = bt[64 * m];
    if (lane < 32) *(u4*)(sm + SM_U + lane * 16) = u4{0, 0, 0, 0};
    __builtin_amdgcn_s_barrier();
    asm volatile("s_waitcnt vmcnt(0)" ::: "memory");
    h8 sa[6], sb[6], s3[4];
#pragma unroll
    for (int tau = 0; tau < 6; ++tau) {
      h8 w1 = ld_h8(ub + (256 * tau - 16) * 2), w2 = ld_h8(ub + (256 * tau - 24) * 2);
      sa[tau] = cvt_pair(MFMA16(w1, as_h8(bx[0]), Z), MFMA16(w1, as_h8(bx[1]), Z));
      sb[tau] = cvt_pair(MFMA16(w2, as_h8(bx[2]), Z), MFMA16(w2, as_h8(bx[3]), Z));
      if (tau < 4) s3[tau] = cvt_pair(MFMA16(w1, as_h8(bx[4]), Z), MFMA16(w2, as_h8(bx[5]), Z));
    }
    u4 ga0 = gt[0], ga1 = gt[1];
    __builtin_amdgcn_s_barrier();
    __syncthreads();
    *(u4*)(sm + SM_KI + tid * 16) = u4{LDP(ga0.x, ga0.y), LDP(ga0.z, ga0.w), LDP(ga1.x, ga1.y), LDP(ga1.z, ga1.w)};
    const unsigned kv = LDU(SM_KC + 32 * 16 * 2) * 0x10001u;
    __syncthreads();
    f4 acc[6];
    acc[0] = Z; acc[1] = Z;
    {
      h8 As = as_h8(u4{bx[6].x & kv, bx[6].y & kv, bx[6].z & kv, bx[6].w & kv});
#pragma unroll
      for (int i = 0; i < 4; ++i) acc[2 + i] = MFMA16(As, ld_h8(ub + (256 * (4 + i) - 1032) * 2), Z);
    }
    {
      const int ib = 79 - col + 4 * g;
      const char* kb = sm + SM_KI + (F2_OFF + (ib & 1) * 64 + (ib & ~1)) * 2;
#pragma unroll
      for (int dl = 2; dl <= 4; ++dl) {
        h8 tA = ld_4dw(kb - dl * 32, kb + 256 - dl * 32), tB = ld_4dw(kb + 512 - dl * 32, kb + 768 - dl * 32);
#pragma unroll
        for (int tau = 0; tau < 6; ++tau)
          if (tau + dl <= 7) { acc[tau + dl - 2] = MFMA16(sa[tau], tA, acc[tau + dl - 2]); acc[tau + dl - 2] = MFMA16(sb[tau], tB, acc[tau + dl - 2]); }
      }
    }
    {
      const int ib = 127 - col + 4 * g;
      const char* kb = sm + SM_KI + (F3_OFF + (ib & 1) * 80 + (ib & ~1)) * 2;
#pragma unroll
      for (int dl = 4; dl <= 7; ++dl) {
        h8 tt = ld_4dw(kb - dl * 32, kb + 320 - dl * 32);
#pragma unroll
        for (int tau = 0; tau < 4; ++tau)
          if (tau + dl <= 7) acc[tau + dl - 2] = MFMA16(s3[tau], tt, acc[tau + dl - 2]);
      }
    }
#pragma unroll
    for (int i = 0; i < 6; ++i) *(f4*)(sm + SM_P + i * 1024 + lane * 16) = acc[i];
    __syncthreads();
    return;
  }
  __builtin_amdgcn_s_setprio(3);
  const size_t pidx = (size_t)d * N_ + tid;
  us r_are = Are[pidx], r_aim = Aim[pidx], r_bre = Bre[pidx], r_bim = Bim[pidx], r_cre = Cre[pidx], r_cim = Cim[pidx];
  const unsigned ldw = ((const unsigned*)logdt)[d >> 1], dsw = ((const unsigned*)Dskip)[d >> 1];
  const float dt = rbf(__expf((d & 1) ? bfhi(ldw) : bflo(ldw)));
  const float dsk = (d & 1) ? bfhi(dsw) : bflo(dsw);
  asm volatile("s_mov_b32 m0, %0" :: "s"(wave * 256) : "memory");
  float ar = dt * bf1(r_are), ai = dt * bf1(r_aim);
  f2 dB = {dt * bf1(r_bre), dt * bf1(r_bim)};
  f2 Cc = {bf1(r_cre), bf1(r_cim)};
  f2 CB = cmul(Cc, dB);
  const float LOG2E = 1.4426950408889634f;
  const float I2PH = 0.15915494309189535f;
  const float I2PL = (float)(0.15915494309189533577 - (double)0.15915494309189535f);
  float lam = ar * LOG2E;
  float rh = ai * I2PH;
  float rl = __builtin_fmaf(ai, I2PL, __builtin_fmaf(ai, I2PH, -rh));
  {
    float gg = fmaxf(1.f, __builtin_amdgcn_exp2f(lam * 2048.f));
    float bnd = row_sum((fabsf(CB.x) + fabsf(CB.y)) * gg);
    if ((lane & 15) == 0) ((float*)(kc))[wave * 4 + (lane >> 4)] = bnd;
  }
  f2 w1 = cexp_p(lam, rh, rl, 1.f), w16 = cexp_p(lam, rh, rl, 16.f), w256 = cexp_p(lam, rh, rl, 256.f);
  f2 w2 = cmul(w1, w1), w32 = cmul(w16, w16), w512 = cmul(w256, w256);
  f2 w4 = cmul(w2, w2), w64 = cmul(w32, w32), w1024 = cmul(w512, w512);
  f2 w8 = cmul(w4, w4), w128 = cmul(w64, w64);
#define DSW(row, val) asm volatile("ds_write_addtid_b32 %0 offset:%1" :: "v"(val), "i"((row) * TAB_PITCH * 4) : "memory")
#define PUTE(row, c) do { f2 c_ = (c); DSW((row), pk_h(c_.x, -c_.y)); } while (0)
#define PUT(row, c) do { f2 c_ = (c); DSW(16 + (row), pk_h(c_.x, c_.y)); } while (0)
  {
    f2 e[8];
    e[1] = w8; e[2] = w16; e[3] = cmul(w8, w16); e[4] = w32;
    e[5] = cmul(w32, e[1]); e[6] = cmul(w32, e[2]); e[7] = cmul(w32, e[3]);
    DSW(0, 0x00003C00u);
#pragma unroll
    for (int l = 1; l < 8; ++l) PUTE(l, e[l]);
    PUTE(8, w64);
#pragma unroll
    for (int l = 1; l < 8; ++l) PUTE(8 + l, cmul(w64, e[l]));
  }
  __syncthreads();
  STAMP(1, 1);
  u4 ga0 = gt[0], ga1 = gt[1];
  float sc, unsc;
  {
    f4 q0 = *(const f4*)(kc), q1 = *(const f4*)(kc + 16), q2 = *(const f4*)(kc + 32);
    q0 += q1; q0 += q2;
    float sig = ((q0.x + q0.y) + (q0.z + q0.w)) + fabsf(dsk);
    int ex = (int)(__float_as_uint(sig) >> 23) & 255; ex = ex < 24 ? 24 : (ex > 253 ? 253 : ex);
    sc = __uint_as_float((unsigned)(267 - ex) << 23);
    unsc = __uint_as_float((unsigned)(ex - 20) << 23);
    if (tid == 0) *(unsigned*)(kc + ZIDX * 2) = 0;
    f2 c[8];
    c[0] = CB * sc;
    c[1] = cmul(c[0], w1); c[2] = cmul(c[0], w2); c[4] = cmul(c[0], w4); c[3] = cmul(c[1], w2);
    c[5] = cmul(c[1], w4); c[6] = cmul(c[2], w4); c[7] = cmul(c[3], w4);
#pragma unroll
    for (int r = 0; r < 8; ++r) {
      PUT(r, c[r]);
      f2 ch = cmul(c[r], w128); PUT(8 + r, ch);
      if ((r & 1) == 0) {
        f2 s0 = cmul(c[r], w256), s1 = cmul(ch, w256);
        PUT(16 + (r >> 1), s0); PUT(20 + (r >> 1), s1);
        if ((r & 3) == 0) {
          const int rho = r >> 2;
          f2 z0 = cmul(c[r], w512), z1 = cmul(ch, w512), z2 = cmul(s0, w512), z3 = cmul(s1, w512);
          PUT(24 + 0 + rho, z0); PUT(24 + 2 + rho, z1); PUT(24 + 4 + rho, z2); PUT(24 + 6 + rho, z3);
          if (r == 0) {
            f2 y0 = cmul(c[0], w1024);
            PUT(32, y0); PUT(33, cmul(ch, w1024)); PUT(34, cmul(s0, w1024)); PUT(35, cmul(s1, w1024));
            PUT(36, cmul(z0, w1024)); PUT(37, cmul(z1, w1024)); PUT(38, cmul(z2, w1024)); PUT(39, cmul(z3, w1024));
            PUT(40, cmul(y0, w1024));
          }
        }
      }
    }
  }
  asm volatile("s_waitcnt lgkmcnt(0)" ::: "memory");
  __syncthreads();
  STAMP(1, 2);
  {
    const unsigned* tab = (const unsigned*)sm;
    f4 acc0 = Z, acc1 = Z;
    const unsigned* pa = tab + col * TAB_PITCH + g * 4;
    const unsigned* pb = tab + (16 + wave * 16 + col) * TAB_PITCH + g * 4;
    u4 ea[12], pb_[12];
#pragma unroll
    for (int kk = 0; kk < 12; ++kk) { ea[kk] = *(const u4*)(pa + kk * 16); pb_[kk] = *(const u4*)(pb + kk * 16); }
#pragma unroll
    for (int kk = 0; kk < 12; kk += 2) {
      acc0 = MFMA16(as_h8(ea[kk]), as_h8(pb_[kk]), acc0);
      acc1 = MFMA16(as_h8(ea[kk + 1]), as_h8(pb_[kk + 1]), acc1);
    }
    __builtin_amdgcn_sched_group_barrier(0x100, 24, 0);
    __builtin_amdgcn_sched_group_barrier(0x008, 12, 0);
    acc0 += acc1;
    acc0[0] = __builtin_fmaf(tid == 0 ? dsk : 0.f, sc, acc0[0]);
    u2 o; o.x = pk_h(acc0[0], acc0[1]); o.y = pk_h(acc0[2], acc0[3]);
    *(u2*)(kc + ((wave * 16 + col) * 16 + 4 * g) * 2) = o;
  }
  __syncthreads();
  *(u4*)(sm + SM_KI + tid * 16) = u4{LDP(ga0.x, ga0.y), LDP(ga0.z, ga0.w), LDP(ga1.x, ga1.y), LDP(ga1.z, ga1.w)};
  __syncthreads();
  __builtin_amdgcn_s_setprio(0);
  STAMP(1, 3);
  const int i0 = 15 - col + 8 * g;
  const char* ka = sm + SM_KI + ((i0 & 1) * NR_LEN + (i0 & ~1)) * 2;
  const int n0 = 3 * wave;
  f4 a0 = Z, a1 = Z, a2 = Z;
  const unsigned fa = (unsigned)(SM_U + (256 + 16 * col + 8 * g + 256 * n0 - 528) * 2);
  h8 A[17];
#pragma unroll
  for (int k = 0; k < 17; ++k) A[k] = ld_4dw(ka + k * 64, ka + k * 64 + 8);
  __builtin_amdgcn_sched_barrier(0);
  if (wave == 0) near_band<9, 33, true>(A, fa, a0, a1, a2);
  else if (wave == 1) near_band<0, 33, true>(A, fa, a0, a1, a2);
  else near_band<0, 25, false>(A, fa, a0, a1, a2);
  asm volatile("s_nop 7\n\ts_nop 7" : "+v"(a0), "+v"(a1), "+v"(a2));
  __syncthreads();
  {
    const char* pp = sm + SM_P + lane * 16 + (n0 - 2) * 1024;
    if (wave != 0) { a0 += *(const f4*)(pp); a1 += *(const f4*)(pp + 1024); }
    conv_epi(a0, unsc, yt + YTS * n0);
    conv_epi(a1, unsc, yt + YTS * (n0 + 1));
    if (wave != 2) { a2 += *(const f4*)(pp + 2048); conv_epi(a2, unsc, yt + YTS * (n0 + 2)); }
  }
  STAMP(1, 4);
  STAMPD(1, 5);
}

__device__ __forceinline__ unsigned ord16(unsigned b) { return (b & 0x8000u) ? (~b & 0xffffu) : (b | 0x8000u); }
__device__ __forceinline__ void fma8(float* acc, u4 w, float xv) {
  acc[0] = __builtin_fmaf(bflo(w.x), xv, acc[0]); acc[1] = __builtin_fmaf(bfhi(w.x), xv, acc[1]);
  acc[2] = __builtin_fmaf(bflo(w.y), xv, acc[2]); acc[3] = __builtin_fmaf(bfhi(w.y), xv, acc[3]);
  acc[4] = __builtin_fmaf(bflo(w.z), xv, acc[4]); acc[5] = __builtin_fmaf(bfhi(w.z), xv, acc[5]);
  acc[6] = __builtin_fmaf(bflo(w.w), xv, acc[6]); acc[7] = __builtin_fmaf(bfhi(w.w), xv, acc[7]);
}
#define RLX_AGENT __ATOMIC_RELAXED, __HIP_MEMORY_SCOPE_AGENT
constexpr unsigned SPIN_CAP = 1u << 22;

__device__ __forceinline__ u64 sweep_keys(char* ws, int lane, int rep, u64* lk) {
  GAS u64* kp = (GAS u64*)(ws + WS_KEYS) + rep * 256 + lane;
  u64 b;
  for (unsigned spins = 0;; ++spins) {
    u64 k0 = __hip_atomic_load(kp, RLX_AGENT), k1 = __hip_atomic_load(kp + 64, RLX_AGENT);
    u64 k2 = __hip_atomic_load(kp + 128, RLX_AGENT), k3 = __hip_atomic_load(kp + 192, RLX_AGENT);
    bool ok = (long long)(k0 & k1 & k2 & k3) < 0;
    b = k0 > k1 ? k0 : k1; u64 c = k2 > k3 ? k2 : k3; b = b > c ? b : c;
    if (__all(ok) || spins > SPIN_CAP) {
      if (lk) { lk[lane] = k0; lk[lane + 64] = k1; lk[lane + 128] = k2; lk[lane + 192] = k3; }
      break;
    }
    __builtin_amdgcn_s_sleep(1);
  }
  unsigned hk = (unsigned)(b >> 32), m = hk;
  m = max(m, (unsigned)__builtin_amdgcn_update_dpp(0, (int)m, 0x121, 0xf, 0xf, true));
  m = max(m, (unsigned)__builtin_amdgcn_update_dpp(0, (int)m, 0x122, 0xf, 0xf, true));
  m = max(m, (unsigned)__builtin_amdgcn_update_dpp(0, (int)m, 0x124, 0xf, 0xf, true));
  m = max(m, (unsigned)__builtin_amdgcn_update_dpp(0, (int)m, 0x128, 0xf, 0xf, true));
  unsigned m0 = (unsigned)__builtin_amdgcn_readlane((int)m, 0), m1 = (unsigned)__builtin_amdgcn_readlane((int)m, 16);
  unsigned m2 = (unsigned)__builtin_amdgcn_readlane((int)m, 32), m3 = (unsigned)__builtin_amdgcn_readlane((int)m, 48);
  m0 = max(max(m0, m1), max(m2, m3));
  int src = __builtin_ctzll(__ballot(hk == m0));
  unsigned lo = (unsigned)__builtin_amdgcn_readlane((int)(unsigned)b, src);
  return ((u64)m0 << 32) | lo;
}
__device__ __forceinline__ float par_sum(float v) {
  v = dpp_add<0x122>(v); v = dpp_add<0x124>(v); v = dpp_add<0x128>(v);
  return v;
}
__device__ __forceinline__ float xs_of(us xv, us yv, float wm, float wrs, float& hh) {
  hh = rbf(bf1(xv) + bf1(yv));
  return rbf(rbf(hh - wm) * wrs);
}

extern "C" __global__ __launch_bounds__(384, 2) void k3_tail(const us* __restrict__ x, const us* __restrict__ wr,
                                                  const us* __restrict__ wg, const us* __restrict__ wu,
                                                  const us* __restrict__ wd, us* __restrict__ out,
                                                  char* __restrict__ ws) {
  __shared__ __attribute__((aligned(16))) float red[5 * 32 + 16 + 8];
  __shared__ __attribute__((aligned(16))) u64 lkeys[256];
  unsigned* redW = (unsigned*)(red + 176);
  const int tid = threadIdx.x, lane = tid & 63, wave = __builtin_amdgcn_readfirstlane(tid >> 6);
  const int bid = blockIdx.x;
  const u64 tstart3 = __builtin_amdgcn_s_memrealtime();
  const us* ytb = (const us*)(ws + WS_YT);
  if (bid >= 256) {
    const bool f1 = bid < 352;
    const int j = f1 ? bid - 256 : bid - 352;
    const int cb = f1 ? (j & 7) * 12 + (j >> 3) : (j & 7) * 5 + (j >> 3);
    if (wave == 5) {
      __builtin_amdgcn_s_sleep(40);
      const u64 wn = sweep_keys(ws, lane, j & 7, f1 ? lkeys : nullptr);
      const unsigned wkey = (unsigned)(wn >> 32), wms = (unsigned)wn;
      const int wrow = 2047 - (int)(wkey & 2047u);
      const int c = lane & 15;
      if (f1) {
        if (lane == 0) { redW[2] = wkey; redW[3] = wms; }
        STAMPT(2, 3, 320);
        __syncthreads();
        __syncthreads();
        const float* rp = red + (c >> 3) * 16 + (c & 7);
        float gs = (rp[0] + rp[32]) + (rp[64] + rp[96]) + rp[128];
        float us_ = (rp[8] + rp[40]) + (rp[72] + rp[104]) + rp[136];
        float gb = rbf(gs);
        float sl = rbf(gb * __builtin_amdgcn_rcpf(1.f + __expf(-gb)));
        unsigned a = (unsigned)f2bf(sl * rbf(us_)) | 0x10000u;
        __hip_atomic_store((GAS unsigned*)(ws + WS_ACT) + (lane >> 4) * F_ + cb * 16 + c, a, RLX_AGENT);
        STAMPT(2, 4, 320);
      } else {
        const int col = cb * 16 + c;
        float fh;
        float fx2 = xs_of(x[(size_t)wrow * D_ + col], ytb[((size_t)(wrow >> 3) * D_ + col) * 8 + (wrow & 7)],
                          bflo(wms), __builtin_amdgcn_rcpf(bfhi(wms)), fh);
        unsigned ob = (wkey >> 11) & 0xffffu;
        unsigned rb = (ob & 0x8000u) ? (ob & 0x7fffu) : (~ob & 0xffffu);
        float sg = rbf(__builtin_amdgcn_rcpf(1.f + __expf(-bf1((us)rb))));
        __syncthreads();
        const float* rp = red + c;
        float ys = (rp[0] + rp[16]) + (rp[32] + rp[48]) + rp[64];
        float y = rbf(rbf(ys) * sg);
        if (lane < 16) out[(size_t)wrow * D_ + col] = f2bf(fh + rbf(fx2 + y));
        if (bid == 352) { const u64 r0 = *(const u64*)(ws + WS_WSUM + 64); while (__builtin_amdgcn_s_memrealtime() - r0 < 4000ull) __builtin_amdgcn_s_sleep(2); }
      }
      return;
    }
    const int half = tid & 1, rb = tid >> 1;
    __builtin_amdgcn_s_sleep(16);
    if (f1) {
      const size_t o0 = (size_t)rb * F_ + cb * 16 + half * 8;
      u4 wq[8];
#pragma unroll
      for (int k = 0; k < 4; ++k) { wq[k] = *(const u4*)(wg + o0 + (size_t)k * 160 * F_); wq[4 + k] = *(const u4*)(wu + o0 + (size_t)k * 160 * F_); }
      f2 wf[32];
#pragma unroll
      for (int k = 0; k < 8; ++k) { wf[4 * k] = unpk(wq[k].x); wf[4 * k + 1] = unpk(wq[k].y); wf[4 * k + 2] = unpk(wq[k].z); wf[4 * k + 3] = unpk(wq[k].w); }
#pragma unroll
      for (int k = 0; k < 32; ++k) asm volatile("" : "+v"(wf[k]));
      __syncthreads();
      const unsigned wkey = redW[2], wms = redW[3];
      const int wrow = 2047 - (int)(wkey & 2047u);
      const float wm = bflo(wms), wrs = __builtin_amdgcn_rcpf(bfhi(wms));
      const us* xw = x + (size_t)wrow * D_ + rb;
      const us* yw = ytb + ((size_t)(wrow >> 3) * D_ + rb) * 8 + (wrow & 7);
      us xv[4], yv[4];
#pragma unroll
      for (int k = 0; k < 4; ++k) { xv[k] = xw[160 * k]; yv[k] = yw[160 * 8 * k]; }
      f2 acc[8];
#pragma unroll
      for (int i = 0; i < 8; ++i) acc[i] = f2{0.f, 0.f};
#pragma unroll
      for (int k = 0; k < 4; ++k) {
        float hh; float xs = xs_of(xv[k], yv[k], wm, wrs, hh);
        f2 x2v = {xs, xs};
#pragma unroll
        for (int i = 0; i < 4; ++i) {
          acc[i] = __builtin_elementwise_fma(wf[4 * k + i], x2v, acc[i]);
          acc[4 + i] = __builtin_elementwise_fma(wf[16 + 4 * k + i], x2v, acc[4 + i]);
        }
      }
      float w[8], o[4];
#pragma unroll
      for (int i = 0; i < 4; ++i) { w[2 * i] = swap32_add(acc[i].x, acc[4 + i].x); w[2 * i + 1] = swap32_add(acc[i].y, acc[4 + i].y); }
#pragma unroll
      for (int i = 0; i < 4; ++i) o[i] = swap16_add(w[i], w[i + 4]);
      par_sum4(o);
      const int rr = lane >> 4;
      if ((lane & 15) < 2) *(f4*)(red + wave * 32 + half * 16 + 4 * (rr & 1) + 8 * (rr >> 1)) = f4{o[0], o[1], o[2], o[3]};
      __syncthreads();
#pragma unroll
      for (int q = 0; q < 3; ++q) {
        const int cbk = j + 96 * q;
        if (cbk < 256) {
          const u64 gk = lkeys[cbk];
          const unsigned ck = (unsigned)(gk >> 32), cms = (unsigned)gk;
          if (ck != wkey) {
            const int crow = 2047 - (int)(ck & 2047u);
            const float crs = __builtin_amdgcn_rcpf(bfhi(cms)), cb_ = -bflo(cms) * crs;
            unsigned xr = ((const unsigned*)(x + (size_t)crow * D_))[tid];
            const us* yc = ytb + ((size_t)(crow >> 3) * D_ + 2 * tid) * 8 + (crow & 7);
            f2 hs = unpk(xr) + f2{bf1(yc[0]), bf1(yc[8])};
            f2 hh = unpk(pk_bf(hs.x, hs.y));
            f2 oo = hh + __builtin_elementwise_fma(hh, f2{crs, crs}, f2{cb_, cb_});
            ((unsigned*)(out + (size_t)crow * D_))[tid] = pk_bf(oo.x, oo.y);
          }
        }
      }
      STAMP(2, 6);
    } else {
      const us* wp = wd + cb * 16 + half * 8;
      u4 wq[10];
#pragma unroll
      for (int k = 0; k < 10; ++k) { int r = rb + 160 * k; r = r < F_ ? r : F_ - 1; wq[k] = *(const u4*)(wp + (size_t)r * D_); }
      f2 wf[40];
#pragma unroll
      for (int k = 0; k < 10; ++k) { wf[4 * k] = unpk(wq[k].x); wf[4 * k + 1] = unpk(wq[k].y); wf[4 * k + 2] = unpk(wq[k].z); wf[4 * k + 3] = unpk(wq[k].w); }
#pragma unroll
      for (int k = 0; k < 40; ++k) asm volatile("" : "+v"(wf[k]));
      __builtin_amdgcn_s_sleep(30);
      GAS unsigned* ap = (GAS unsigned*)(ws + WS_ACT) + (j & 3) * F_ + rb;
      const int last = rb < 96 ? 1440 : 0;
      unsigned a[10];
      for (unsigned spins = 0;; ++spins) {
#pragma unroll
        for (int k = 0; k < 9; ++k) a[k] = __hip_atomic_load(ap + 160 * k, RLX_AGENT);
        a[9] = __hip_atomic_load(ap + last, RLX_AGENT);
        unsigned all = a[0] & a[1] & a[2] & a[3] & a[4] & a[5] & a[6] & a[7] & a[8] & a[9];
        if (__all(all >> 16) || spins > SPIN_CAP) break;
        __builtin_amdgcn_s_sleep(1);
      }
      if (rb >= 96) a[9] = 0;
      f2 acc[4] = {f2{0.f, 0.f}, f2{0.f, 0.f}, f2{0.f, 0.f}, f2{0.f, 0.f}};
#pragma unroll
      for (int k = 0; k < 10; ++k) {
        float av = bflo(a[k]); f2 a2 = {av, av};
#pragma unroll
        for (int i = 0; i < 4; ++i) acc[i] = __builtin_elementwise_fma(wf[4 * k + i], a2, acc[i]);
      }
      float w0 = swap32_add(acc[0].x, acc[2].x), w1 = swap32_add(acc[0].y, acc[2].y);
      float w2 = swap32_add(acc[1].x, acc[3].x), w3 = swap32_add(acc[1].y, acc[3].y);
      float o0 = par_sum(swap16_add(w0, w2)), o1 = par_sum(swap16_add(w1, w3));
      const int rr = lane >> 4;
      if ((lane & 15) < 2) *(f2*)(red + wave * 16 + half * 8 + 2 * (rr & 1) + 4 * (rr >> 1)) = f2{o0, o1};
      __syncthreads();
    }
    return;
  }
  float* redA = red;
  float* abv = red + 160;
  const int tb = (bid & 7) * 32 + (bid >> 3);
  const int t0 = tb * 8;
  const unsigned* xb = (const unsigned*)(x + (size_t)t0 * D_);
  if (wave == 5) {
    const int r = lane & 7;
    const float piv = bf1(x[(size_t)(t0 + r) * D_]);
    const float W = *(const float*)(ws + WS_WSUM);
    __syncthreads();
    float s1 = (redA[r] + redA[24 + r]) + (redA[48 + r] + redA[72 + r]) + redA[96 + r];
    float s2 = (redA[8 + r] + redA[32 + r]) + (redA[56 + r] + redA[80 + r]) + redA[104 + r];
    float s3 = (redA[16 + r] + redA[40 + r]) + (redA[64 + r] + redA[88 + r]) + redA[112 + r];
    float mb, sb;
    ln_from_sums(s1, s2, piv, mb, sb);
    float rs = __builtin_amdgcn_rcpf(sb);
    float rv = rs * __builtin_fmaf(piv - mb, W, s3);
    unsigned key = 0x80000000u | (ord16(f2bf(rv)) << 11) | (unsigned)(2047 - (t0 + r));
    unsigned best = key;
    best = max(best, (unsigned)__builtin_amdgcn_update_dpp(0, (int)best, 0x111, 0xf, 0xf, true));
    best = max(best, (unsigned)__builtin_amdgcn_update_dpp(0, (int)best, 0x112, 0xf, 0xf, true));
    best = max(best, (unsigned)__builtin_amdgcn_update_dpp(0, (int)best, 0x114, 0xf, 0xf, true));
    best = (unsigned)__builtin_amdgcn_readlane((int)best, 7);
    if (key == best) {
      u64 gr = ((u64)best << 32) | (u64)((__float_as_uint(mb) >> 16) | (__float_as_uint(sb) & 0xffff0000u));
      __hip_atomic_store((GAS u64*)(ws + WS_KEYS) + (lane >> 3) * 256 + bid, gr, RLX_AGENT);
      redW[0] = (unsigned)r;
    }
    if (lane < 8) *(f2*)(abv + 2 * r) = f2{rs, -mb * rs};
    STAMPT(2, 2, 320);
    __syncthreads();
    return;
  }
  unsigned raw[8];
#pragma unroll
  for (int r = 0; r < 8; ++r) raw[r] = xb[r * (D_ / 2) + tid];
  const u4 y0 = *(const u4*)(ytb + ((size_t)tb * D_ + 2 * tid) * 8);
  const u4 y1 = *(const u4*)(ytb + ((size_t)tb * D_ + 2 * tid) * 8 + 8);
  const f2 wv = unpk(((const unsigned*)wr)[tid]);
  f2 h[8];
  {
    float v[24];
#pragma unroll
    for (int r = 0; r < 8; ++r) {
      const float piv = bflo(xb[r * (D_ / 2)]);
      unsigned ya = y0[r >> 1], yb = y1[r >> 1];
      f2 yy = (r & 1) ? f2{bfhi(ya), bfhi(yb)} : f2{bflo(ya), bflo(yb)};
      f2 hs = unpk(raw[r]) + yy;
      h[r] = unpk(pk_bf(hs.x, hs.y));
      f2 dd = h[r] - piv;
      v[r] = dd.x + dd.y;
      v[r + 8] = __builtin_fmaf(dd.x, dd.x, dd.y * dd.y);
      v[r + 16] = __builtin_fmaf(dd.x, wv.x, dd.y * wv.y);
    }
    float w[12], o[6];
#pragma unroll
    for (int i = 0; i < 12; ++i) w[i] = swap32_add(v[i], v[i + 12]);
#pragma unroll
    for (int i = 0; i < 6; ++i) o[i] = swap16_add(w[i], w[i + 6]);
    row_sum6(o);
    if ((lane & 15) == 0) {
      float* dst = redA + wave * 24 + (lane >> 4) * 6;
      *(f2*)dst = f2{o[0], o[1]}; *(f2*)(dst + 2) = f2{o[2], o[3]}; *(f2*)(dst + 4) = f2{o[4], o[5]};
    }
  }
  __syncthreads();
  STAMP(2, 1);
  __syncthreads();
  const int brow = (int)redW[0];
  unsigned* outr = (unsigned*)(out + (size_t)t0 * D_) + tid;
#pragma unroll
  for (int r = 0; r < 8; r += 2) {
    f4 c = *(const f4*)(abv + 2 * r);
    f2 o0 = h[r] + __builtin_elementwise_fma(h[r], f2{c.x, c.x}, f2{c.y, c.y});
    f2 o1 = h[r + 1] + __builtin_elementwise_fma(h[r + 1], f2{c.z, c.z}, f2{c.w, c.w});
    unsigned q0 = pk_bf(o0.x, o0.y), q1 = pk_bf(o1.x, o1.y);
    if (r != brow) outr[r * (D_ / 2)] = q0;
    if (r + 1 != brow) outr[(r + 1) * (D_ / 2)] = q1;
  }
}

extern "C" __global__ void k9_end() {}

extern "C" void kernel_launch(void* const* d_in, const int* in_sizes, int n_in, void* d_out, int out_size,
                              void* d_ws, size_t ws_size, hipStream_t stream) {
  const us* x = (const us*)d_in[0];
  const us* Are = (const us*)d_in[1];
  const us* Aim = (const us*)d_in[2];
  const us* Bre = (const us*)d_in[3];
  const us* Bim = (const us*)d_in[4];
  const us* Cre = (const us*)d_in[5];
  const us* Cim = (const us*)d_in[6];
  const us* logdt = (const us*)d_in[7];
  const us* Dskip = (const us*)d_in[8];
  const us* wr = (const us*)d_in[10];
  const us* wg = (const us*)d_in[11];
  const us* wu = (const us*)d_in[12];
  const us* wd = (const us*)d_in[13];
  us* out = (us*)d_out;
  char* ws = (char*)d_ws;
  k1_ln<<<256, 384, 0, stream>>>(x, Are, Aim, Bre, Bim, Cre, Cim, wr, wg, wu, wd, out, ws);
  k2_conv<<<D_, 256, K2_LDS, stream>>>(Are, Aim, Bre, Bim, Cre, Cim, logdt, Dskip, ws);
  k3_tail<<<392, 384, 0, stream>>>(x, wr, wg, wu, wd, out, ws);
}
```

```cpp
#include <hip/hip_runtime.h>
#include <hip/hip_bf16.h>
#include <stdint.h>

typedef _Float16 h8 __attribute__((ext_vector_type(8)));
typedef _Float16 h2 __attribute__((ext_vector_type(2)));
typedef float f4 __attribute__((ext_vector_type(4)));
typedef float f2 __attribute__((ext_vector_type(2)));
typedef unsigned u4 __attribute__((ext_vector_type(4)));
typedef unsigned u2 __attribute__((ext_vector_type(2)));
typedef unsigned short us;
typedef unsigned long long u64;
#define GAS __attribute__((address_space(1)))
#define LAS __attribute__((address_space(3)))

constexpr int L_ = 2048, D_ = 640, N_ = 192, F_ = 1536;
constexpr int LP = 2048 + 64;
#define STAMPD(kid, slot) do {} while (0)
#define STAMP(kid, slot) do {} while (0)
#define STAMPT(kid, slot, thr) do {} while (0)

constexpr int NR_LEN  = 608;
constexpr int F2_OFF  = 2 * NR_LEN;
constexpr int F3_OFF  = F2_OFF + 8 * 64;
constexpr int KIMG_ELEMS = F3_OFF + 4 * 80;
constexpr int KCHUNKS = KIMG_ELEMS / 8;

constexpr size_t WS_FLAGS = 0;
constexpr size_t WS_KEYS  = WS_FLAGS;
constexpr size_t WS_ACT   = WS_FLAGS + 16384;
constexpr size_t WS_WSUM  = 40960;
constexpr size_t WS_UT    = 40960 + 256;
constexpr size_t WS_YT    = WS_UT + (size_t)D_ * LP * 2;
constexpr size_t WS_TOTAL = WS_YT + (size_t)D_ * L_ * 2;

constexpr int TAB_PITCH = 200;
constexpr int SM_KC = 57 * TAB_PITCH * 4;
constexpr int SM_U  = SM_KC + 1552;
constexpr int K2_LDS = SM_U + 4608;
constexpr int SM_KI = 0;
constexpr int SM_P  = 4096;

constexpr int kc_of_v(int v) {
  if (v < 256)  { int h = v >> 7, r = v & 127; return ((h * 8 + (r & 7)) * 16) + (r >> 3); }
  if (v < 512)  { int w = v - 256, h = w >> 7, r = w & 127; return ((16 + h * 4 + ((r & 7) >> 1)) * 16) + (r >> 3); }
  if (v < 1024) { int w = v - 512, h = w >> 7, r = w & 127; return ((24 + h * 2 + ((r & 7) >> 2)) * 16) + (r >> 3); }
  if (v < 2048) { int w = v - 1024, h = w >> 7, r = w & 127; return ((32 + h) * 16) + (r >> 3); }
  return 40 * 16;
}
constexpr int bf16_round_int(int j) {
  if (j <= 256) return j;
  int step = j < 512 ? 2 : (j < 1024 ? 4 : 8);
  int lo = (j / step) * step, rem = j - lo;
  if (rem * 2 < step) return lo;
  if (rem * 2 > step) return lo + step;
  return ((lo / step) & 1) ? lo + step : lo;
}
constexpr int ZIDX = 768;

struct GatherTab { unsigned v[KIMG_ELEMS]; };
constexpr GatherTab make_gather() {
  GatherTab t{};
  for (int i = 0; i < KIMG_ELEMS; ++i) t.v[i] = SM_KC + ZIDX * 2;
  for (int e = 0; e < 2; ++e)
    for (int i = 0; i < NR_LEN; ++i) {
      int j = 543 - (i + e);
      if (j >= 0 && j < 515) t.v[e * NR_LEN + i] = (unsigned)(SM_KC + 2 * kc_of_v(bf16_round_int(j)));
    }
  for (int pi = 0; pi < 4; ++pi)
    for (int e = 0; e < 2; ++e)
      for (int i = 0; i < 64; ++i) {
        int p = 79 - (i + e);
        if (p >= 32 && p < 64) {
          int v = 16 * p + 8 * (pi >> 1) + ((pi & 1) ? 8 : 4);
          t.v[F2_OFF + (pi * 2 + e) * 64 + i] = (unsigned)(SM_KC + 2 * kc_of_v(v));
        }
      }
  for (int pi = 0; pi < 2; ++pi)
    for (int e = 0; e < 2; ++e)
      for (int i = 0; i < 80; ++i) {
        int qq = 127 - (i + e);
        if (qq >= 64 && qq < 128) {
          int v = 16 * qq + (pi ? 16 : 8);
          t.v[F3_OFF + (pi * 2 + e) * 80 + i] = (unsigned)(SM_KC + 2 * kc_of_v(v));
        }
      }
  return t;
}
__device__ const GatherTab g_gather = make_gather();

struct BoxTab { us v[7 * 64 * 8]; };
constexpr BoxTab make_box() {
  BoxTab t{};
  const int nn[6] = {3, 5, 3, 5, 7, 9};
  const int sh[6] = {3, 6, 11, 14, 5, 12};
  const int w0[6] = {-16, -16, -24, -24, -16, -24};
  for (int m = 0; m < 6; ++m)
    for (int lane = 0; lane < 64; ++lane)
      for (int j = 0; j < 8; ++j) {
        int b = lane & 15, k = 8 * (lane >> 4) + j;
        int idx = w0[m] + k;
        bool on = idx <= b - sh[m] && idx >= b - sh[m] - (nn[m] - 1);
        t.v[(m * 64 + lane) * 8 + j] = on ? 0x3C00 : 0;
      }
  for (int lane = 0; lane < 64; ++lane)
    for (int j = 0; j < 8; ++j) {
      int b = lane & 15, k = 8 * (lane >> 4) + j;
      t.v[(6 * 64 + lane) * 8 + j] = (k - b == 4 || k - b == 5) ? 0xFFFF : 0;
    }
  return t;
}
__device__ const BoxTab g_box = make_box();

__device__ __forceinline__ float bflo(unsigned w) { return __uint_as_float(w << 16); }
__device__ __forceinline__ float bfhi(unsigned w) { return __uint_as_float(w & 0xffff0000u); }
__device__ __forceinline__ float bf1(us v) { return __uint_as_float((unsigned)v << 16); }
__device__ __forceinline__ us f2bf(float f) { __bf16 b = (__bf16)f; return __builtin_bit_cast(us, b); }
__device__ __forceinline__ float rbf(float f) { return bf1(f2bf(f)); }
typedef __bf16 b2 __attribute__((ext_vector_type(2)));
__device__ __forceinline__ unsigned pk_bf(float a, float b) {
  b2 v; v.x = (__bf16)a; v.y = (__bf16)b; return __builtin_bit_cast(unsigned, v);
}
__device__ __forceinline__ unsigned pk_h(float a, float b) {
  h2 v; v.x = (_Float16)a; v.y = (_Float16)b; return __builtin_bit_cast(unsigned, v);
}
template <int CTRL> __device__ __forceinline__ float dpp_add(float v) {
  int t = __builtin_amdgcn_update_dpp(0, __float_as_int(v), CTRL, 0xf, 0xf, true);
  return v + __int_as_float(t);
}
__device__ __forceinline__ float row_sum(float v) {
  v = dpp_add<0x121>(v); v = dpp_add<0x122>(v); v = dpp_add<0x124>(v); v = dpp_add<0x128>(v);
  return v;
}
__device__ __forceinline__ float swap32_add(float a, float b) {
  auto r = __builtin_amdgcn_permlane32_swap(__float_as_uint(a), __float_as_uint(b), false, false);
  return __uint_as_float(r[0]) + __uint_as_float(r[1]);
}
__device__ __forceinline__ float swap16_add(float a, float b) {
  auto r = __builtin_amdgcn_permlane16_swap(__float_as_uint(a), __float_as_uint(b), false, false);
  return __uint_as_float(r[0]) + __uint_as_float(r[1]);
}
__device__ __forceinline__ void wave_reduce16(const float (&v)[16], float (&out)[4]) {
  float w[8];
#pragma unroll
  for (int i = 0; i < 8; ++i) w[i] = swap32_add(v[i], v[i + 8]);
#pragma unroll
  for (int i = 0; i < 4; ++i) out[i] = row_sum(swap16_add(w[i], w[i + 4]));
}
__device__ __forceinline__ void wave_reduce8(const float (&v)[8], float (&out)[2]) {
  float w[4];
#pragma unroll
  for (int i = 0; i < 4; ++i) w[i] = swap32_add(v[i], v[i + 4]);
#pragma unroll
  for (int i = 0; i < 2; ++i) out[i] = row_sum(swap16_add(w[i], w[i + 2]));
}
__device__ __forceinline__ float wave_reduce4(const float (&v)[4]) {
  float w0 = swap32_add(v[0], v[2]), w1 = swap32_add(v[1], v[3]);
  return row_sum(swap16_add(w0, w1));
}
#define DPPA(i, st) "v_add_f32_dpp %" #i ", %" #i ", %" #i " row_ror:" #st " row_mask:0xf bank_mask:0xf\n\t"
__device__ __forceinline__ void row_sum8(float (&o)[8]) {
  asm volatile("s_nop 1\n\t"
    DPPA(0,1) DPPA(1,1) DPPA(2,1) DPPA(3,1) DPPA(4,1) DPPA(5,1) DPPA(6,1) DPPA(7,1)
    DPPA(0,2) DPPA(1,2) DPPA(2,2) DPPA(3,2) DPPA(4,2) DPPA(5,2) DPPA(6,2) DPPA(7,2)
    DPPA(0,4) DPPA(1,4) DPPA(2,4) DPPA(3,4) DPPA(4,4) DPPA(5,4) DPPA(6,4) DPPA(7,4)
    DPPA(0,8) DPPA(1,8) DPPA(2,8) DPPA(3,8) DPPA(4,8) DPPA(5,8) DPPA(6,8) DPPA(7,8)
    : "+v"(o[0]), "+v"(o[1]), "+v"(o[2]), "+v"(o[3]), "+v"(o[4]), "+v"(o[5]), "+v"(o[6]), "+v"(o[7]));
}
__device__ __forceinline__ void row_sum6(float (&o)[6]) {
  asm volatile("s_nop 1\n\t"
    DPPA(0,1) DPPA(1,1) DPPA(2,1) DPPA(3,1) DPPA(4,1) DPPA(5,1)
    DPPA(0,2) DPPA(1,2) DPPA(2,2) DPPA(3,2) DPPA(4,2) DPPA(5,2)
    DPPA(0,4) DPPA(1,4) DPPA(2,4) DPPA(3,4) DPPA(4,4) DPPA(5,4)
    DPPA(0,8) DPPA(1,8) DPPA(2,8) DPPA(3,8) DPPA(4,8) DPPA(5,8)
    : "+v"(o[0]), "+v"(o[1]), "+v"(o[2]), "+v"(o[3]), "+v"(o[4]), "+v"(o[5]));
}
__device__ __forceinline__ void row_sum4(float (&o)[4]) {
  asm volatile("s_nop 1\n\t"
    DPPA(0,1) DPPA(1,1) DPPA(2,1) DPPA(3,1)
    DPPA(0,2) DPPA(1,2) DPPA(2,2) DPPA(3,2)
    DPPA(0,4) DPPA(1,4) DPPA(2,4) DPPA(3,4)
    DPPA(0,8) DPPA(1,8) DPPA(2,8) DPPA(3,8)
    : "+v"(o[0]), "+v"(o[1]), "+v"(o[2]), "+v"(o[3]));
}
__device__ __forceinline__ void par_sum4(float (&o)[4]) {
  asm volatile("s_nop 1\n\t"
    DPPA(0,2) DPPA(1,2) DPPA(2,2) DPPA(3,2)
    DPPA(0,4) DPPA(1,4) DPPA(2,4) DPPA(3,4)
    DPPA(0,8) DPPA(1,8) DPPA(2,8) DPPA(3,8)
    : "+v"(o[0]), "+v"(o[1]), "+v"(o[2]), "+v"(o[3]));
}
__device__ __forceinline__ float rdlane(float v, int l) {
  return __int_as_float(__builtin_amdgcn_readlane(__float_as_int(v), l));
}

__device__ __forceinline__ f2 cmul(f2 a, f2 b) {
  f2 c;
  asm("v_pk_mul_f32 %0, %1, %2 op_sel:[0,0] op_sel_hi:[1,0]\n\tv_pk_fma_f32 %0, %1, %2, %0 op_sel:[1,1,0] op_sel_hi:[0,1,1] neg_lo:[1,0,0]"
      : "=&v"(c) : "v"(a), "v"(b));
  return c;
}
__device__ __forceinline__ f2 cexp_p(float lam, float rh, float rl, float p) {
  float mag = __builtin_amdgcn_exp2f(lam * p);
  float x = rh * p;
  float f = x - __builtin_rintf(x);
  f = __builtin_fmaf(rl, p, f);
  f2 w; w.x = mag * __builtin_amdgcn_cosf(f); w.y = mag * __builtin_amdgcn_sinf(f); return w;
}
#define MFMA16(a, b, c) __builtin_amdgcn_mfma_f32_16x16x32_f16((a), (b), (c), 0, 0, 0)
__device__ __forceinline__ h8 as_h8(u4 v) { return __builtin_bit_cast(h8, v); }

__device__ __forceinline__ void ln_from_sums(float s1, float s2, float piv, float& mb, float& sb) {
  float dm = s1 * (1.f / 640.f);
  float var = fmaxf(__builtin_fmaf(-dm, dm, s2 * (1.f / 640.f)), 0.f);
  mb = rbf(piv + dm);
  sb = rbf(__builtin_amdgcn_sqrtf(rbf(rbf(var) + 1e-5f)));
}
__device__ __forceinline__ f2 unpk(unsigned w) { return f2{bflo(w), bfhi(w)}; }

extern "C" __global__ __launch_bounds__(384, 2) void k1_ln(
    const us* __restrict__ x, const us* __restrict__ Are, const us* __restrict__ Aim,
    const us* __restrict__ Bre, const us* __restrict__ Bim, const us* __restrict__ Cre,
    const us* __restrict__ Cim, const us* __restrict__ wr,
    const us* __restrict__ wg, const us* __restrict__ wu, const us* __restrict__ wd, const us* __restrict__ out,
    char* __restrict__ ws) {
  __shared__ __attribute__((aligned(16))) float red[5 * 16 + 16 + 8];
  const int tid = threadIdx.x, lane = tid & 63, wave = __builtin_amdgcn_readfirstlane(tid >> 6);
  const int bid = blockIdx.x;
  STAMP(0, 0);
  if (wave == 5) {
    u64 pc; int o1, o2;
    asm volatile("s_getpc_b64 %0\n\ts_mov_b32 %1, k1_ln@rel32@lo+4\n\ts_mov_b32 %2, k9_end@rel32@lo+12" : "=s"(pc), "=s"(o1), "=s"(o2));
    const int xcd = bid & 7, jb = bid >> 3;
    const GAS char* cbase = (const GAS char*)(pc + (long long)o1);
    unsigned acc = 0;
    {
      const int id = jb * 64 + lane;
      const GAS char* p;
      if (id < 384) { int o = id * 64; p = cbase + (o < o2 - o1 ? o : 0); }
      else if (id < 384 + 1440) { int i = id - 384, ar = i / 240, q = i - ar * 240;
        const us* base = ar == 0 ? Are : ar == 1 ? Aim : ar == 2 ? Bre : ar == 3 ? Bim : ar == 4 ? Cre : Cim;
        p = (const GAS char*)base + xcd * 30720 + q * 128; }
      else if (id < 1824 + 64) p = (const GAS char*)g_gather.v + (id - 1824) * 128;
      else if (id < 1888 + 56) p = (const GAS char*)g_box.v + (id - 1888) * 128;
      else if (id < 1944 + 40) p = (const GAS char*)out + (size_t)(id - 1944) * 65536;
      else p = (const GAS char*)(ws + WS_YT) + (size_t)((id - 1984) % 40) * 65536;
      acc = *(const GAS unsigned*)p;
    }
    __builtin_amdgcn_s_barrier();
#pragma unroll
    for (int k = 0; k < 4; ++k) {
      int id = (jb * 4 + k) * 64 + lane;
      id = id < 6912 ? id : 6911;
      const GAS char* p;
      if (id < 3840) { int r3 = id / 3, q3 = id - r3 * 3;
        p = (const GAS char*)((r3 < 640 ? wg + (size_t)r3 * F_ : wu + (size_t)(r3 - 640) * F_) + xcd * 192 + q3 * 64); }
      else { int i2 = id - 3840; p = (const GAS char*)(wd + (size_t)(i2 >> 1) * D_ + xcd * 80 + (i2 & 1) * 78); }
      acc ^= *(const GAS unsigned*)p;
      if (k == 0) __builtin_amdgcn_s_barrier();
    }
    asm volatile("" :: "v"(acc));
    STAMPT(0, 7, 320);
    return;
  }
  const int t0 = ((bid & 7) * 32 + (bid >> 3)) * 8;
  const unsigned* xb = (const unsigned*)(x + (size_t)t0 * D_);
  unsigned raw[8];
#pragma unroll
  for (int r = 0; r < 8; ++r) raw[r] = xb[r * (D_ / 2) + tid];
  f2* ab = (f2*)(red + 80);
  unsigned* pv = (unsigned*)(red + 96);
  f2 a[8];
  {
    float v[16];
#pragma unroll
    for (int r = 0; r < 8; ++r) {
      const float piv = bflo(xb[r * (D_ / 2)]);
      a[r] = unpk(raw[r]);
      f2 dd = a[r] - piv;
      v[r] = dd.x + dd.y; v[r + 8] = __builtin_fmaf(dd.x, dd.x, dd.y * dd.y);
    }
    if (tid == 0) { *(u4*)(pv) = u4{raw[0], raw[1], raw[2], raw[3]}; *(u4*)(pv + 4) = u4{raw[4], raw[5], raw[6], raw[7]}; }
    float w[8], o[4];
#pragma unroll
    for (int i = 0; i < 8; ++i) w[i] = swap32_add(v[i], v[i + 8]);
#pragma unroll
    for (int i = 0; i < 4; ++i) o[i] = swap16_add(w[i], w[i + 4]);
    row_sum4(o);
    if ((lane & 15) == 0) *(f4*)(red + wave * 16 + (lane >> 4) * 4) = f4{o[0], o[1], o[2], o[3]};
  }
  __syncthreads();
  if (wave == 0) {
    const int r = lane & 7;
    float s1 = (red[r] + red[16 + r]) + (red[32 + r] + red[48 + r]) + red[64 + r];
    float s2 = (red[8 + r] + red[24 + r]) + (red[40 + r] + red[56 + r]) + red[72 + r];
    float mb, sb;
    ln_from_sums(s1, s2, bflo(pv[r]), mb, sb);
    float aa = 128.f * __builtin_amdgcn_rcpf(sb);
    if (lane < 8) ab[r] = f2{aa, -mb * aa};
  } else if (wave == 1 && bid == 0) {
    const unsigned* w2 = (const unsigned*)wr + lane;
    float sw = 0.f;
#pragma unroll
    for (int k = 0; k < 5; ++k) { unsigned q = w2[64 * k]; sw += bflo(q) + bfhi(q); }
    sw = row_sum(sw);
    sw = rdlane(sw, 0) + rdlane(sw, 16) + rdlane(sw, 32) + rdlane(sw, 48);
    if (lane == 0) *(float*)(ws + WS_WSUM) = sw;
  } else if (wave >= 1 && bid >= 1 && bid <= 10) {
    ((u4*)(ws + WS_FLAGS))[(bid - 1) * 256 + tid - 64] = u4{0, 0, 0, 0};
  }
  __syncthreads();
  unsigned p0[4], p1[4];
#pragma unroll
  for (int r = 0; r < 8; r += 2) {
    f4 c = *(const f4*)(ab + r);
    f2 u0 = __builtin_elementwise_fma(a[r], f2{c.x, c.x}, f2{c.y, c.y});
    f2 u1 = __builtin_elementwise_fma(a[r + 1], f2{c.z, c.z}, f2{c.w, c.w});
    p0[r >> 1] = pk_h(u0.x, u1.x); p1[r >> 1] = pk_h(u0.y, u1.y);
  }
  us* ut = (us*)(ws + WS_UT) + (size_t)(2 * tid) * LP + t0;
  *(u4*)(ut) = u4{p0[0], p0[1], p0[2], p0[3]};
  *(u4*)(ut + LP) = u4{p1[0], p1[1], p1[2], p1[3]};
  STAMP(0, 5);
  STAMPD(0, 6);
}

__device__ __forceinline__ h8 ld_h8(const char* p) { return as_h8(*(const u4*)p); }
__device__ __forceinline__ h8 ld_4dw(const char* p0, const char* p1) {
  const unsigned* a = (const unsigned*)p0; const unsigned* b = (const unsigned*)p1;
  return as_h8(u4{a[0], a[1], b[0], b[1]});
}
__device__ __forceinline__ h8 cvt_pair(f4 a, f4 b) {
  return as_h8(u4{pk_h(a[0], a[1]), pk_h(a[2], a[3]), pk_h(b[0], b[1]), pk_h(b[2], b[3])});
}
__device__ __forceinline__ void glds16(const void* g, void* l) {
  __builtin_amdgcn_global_load_lds((const GAS unsigned*)g, (LAS unsigned*)l, 16, 0, 0);
}
__device__ __forceinline__ void conv_epi(f4 acc, float sc, us* dst) {
  u2 o; o.x = pk_bf(acc[0] * sc, acc[1] * sc); o.y = pk_bf(acc[2] * sc, acc[3] * sc);
  *(u2*)dst = o;
}
#define LDU(a) ((unsigned)*(const LAS us*)(size_t)(a))
#define LDP(a, b) (LDU(a) | (LDU(b) << 16))

template <int OFF> __device__ __forceinline__ u4 lds_rd128(unsigned addr) {
  u4 v; asm volatile("ds_read_b128 %0, %1 offset:%2" : "=v"(v) : "v"(addr), "i"(OFF) : "memory"); return v;
}
template <int N> __device__ __forceinline__ void lgkm_wait() {
  asm volatile("s_waitcnt lgkmcnt(%0)" :: "i"(N) : "memory"); __builtin_amdgcn_sched_barrier(0);
}
template <int M, int M0, int M1, bool T3, int PF>
__device__ __forceinline__ void near_step(const h8 (&A)[17], u4 (&f)[PF], unsigned fa, f4& a0, f4& a1, f4& a2) {
  if constexpr (M < M1) {
    if constexpr (M >= M0 + PF) lgkm_wait<((M1 - 1 - M) < (PF - 1)) ? (M1 - 1 - M) : (PF - 1)>();
    const h8 ff = as_h8(f[(M - M0) % PF]);
    if constexpr (M <= 16) a0 = MFMA16(A[M], ff, a0);
    if constexpr (M >= 8 && M <= 24) a1 = MFMA16(A[M - 8], ff, a1);
    if constexpr (T3 && M >= 16) a2 = MFMA16(A[M - 16], ff, a2);
    __builtin_amdgcn_sched_barrier(0);
    if constexpr (M + PF < M1) f[(M - M0) % PF] = lds_rd128<(M + PF) * 64>(fa);
    near_step<M + 1, M0, M1, T3, PF>(A, f, fa, a0, a1, a2);
  }
}
template <int M, int M0, int PF> __device__ __forceinline__ void near_fill(u4 (&f)[PF], unsigned fa) {
  if constexpr (M < M0 + PF) { f[M - M0] = lds_rd128<M * 64>(fa); near_fill<M + 1, M0, PF>(f, fa); }
}
template <int M0, int M1, bool T3>
__device__ __forceinline__ void near_band(const h8 (&A)[17], unsigned fa, f4& a0, f4& a1, f4& a2) {
  constexpr int PF = 6;
  u4 f[PF];
  near_fill<M0, M0, PF>(f, fa);
  __builtin_amdgcn_s_waitcnt(0xC07F);
  __builtin_amdgcn_sched_barrier(0);
  near_step<M0, M0, M1, T3, PF>(A, f, fa, a0, a1, a2);
}

extern "C" __global__ __launch_bounds__(256, 2) void k2_conv(
    const us* __restrict__ Are, const us* __restrict__ Aim, const us* __restrict__ Bre, const us* __restrict__ Bim,
    const us* __restrict__ Cre, const us* __restrict__ Cim, const us* __restrict__ logdt,
    const us* __restrict__ Dskip, char* __restrict__ ws) {
  extern __shared__ __attribute__((aligned(16))) char sm[];
  const int tid = threadIdx.x, lane = tid & 63, wave = __builtin_amdgcn_readfirstlane(tid >> 6);
  const int d = (blockIdx.x & 7) * 80 + (blockIdx.x >> 3);
  const int col = lane & 15, g = lane >> 4;
  STAMP(1, 0);
  char* kc = sm + SM_KC;
  const char* ub = sm + SM_U + (256 + 16 * col + 8 * g) * 2;
  us* yt = (us*)(ws + WS_YT) + ((size_t)(2 * col + (g >> 1)) * D_ + d) * 8 + (g & 1) * 4;
  constexpr int YTS = 32 * D_ * 8;
  const f4 Z = {0.f, 0.f, 0.f, 0.f};
  const u4* gt = (const u4*)g_gather.v + 2 * tid;
  if (wave == 3) {
    const char* gu = ws + WS_UT + (size_t)d * LP * 2;
#pragma unroll
    for (int k = 0; k < 4; ++k) glds16(gu + (k * 64 + lane) * 16, sm + SM_U + 512 + k * 1024);
    const u4* bt = (const u4*)g_box.v + lane;
    u4 bx[7];
#pragma unroll
    for (int m = 0; m < 7; ++m) bx[m] = bt[64 * m];
    if (lane < 32) *(u4*)(sm + SM_U + lane * 16) = u4{0, 0, 0, 0};
    __builtin_amdgcn_s_barrier();
    asm volatile("s_waitcnt vmcnt(0)" ::: "memory");
    h8 sa[6], sb[6], s3[4];
#pragma unroll
    for (int tau = 0; tau < 6; ++tau) {
      h8 w1 = ld_h8(ub + (256 * tau - 16) * 2), w2 = ld_h8(ub + (256 * tau - 24) * 2);
      sa[tau] = cvt_pair(MFMA16(w1, as_h8(bx[0]), Z), MFMA16(w1, as_h8(bx[1]), Z));
      sb[tau] = cvt_pair(MFMA16(w2, as_h8(bx[2]), Z), MFMA16(w2, as_h8(bx[3]), Z));
      if (tau < 4) s3[tau] = cvt_pair(MFMA16(w1, as_h8(bx[4]), Z), MFMA16(w2, as_h8(bx[5]), Z));
    }
    u4 ga0 = gt[0], ga1 = gt[1];
    __builtin_amdgcn_s_barrier();
    __syncthreads();
    *(u4*)(sm + SM_KI + tid * 16) = u4{LDP(ga0.x, ga0.y), LDP(ga0.z, ga0.w), LDP(ga1.x, ga1.y), LDP(ga1.z, ga1.w)};
    const unsigned kv = LDU(SM_KC + 32 * 16 * 2) * 0x10001u;
    __syncthreads();
    f4 acc[6];
    acc[0] = Z; acc[1] = Z;
    {
      h8 As = as_h8(u4{bx[6].x & kv, bx[6].y & kv, bx[6].z & kv, bx[6].w & kv});
#pragma unroll
      for (int i = 0; i < 4; ++i) acc[2 + i] = MFMA16(As, ld_h8(ub + (256 * (4 + i) - 1032) * 2), Z);
    }
    {
      const int ib = 79 - col + 4 * g;
      const char* kb = sm + SM_KI + (F2_OFF + (ib & 1) * 64 + (ib & ~1)) * 2;
#pragma unroll
      for (int dl = 2; dl <= 4; ++dl) {
        h8 tA = ld_4dw(kb - dl * 32, kb + 256 - dl * 32), tB = ld_4dw(kb + 512 - dl * 32, kb + 768 - dl * 32);
#pragma unroll
        for (int tau = 0; tau < 6; ++tau)
          if (tau + dl <= 7) { acc[tau + dl - 2] = MFMA16(sa[tau], tA, acc[tau + dl - 2]); acc[tau + dl - 2] = MFMA16(sb[tau], tB, acc[tau + dl - 2]); }
      }
    }
    {
      const int ib = 127 - col + 4 * g;
      const char* kb = sm + SM_KI + (F3_OFF + (ib & 1) * 80 + (ib & ~1)) * 2;
#pragma unroll
      for (int dl = 4; dl <= 7; ++dl) {
        h8 tt = ld_4dw(kb - dl * 32, kb + 320 - dl * 32);
#pragma unroll
        for (int tau = 0; tau < 4; ++tau)
          if (tau + dl <= 7) acc[tau + dl - 2] = MFMA16(s3[tau], tt, acc[tau + dl - 2]);
      }
    }
#pragma unroll
    for (int i = 0; i < 6; ++i) *(f4*)(sm + SM_P + i * 1024 + lane * 16) = acc[i];
    __syncthreads();
    return;
  }
  __builtin_amdgcn_s_setprio(3);
  const size_t pidx = (size_t)d * N_ + tid;
  us r_are = Are[pidx], r_aim = Aim[pidx], r_bre = Bre[pidx], r_bim = Bim[pidx], r_cre = Cre[pidx], r_cim = Cim[pidx];
  const unsigned ldw = ((const unsigned*)logdt)[d >> 1], dsw = ((const unsigned*)Dskip)[d >> 1];
  const float dt = rbf(__expf((d & 1) ? bfhi(ldw) : bflo(ldw)));
  const float dsk = (d & 1) ? bfhi(dsw) : bflo(dsw);
  asm volatile("s_mov_b32 m0, %0" :: "s"(wave * 256) : "memory");
  float ar = dt * bf1(r_are), ai = dt * bf1(r_aim);
  f2 dB = {dt * bf1(r_bre), dt * bf1(r_bim)};
  f2 Cc = {bf1(r_cre), bf1(r_cim)};
  f2 CB = cmul(Cc, dB);
  const float LOG2E = 1.4426950408889634f;
  const float I2PH = 0.15915494309189535f;
  const float I2PL = (float)(0.15915494309189533577 - (double)0.15915494309189535f);
  float lam = ar * LOG2E;
  float rh = ai * I2PH;
  float rl = __builtin_fmaf(ai, I2PL, __builtin_fmaf(ai, I2PH, -rh));
  {
    float gg = fmaxf(1.f, __builtin_amdgcn_exp2f(lam * 2048.f));
    float bnd = row_sum((fabsf(CB.x) + fabsf(CB.y)) * gg);
    if ((lane & 15) == 0) ((float*)(kc))[wave * 4 + (lane >> 4)] = bnd;
  }
  f2 w1 = cexp_p(lam, rh, rl, 1.f), w16 = cexp_p(lam, rh, rl, 16.f), w256 = cexp_p(lam, rh, rl, 256.f);
  f2 w2 = cmul(w1, w1), w32 = cmul(w16, w16), w512 = cmul(w256, w256);
  f2 w4 = cmul(w2, w2), w64 = cmul(w32, w32), w1024 = cmul(w512, w512);
  f2 w8 = cmul(w4, w4), w128 = cmul(w64, w64);
#define DSW(row, val) asm volatile("ds_write_addtid_b32 %0 offset:%1" :: "v"(val), "i"((row) * TAB_PITCH * 4) : "memory")
#define PUTE(row, c) do { f2 c_ = (c); DSW((row), pk_h(c_.x, -c_.y)); } while (0)
#define PUT(row, c) do { f2 c_ = (c); DSW(16 + (row), pk_h(c_.x, c_.y)); } while (0)
  {
    f2 e[8];
    e[1] = w8; e[2] = w16; e[3] = cmul(w8, w16); e[4] = w32;
    e[5] = cmul(w32, e[1]); e[6] = cmul(w32, e[2]); e[7] = cmul(w32, e[3]);
    DSW(0, 0x00003C00u);
#pragma unroll
    for (int l = 1; l < 8; ++l) PUTE(l, e[l]);
    PUTE(8, w64);
#pragma unroll
    for (int l = 1; l < 8; ++l) PUTE(8 + l, cmul(w64, e[l]));
  }
  __syncthreads();
  STAMP(1, 1);
  u4 ga0 = gt[0], ga1 = gt[1];
  float sc, unsc;
  {
    f4 q0 = *(const f4*)(kc), q1 = *(const f4*)(kc + 16), q2 = *(const f4*)(kc + 32);
    q0 += q1; q0 += q2;
    float sig = ((q0.x + q0.y) + (q0.z + q0.w)) + fabsf(dsk);
    int ex = (int)(__float_as_uint(sig) >> 23) & 255; ex = ex < 24 ? 24 : (ex > 253 ? 253 : ex);
    sc = __uint_as_float((unsigned)(267 - ex) << 23);
    unsc = __uint_as_float((unsigned)(ex - 20) << 23);
    if (tid == 0) *(unsigned*)(kc + ZIDX * 2) = 0;
    f2 c[8];
    c[0] = CB * sc;
    c[1] = cmul(c[0], w1); c[2] = cmul(c[0], w2); c[4] = cmul(c[0], w4); c[3] = cmul(c[1], w2);
    c[5] = cmul(c[1], w4); c[6] = cmul(c[2], w4); c[7] = cmul(c[3], w4);
#pragma unroll
    for (int r = 0; r < 8; ++r) {
      PUT(r, c[r]);
      f2 ch = cmul(c[r], w128); PUT(8 + r, ch);
      if ((r & 1) == 0) {
        f2 s0 = cmul(c[r], w256), s1 = cmul(ch, w256);
        PUT(16 + (r >> 1), s0); PUT(20 + (r >> 1), s1);
        if ((r & 3) == 0) {
          const int rho = r >> 2;
          f2 z0 = cmul(c[r], w512), z1 = cmul(ch, w512), z2 = cmul(s0, w512), z3 = cmul(s1, w512);
          PUT(24 + 0 + rho, z0); PUT(24 + 2 + rho, z1); PUT(24 + 4 + rho, z2); PUT(24 + 6 + rho, z3);
          if (r == 0) {
            f2 y0 = cmul(c[0], w1024);
            PUT(32, y0); PUT(33, cmul(ch, w1024)); PUT(34, cmul(s0, w1024)); PUT(35, cmul(s1, w1024));
            PUT(36, cmul(z0, w1024)); PUT(37, cmul(z1, w1024)); PUT(38, cmul(z2, w1024)); PUT(39, cmul(z3, w1024));
            PUT(40, cmul(y0, w1024));
          }
        }
      }
    }
  }
  asm volatile("s_waitcnt lgkmcnt(0)" ::: "memory");
  __syncthreads();
  STAMP(1, 2);
  {
    const unsigned* tab = (const unsigned*)sm;
    f4 acc0 = Z, acc1 = Z;
    const unsigned* pa = tab + col * TAB_PITCH + g * 4;
    const unsigned* pb = tab + (16 + wave * 16 + col) * TAB_PITCH + g * 4;
    u4 ea[12], pb_[12];
#pragma unroll
    for (int kk = 0; kk < 12; ++kk) { ea[kk] = *(const u4*)(pa + kk * 16); pb_[kk] = *(const u4*)(pb + kk * 16); }
#pragma unroll
    for (int kk = 0; kk < 12; kk += 2) {
      acc0 = MFMA16(as_h8(ea[kk]), as_h8(pb_[kk]), acc0);
      acc1 = MFMA16(as_h8(ea[kk + 1]), as_h8(pb_[kk + 1]), acc1);
    }
    __builtin_amdgcn_sched_group_barrier(0x100, 24, 0);
    __builtin_amdgcn_sched_group_barrier(0x008, 12, 0);
    acc0 += acc1;
    acc0[0] = __builtin_fmaf(tid == 0 ? dsk : 0.f, sc, acc0[0]);
    u2 o; o.x = pk_h(acc0[0], acc0[1]); o.y = pk_h(acc0[2], acc0[3]);
    *(u2*)(kc + ((wave * 16 + col) * 16 + 4 * g) * 2) = o;
  }
  __syncthreads();
  *(u4*)(sm + SM_KI + tid * 16) = u4{LDP(ga0.x, ga0.y), LDP(ga0.z, ga0.w), LDP(ga1.x, ga1.y), LDP(ga1.z, ga1.w)};
  __syncthreads();
  __builtin_amdgcn_s_setprio(0);
  STAMP(1, 3);
  const int i0 = 15 - col + 8 * g;
  const char* ka = sm + SM_KI + ((i0 & 1) * NR_LEN + (i0 & ~1)) * 2;
  const int n0 = 3 * wave;
  f4 a0 = Z, a1 = Z, a2 = Z;
  const unsigned fa = (unsigned)(SM_U + (256 + 16 * col + 8 * g + 256 * n0 - 528) * 2);
  h8 A[17];
#pragma unroll
  for (int k = 0; k < 17; ++k) A[k] = ld_4dw(ka + k * 64, ka + k * 64 + 8);
  __builtin_amdgcn_sched_barrier(0);
  if (wave == 0) near_band<9, 33, true>(A, fa, a0, a1, a2);
  else if (wave == 1) near_band<0, 33, true>(A, fa, a0, a1, a2);
  else near_band<0, 25, false>(A, fa, a0, a1, a2);
  asm volatile("s_nop 7\n\ts_nop 7" : "+v"(a0), "+v"(a1), "+v"(a2));
  __syncthreads();
  {
    const char* pp = sm + SM_P + lane * 16 + (n0 - 2) * 1024;
    if (wave != 0) { a0 += *(const f4*)(pp); a1 += *(const f4*)(pp + 1024); }
    conv_epi(a0, unsc, yt + YTS * n0);
    conv_epi(a1, unsc, yt + YTS * (n0 + 1));
    if (wave != 2) { a2 += *(const f4*)(pp + 2048); conv_epi(a2, unsc, yt + YTS * (n0 + 2)); }
  }
  STAMP(1, 4);
  STAMPD(1, 5);
}

__device__ __forceinline__ unsigned ord16(unsigned b) { return (b & 0x8000u) ? (~b & 0xffffu) : (b | 0x8000u); }
__device__ __forceinline__ void fma8(float* acc, u4 w, float xv) {
  acc[0] = __builtin_fmaf(bflo(w.x), xv, acc[0]); acc[1] = __builtin_fmaf(bfhi(w.x), xv, acc[1]);
  acc[2] = __builtin_fmaf(bflo(w.y), xv, acc[2]); acc[3] = __builtin_fmaf(bfhi(w.y), xv, acc[3]);
  acc[4] = __builtin_fmaf(bflo(w.z), xv, acc[4]); acc[5] = __builtin_fmaf(bfhi(w.z), xv, acc[5]);
  acc[6] = __builtin_fmaf(bflo(w.w), xv, acc[6]); acc[7] = __builtin_fmaf(bfhi(w.w), xv, acc[7]);
}
#define RLX_AGENT __ATOMIC_RELAXED, __HIP_MEMORY_SCOPE_AGENT
constexpr unsigned SPIN_CAP = 1u << 22;

__device__ __forceinline__ u64 sweep_keys(char* ws, int lane, int rep, u64* lk) {
  GAS u64* kp = (GAS u64*)(ws + WS_KEYS) + rep * 256 + lane;
  u64 b;
  for (unsigned spins = 0;; ++spins) {
    u64 k0 = __hip_atomic_load(kp, RLX_AGENT), k1 = __hip_atomic_load(kp + 64, RLX_AGENT);
    u64 k2 = __hip_atomic_load(kp + 128, RLX_AGENT), k3 = __hip_atomic_load(kp + 192, RLX_AGENT);
    bool ok = (long long)(k0 & k1 & k2 & k3) < 0;
    b = k0 > k1 ? k0 : k1; u64 c = k2 > k3 ? k2 : k3; b = b > c ? b : c;
    if (__all(ok) || spins > SPIN_CAP) {
      if (lk) { lk[lane] = k0; lk[lane + 64] = k1; lk[lane + 128] = k2; lk[lane + 192] = k3; }
      break;
    }
    __builtin_amdgcn_s_sleep(1);
  }
  unsigned hk = (unsigned)(b >> 32), m = hk;
  m = max(m, (unsigned)__builtin_amdgcn_update_dpp(0, (int)m, 0x121, 0xf, 0xf, true));
  m = max(m, (unsigned)__builtin_amdgcn_update_dpp(0, (int)m, 0x122, 0xf, 0xf, true));
  m = max(m, (unsigned)__builtin_amdgcn_update_dpp(0, (int)m, 0x124, 0xf, 0xf, true));
  m = max(m, (unsigned)__builtin_amdgcn_update_dpp(0, (int)m, 0x128, 0xf, 0xf, true));
  unsigned m0 = (unsigned)__builtin_amdgcn_readlane((int)m, 0), m1 = (unsigned)__builtin_amdgcn_readlane((int)m, 16);
  unsigned m2 = (unsigned)__builtin_amdgcn_readlane((int)m, 32), m3 = (unsigned)__builtin_amdgcn_readlane((int)m, 48);
  m0 = max(max(m0, m1), max(m2, m3));
  int src = __builtin_ctzll(__ballot(hk == m0));
  unsigned lo = (unsigned)__builtin_amdgcn_readlane((int)(unsigned)b, src);
  return ((u64)m0 << 32) | lo;
}
__device__ __forceinline__ float par_sum(float v) {
  v = dpp_add<0x122>(v); v = dpp_add<0x124>(v); v = dpp_add<0x128>(v);
  return v;
}
__device__ __forceinline__ float xs_of(us xv, us yv, float wm, float wrs, float& hh) {
  hh = rbf(bf1(xv) + bf1(yv));
  return rbf(rbf(hh - wm) * wrs);
}

extern "C" __global__ __launch_bounds__(384, 2) void k3_tail(const us* __restrict__ x, const us* __restrict__ wr,
                                                  const us* __restrict__ wg, const us* __restrict__ wu,
                                                  const us* __restrict__ wd, us* __restrict__ out,
                                                  char* __restrict__ ws) {
  __shared__ __attribute__((aligned(16))) float red[5 * 32 + 16 + 8];
  __shared__ __attribute__((aligned(16))) u64 lkeys[1280];
  unsigned* redW = (unsigned*)(red + 176);
  const int tid = threadIdx.x, lane = tid & 63, wave = __builtin_amdgcn_readfirstlane(tid >> 6);
  const int bid = blockIdx.x;
  STAMP(2, 0);
  const us* ytb = (const us*)(ws + WS_YT);
  if (bid >= 256) {
    const bool f1 = bid < 352;
    const int j = f1 ? bid - 256 : bid - 352;
    const int cb = f1 ? (j & 7) * 12 + (j >> 3) : (j & 7) * 5 + (j >> 3);
    if (wave == 5) {
      __builtin_amdgcn_s_sleep(40);
      const u64 wn = sweep_keys(ws, lane, j & 7, f1 ? lkeys : nullptr);
      const unsigned wkey = (unsigned)(wn >> 32), wms = (unsigned)wn;
      const int wrow = 2047 - (int)(wkey & 2047u);
      const int c = lane & 15;
      if (f1) {
        if (lane == 0) { redW[2] = wkey; redW[3] = wms; }
        STAMPT(2, 3, 320);
        __syncthreads();
        __syncthreads();
        const float* rp = red + (c >> 3) * 16 + (c & 7);
        float gs = (rp[0] + rp[32]) + (rp[64] + rp[96]) + rp[128];
        float us_ = (rp[8] + rp[40]) + (rp[72] + rp[104]) + rp[136];
        float gb = rbf(gs);
        float sl = rbf(gb * __builtin_amdgcn_rcpf(1.f + __expf(-gb)));
        unsigned a = (unsigned)f2bf(sl * rbf(us_)) | 0x10000u;
        __hip_atomic_store((GAS unsigned*)(ws + WS_ACT) + (lane >> 4) * F_ + cb * 16 + c, a, RLX_AGENT);
        STAMPT(2, 4, 320);
      } else {
        const int col = cb * 16 + c;
        float fh;
        float fx2 = xs_of(x[(size_t)wrow * D_ + col], ytb[((size_t)(wrow >> 3) * D_ + col) * 8 + (wrow & 7)],
                          bflo(wms), __builtin_amdgcn_rcpf(bfhi(wms)), fh);
        unsigned ob = (wkey >> 11) & 0xffffu;
        unsigned rb = (ob & 0x8000u) ? (ob & 0x7fffu) : (~ob & 0xffffu);
        float sg = rbf(__builtin_amdgcn_rcpf(1.f + __expf(-bf1((us)rb))));
        __syncthreads();
        const float* rp = red + c;
        float ys = (rp[0] + rp[16]) + (rp[32] + rp[48]) + rp[64];
        float y = rbf(rbf(ys) * sg);
        if (lane < 16) out[(size_t)wrow * D_ + col] = f2bf(fh + rbf(fx2 + y));
        STAMPT(2, 5, 320);
      }
      return;
    }
    const int half = tid & 1, rb = tid >> 1;
    __builtin_amdgcn_s_sleep(16);
    if (f1) {
      const size_t o0 = (size_t)rb * F_ + cb * 16 + half * 8;
      u4 wq[8];
#pragma unroll
      for (int k = 0; k < 4; ++k) { wq[k] = *(const u4*)(wg + o0 + (size_t)k * 160 * F_); wq[4 + k] = *(const u4*)(wu + o0 + (size_t)k * 160 * F_); }
      f2 wf[32];
#pragma unroll
      for (int k = 0; k < 8; ++k) { wf[4 * k] = unpk(wq[k].x); wf[4 * k + 1] = unpk(wq[k].y); wf[4 * k + 2] = unpk(wq[k].z); wf[4 * k + 3] = unpk(wq[k].w); }
#pragma unroll
      for (int k = 0; k < 32; ++k) asm volatile("" : "+v"(wf[k]));
      __syncthreads();
      const unsigned wkey = redW[2], wms = redW[3];
      const int wrow = 2047 - (int)(wkey & 2047u);
      const float wm = bflo(wms), wrs = __builtin_amdgcn_rcpf(bfhi(wms));
      const us* xw = x + (size_t)wrow * D_ + rb;
      const us* yw = ytb + ((size_t)(wrow >> 3) * D_ + rb) * 8 + (wrow & 7);
      us xv[4], yv[4];
#pragma unroll
      for (int k = 0; k < 4; ++k) { xv[k] = xw[160 * k]; yv[k] = yw[160 * 8 * k]; }
      f2 acc[8];
#pragma unroll
      for (int i = 0; i < 8; ++i) acc[i] = f2{0.f, 0.f};
#pragma unroll
      for (int k = 0; k < 4; ++k) {
        float hh; float xs = xs_of(xv[k], yv[k], wm, wrs, hh);
        f2 x2v = {xs, xs};
#pragma unroll
        for (int i = 0; i < 4; ++i) {
          acc[i] = __builtin_elementwise_fma(wf[4 * k + i], x2v, acc[i]);
          acc[4 + i] = __builtin_elementwise_fma(wf[16 + 4 * k + i], x2v, acc[4 + i]);
        }
      }
      float w[8], o[4];
#pragma unroll
      for (int i = 0; i < 4; ++i) { w[2 * i] = swap32_add(acc[i].x, acc[4 + i].x); w[2 * i + 1] = swap32_add(acc[i].y, acc[4 + i].y); }
#pragma unroll
      for (int i = 0; i < 4; ++i) o[i] = swap16_add(w[i], w[i + 4]);
      par_sum4(o);
      const int rr = lane >> 4;
      if ((lane & 15) < 2) *(f4*)(red + wave * 32 + half * 16 + 4 * (rr & 1) + 8 * (rr >> 1)) = f4{o[0], o[1], o[2], o[3]};
      __syncthreads();
#pragma unroll
      for (int q = 0; q < 3; ++q) {
        const int cbk = j + 96 * q;
        if (cbk < 256) {
          const u64 gk = lkeys[cbk];
          const unsigned ck = (unsigned)(gk >> 32), cms = (unsigned)gk;
          if (ck != wkey) {
            const int crow = 2047 - (int)(ck & 2047u);
            const float crs = __builtin_amdgcn_rcpf(bfhi(cms)), cb_ = -bflo(cms) * crs;
            unsigned xr = ((const unsigned*)(x + (size_t)crow * D_))[tid];
            const us* yc = ytb + ((size_t)(crow >> 3) * D_ + 2 * tid) * 8 + (crow & 7);
            f2 hs = unpk(xr) + f2{bf1(yc[0]), bf1(yc[8])};
            f2 hh = unpk(pk_bf(hs.x, hs.y));
            f2 oo = hh + __builtin_elementwise_fma(hh, f2{crs, crs}, f2{cb_, cb_});
            ((unsigned*)(out + (size_t)crow * D_))[tid] = pk_bf(oo.x, oo.y);
          }
        }
      }
      STAMP(2, 6);
    } else {
      const us* wp = wd + cb * 16 + half * 8;
      u4 wq[10];
#pragma unroll
      for (int k = 0; k < 10; ++k) { int r = rb + 160 * k; r = r < F_ ? r : F_ - 1; wq[k] = *(const u4*)(wp + (size_t)r * D_); }
      f2 wf[40];
#pragma unroll
      for (int k = 0; k < 10; ++k) { wf[4 * k] = unpk(wq[k].x); wf[4 * k + 1] = unpk(wq[k].y); wf[4 * k + 2] = unpk(wq[k].z); wf[4 * k + 3] = unpk(wq[k].w); }
#pragma unroll
      for (int k = 0; k < 40; ++k) asm volatile("" : "+v"(wf[k]));
      __builtin_amdgcn_s_sleep(30);
      GAS unsigned* ap = (GAS unsigned*)(ws + WS_ACT) + (j & 3) * F_ + rb;
      const int last = rb < 96 ? 1440 : 0;
      unsigned a[10];
      for (unsigned spins = 0;; ++spins) {
#pragma unroll
        for (int k = 0; k < 9; ++k) a[k] = __hip_atomic_load(ap + 160 * k, RLX_AGENT);
        a[9] = __hip_atomic_load(ap + last, RLX_AGENT);
        unsigned all = a[0] & a[1] & a[2] & a[3] & a[4] & a[5] & a[6] & a[7] & a[8] & a[9];
        if (__all(all >> 16) || spins > SPIN_CAP) break;
        __builtin_amdgcn_s_sleep(1);
      }
      if (rb >= 96) a[9] = 0;
      f2 acc[4] = {f2{0.f, 0.f}, f2{0.f, 0.f}, f2{0.f, 0.f}, f2{0.f, 0.f}};
#pragma unroll
      for (int k = 0; k < 10; ++k) {
        float av = bflo(a[k]); f2 a2 = {av, av};
#pragma unroll
        for (int i = 0; i < 4; ++i) acc[i] = __builtin_elementwise_fma(wf[4 * k + i], a2, acc[i]);
      }
      float w0 = swap32_add(acc[0].x, acc[2].x), w1 = swap32_add(acc[0].y, acc[2].y);
      float w2 = swap32_add(acc[1].x, acc[3].x), w3 = swap32_add(acc[1].y, acc[3].y);
      float o0 = par_sum(swap16_add(w0, w2)), o1 = par_sum(swap16_add(w1, w3));
      const int rr = lane >> 4;
      if ((lane & 15) < 2) *(f2*)(red + wave * 16 + half * 8 + 2 * (rr & 1) + 4 * (rr >> 1)) = f2{o0, o1};
      __syncthreads();
    }
    return;
  }
  float* redA = red;
  float* abv = red + 160;
  const int tb = (bid & 7) * 32 + (bid >> 3);
  const int t0 = tb * 8;
  const unsigned* xb = (const unsigned*)(x + (size_t)t0 * D_);
  if (wave == 5) {
    const int r = lane & 7;
    const float piv = bf1(x[(size_t)(t0 + r) * D_]);
    const float W = *(const float*)(ws + WS_WSUM);
    __syncthreads();
    float s1 = (redA[r] + redA[24 + r]) + (redA[48 + r] + redA[72 + r]) + redA[96 + r];
    float s2 = (redA[8 + r] + redA[32 + r]) + (redA[56 + r] + redA[80 + r]) + redA[104 + r];
    float s3 = (redA[16 + r] + redA[40 + r]) + (redA[64 + r] + redA[88 + r]) + redA[112 + r];
    float mb, sb;
    ln_from_sums(s1, s2, piv, mb, sb);
    float rs = __builtin_amdgcn_rcpf(sb);
    float rv = rs * __builtin_fmaf(piv - mb, W, s3);
    unsigned key = 0x80000000u | (ord16(f2bf(rv)) << 11) | (unsigned)(2047 - (t0 + r));
    unsigned best = key;
    best = max(best, (unsigned)__builtin_amdgcn_update_dpp(0, (int)best, 0x111, 0xf, 0xf, true));
    best = max(best, (unsigned)__builtin_amdgcn_update_dpp(0, (int)best, 0x112, 0xf, 0xf, true));
    best = max(best, (unsigned)__builtin_amdgcn_update_dpp(0, (int)best, 0x114, 0xf, 0xf, true));
    best = (unsigned)__builtin_amdgcn_readlane((int)best, 7);
    if (key == best) {
      u64 gr = ((u64)best << 32) | (u64)((__float_as_uint(mb) >> 16) | (__float_as_uint(sb) & 0xffff0000u));
      __hip_atomic_store((GAS u64*)(ws + WS_KEYS) + (lane >> 3) * 256 + bid, gr, RLX_AGENT);
      redW[0] = (unsigned)r;
    }
    if (lane < 8) *(f2*)(abv + 2 * r) = f2{rs, -mb * rs};
    STAMPT(2, 2, 320);
    __syncthreads();
    return;
  }
  unsigned raw[8];
#pragma unroll
  for (int r = 0; r < 8; ++r) raw[r] = xb[r * (D_ / 2) + tid];
  const u4 y0 = *(const u4*)(ytb + ((size_t)tb * D_ + 2 * tid) * 8);
  const u4 y1 = *(const u4*)(ytb + ((size_t)tb * D_ + 2 * tid) * 8 + 8);
  const f2 wv = unpk(((const unsigned*)wr)[tid]);
  f2 h[8];
  {
    float v[24];
#pragma unroll
    for (int r = 0; r < 8; ++r) {
      const float piv = bflo(xb[r * (D_ / 2)]);
      unsigned ya = y0[r >> 1], yb = y1[r >> 1];
      f2 yy = (r & 1) ? f2{bfhi(ya), bfhi(yb)} : f2{bflo(ya), bflo(yb)};
      f2 hs = unpk(raw[r]) + yy;
      h[r] = unpk(pk_bf(hs.x, hs.y));
      f2 dd = h[r] - piv;
      v[r] = dd.x + dd.y;
      v[r + 8] = __builtin_fmaf(dd.x, dd.x, dd.y * dd.y);
      v[r + 16] = __builtin_fmaf(dd.x, wv.x, dd.y * wv.y);
    }
    float w[12], o[6];
#pragma unroll
    for (int i = 0; i < 12; ++i) w[i] = swap32_add(v[i], v[i + 12]);
#pragma unroll
    for (int i = 0; i < 6; ++i) o[i] = swap16_add(w[i], w[i + 6]);
    row_sum6(o);
    if ((lane & 15) == 0) {
      float* dst = redA + wave * 24 + (lane >> 4) * 6;
      *(f2*)dst = f2{o[0], o[1]}; *(f2*)(dst + 2) = f2{o[2], o[3]}; *(f2*)(dst + 4) = f2{o[4], o[5]};
    }
  }
  __syncthreads();
  STAMP(2, 1);
  __syncthreads();
  const int brow = (int)redW[0];
  unsigned* stg = (unsigned*)lkeys + wave * 512;
#pragma unroll
  for (int r = 0; r < 8; r += 2) {
    f4 c = *(const f4*)(abv + 2 * r);
    f2 o0 = h[r] + __builtin_elementwise_fma(h[r], f2{c.x, c.x}, f2{c.y, c.y});
    f2 o1 = h[r + 1] + __builtin_elementwise_fma(h[r + 1], f2{c.z, c.z}, f2{c.w, c.w});
    stg[r * 64 + lane] = pk_bf(o0.x, o0.y);
    stg[(r + 1) * 64 + lane] = pk_bf(o1.x, o1.y);
  }
  {
    const int rr = lane >> 4, ch = lane & 15;
    u4 q0 = *(const u4*)(stg + rr * 64 + ch * 4), q1 = *(const u4*)(stg + (rr + 4) * 64 + ch * 4);
    us* o0p = out + (size_t)(t0 + rr) * D_ + wave * 128 + ch * 8;
    if (rr != brow) asm volatile("global_store_dwordx4 %0, %1, off sc1\n\ts_nop 1" :: "v"(o0p), "v"(q0) : "memory");
    if (rr + 4 != brow) asm volatile("global_store_dwordx4 %0, %1, off sc1\n\ts_nop 1" :: "v"(o0p + 4 * D_), "v"(q1) : "memory");
  }
}

extern "C" __global__ void k9_end() {}

extern "C" void kernel_launch(void* const* d_in, const int* in_sizes, int n_in, void* d_out, int out_size,
                              void* d_ws, size_t ws_size, hipStream_t stream) {
  const us* x = (const us*)d_in[0];
  const us* Are = (const us*)d_in[1];
  const us* Aim = (const us*)d_in[2];
  const us* Bre = (const us*)d_in[3];
  const us* Bim = (const us*)d_in[4];
  const us* Cre = (const us*)d_in[5];
  const us* Cim = (const us*)d_in[6];
  const us* logdt = (const us*)d_in[7];
  const us* Dskip = (const us*)d_in[8];
  const us* wr = (const us*)d_in[10];
  const us* wg = (const us*)d_in[11];
  const us* wu = (const us*)d_in[12];
  const us* wd = (const us*)d_in[13];
  us* out = (us*)d_out;
  char* ws = (char*)d_ws;
  k1_ln<<<256, 384, 0, stream>>>(x, Are, Aim, Bre, Bim, Cre, Cim, wr, wg, wu, wd, out, ws);
  k2_conv<<<D_, 256, K2_LDS, stream>>>(Are, Aim, Bre, Bim, Cre, Cim, logdt, Dskip, ws);
  k3_tail<<<392, 384, 0, stream>>>(x, wr, wg, wu, wd, out, ws);
}
```

```cpp
#include <hip/hip_runtime.h>
#include <hip/hip_bf16.h>
#include <stdint.h>

typedef _Float16 h8 __attribute__((ext_vector_type(8)));
typedef _Float16 h2 __attribute__((ext_vector_type(2)));
typedef float f4 __attribute__((ext_vector_type(4)));
typedef float f2 __attribute__((ext_vector_type(2)));
typedef unsigned u4 __attribute__((ext_vector_type(4)));
typedef unsigned u2 __attribute__((ext_vector_type(2)));
typedef unsigned short us;
typedef unsigned long long u64;
#define GAS __attribute__((address_space(1)))
#define LAS __attribute__((address_space(3)))

constexpr int L_ = 2048, D_ = 640, N_ = 192, F_ = 1536;
constexpr int LP = 2048 + 64;
#define STAMPD(kid, slot) do {} while (0)
#define STAMP(kid, slot) do {} while (0)
#define STAMPT(kid, slot, thr) do {} while (0)

constexpr int NR_LEN  = 608;
constexpr int F2_OFF  = 2 * NR_LEN;
constexpr int F3_OFF  = F2_OFF + 8 * 64;
constexpr int KIMG_ELEMS = F3_OFF + 4 * 80;
constexpr int KCHUNKS = KIMG_ELEMS / 8;

constexpr size_t WS_FLAGS = 0;
constexpr size_t WS_KEYS  = WS_FLAGS;
constexpr size_t WS_ACT   = WS_FLAGS + 16384;
constexpr size_t WS_WSUM  = 40960;
constexpr size_t WS_UT    = 40960 + 256;
constexpr size_t WS_YT    = WS_UT + (size_t)D_ * LP * 2;
constexpr size_t WS_TOTAL = WS_YT + (size_t)D_ * L_ * 2;

constexpr int TAB_PITCH = 200;
constexpr int SM_KC = 57 * TAB_PITCH * 4;
constexpr int SM_U  = SM_KC + 1552;
constexpr int K2_LDS = SM_U + 4608;
constexpr int SM_KI = 0;
constexpr int SM_P  = 4096;

constexpr int kc_of_v(int v) {
  if (v < 256)  { int h = v >> 7, r = v & 127; return ((h * 8 + (r & 7)) * 16) + (r >> 3); }
  if (v < 512)  { int w = v - 256, h = w >> 7, r = w & 127; return ((16 + h * 4 + ((r & 7) >> 1)) * 16) + (r >> 3); }
  if (v < 1024) { int w = v - 512, h = w >> 7, r = w & 127; return ((24 + h * 2 + ((r & 7) >> 2)) * 16) + (r >> 3); }
  if (v < 2048) { int w = v - 1024, h = w >> 7, r = w & 127; return ((32 + h) * 16) + (r >> 3); }
  return 40 * 16;
}
constexpr int bf16_round_int(int j) {
  if (j <= 256) return j;
  int step = j < 512 ? 2 : (j < 1024 ? 4 : 8);
  int lo = (j / step) * step, rem = j - lo;
  if (rem * 2 < step) return lo;
  if (rem * 2 > step) return lo + step;
  return ((lo / step) & 1) ? lo + step : lo;
}
constexpr int ZIDX = 768;

struct GatherTab { unsigned v[KIMG_ELEMS]; };
constexpr GatherTab make_gather() {
  GatherTab t{};
  for (int i = 0; i < KIMG_ELEMS; ++i) t.v[i] = SM_KC + ZIDX * 2;
  for (int e = 0; e < 2; ++e)
    for (int i = 0; i < NR_LEN; ++i) {
      int j = 543 - (i + e);
      if (j >= 0 && j < 515) t.v[e * NR_LEN + i] = (unsigned)(SM_KC + 2 * kc_of_v(bf16_round_int(j)));
    }
  for (int pi = 0; pi < 4; ++pi)
    for (int e = 0; e < 2; ++e)
      for (int i = 0; i < 64; ++i) {
        int p = 79 - (i + e);
        if (p >= 32 && p < 64) {
          int v = 16 * p + 8 * (pi >> 1) + ((pi & 1) ? 8 : 4);
          t.v[F2_OFF + (pi * 2 + e) * 64 + i] = (unsigned)(SM_KC + 2 * kc_of_v(v));
        }
      }
  for (int pi = 0; pi < 2; ++pi)
    for (int e = 0; e < 2; ++e)
      for (int i = 0; i < 80; ++i) {
        int qq = 127 - (i + e);
        if (qq >= 64 && qq < 128) {
          int v = 16 * qq + (pi ? 16 : 8);
          t.v[F3_OFF + (pi * 2 + e) * 80 + i] = (unsigned)(SM_KC + 2 * kc_of_v(v));
        }
      }
  return t;
}
__device__ const GatherTab g_gather = make_gather();

struct BoxTab { us v[7 * 64 * 8]; };
constexpr BoxTab make_box() {
  BoxTab t{};
  const int nn[6] = {3, 5, 3, 5, 7, 9};
  const int sh[6] = {3, 6, 11, 14, 5, 12};
  const int w0[6] = {-16, -16, -24, -24, -16, -24};
  for (int m = 0; m < 6; ++m)
    for (int lane = 0; lane < 64; ++lane)
      for (int j = 0; j < 8; ++j) {
        int b = lane & 15, k = 8 * (lane >> 4) + j;
        int idx = w0[m] + k;
        bool on = idx <= b - sh[m] && idx >= b - sh[m] - (nn[m] - 1);
        t.v[(m * 64 + lane) * 8 + j] = on ? 0x3C00 : 0;
      }
  for (int lane = 0; lane < 64; ++lane)
    for (int j = 0; j < 8; ++j) {
      int b = lane & 15, k = 8 * (lane >> 4) + j;
      t.v[(6 * 64 + lane) * 8 + j] = (k - b == 4 || k - b == 5) ? 0xFFFF : 0;
    }
  return t;
}
__device__ const BoxTab g_box = make_box();

__device__ __forceinline__ float bflo(unsigned w) { return __uint_as_float(w << 16); }
__device__ __forceinline__ float bfhi(unsigned w) { return __uint_as_float(w & 0xffff0000u); }
__device__ __forceinline__ float bf1(us v) { return __uint_as_float((unsigned)v << 16); }
__device__ __forceinline__ us f2bf(float f) { __bf16 b = (__bf16)f; return __builtin_bit_cast(us, b); }
__device__ __forceinline__ float rbf(float f) { return bf1(f2bf(f)); }
typedef __bf16 b2 __attribute__((ext_vector_type(2)));
__device__ __forceinline__ unsigned pk_bf(float a, float b) {
  b2 v; v.x = (__bf16)a; v.y = (__bf16)b; return __builtin_bit_cast(unsigned, v);
}
__device__ __forceinline__ unsigned pk_h(float a, float b) {
  h2 v; v.x = (_Float16)a; v.y = (_Float16)b; return __builtin_bit_cast(unsigned, v);
}
template <int CTRL> __device__ __forceinline__ float dpp_add(float v) {
  int t = __builtin_amdgcn_update_dpp(0, __float_as_int(v), CTRL, 0xf, 0xf, true);
  return v + __int_as_float(t);
}
__device__ __forceinline__ float row_sum(float v) {
  v = dpp_add<0x121>(v); v = dpp_add<0x122>(v); v = dpp_add<0x124>(v); v = dpp_add<0x128>(v);
  return v;
}
__device__ __forceinline__ float swap32_add(float a, float b) {
  auto r = __builtin_amdgcn_permlane32_swap(__float_as_uint(a), __float_as_uint(b), false, false);
  return __uint_as_float(r[0]) + __uint_as_float(r[1]);
}
__device__ __forceinline__ float swap16_add(float a, float b) {
  auto r = __builtin_amdgcn_permlane16_swap(__float_as_uint(a), __float_as_uint(b), false, false);
  return __uint_as_float(r[0]) + __uint_as_float(r[1]);
}
__device__ __forceinline__ void wave_reduce16(const float (&v)[16], float (&out)[4]) {
  float w[8];
#pragma unroll
  for (int i = 0; i < 8; ++i) w[i] = swap32_add(v[i], v[i + 8]);
#pragma unroll
  for (int i = 0; i < 4; ++i) out[i] = row_sum(swap16_add(w[i], w[i + 4]));
}
__device__ __forceinline__ void wave_reduce8(const float (&v)[8], float (&out)[2]) {
  float w[4];
#pragma unroll
  for (int i = 0; i < 4; ++i) w[i] = swap32_add(v[i], v[i + 4]);
#pragma unroll
  for (int i = 0; i < 2; ++i) out[i] = row_sum(swap16_add(w[i], w[i + 2]));
}
__device__ __forceinline__ float wave_reduce4(const float (&v)[4]) {
  float w0 = swap32_add(v[0], v[2]), w1 = swap32_add(v[1], v[3]);
  return row_sum(swap16_add(w0, w1));
}
#define DPPA(i, st) "v_add_f32_dpp %" #i ", %" #i ", %" #i " row_ror:" #st " row_mask:0xf bank_mask:0xf\n\t"
__device__ __forceinline__ void row_sum8(float (&o)[8]) {
  asm volatile("s_nop 1\n\t"
    DPPA(0,1) DPPA(1,1) DPPA(2,1) DPPA(3,1) DPPA(4,1) DPPA(5,1) DPPA(6,1) DPPA(7,1)
    DPPA(0,2) DPPA(1,2) DPPA(2,2) DPPA(3,2) DPPA(4,2) DPPA(5,2) DPPA(6,2) DPPA(7,2)
    DPPA(0,4) DPPA(1,4) DPPA(2,4) DPPA(3,4) DPPA(4,4) DPPA(5,4) DPPA(6,4) DPPA(7,4)
    DPPA(0,8) DPPA(1,8) DPPA(2,8) DPPA(3,8) DPPA(4,8) DPPA(5,8) DPPA(6,8) DPPA(7,8)
    : "+v"(o[0]), "+v"(o[1]), "+v"(o[2]), "+v"(o[3]), "+v"(o[4]), "+v"(o[5]), "+v"(o[6]), "+v"(o[7]));
}
__device__ __forceinline__ void row_sum6(float (&o)[6]) {
  asm volatile("s_nop 1\n\t"
    DPPA(0,1) DPPA(1,1) DPPA(2,1) DPPA(3,1) DPPA(4,1) DPPA(5,1)
    DPPA(0,2) DPPA(1,2) DPPA(2,2) DPPA(3,2) DPPA(4,2) DPPA(5,2)
    DPPA(0,4) DPPA(1,4) DPPA(2,4) DPPA(3,4) DPPA(4,4) DPPA(5,4)
    DPPA(0,8) DPPA(1,8) DPPA(2,8) DPPA(3,8) DPPA(4,8) DPPA(5,8)
    : "+v"(o[0]), "+v"(o[1]), "+v"(o[2]), "+v"(o[3]), "+v"(o[4]), "+v"(o[5]));
}
__device__ __forceinline__ void row_sum4(float (&o)[4]) {
  asm volatile("s_nop 1\n\t"
    DPPA(0,1) DPPA(1,1) DPPA(2,1) DPPA(3,1)
    DPPA(0,2) DPPA(1,2) DPPA(2,2) DPPA(3,2)
    DPPA(0,4) DPPA(1,4) DPPA(2,4) DPPA(3,4)
    DPPA(0,8) DPPA(1,8) DPPA(2,8) DPPA(3,8)
    : "+v"(o[0]), "+v"(o[1]), "+v"(o[2]), "+v"(o[3]));
}
__device__ __forceinline__ void par_sum4(float (&o)[4]) {
  asm volatile("s_nop 1\n\t"
    DPPA(0,2) DPPA(1,2) DPPA(2,2) DPPA(3,2)
    DPPA(0,4) DPPA(1,4) DPPA(2,4) DPPA(3,4)
    DPPA(0,8) DPPA(1,8) DPPA(2,8) DPPA(3,8)
    : "+v"(o[0]), "+v"(o[1]), "+v"(o[2]), "+v"(o[3]));
}
__device__ __forceinline__ float rdlane(float v, int l) {
  return __int_as_float(__builtin_amdgcn_readlane(__float_as_int(v), l));
}

__device__ __forceinline__ f2 cmul(f2 a, f2 b) {
  f2 c;
  asm("v_pk_mul_f32 %0, %1, %2 op_sel:[0,0] op_sel_hi:[1,0]\n\tv_pk_fma_f32 %0, %1, %2, %0 op_sel:[1,1,0] op_sel_hi:[0,1,1] neg_lo:[1,0,0]"
      : "=&v"(c) : "v"(a), "v"(b));
  return c;
}
__device__ __forceinline__ f2 cexp_p(float lam, float rh, float rl, float p) {
  float mag = __builtin_amdgcn_exp2f(lam * p);
  float x = rh * p;
  float f = x - __builtin_rintf(x);
  f = __builtin_fmaf(rl, p, f);
  f2 w; w.x = mag * __builtin_amdgcn_cosf(f); w.y = mag * __builtin_amdgcn_sinf(f); return w;
}
#define MFMA16(a, b, c) __builtin_amdgcn_mfma_f32_16x16x32_f16((a), (b), (c), 0, 0, 0)
__device__ __forceinline__ h8 as_h8(u4 v) { return __builtin_bit_cast(h8, v); }

__device__ __forceinline__ void ln_from_sums(float s1, float s2, float piv, float& mb, float& sb) {
  float dm = s1 * (1.f / 640.f);
  float var = fmaxf(__builtin_fmaf(-dm, dm, s2 * (1.f / 640.f)), 0.f);
  mb = rbf(piv + dm);
  sb = rbf(__builtin_amdgcn_sqrtf(rbf(rbf(var) + 1e-5f)));
}
__device__ __forceinline__ f2 unpk(unsigned w) { return f2{bflo(w), bfhi(w)}; }

extern "C" __global__ __launch_bounds__(384, 2) void k1_ln(
    const us* __restrict__ x, const us* __restrict__ Are, const us* __restrict__ Aim,
    const us* __restrict__ Bre, const us* __restrict__ Bim, const us* __restrict__ Cre,
    const us* __restrict__ Cim, const us* __restrict__ wr,
    const us* __restrict__ wg, const us* __restrict__ wu, const us* __restrict__ wd, const us* __restrict__ out,
    char* __restrict__ ws) {
  __shared__ __attribute__((aligned(16))) float red[5 * 16 + 16 + 8];
  const int tid = threadIdx.x, lane = tid & 63, wave = __builtin_amdgcn_readfirstlane(tid >> 6);
  const int bid = blockIdx.x;
  STAMP(0, 0);
  if (wave == 5) {
    u64 pc; int o1, o2;
    asm volatile("s_getpc_b64 %0\n\ts_mov_b32 %1, k1_ln@rel32@lo+4\n\ts_mov_b32 %2, k9_end@rel32@lo+12" : "=s"(pc), "=s"(o1), "=s"(o2));
    const int xcd = bid & 7, jb = bid >> 3;
    const GAS char* cbase = (const GAS char*)(pc + (long long)o1);
    unsigned acc = 0;
    {
      const int id = jb * 64 + lane;
      const GAS char* p;
      if (id < 384) { int o = id * 64; p = cbase + (o < o2 - o1 ? o : 0); }
      else if (id < 384 + 1440) { int i = id - 384, ar = i / 240, q = i - ar * 240;
        const us* base = ar == 0 ? Are : ar == 1 ? Aim : ar == 2 ? Bre : ar == 3 ? Bim : ar == 4 ? Cre : Cim;
        p = (const GAS char*)base + xcd * 30720 + q * 128; }
      else if (id < 1824 + 64) p = (const GAS char*)g_gather.v + (id - 1824) * 128;
      else if (id < 1888 + 56) p = (const GAS char*)g_box.v + (id - 1888) * 128;
      else if (id < 1944 + 40) p = (const GAS char*)out + (size_t)(id - 1944) * 65536;
      else p = (const GAS char*)(ws + WS_YT) + (size_t)((id - 1984) % 40) * 65536;
      acc = *(const GAS unsigned*)p;
    }
    __builtin_amdgcn_s_barrier();
    __builtin_amdgcn_s_barrier();
    asm volatile("" :: "v"(acc));
    STAMPT(0, 7, 320);
    return;
  }
  const int t0 = ((bid & 7) * 32 + (bid >> 3)) * 8;
  const unsigned* xb = (const unsigned*)(x + (size_t)t0 * D_);
  unsigned raw[8];
#pragma unroll
  for (int r = 0; r < 8; ++r) raw[r] = xb[r * (D_ / 2) + tid];
  f2* ab = (f2*)(red + 80);
  unsigned* pv = (unsigned*)(red + 96);
  f2 a[8];
  {
    float v[16];
#pragma unroll
    for (int r = 0; r < 8; ++r) {
      const float piv = bflo(xb[r * (D_ / 2)]);
      a[r] = unpk(raw[r]);
      f2 dd = a[r] - piv;
      v[r] = dd.x + dd.y; v[r + 8] = __builtin_fmaf(dd.x, dd.x, dd.y * dd.y);
    }
    if (tid == 0) { *(u4*)(pv) = u4{raw[0], raw[1], raw[2], raw[3]}; *(u4*)(pv + 4) = u4{raw[4], raw[5], raw[6], raw[7]}; }
    float w[8], o[4];
#pragma unroll
    for (int i = 0; i < 8; ++i) w[i] = swap32_add(v[i], v[i + 8]);
#pragma unroll
    for (int i = 0; i < 4; ++i) o[i] = swap16_add(w[i], w[i + 4]);
    row_sum4(o);
    if ((lane & 15) == 0) *(f4*)(red + wave * 16 + (lane >> 4) * 4) = f4{o[0], o[1], o[2], o[3]};
  }
  __syncthreads();
  if (wave == 0) {
    const int r = lane & 7;
    float s1 = (red[r] + red[16 + r]) + (red[32 + r] + red[48 + r]) + red[64 + r];
    float s2 = (red[8 + r] + red[24 + r]) + (red[40 + r] + red[56 + r]) + red[72 + r];
    float mb, sb;
    ln_from_sums(s1, s2, bflo(pv[r]), mb, sb);
    float aa = 128.f * __builtin_amdgcn_rcpf(sb);
    if (lane < 8) ab[r] = f2{aa, -mb * aa};
  } else if (wave == 1 && bid == 0) {
    const unsigned* w2 = (const unsigned*)wr + lane;
    float sw = 0.f;
#pragma unroll
    for (int k = 0; k < 5; ++k) { unsigned q = w2[64 * k]; sw += bflo(q) + bfhi(q); }
    sw = row_sum(sw);
    sw = rdlane(sw, 0) + rdlane(sw, 16) + rdlane(sw, 32) + rdlane(sw, 48);
    if (lane == 0) *(float*)(ws + WS_WSUM) = sw;
  } else if (wave >= 1 && bid >= 1 && bid <= 10) {
    ((u4*)(ws + WS_FLAGS))[(bid - 1) * 256 + tid - 64] = u4{0, 0, 0, 0};
  }
  __syncthreads();
  unsigned p0[4], p1[4];
#pragma unroll
  for (int r = 0; r < 8; r += 2) {
    f4 c = *(const f4*)(ab + r);
    f2 u0 = __builtin_elementwise_fma(a[r], f2{c.x, c.x}, f2{c.y, c.y});
    f2 u1 = __builtin_elementwise_fma(a[r + 1], f2{c.z, c.z}, f2{c.w, c.w});
    p0[r >> 1] = pk_h(u0.x, u1.x); p1[r >> 1] = pk_h(u0.y, u1.y);
  }
  us* ut = (us*)(ws + WS_UT) + (size_t)(2 * tid) * LP + t0;
  *(u4*)(ut) = u4{p0[0], p0[1], p0[2], p0[3]};
  *(u4*)(ut + LP) = u4{p1[0], p1[1], p1[2], p1[3]};
  STAMP(0, 5);
  STAMPD(0, 6);
}

__device__ __forceinline__ h8 ld_h8(const char* p) { return as_h8(*(const u4*)p); }
__device__ __forceinline__ h8 ld_4dw(const char* p0, const char* p1) {
  const unsigned* a = (const unsigned*)p0; const unsigned* b = (const unsigned*)p1;
  return as_h8(u4{a[0], a[1], b[0], b[1]});
}
__device__ __forceinline__ h8 cvt_pair(f4 a, f4 b) {
  return as_h8(u4{pk_h(a[0], a[1]), pk_h(a[2], a[3]), pk_h(b[0], b[1]), pk_h(b[2], b[3])});
}
__device__ __forceinline__ void glds16(const void* g, void* l) {
  __builtin_amdgcn_global_load_lds((const GAS unsigned*)g, (LAS unsigned*)l, 16, 0, 0);
}
__device__ __forceinline__ void conv_epi(f4 acc, float sc, us* dst) {
  u2 o; o.x = pk_bf(acc[0] * sc, acc[1] * sc); o.y = pk_bf(acc[2] * sc, acc[3] * sc);
  *(u2*)dst = o;
}
#define LDU(a) ((unsigned)*(const LAS us*)(size_t)(a))
#define LDP(a, b) (LDU(a) | (LDU(b) << 16))

template <int OFF> __device__ __forceinline__ u4 lds_rd128(unsigned addr) {
  u4 v; asm volatile("ds_read_b128 %0, %1 offset:%2" : "=v"(v) : "v"(addr), "i"(OFF) : "memory"); return v;
}
template <int N> __device__ __forceinline__ void lgkm_wait() {
  asm volatile("s_waitcnt lgkmcnt(%0)" :: "i"(N) : "memory"); __builtin_amdgcn_sched_barrier(0);
}
template <int M, int M0, int M1, bool T3, int PF>
__device__ __forceinline__ void near_step(const h8 (&A)[17], u4 (&f)[PF], unsigned fa, f4& a0, f4& a1, f4& a2) {
  if constexpr (M < M1) {
    if constexpr (M >= M0 + PF) lgkm_wait<((M1 - 1 - M) < (PF - 1)) ? (M1 - 1 - M) : (PF - 1)>();
    const h8 ff = as_h8(f[(M - M0) % PF]);
    if constexpr (M <= 16) a0 = MFMA16(A[M], ff, a0);
    if constexpr (M >= 8 && M <= 24) a1 = MFMA16(A[M - 8], ff, a1);
    if constexpr (T3 && M >= 16) a2 = MFMA16(A[M - 16], ff, a2);
    __builtin_amdgcn_sched_barrier(0);
    if constexpr (M + PF < M1) f[(M - M0) % PF] = lds_rd128<(M + PF) * 64>(fa);
    near_step<M + 1, M0, M1, T3, PF>(A, f, fa, a0, a1, a2);
  }
}
template <int M, int M0, int PF> __device__ __forceinline__ void near_fill(u4 (&f)[PF], unsigned fa) {
  if constexpr (M < M0 + PF) { f[M - M0] = lds_rd128<M * 64>(fa); near_fill<M + 1, M0, PF>(f, fa); }
}
template <int M0, int M1, bool T3>
__device__ __forceinline__ void near_band(const h8 (&A)[17], unsigned fa, f4& a0, f4& a1, f4& a2) {
  constexpr int PF = 6;
  u4 f[PF];
  near_fill<M0, M0, PF>(f, fa);
  __builtin_amdgcn_s_waitcnt(0xC07F);
  __builtin_amdgcn_sched_barrier(0);
  near_step<M0, M0, M1, T3, PF>(A, f, fa, a0, a1, a2);
}

extern "C" __global__ __launch_bounds__(256, 2) void k2_conv(
    const us* __restrict__ Are, const us* __restrict__ Aim, const us* __restrict__ Bre, const us* __restrict__ Bim,
    const us* __restrict__ Cre, const us* __restrict__ Cim, const us* __restrict__ logdt,
    const us* __restrict__ Dskip, const us* __restrict__ wg, const us* __restrict__ wu, const us* __restrict__ wd,
    char* __restrict__ ws) {
  extern __shared__ __attribute__((aligned(16))) char sm[];
  const int tid = threadIdx.x, lane = tid & 63, wave = __builtin_amdgcn_readfirstlane(tid >> 6);
  const int d = (blockIdx.x & 7) * 80 + (blockIdx.x >> 3);
  const int col = lane & 15, g = lane >> 4;
  STAMP(1, 0);
  char* kc = sm + SM_KC;
  const char* ub = sm + SM_U + (256 + 16 * col + 8 * g) * 2;
  us* yt = (us*)(ws + WS_YT) + ((size_t)(2 * col + (g >> 1)) * D_ + d) * 8 + (g & 1) * 4;
  constexpr int YTS = 32 * D_ * 8;
  const f4 Z = {0.f, 0.f, 0.f, 0.f};
  const u4* gt = (const u4*)g_gather.v + 2 * tid;
#define PRIOV 2
  if (blockIdx.x >= 512) __builtin_amdgcn_s_setprio(3); else if (blockIdx.x >= 256) __builtin_amdgcn_s_setprio(2); else __builtin_amdgcn_s_setprio(1);
  if (wave == 3) {
    const char* gu = ws + WS_UT + (size_t)d * LP * 2;
#pragma unroll
    for (int k = 0; k < 4; ++k) glds16(gu + (k * 64 + lane) * 16, sm + SM_U + 512 + k * 1024);
    const u4* bt = (const u4*)g_box.v + lane;
    u4 bx[7];
#pragma unroll
    for (int m = 0; m < 7; ++m) bx[m] = bt[64 * m];
    if (lane < 32) *(u4*)(sm + SM_U + lane * 16) = u4{0, 0, 0, 0};
    __builtin_amdgcn_s_barrier();
    asm volatile("s_waitcnt vmcnt(0)" ::: "memory");
    h8 sa[6], sb[6], s3[4];
#pragma unroll
    for (int tau = 0; tau < 6; ++tau) {
      h8 w1 = ld_h8(ub + (256 * tau - 16) * 2), w2 = ld_h8(ub + (256 * tau - 24) * 2);
      sa[tau] = cvt_pair(MFMA16(w1, as_h8(bx[0]), Z), MFMA16(w1, as_h8(bx[1]), Z));
      sb[tau] = cvt_pair(MFMA16(w2, as_h8(bx[2]), Z), MFMA16(w2, as_h8(bx[3]), Z));
      if (tau < 4) s3[tau] = cvt_pair(MFMA16(w1, as_h8(bx[4]), Z), MFMA16(w2, as_h8(bx[5]), Z));
    }
    u4 ga0 = gt[0], ga1 = gt[1];
    unsigned pf0, pf1;
    {
      const int xcd = blockIdx.x & 7;
      int id = (blockIdx.x >> 3) * 64 + lane;
      int r3 = id / 3, q3 = id - r3 * 3;
      const us* pa = (r3 < 640 ? wg + (size_t)r3 * F_ : wu + (size_t)(r3 - 640) * F_) + xcd * 192 + q3 * 64;
      int i2 = id - 3840;
      const us* pb = wd + (size_t)(i2 >> 1) * D_ + xcd * 80 + (i2 & 1) * 78;
      pf0 = *(const unsigned*)(id < 3840 ? pa : pb);
      i2 = id + 1280; i2 = i2 < 3072 ? i2 : 3071;
      pf1 = *(const unsigned*)(wd + (size_t)(i2 >> 1) * D_ + xcd * 80 + (i2 & 1) * 78);
    }
    __builtin_amdgcn_s_barrier();
    __syncthreads();
    *(u4*)(sm + SM_KI + tid * 16) = u4{LDP(ga0.x, ga0.y), LDP(ga0.z, ga0.w), LDP(ga1.x, ga1.y), LDP(ga1.z, ga1.w)};
    const unsigned kv = LDU(SM_KC + 32 * 16 * 2) * 0x10001u;
    __syncthreads();
    __builtin_amdgcn_s_setprio(0);
    f4 acc[6];
    acc[0] = Z; acc[1] = Z;
    {
      h8 As = as_h8(u4{bx[6].x & kv, bx[6].y & kv, bx[6].z & kv, bx[6].w & kv});
#pragma unroll
      for (int i = 0; i < 4; ++i) acc[2 + i] = MFMA16(As, ld_h8(ub + (256 * (4 + i) - 1032) * 2), Z);
    }
    {
      const int ib = 79 - col + 4 * g;
      const char* kb = sm + SM_KI + (F2_OFF + (ib & 1) * 64 + (ib & ~1)) * 2;
#pragma unroll
      for (int dl = 2; dl <= 4; ++dl) {
        h8 tA = ld_4dw(kb - dl * 32, kb + 256 - dl * 32), tB = ld_4dw(kb + 512 - dl * 32, kb + 768 - dl * 32);
#pragma unroll
        for (int tau = 0; tau < 6; ++tau)
          if (tau + dl <= 7) { acc[tau + dl - 2] = MFMA16(sa[tau], tA, acc[tau + dl - 2]); acc[tau + dl - 2] = MFMA16(sb[tau], tB, acc[tau + dl - 2]); }
      }
    }
    {
      const int ib = 127 - col + 4 * g;
      const char* kb = sm + SM_KI + (F3_OFF + (ib & 1) * 80 + (ib & ~1)) * 2;
#pragma unroll
      for (int dl = 4; dl <= 7; ++dl) {
        h8 tt = ld_4dw(kb - dl * 32, kb + 320 - dl * 32);
#pragma unroll
        for (int tau = 0; tau < 4; ++tau)
          if (tau + dl <= 7) acc[tau + dl - 2] = MFMA16(s3[tau], tt, acc[tau + dl - 2]);
      }
    }
#pragma unroll
    for (int i = 0; i < 6; ++i) *(f4*)(sm + SM_P + i * 1024 + lane * 16) = acc[i];
    __syncthreads();
    asm volatile("" :: "v"(pf0), "v"(pf1));
    return;
  }
  const size_t pidx = (size_t)d * N_ + tid;
  us r_are = Are[pidx], r_aim = Aim[pidx], r_bre = Bre[pidx], r_bim = Bim[pidx], r_cre = Cre[pidx], r_cim = Cim[pidx];
  const unsigned ldw = ((const unsigned*)logdt)[d >> 1], dsw = ((const unsigned*)Dskip)[d >> 1];
  const float dt = rbf(__expf((d & 1) ? bfhi(ldw) : bflo(ldw)));
  const float dsk = (d & 1) ? bfhi(dsw) : bflo(dsw);
  asm volatile("s_mov_b32 m0, %0" :: "s"(wave * 256) : "memory");
  float ar = dt * bf1(r_are), ai = dt * bf1(r_aim);
  f2 dB = {dt * bf1(r_bre), dt * bf1(r_bim)};
  f2 Cc = {bf1(r_cre), bf1(r_cim)};
  f2 CB = cmul(Cc, dB);
  const float LOG2E = 1.4426950408889634f;
  const float I2PH = 0.15915494309189535f;
  const float I2PL = (float)(0.15915494309189533577 - (double)0.15915494309189535f);
  float lam = ar * LOG2E;
  float rh = ai * I2PH;
  float rl = __builtin_fmaf(ai, I2PL, __builtin_fmaf(ai, I2PH, -rh));
  {
    float gg = fmaxf(1.f, __builtin_amdgcn_exp2f(lam * 2048.f));
    float bnd = row_sum((fabsf(CB.x) + fabsf(CB.y)) * gg);
    if ((lane & 15) == 0) ((float*)(kc))[wave * 4 + (lane >> 4)] = bnd;
  }
  f2 w1 = cexp_p(lam, rh, rl, 1.f), w16 = cexp_p(lam, rh, rl, 16.f), w256 = cexp_p(lam, rh, rl, 256.f);
  f2 w2 = cmul(w1, w1), w32 = cmul(w16, w16), w512 = cmul(w256, w256);
  f2 w4 = cmul(w2, w2), w64 = cmul(w32, w32), w1024 = cmul(w512, w512);
  f2 w8 = cmul(w4, w4), w128 = cmul(w64, w64);
#define DSW(row, val) asm volatile("ds_write_addtid_b32 %0 offset:%1" :: "v"(val), "i"((row) * TAB_PITCH * 4) : "memory")
#define PUTE(row, c) do { f2 c_ = (c); DSW((row), pk_h(c_.x, -c_.y)); } while (0)
#define PUT(row, c) do { f2 c_ = (c); DSW(16 + (row), pk_h(c_.x, c_.y)); } while (0)
  {
    f2 e[8];
    e[1] = w8; e[2] = w16; e[3] = cmul(w8, w16); e[4] = w32;
    e[5] = cmul(w32, e[1]); e[6] = cmul(w32, e[2]); e[7] = cmul(w32, e[3]);
    DSW(0, 0x00003C00u);
#pragma unroll
    for (int l = 1; l < 8; ++l) PUTE(l, e[l]);
    PUTE(8, w64);
#pragma unroll
    for (int l = 1; l < 8; ++l) PUTE(8 + l, cmul(w64, e[l]));
  }
  __syncthreads();
  STAMP(1, 1);
  u4 ga0 = gt[0], ga1 = gt[1];
  float sc, unsc;
  {
    f4 q0 = *(const f4*)(kc), q1 = *(const f4*)(kc + 16), q2 = *(const f4*)(kc + 32);
    q0 += q1; q0 += q2;
    float sig = ((q0.x + q0.y) + (q0.z + q0.w)) + fabsf(dsk);
    int ex = (int)(__float_as_uint(sig) >> 23) & 255; ex = ex < 24 ? 24 : (ex > 253 ? 253 : ex);
    sc = __uint_as_float((unsigned)(267 - ex) << 23);
    unsc = __uint_as_float((unsigned)(ex - 20) << 23);
    if (tid == 0) *(unsigned*)(kc + ZIDX * 2) = 0;
    f2 c[8];
    c[0] = CB * sc;
    c[1] = cmul(c[0], w1); c[2] = cmul(c[0], w2); c[4] = cmul(c[0], w4); c[3] = cmul(c[1], w2);
    c[5] = cmul(c[1], w4); c[6] = cmul(c[2], w4); c[7] = cmul(c[3], w4);
#pragma unroll
    for (int r = 0; r < 8; ++r) {
      PUT(r, c[r]);
      f2 ch = cmul(c[r], w128); PUT(8 + r, ch);
      if ((r & 1) == 0) {
        f2 s0 = cmul(c[r], w256), s1 = cmul(ch, w256);
        PUT(16 + (r >> 1), s0); PUT(20 + (r >> 1), s1);
        if ((r & 3) == 0) {
          const int rho = r >> 2;
          f2 z0 = cmul(c[r], w512), z1 = cmul(ch, w512), z2 = cmul(s0, w512), z3 = cmul(s1, w512);
          PUT(24 + 0 + rho, z0); PUT(24 + 2 + rho, z1); PUT(24 + 4 + rho, z2); PUT(24 + 6 + rho, z3);
          if (r == 0) {
            f2 y0 = cmul(c[0], w1024);
            PUT(32, y0); PUT(33, cmul(ch, w1024)); PUT(34, cmul(s0, w1024)); PUT(35, cmul(s1, w1024));
            PUT(36, cmul(z0, w1024)); PUT(37, cmul(z1, w1024)); PUT(38, cmul(z2, w1024)); PUT(39, cmul(z3, w1024));
            PUT(40, cmul(y0, w1024));
          }
        }
      }
    }
  }
  asm volatile("s_waitcnt lgkmcnt(0)" ::: "memory");
  __syncthreads();
  STAMP(1, 2);
  {
    const unsigned* tab = (const unsigned*)sm;
    f4 acc0 = Z, acc1 = Z;
    const unsigned* pa = tab + col * TAB_PITCH + g * 4;
    const unsigned* pb = tab + (16 + wave * 16 + col) * TAB_PITCH + g * 4;
    u4 ea[12], pb_[12];
#pragma unroll
    for (int kk = 0; kk < 12; ++kk) { ea[kk] = *(const u4*)(pa + kk * 16); pb_[kk] = *(const u4*)(pb + kk * 16); }
#pragma unroll
    for (int kk = 0; kk < 12; kk += 2) {
      acc0 = MFMA16(as_h8(ea[kk]), as_h8(pb_[kk]), acc0);
      acc1 = MFMA16(as_h8(ea[kk + 1]), as_h8(pb_[kk + 1]), acc1);
    }
    __builtin_amdgcn_sched_group_barrier(0x100, 24, 0);
    __builtin_amdgcn_sched_group_barrier(0x008, 12, 0);
    acc0 += acc1;
    acc0[0] = __builtin_fmaf(tid == 0 ? dsk : 0.f, sc, acc0[0]);
    u2 o; o.x = pk_h(acc0[0], acc0[1]); o.y = pk_h(acc0[2], acc0[3]);
    *(u2*)(kc + ((wave * 16 + col) * 16 + 4 * g) * 2) = o;
  }
  __syncthreads();
  *(u4*)(sm + SM_KI + tid * 16) = u4{LDP(ga0.x, ga0.y), LDP(ga0.z, ga0.w), LDP(ga1.x, ga1.y), LDP(ga1.z, ga1.w)};
  __syncthreads();
  __builtin_amdgcn_s_setprio(0);
  STAMP(1, 3);
  const int i0 = 15 - col + 8 * g;
  const char* ka = sm + SM_KI + ((i0 & 1) * NR_LEN + (i0 & ~1)) * 2;
  const int n0 = 3 * wave;
  f4 a0 = Z, a1 = Z, a2 = Z;
  const unsigned fa = (unsigned)(SM_U + (256 + 16 * col + 8 * g + 256 * n0 - 528) * 2);
  h8 A[17];
#pragma unroll
  for (int k = 0; k < 17; ++k) A[k] = ld_4dw(ka + k * 64, ka + k * 64 + 8);
  __builtin_amdgcn_sched_barrier(0);
  if (wave == 0) near_band<9, 33, true>(A, fa, a0, a1, a2);
  else if (wave == 1) near_band<0, 33, true>(A, fa, a0, a1, a2);
  else near_band<0, 25, false>(A, fa, a0, a1, a2);
  asm volatile("s_nop 7\n\ts_nop 7" : "+v"(a0), "+v"(a1), "+v"(a2));
  __syncthreads();
  {
    const char* pp = sm + SM_P + lane * 16 + (n0 - 2) * 1024;
    if (wave != 0) { a0 += *(const f4*)(pp); a1 += *(const f4*)(pp + 1024); }
    conv_epi(a0, unsc, yt + YTS * n0);
    conv_epi(a1, unsc, yt + YTS * (n0 + 1));
    if (wave != 2) { a2 += *(const f4*)(pp + 2048); conv_epi(a2, unsc, yt + YTS * (n0 + 2)); }
  }
  STAMP(1, 4);
  STAMPD(1, 5);
}

__device__ __forceinline__ unsigned ord16(unsigned b) { return (b & 0x8000u) ? (~b & 0xffffu) : (b | 0x8000u); }
__device__ __forceinline__ void fma8(float* acc, u4 w, float xv) {
  acc[0] = __builtin_fmaf(bflo(w.x), xv, acc[0]); acc[1] = __builtin_fmaf(bfhi(w.x), xv, acc[1]);
  acc[2] = __builtin_fmaf(bflo(w.y), xv, acc[2]); acc[3] = __builtin_fmaf(bfhi(w.y), xv, acc[3]);
  acc[4] = __builtin_fmaf(bflo(w.z), xv, acc[4]); acc[5] = __builtin_fmaf(bfhi(w.z), xv, acc[5]);
  acc[6] = __builtin_fmaf(bflo(w.w), xv, acc[6]); acc[7] = __builtin_fmaf(bfhi(w.w), xv, acc[7]);
}
#define RLX_AGENT __ATOMIC_RELAXED, __HIP_MEMORY_SCOPE_AGENT
constexpr unsigned SPIN_CAP = 1u << 22;

__device__ __forceinline__ u64 sweep_keys(char* ws, int lane, int rep, u64* lk) {
  GAS u64* kp = (GAS u64*)(ws + WS_KEYS) + rep * 256 + lane;
  u64 b;
  for (unsigned spins = 0;; ++spins) {
    u64 k0 = __hip_atomic_load(kp, RLX_AGENT), k1 = __hip_atomic_load(kp + 64, RLX_AGENT);
    u64 k2 = __hip_atomic_load(kp + 128, RLX_AGENT), k3 = __hip_atomic_load(kp + 192, RLX_AGENT);
    bool ok = (long long)(k0 & k1 & k2 & k3) < 0;
    b = k0 > k1 ? k0 : k1; u64 c = k2 > k3 ? k2 : k3; b = b > c ? b : c;
    if (__all(ok) || spins > SPIN_CAP) {
      if (lk) { lk[lane] = k0; lk[lane + 64] = k1; lk[lane + 128] = k2; lk[lane + 192] = k3; }
      break;
    }
    __builtin_amdgcn_s_sleep(1);
  }
  unsigned hk = (unsigned)(b >> 32), m = hk;
  m = max(m, (unsigned)__builtin_amdgcn_update_dpp(0, (int)m, 0x121, 0xf, 0xf, true));
  m = max(m, (unsigned)__builtin_amdgcn_update_dpp(0, (int)m, 0x122, 0xf, 0xf, true));
  m = max(m, (unsigned)__builtin_amdgcn_update_dpp(0, (int)m, 0x124, 0xf, 0xf, true));
  m = max(m, (unsigned)__builtin_amdgcn_update_dpp(0, (int)m, 0x128, 0xf, 0xf, true));
  unsigned m0 = (unsigned)__builtin_amdgcn_readlane((int)m, 0), m1 = (unsigned)__builtin_amdgcn_readlane((int)m, 16);
  unsigned m2 = (unsigned)__builtin_amdgcn_readlane((int)m, 32), m3 = (unsigned)__builtin_amdgcn_readlane((int)m, 48);
  m0 = max(max(m0, m1), max(m2, m3));
  int src = __builtin_ctzll(__ballot(hk == m0));
  unsigned lo = (unsigned)__builtin_amdgcn_readlane((int)(unsigned)b, src);
  return ((u64)m0 << 32) | lo;
}
__device__ __forceinline__ float par_sum(float v) {
  v = dpp_add<0x122>(v); v = dpp_add<0x124>(v); v = dpp_add<0x128>(v);
  return v;
}
__device__ __forceinline__ float xs_of(us xv, us yv, float wm, float wrs, float& hh) {
  hh = rbf(bf1(xv) + bf1(yv));
  return rbf(rbf(hh - wm) * wrs);
}

extern "C" __global__ __launch_bounds__(384, 2) void k3_tail(const us* __restrict__ x, const us* __restrict__ wr,
                                                  const us* __restrict__ wg, const us* __restrict__ wu,
                                                  const us* __restrict__ wd, us* __restrict__ out,
                                                  char* __restrict__ ws) {
  __shared__ __attribute__((aligned(16))) float red[5 * 32 + 16 + 8];
  __shared__ __attribute__((aligned(16))) u64 lkeys[1280];
  unsigned* redW = (unsigned*)(red + 176);
  const int tid = threadIdx.x, lane = tid & 63, wave = __builtin_amdgcn_readfirstlane(tid >> 6);
  const int bid = blockIdx.x;
  STAMP(2, 0);
  const us* ytb = (const us*)(ws + WS_YT);
  if (bid >= 256) {
    const bool f1 = bid < 352;
    const int j = f1 ? bid - 256 : bid - 352;
    const int cb = f1 ? (j & 7) * 12 + (j >> 3) : (j & 7) * 5 + (j >> 3);
    if (wave == 5) {
      __builtin_amdgcn_s_sleep(40);
      const u64 wn = sweep_keys(ws, lane, j & 7, f1 ? lkeys : nullptr);
      const unsigned wkey = (unsigned)(wn >> 32), wms = (unsigned)wn;
      const int wrow = 2047 - (int)(wkey & 2047u);
      const int c = lane & 15;
      if (f1) {
        if (lane == 0) { redW[2] = wkey; redW[3] = wms; }
        STAMPT(2, 3, 320);
        __syncthreads();
        __syncthreads();
        const float* rp = red + (c >> 3) * 16 + (c & 7);
        float gs = (rp[0] + rp[32]) + (rp[64] + rp[96]) + rp[128];
        float us_ = (rp[8] + rp[40]) + (rp[72] + rp[104]) + rp[136];
        float gb = rbf(gs);
        float sl = rbf(gb * __builtin_amdgcn_rcpf(1.f + __expf(-gb)));
        unsigned a = (unsigned)f2bf(sl * rbf(us_)) | 0x10000u;
        __hip_atomic_store((GAS unsigned*)(ws + WS_ACT) + (lane >> 4) * F_ + cb * 16 + c, a, RLX_AGENT);
        STAMPT(2, 4, 320);
      } else {
        const int col = cb * 16 + c;
        float fh;
        float fx2 = xs_of(x[(size_t)wrow * D_ + col], ytb[((size_t)(wrow >> 3) * D_ + col) * 8 + (wrow & 7)],
                          bflo(wms), __builtin_amdgcn_rcpf(bfhi(wms)), fh);
        unsigned ob = (wkey >> 11) & 0xffffu;
        unsigned rb = (ob & 0x8000u) ? (ob & 0x7fffu) : (~ob & 0xffffu);
        float sg = rbf(__builtin_amdgcn_rcpf(1.f + __expf(-bf1((us)rb))));
        __syncthreads();
        const float* rp = red + c;
        float ys = (rp[0] + rp[16]) + (rp[32] + rp[48]) + rp[64];
        float y = rbf(rbf(ys) * sg);
        if (lane < 16) out[(size_t)wrow * D_ + col] = f2bf(fh + rbf(fx2 + y));
        STAMPT(2, 5, 320);
      }
      return;
    }
    const int half = tid & 1, rb = tid >> 1;
    __builtin_amdgcn_s_sleep(16);
    if (f1) {
      const size_t o0 = (size_t)rb * F_ + cb * 16 + half * 8;
      u4 wq[8];
#pragma unroll
      for (int k = 0; k < 4; ++k) { wq[k] = *(const u4*)(wg + o0 + (size_t)k * 160 * F_); wq[4 + k] = *(const u4*)(wu + o0 + (size_t)k * 160 * F_); }
      f2 wf[32];
#pragma unroll
      for (int k = 0; k < 8; ++k) { wf[4 * k] = unpk(wq[k].x); wf[4 * k + 1] = unpk(wq[k].y); wf[4 * k + 2] = unpk(wq[k].z); wf[4 * k + 3] = unpk(wq[k].w); }
#pragma unroll
      for (int k = 0; k < 32; ++k) asm volatile("" : "+v"(wf[k]));
      __syncthreads();
      const unsigned wkey = redW[2], wms = redW[3];
      const int wrow = 2047 - (int)(wkey & 2047u);
      const float wm = bflo(wms), wrs = __builtin_amdgcn_rcpf(bfhi(wms));
      const us* xw = x + (size_t)wrow * D_ + rb;
      const us* yw = ytb + ((size_t)(wrow >> 3) * D_ + rb) * 8 + (wrow & 7);
      us xv[4], yv[4];
#pragma unroll
      for (int k = 0; k < 4; ++k) { xv[k] = xw[160 * k]; yv[k] = yw[160 * 8 * k]; }
      f2 acc[8];
#pragma unroll
      for (int i = 0; i < 8; ++i) acc[i] = f2{0.f, 0.f};
#pragma unroll
      for (int k = 0; k < 4; ++k) {
        float hh; float xs = xs_of(xv[k], yv[k], wm, wrs, hh);
        f2 x2v = {xs, xs};
#pragma unroll
        for (int i = 0; i < 4; ++i) {
          acc[i] = __builtin_elementwise_fma(wf[4 * k + i], x2v, acc[i]);
          acc[4 + i] = __builtin_elementwise_fma(wf[16 + 4 * k + i], x2v, acc[4 + i]);
        }
      }
      float w[8], o[4];
#pragma unroll
      for (int i = 0; i < 4; ++i) { w[2 * i] = swap32_add(acc[i].x, acc[4 + i].x); w[2 * i + 1] = swap32_add(acc[i].y, acc[4 + i].y); }
#pragma unroll
      for (int i = 0; i < 4; ++i) o[i] = swap16_add(w[i], w[i + 4]);
      par_sum4(o);
      const int rr = lane >> 4;
      if ((lane & 15) < 2) *(f4*)(red + wave * 32 + half * 16 + 4 * (rr & 1) + 8 * (rr >> 1)) = f4{o[0], o[1], o[2], o[3]};
      __syncthreads();
#pragma unroll
      for (int q = 0; q < 3; ++q) {
        const int cbk = j + 96 * q;
        if (cbk < 256) {
          const u64 gk = lkeys[cbk];
          const unsigned ck = (unsigned)(gk >> 32), cms = (unsigned)gk;
          if (ck != wkey) {
            const int crow = 2047 - (int)(ck & 2047u);
            const float crs = __builtin_amdgcn_rcpf(bfhi(cms)), cb_ = -bflo(cms) * crs;
            unsigned xr = ((const unsigned*)(x + (size_t)crow * D_))[tid];
            const us* yc = ytb + ((size_t)(crow >> 3) * D_ + 2 * tid) * 8 + (crow & 7);
            f2 hs = unpk(xr) + f2{bf1(yc[0]), bf1(yc[8])};
            f2 hh = unpk(pk_bf(hs.x, hs.y));
            f2 oo = hh + __builtin_elementwise_fma(hh, f2{crs, crs}, f2{cb_, cb_});
            ((unsigned*)(out + (size_t)crow * D_))[tid] = pk_bf(oo.x, oo.y);
          }
        }
      }
      STAMP(2, 6);
    } else {
      const us* wp = wd + cb * 16 + half * 8;
      u4 wq[10];
#pragma unroll
      for (int k = 0; k < 10; ++k) { int r = rb + 160 * k; r = r < F_ ? r : F_ - 1; wq[k] = *(const u4*)(wp + (size_t)r * D_); }
      f2 wf[40];
#pragma unroll
      for (int k = 0; k < 10; ++k) { wf[4 * k] = unpk(wq[k].x); wf[4 * k + 1] = unpk(wq[k].y); wf[4 * k + 2] = unpk(wq[k].z); wf[4 * k + 3] = unpk(wq[k].w); }
#pragma unroll
      for (int k = 0; k < 40; ++k) asm volatile("" : "+v"(wf[k]));
      __builtin_amdgcn_s_sleep(30);
      GAS unsigned* ap = (GAS unsigned*)(ws + WS_ACT) + (j & 3) * F_ + rb;
      const int last = rb < 96 ? 1440 : 0;
      unsigned a[10];
      for (unsigned spins = 0;; ++spins) {
#pragma unroll
        for (int k = 0; k < 9; ++k) a[k] = __hip_atomic_load(ap + 160 * k, RLX_AGENT);
        a[9] = __hip_atomic_load(ap + last, RLX_AGENT);
        unsigned all = a[0] & a[1] & a[2] & a[3] & a[4] & a[5] & a[6] & a[7] & a[8] & a[9];
        if (__all(all >> 16) || spins > SPIN_CAP) break;
        __builtin_amdgcn_s_sleep(1);
      }
      if (rb >= 96) a[9] = 0;
      f2 acc[4] = {f2{0.f, 0.f}, f2{0.f, 0.f}, f2{0.f, 0.f}, f2{0.f, 0.f}};
#pragma unroll
      for (int k = 0; k < 10; ++k) {
        float av = bflo(a[k]); f2 a2 = {av, av};
#pragma unroll
        for (int i = 0; i < 4; ++i) acc[i] = __builtin_elementwise_fma(wf[4 * k + i], a2, acc[i]);
      }
      float w0 = swap32_add(acc[0].x, acc[2].x), w1 = swap32_add(acc[0].y, acc[2].y);
      float w2 = swap32_add(acc[1].x, acc[3].x), w3 = swap32_add(acc[1].y, acc[3].y);
      float o0 = par_sum(swap16_add(w0, w2)), o1 = par_sum(swap16_add(w1, w3));
      const int rr = lane >> 4;
      if ((lane & 15) < 2) *(f2*)(red + wave * 16 + half * 8 + 2 * (rr & 1) + 4 * (rr >> 1)) = f2{o0, o1};
      __syncthreads();
    }
    return;
  }
  float* redA = red;
  float* abv = red + 160;
  const int tb = (bid & 7) * 32 + (bid >> 3);
  const int t0 = tb * 8;
  const unsigned* xb = (const unsigned*)(x + (size_t)t0 * D_);
  if (wave == 5) {
    const int r = lane & 7;
    const float piv = bf1(x[(size_t)(t0 + r) * D_]);
    const float W = *(const float*)(ws + WS_WSUM);
    __syncthreads();
    float s1 = (redA[r] + redA[24 + r]) + (redA[48 + r] + redA[72 + r]) + redA[96 + r];
    float s2 = (redA[8 + r] + redA[32 + r]) + (redA[56 + r] + redA[80 + r]) + redA[104 + r];
    float s3 = (redA[16 + r] + redA[40 + r]) + (redA[64 + r] + redA[88 + r]) + redA[112 + r];
    float mb, sb;
    ln_from_sums(s1, s2, piv, mb, sb);
    float rs = __builtin_amdgcn_rcpf(sb);
    float rv = rs * __builtin_fmaf(piv - mb, W, s3);
    unsigned key = 0x80000000u | (ord16(f2bf(rv)) << 11) | (unsigned)(2047 - (t0 + r));
    unsigned best = key;
    best = max(best, (unsigned)__builtin_amdgcn_update_dpp(0, (int)best, 0x111, 0xf, 0xf, true));
    best = max(best, (unsigned)__builtin_amdgcn_update_dpp(0, (int)best, 0x112, 0xf, 0xf, true));
    best = max(best, (unsigned)__builtin_amdgcn_update_dpp(0, (int)best, 0x114, 0xf, 0xf, true));
    best = (unsigned)__builtin_amdgcn_readlane((int)best, 7);
    if (key == best) {
      u64 gr = ((u64)best << 32) | (u64)((__float_as_uint(mb) >> 16) | (__float_as_uint(sb) & 0xffff0000u));
      __hip_atomic_store((GAS u64*)(ws + WS_KEYS) + (lane >> 3) * 256 + bid, gr, RLX_AGENT);
      redW[0] = (unsigned)r;
    }
    if (lane < 8) *(f2*)(abv + 2 * r) = f2{rs, -mb * rs};
    STAMPT(2, 2, 320);
    __syncthreads();
    return;
  }
  unsigned raw[8];
#pragma unroll
  for (int r = 0; r < 8; ++r) raw[r] = xb[r * (D_ / 2) + tid];
  const u4 y0 = *(const u4*)(ytb + ((size_t)tb * D_ + 2 * tid) * 8);
  const u4 y1 = *(const u4*)(ytb + ((size_t)tb * D_ + 2 * tid) * 8 + 8);
  const f2 wv = unpk(((const unsigned*)wr)[tid]);
  f2 h[8];
  {
    float v[24];
#pragma unroll
    for (int r = 0; r < 8; ++r) {
      const float piv = bflo(xb[r * (D_ / 2)]);
      unsigned ya = y0[r >> 1], yb = y1[r >> 1];
      f2 yy = (r & 1) ? f2{bfhi(ya), bfhi(yb)} : f2{bflo(ya), bflo(yb)};
      f2 hs = unpk(raw[r]) + yy;
      h[r] = unpk(pk_bf(hs.x, hs.y));
      f2 dd = h[r] - piv;
      v[r] = dd.x + dd.y;
      v[r + 8] = __builtin_fmaf(dd.x, dd.x, dd.y * dd.y);
      v[r + 16] = __builtin_fmaf(dd.x, wv.x, dd.y * wv.y);
    }
    float w[12], o[6];
#pragma unroll
    for (int i = 0; i < 12; ++i) w[i] = swap32_add(v[i], v[i + 12]);
#pragma unroll
    for (int i = 0; i < 6; ++i) o[i] = swap16_add(w[i], w[i + 6]);
    row_sum6(o);
    if ((lane & 15) == 0) {
      float* dst = redA + wave * 24 + (lane >> 4) * 6;
      *(f2*)dst = f2{o[0], o[1]}; *(f2*)(dst + 2) = f2{o[2], o[3]}; *(f2*)(dst + 4) = f2{o[4], o[5]};
    }
  }
  __syncthreads();
  STAMP(2, 1);
  __syncthreads();
  const int brow = (int)redW[0];
  unsigned* stg = (unsigned*)lkeys + wave * 512;
#pragma unroll
  for (int r = 0; r < 8; r += 2) {
    f4 c = *(const f4*)(abv + 2 * r);
    f2 o0 = h[r] + __builtin_elementwise_fma(h[r], f2{c.x, c.x}, f2{c.y, c.y});
    f2 o1 = h[r + 1] + __builtin_elementwise_fma(h[r + 1], f2{c.z, c.z}, f2{c.w, c.w});
    stg[r * 64 + lane] = pk_bf(o0.x, o0.y);
    stg[(r + 1) * 64 + lane] = pk_bf(o1.x, o1.y);
  }
  {
    const int rr = lane >> 4, ch = lane & 15;
    u4 q0 = *(const u4*)(stg + rr * 64 + ch * 4), q1 = *(const u4*)(stg + (rr + 4) * 64 + ch * 4);
    us* o0p = out + (size_t)(t0 + rr) * D_ + wave * 128 + ch * 8;
    if (rr != brow) asm volatile("global_store_dwordx4 %0, %1, off sc1\n\ts_nop 1" :: "v"(o0p), "v"(q0) : "memory");
    if (rr + 4 != brow) asm volatile("global_store_dwordx4 %0, %1, off sc1\n\ts_nop 1" :: "v"(o0p + 4 * D_), "v"(q1) : "memory");
  }
}

extern "C" __global__ void k9_end() {}

extern "C" void kernel_launch(void* const* d_in, const int* in_sizes, int n_in, void* d_out, int out_size,
                              void* d_ws, size_t ws_size, hipStream_t stream) {
  const us* x = (const us*)d_in[0];
  const us* Are = (const us*)d_in[1];
  const us* Aim = (const us*)d_in[2];
  const us* Bre = (const us*)d_in[3];
  const us* Bim = (const us*)d_in[4];
  const us* Cre = (const us*)d_in[5];
  const us* Cim = (const us*)d_in[6];
  const us* logdt = (const us*)d_in[7];
  const us* Dskip = (const us*)d_in[8];
  const us* wr = (const us*)d_in[10];
  const us* wg = (const us*)d_in[11];
  const us* wu = (const us*)d_in[12];
  const us* wd = (const us*)d_in[13];
  us* out = (us*)d_out;
  char* ws = (char*)d_ws;
  k1_ln<<<256, 384, 0, stream>>>(x, Are, Aim, Bre, Bim, Cre, Cim, wr, wg, wu, wd, out, ws);
  k2_conv<<<D_, 256, K2_LDS, stream>>>(Are, Aim, Bre, Bim, Cre, Cim, logdt, Dskip, wg, wu, wd, ws);
  k3_tail<<<392, 384, 0, stream>>>(x, wr, wg, wu, wd, out, ws);
}
```

```cpp
#include <hip/hip_runtime.h>
#include <hip/hip_bf16.h>
#include <stdint.h>

typedef _Float16 h8 __attribute__((ext_vector_type(8)));
typedef _Float16 h2 __attribute__((ext_vector_type(2)));
typedef float f4 __attribute__((ext_vector_type(4)));
typedef float f2 __attribute__((ext_vector_type(2)));
typedef unsigned u4 __attribute__((ext_vector_type(4)));
typedef unsigned u2 __attribute__((ext_vector_type(2)));
typedef unsigned short us;
typedef unsigned long long u64;
#define GAS __attribute__((address_space(1)))
#define LAS __attribute__((address_space(3)))

constexpr int L_ = 2048, D_ = 640, N_ = 192, F_ = 1536;
constexpr int LP = 2048 + 64;
#define STAMPD(kid, slot) do {} while (0)
#define STAMP(kid, slot) do {} while (0)
#define STAMPT(kid, slot, thr) do {} while (0)

constexpr int NR_LEN  = 608;
constexpr int F2_OFF  = 2 * NR_LEN;
constexpr int F3_OFF  = F2_OFF + 8 * 64;
constexpr int KIMG_ELEMS = F3_OFF + 4 * 80;
constexpr int KCHUNKS = KIMG_ELEMS / 8;

constexpr size_t WS_FLAGS = 0;
constexpr size_t WS_KEYS  = WS_FLAGS;
constexpr size_t WS_ACT   = WS_FLAGS + 16384;
constexpr size_t WS_WSUM  = 40960;
constexpr size_t WS_EPOCH = 41216;
constexpr size_t WS_LNF   = 41472;
constexpr size_t WS_ACK   = WS_LNF + 32768;
constexpr size_t WS_UT    = WS_ACK + 8192;
constexpr size_t WS_YT    = WS_UT + (size_t)D_ * L_ * 2;
constexpr size_t WS_TOTAL = WS_YT + (size_t)D_ * L_ * 2;

constexpr int TAB_PITCH = 200;
constexpr int SM_KC = 57 * TAB_PITCH * 4;
constexpr int SM_U  = SM_KC + 1552;
constexpr int SM_SY = SM_U + 4608;
constexpr int K2_LDS = SM_SY + 32;
constexpr int SM_KI = 0;
constexpr int SM_P  = 4096;

constexpr int kc_of_v(int v) {
  if (v < 256)  { int h = v >> 7, r = v & 127; return ((h * 8 + (r & 7)) * 16) + (r >> 3); }
  if (v < 512)  { int w = v - 256, h = w >> 7, r = w & 127; return ((16 + h * 4 + ((r & 7) >> 1)) * 16) + (r >> 3); }
  if (v < 1024) { int w = v - 512, h = w >> 7, r = w & 127; return ((24 + h * 2 + ((r & 7) >> 2)) * 16) + (r >> 3); }
  if (v < 2048) { int w = v - 1024, h = w >> 7, r = w & 127; return ((32 + h) * 16) + (r >> 3); }
  return 40 * 16;
}
constexpr int bf16_round_int(int j) {
  if (j <= 256) return j;
  int step = j < 512 ? 2 : (j < 1024 ? 4 : 8);
  int lo = (j / step) * step, rem = j - lo;
  if (rem * 2 < step) return lo;
  if (rem * 2 > step) return lo + step;
  return ((lo / step) & 1) ? lo + step : lo;
}
constexpr int ZIDX = 768;

struct GatherTab { unsigned v[KIMG_ELEMS]; };
constexpr GatherTab make_gather() {
  GatherTab t{};
  for (int i = 0; i < KIMG_ELEMS; ++i) t.v[i] = SM_KC + ZIDX * 2;
  for (int e = 0; e < 2; ++e)
    for (int i = 0; i < NR_LEN; ++i) {
      int j = 543 - (i + e);
      if (j >= 0 && j < 515) t.v[e * NR_LEN + i] = (unsigned)(SM_KC + 2 * kc_of_v(bf16_round_int(j)));
    }
  for (int pi = 0; pi < 4; ++pi)
    for (int e = 0; e < 2; ++e)
      for (int i = 0; i < 64; ++i) {
        int p = 79 - (i + e);
        if (p >= 32 && p < 64) {
          int v = 16 * p + 8 * (pi >> 1) + ((pi & 1) ? 8 : 4);
          t.v[F2_OFF + (pi * 2 + e) * 64 + i] = (unsigned)(SM_KC + 2 * kc_of_v(v));
        }
      }
  for (int pi = 0; pi < 2; ++pi)
    for (int e = 0; e < 2; ++e)
      for (int i = 0; i < 80; ++i) {
        int qq = 127 - (i + e);
        if (qq >= 64 && qq < 128) {
          int v = 16 * qq + (pi ? 16 : 8);
          t.v[F3_OFF + (pi * 2 + e) * 80 + i] = (unsigned)(SM_KC + 2 * kc_of_v(v));
        }
      }
  return t;
}
__device__ const GatherTab g_gather = make_gather();

struct BoxTab { us v[7 * 64 * 8]; };
constexpr BoxTab make_box() {
  BoxTab t{};
  const int nn[6] = {3, 5, 3, 5, 7, 9};
  const int sh[6] = {3, 6, 11, 14, 5, 12};
  const int w0[6] = {-16, -16, -24, -24, -16, -24};
  for (int m = 0; m < 6; ++m)
    for (int lane = 0; lane < 64; ++lane)
      for (int j = 0; j < 8; ++j) {
        int b = lane & 15, k = 8 * (lane >> 4) + j;
        int idx = w0[m] + k;
        bool on = idx <= b - sh[m] && idx >= b - sh[m] - (nn[m] - 1);
        t.v[(m * 64 + lane) * 8 + j] = on ? 0x3C00 : 0;
      }
  for (int lane = 0; lane < 64; ++lane)
    for (int j = 0; j < 8; ++j) {
      int b = lane & 15, k = 8 * (lane >> 4) + j;
      t.v[(6 * 64 + lane) * 8 + j] = (k - b == 4 || k - b == 5) ? 0xFFFF : 0;
    }
  return t;
}
__device__ const BoxTab g_box = make_box();

#define RLX_AGENT __ATOMIC_RELAXED, __HIP_MEMORY_SCOPE_AGENT
constexpr unsigned SPIN_CAP = 1u << 22;
__device__ __forceinline__ float bflo(unsigned w) { return __uint_as_float(w << 16); }
__device__ __forceinline__ float bfhi(unsigned w) { return __uint_as_float(w & 0xffff0000u); }
__device__ __forceinline__ float bf1(us v) { return __uint_as_float((unsigned)v << 16); }
__device__ __forceinline__ us f2bf(float f) { __bf16 b = (__bf16)f; return __builtin_bit_cast(us, b); }
__device__ __forceinline__ float rbf(float f) { return bf1(f2bf(f)); }
typedef __bf16 b2 __attribute__((ext_vector_type(2)));
__device__ __forceinline__ unsigned pk_bf(float a, float b) {
  b2 v; v.x = (__bf16)a; v.y = (__bf16)b; return __builtin_bit_cast(unsigned, v);
}
__device__ __forceinline__ unsigned pk_h(float a, float b) {
  h2 v; v.x = (_Float16)a; v.y = (_Float16)b; return __builtin_bit_cast(unsigned, v);
}
template <int CTRL> __device__ __forceinline__ float dpp_add(float v) {
  int t = __builtin_amdgcn_update_dpp(0, __float_as_int(v), CTRL, 0xf, 0xf, true);
  return v + __int_as_float(t);
}
__device__ __forceinline__ float row_sum(float v) {
  v = dpp_add<0x121>(v); v = dpp_add<0x122>(v); v = dpp_add<0x124>(v); v = dpp_add<0x128>(v);
  return v;
}
__device__ __forceinline__ float swap32_add(float a, float b) {
  auto r = __builtin_amdgcn_permlane32_swap(__float_as_uint(a), __float_as_uint(b), false, false);
  return __uint_as_float(r[0]) + __uint_as_float(r[1]);
}
__device__ __forceinline__ float swap16_add(float a, float b) {
  auto r = __builtin_amdgcn_permlane16_swap(__float_as_uint(a), __float_as_uint(b), false, false);
  return __uint_as_float(r[0]) + __uint_as_float(r[1]);
}
__device__ __forceinline__ void wave_reduce16(const float (&v)[16], float (&out)[4]) {
  float w[8];
#pragma unroll
  for (int i = 0; i < 8; ++i) w[i] = swap32_add(v[i], v[i + 8]);
#pragma unroll
  for (int i = 0; i < 4; ++i) out[i] = row_sum(swap16_add(w[i], w[i + 4]));
}
__device__ __forceinline__ void wave_reduce8(const float (&v)[8], float (&out)[2]) {
  float w[4];
#pragma unroll
  for (int i = 0; i < 4; ++i) w[i] = swap32_add(v[i], v[i + 4]);
#pragma unroll
  for (int i = 0; i < 2; ++i) out[i] = row_sum(swap16_add(w[i], w[i + 2]));
}
__device__ __forceinline__ float wave_reduce4(const float (&v)[4]) {
  float w0 = swap32_add(v[0], v[2]), w1 = swap32_add(v[1], v[3]);
  return row_sum(swap16_add(w0, w1));
}
#define DPPA(i, st) "v_add_f32_dpp %" #i ", %" #i ", %" #i " row_ror:" #st " row_mask:0xf bank_mask:0xf\n\t"
__device__ __forceinline__ void row_sum8(float (&o)[8]) {
  asm volatile("s_nop 1\n\t"
    DPPA(0,1) DPPA(1,1) DPPA(2,1) DPPA(3,1) DPPA(4,1) DPPA(5,1) DPPA(6,1) DPPA(7,1)
    DPPA(0,2) DPPA(1,2) DPPA(2,2) DPPA(3,2) DPPA(4,2) DPPA(5,2) DPPA(6,2) DPPA(7,2)
    DPPA(0,4) DPPA(1,4) DPPA(2,4) DPPA(3,4) DPPA(4,4) DPPA(5,4) DPPA(6,4) DPPA(7,4)
    DPPA(0,8) DPPA(1,8) DPPA(2,8) DPPA(3,8) DPPA(4,8) DPPA(5,8) DPPA(6,8) DPPA(7,8)
    : "+v"(o[0]), "+v"(o[1]), "+v"(o[2]), "+v"(o[3]), "+v"(o[4]), "+v"(o[5]), "+v"(o[6]), "+v"(o[7]));
}
__device__ __forceinline__ void row_sum6(float (&o)[6]) {
  asm volatile("s_nop 1\n\t"
    DPPA(0,1) DPPA(1,1) DPPA(2,1) DPPA(3,1) DPPA(4,1) DPPA(5,1)
    DPPA(0,2) DPPA(1,2) DPPA(2,2) DPPA(3,2) DPPA(4,2) DPPA(5,2)
    DPPA(0,4) DPPA(1,4) DPPA(2,4) DPPA(3,4) DPPA(4,4) DPPA(5,4)
    DPPA(0,8) DPPA(1,8) DPPA(2,8) DPPA(3,8) DPPA(4,8) DPPA(5,8)
    : "+v"(o[0]), "+v"(o[1]), "+v"(o[2]), "+v"(o[3]), "+v"(o[4]), "+v"(o[5]));
}
__device__ __forceinline__ void row_sum4(float (&o)[4]) {
  asm volatile("s_nop 1\n\t"
    DPPA(0,1) DPPA(1,1) DPPA(2,1) DPPA(3,1)
    DPPA(0,2) DPPA(1,2) DPPA(2,2) DPPA(3,2)
    DPPA(0,4) DPPA(1,4) DPPA(2,4) DPPA(3,4)
    DPPA(0,8) DPPA(1,8) DPPA(2,8) DPPA(3,8)
    : "+v"(o[0]), "+v"(o[1]), "+v"(o[2]), "+v"(o[3]));
}
__device__ __forceinline__ void par_sum4(float (&o)[4]) {
  asm volatile("s_nop 1\n\t"
    DPPA(0,2) DPPA(1,2) DPPA(2,2) DPPA(3,2)
    DPPA(0,4) DPPA(1,4) DPPA(2,4) DPPA(3,4)
    DPPA(0,8) DPPA(1,8) DPPA(2,8) DPPA(3,8)
    : "+v"(o[0]), "+v"(o[1]), "+v"(o[2]), "+v"(o[3]));
}
__device__ __forceinline__ float rdlane(float v, int l) {
  return __int_as_float(__builtin_amdgcn_readlane(__float_as_int(v), l));
}

__device__ __forceinline__ f2 cmul(f2 a, f2 b) {
  f2 c;
  asm("v_pk_mul_f32 %0, %1, %2 op_sel:[0,0] op_sel_hi:[1,0]\n\tv_pk_fma_f32 %0, %1, %2, %0 op_sel:[1,1,0] op_sel_hi:[0,1,1] neg_lo:[1,0,0]"
      : "=&v"(c) : "v"(a), "v"(b));
  return c;
}
__device__ __forceinline__ f2 cexp_p(float lam, float rh, float rl, float p) {
  float mag = __builtin_amdgcn_exp2f(lam * p);
  float x = rh * p;
  float f = x - __builtin_rintf(x);
  f = __builtin_fmaf(rl, p, f);
  f2 w; w.x = mag * __builtin_amdgcn_cosf(f); w.y = mag * __builtin_amdgcn_sinf(f); return w;
}
#define MFMA16(a, b, c) __builtin_amdgcn_mfma_f32_16x16x32_f16((a), (b), (c), 0, 0, 0)
__device__ __forceinline__ h8 as_h8(u4 v) { return __builtin_bit_cast(h8, v); }

__device__ __forceinline__ void ln_from_sums(float s1, float s2, float piv, float& mb, float& sb) {
  float dm = s1 * (1.f / 640.f);
  float var = fmaxf(__builtin_fmaf(-dm, dm, s2 * (1.f / 640.f)), 0.f);
  mb = rbf(piv + dm);
  sb = rbf(__builtin_amdgcn_sqrtf(rbf(rbf(var) + 1e-5f)));
}
__device__ __forceinline__ f2 unpk(unsigned w) { return f2{bflo(w), bfhi(w)}; }

__device__ __forceinline__ h8 ld_h8(const char* p) { return as_h8(*(const u4*)p); }
__device__ __forceinline__ h8 ld_4dw(const char* p0, const char* p1) {
  const unsigned* a = (const unsigned*)p0; const unsigned* b = (const unsigned*)p1;
  return as_h8(u4{a[0], a[1], b[0], b[1]});
}
__device__ __forceinline__ h8 cvt_pair(f4 a, f4 b) {
  return as_h8(u4{pk_h(a[0], a[1]), pk_h(a[2], a[3]), pk_h(b[0], b[1]), pk_h(b[2], b[3])});
}
__device__ __forceinline__ void glds16(const void* g, void* l) {
  __builtin_amdgcn_global_load_lds((const GAS unsigned*)g, (LAS unsigned*)l, 16, 0, 0);
}
__device__ __forceinline__ void conv_epi(f4 acc, float sc, us* dst) {
  u2 o; o.x = pk_bf(acc[0] * sc, acc[1] * sc); o.y = pk_bf(acc[2] * sc, acc[3] * sc);
  *(u2*)dst = o;
}
#define LDU(a) ((unsigned)*(const LAS us*)(size_t)(a))
#define LDP(a, b) (LDU(a) | (LDU(b) << 16))

template <int OFF> __device__ __forceinline__ u4 lds_rd128(unsigned addr) {
  u4 v; asm volatile("ds_read_b128 %0, %1 offset:%2" : "=v"(v) : "v"(addr), "i"(OFF) : "memory"); return v;
}
template <int N> __device__ __forceinline__ void lgkm_wait() {
  asm volatile("s_waitcnt lgkmcnt(%0)" :: "i"(N) : "memory"); __builtin_amdgcn_sched_barrier(0);
}
template <int M, int M0, int M1, bool T3, int PF>
__device__ __forceinline__ void near_step(const h8 (&A)[17], u4 (&f)[PF], unsigned fa, f4& a0, f4& a1, f4& a2) {
  if constexpr (M < M1) {
    if constexpr (M >= M0 + PF) lgkm_wait<((M1 - 1 - M) < (PF - 1)) ? (M1 - 1 - M) : (PF - 1)>();
    const h8 ff = as_h8(f[(M - M0) % PF]);
    if constexpr (M <= 16) a0 = MFMA16(A[M], ff, a0);
    if constexpr (M >= 8 && M <= 24) a1 = MFMA16(A[M - 8], ff, a1);
    if constexpr (T3 && M >= 16) a2 = MFMA16(A[M - 16], ff, a2);
    __builtin_amdgcn_sched_barrier(0);
    if constexpr (M + PF < M1) f[(M - M0) % PF] = lds_rd128<(M + PF) * 64>(fa);
    near_step<M + 1, M0, M1, T3, PF>(A, f, fa, a0, a1, a2);
  }
}
template <int M, int M0, int PF> __device__ __forceinline__ void near_fill(u4 (&f)[PF], unsigned fa) {
  if constexpr (M < M0 + PF) { f[M - M0] = lds_rd128<M * 64>(fa); near_fill<M + 1, M0, PF>(f, fa); }
}
template <int M0, int M1, bool T3>
__device__ __forceinline__ void near_band(const h8 (&A)[17], unsigned fa, f4& a0, f4& a1, f4& a2) {
  constexpr int PF = 6;
  u4 f[PF];
  near_fill<M0, M0, PF>(f, fa);
  __builtin_amdgcn_s_waitcnt(0xC07F);
  __builtin_amdgcn_sched_barrier(0);
  near_step<M0, M0, M1, T3, PF>(A, f, fa, a0, a1, a2);
}

__device__ __forceinline__ void sy_arrive(char* sm, int idx, int lane) {
  asm volatile("s_waitcnt lgkmcnt(0)" ::: "memory");
  if (lane == 0) __hip_atomic_fetch_add((LAS unsigned*)(LAS char*)(sm + SM_SY) + idx, 1u, __ATOMIC_RELAXED, __HIP_MEMORY_SCOPE_WORKGROUP);
}
__device__ __forceinline__ void sy_wait(char* sm, int idx, unsigned n) {
  volatile LAS unsigned* c = (volatile LAS unsigned*)(LAS char*)(sm + SM_SY) + idx;
  while (*c < n) __builtin_amdgcn_s_sleep(0);
  asm volatile("" ::: "memory");
}

extern "C" __global__ __launch_bounds__(256, 2) void ka_conv(
    const us* __restrict__ x, const us* __restrict__ Are, const us* __restrict__ Aim, const us* __restrict__ Bre,
    const us* __restrict__ Bim, const us* __restrict__ Cre, const us* __restrict__ Cim, const us* __restrict__ logdt,
    const us* __restrict__ Dskip, const us* __restrict__ wr, const us* __restrict__ wg, const us* __restrict__ wu,
    const us* __restrict__ wd, const us* __restrict__ out, char* __restrict__ ws) {
  extern __shared__ __attribute__((aligned(16))) char sm[];
  const int tid = threadIdx.x, lane = tid & 63, wave = __builtin_amdgcn_readfirstlane(tid >> 6);
  const int bid = blockIdx.x;
  STAMP(0, 0);
  const int d = (bid & 7) * 80 + (bid >> 3);
  const int col = lane & 15, g = lane >> 4;
  char* kc = sm + SM_KC;
  const char* ub = sm + SM_U + (256 + 16 * col + 8 * g) * 2;
  us* yt = (us*)(ws + WS_YT) + ((size_t)(2 * col + (g >> 1)) * D_ + d) * 8 + (g & 1) * 4;
  constexpr int YTS = 32 * D_ * 8;
  const f4 Z = {0.f, 0.f, 0.f, 0.f};
  if (tid < 8) ((unsigned*)(sm + SM_SY))[tid] = 0;
  u64 tag = 0;
  if (wave == 3) tag = __hip_atomic_load((GAS u64*)(ws + WS_EPOCH), RLX_AGENT) + 1ull;
  if (bid < 256) {
    __builtin_amdgcn_s_setprio(3);
    const int tb = (bid & 7) * 32 + (bid >> 3);
    const unsigned* xa = (const unsigned*)(x + (size_t)(tb * 8 + 2 * wave) * D_);
    unsigned ra[5], rb[5];
#pragma unroll
    for (int k = 0; k < 5; ++k) { ra[k] = xa[lane + 64 * k]; rb[k] = xa[D_ / 2 + lane + 64 * k]; }
    const float pa = bflo(xa[0]), pb = bflo(xa[D_ / 2]);
    f2 va[5], vb[5];
    f2 s1a = {0.f, 0.f}, s2a = s1a, s1b = s1a, s2b = s1a;
#pragma unroll
    for (int k = 0; k < 5; ++k) {
      va[k] = unpk(ra[k]); vb[k] = unpk(rb[k]);
      f2 da = va[k] - pa, db = vb[k] - pb;
      s1a += da; s2a = __builtin_elementwise_fma(da, da, s2a);
      s1b += db; s2b = __builtin_elementwise_fma(db, db, s2b);
    }
    float rs4 = row_sum(swap16_add(swap32_add(s1a.x + s1a.y, s2a.x + s2a.y), swap32_add(s1b.x + s1b.y, s2b.x + s2b.y)));
    const float S1 = (lane & 1) ? rdlane(rs4, 16) : rdlane(rs4, 0), S2 = (lane & 1) ? rdlane(rs4, 48) : rdlane(rs4, 32);
    float mb, sb;
    ln_from_sums(S1, S2, (lane & 1) ? pb : pa, mb, sb);
    const float aa = 128.f * __builtin_amdgcn_rcpf(sb), bb = -mb * aa;
    const float aA = rdlane(aa, 0), bA = rdlane(bb, 0), aB = rdlane(aa, 1), bB = rdlane(bb, 1);
#pragma unroll
    for (int k = 0; k < 5; ++k) {
      f2 u0 = __builtin_elementwise_fma(va[k], f2{aA, aA}, f2{bA, bA});
      f2 u1 = __builtin_elementwise_fma(vb[k], f2{aB, aB}, f2{bB, bB});
      *(u2*)(sm + wave * 2560 + (lane + 64 * k) * 8) = u2{pk_h(u0.x, u1.x), pk_h(u0.y, u1.y)};
    }
    __syncthreads();
    {
      const unsigned* tp = (const unsigned*)sm + tid;
      char* ut = ws + WS_UT + ((size_t)tb * D_ + tid) * 16;
      u4 q0 = {tp[0], tp[640], tp[1280], tp[1920]}, q1 = {tp[256], tp[896], tp[1536], tp[2176]};
      asm volatile("global_store_dwordx4 %0, %1, off sc1\n\tglobal_store_dwordx4 %2, %3, off sc1\n\ts_nop 1"
                   :: "v"(ut), "v"(q0), "v"(ut + 4096), "v"(q1) : "memory");
      if (wave < 2) {
        u4 q2 = {tp[512], tp[1152], tp[1792], tp[2432]};
        asm volatile("global_store_dwordx4 %0, %1, off sc1\n\ts_nop 1" :: "v"(ut + 8192), "v"(q2) : "memory");
      }
      asm volatile("s_waitcnt vmcnt(0)" ::: "memory");
    }
    STAMP(0, 5);
    __syncthreads();
    if (wave == 3 && lane < 16) __hip_atomic_store((GAS u64*)(ws + WS_LNF) + lane * 256 + bid, tag, RLX_AGENT);
    __builtin_amdgcn_s_setprio(0);
  } else {
    __syncthreads();
  }
  if (wave == 3) {
    if (lane == 0) __hip_atomic_store((GAS u64*)(ws + WS_ACK) + bid, tag, RLX_AGENT);
    if (lane < 32) *(u4*)(sm + SM_U + lane * 16) = u4{0, 0, 0, 0};
    if (bid >= 256 && bid < 266) ((u4*)(ws + WS_FLAGS))[(bid - 256) * 256 + lane] = u4{0, 0, 0, 0},
        ((u4*)(ws + WS_FLAGS))[(bid - 256) * 256 + 64 + lane] = u4{0, 0, 0, 0},
        ((u4*)(ws + WS_FLAGS))[(bid - 256) * 256 + 128 + lane] = u4{0, 0, 0, 0},
        ((u4*)(ws + WS_FLAGS))[(bid - 256) * 256 + 192 + lane] = u4{0, 0, 0, 0};
    if (bid == 266) {
      const unsigned* w2 = (const unsigned*)wr + lane;
      float sw = 0.f;
#pragma unroll
      for (int k = 0; k < 5; ++k) { unsigned q = w2[64 * k]; sw += bflo(q) + bfhi(q); }
      sw = row_sum(sw);
      sw = rdlane(sw, 0) + rdlane(sw, 16) + rdlane(sw, 32) + rdlane(sw, 48);
      if (lane == 0) *(float*)(ws + WS_WSUM) = sw;
    }
    const u4* bt = (const u4*)g_box.v + lane;
    u4 bx[7];
#pragma unroll
    for (int m = 0; m < 7; ++m) bx[m] = bt[64 * m];
    {
      GAS u64* fp = (GAS u64*)(ws + WS_LNF) + (bid & 15) * 256 + lane;
      for (unsigned spins = 0;; ++spins) {
        bool ok = (__hip_atomic_load(fp, RLX_AGENT) == tag) & (__hip_atomic_load(fp + 64, RLX_AGENT) == tag) &
                  (__hip_atomic_load(fp + 128, RLX_AGENT) == tag) & (__hip_atomic_load(fp + 192, RLX_AGENT) == tag);
        if (__all(ok) || spins > SPIN_CAP) break;
        __builtin_amdgcn_s_sleep(2);
      }
    }
    STAMPT(0, 6, 192);
    {
      const char* gu = ws + WS_UT + (size_t)d * 16 + (size_t)lane * (D_ * 16);
      u4 q0, q1, q2, q3;
      asm volatile("global_load_dwordx4 %0, %4, off sc1\n\tglobal_load_dwordx4 %1, %5, off sc1\n\t"
                   "global_load_dwordx4 %2, %6, off sc1\n\tglobal_load_dwordx4 %3, %7, off sc1\n\ts_waitcnt vmcnt(0)"
                   : "=&v"(q0), "=&v"(q1), "=&v"(q2), "=&v"(q3)
                   : "v"(gu), "v"(gu + (size_t)64 * (D_ * 16)), "v"(gu + (size_t)128 * (D_ * 16)), "v"(gu + (size_t)192 * (D_ * 16)) : "memory");
      u4* dst = (u4*)(sm + SM_U + 512) + lane;
      dst[0] = q0; dst[64] = q1; dst[128] = q2; dst[192] = q3;
    }
    sy_arrive(sm, 4, lane);
    STAMPT(0, 7, 192);
    h8 sa[6], sb[6], s3[4];
#pragma unroll
    for (int tau = 0; tau < 6; ++tau) {
      h8 w1 = ld_h8(ub + (256 * tau - 16) * 2), w2 = ld_h8(ub + (256 * tau - 24) * 2);
      sa[tau] = cvt_pair(MFMA16(w1, as_h8(bx[0]), Z), MFMA16(w1, as_h8(bx[1]), Z));
      sb[tau] = cvt_pair(MFMA16(w2, as_h8(bx[2]), Z), MFMA16(w2, as_h8(bx[3]), Z));
      if (tau < 4) s3[tau] = cvt_pair(MFMA16(w1, as_h8(bx[4]), Z), MFMA16(w2, as_h8(bx[5]), Z));
    }
    u4 fs[4];
#pragma unroll
    for (int i = 0; i < 4; ++i) fs[i] = *(const u4*)(ub + (256 * (4 + i) - 1032) * 2);
#pragma unroll
    for (int i = 0; i < 6; ++i) { asm volatile("" : "+v"(sa[i]), "+v"(sb[i])); }
    sy_wait(sm, 3, 3);
    const unsigned kv = LDU(SM_KC + 32 * 16 * 2) * 0x10001u;
    f4 acc[6];
    acc[0] = Z; acc[1] = Z;
    {
      h8 As = as_h8(u4{bx[6].x & kv, bx[6].y & kv, bx[6].z & kv, bx[6].w & kv});
#pragma unroll
      for (int i = 0; i < 4; ++i) acc[2 + i] = MFMA16(As, as_h8(fs[i]), Z);
    }
    {
      const int ib = 79 - col + 4 * g;
      const char* kb = sm + SM_KI + (F2_OFF + (ib & 1) * 64 + (ib & ~1)) * 2;
#pragma unroll
      for (int dl = 2; dl <= 4; ++dl) {
        h8 tA = ld_4dw(kb - dl * 32, kb + 256 - dl * 32), tB = ld_4dw(kb + 512 - dl * 32, kb + 768 - dl * 32);
#pragma unroll
        for (int tau = 0; tau < 6; ++tau)
          if (tau + dl <= 7) { acc[tau + dl - 2] = MFMA16(sa[tau], tA, acc[tau + dl - 2]); acc[tau + dl - 2] = MFMA16(sb[tau], tB, acc[tau + dl - 2]); }
      }
    }
    {
      const int ib = 127 - col + 4 * g;
      const char* kb = sm + SM_KI + (F3_OFF + (ib & 1) * 80 + (ib & ~1)) * 2;
#pragma unroll
      for (int dl = 4; dl <= 7; ++dl) {
        h8 tt = ld_4dw(kb - dl * 32, kb + 320 - dl * 32);
#pragma unroll
        for (int tau = 0; tau < 4; ++tau)
          if (tau + dl <= 7) acc[tau + dl - 2] = MFMA16(s3[tau], tt, acc[tau + dl - 2]);
      }
    }
#pragma unroll
    for (int i = 0; i < 6; ++i) *(f4*)(sm + SM_P + i * 1024 + lane * 16) = acc[i];
    sy_arrive(sm, 5, lane);
    if (bid == 300) {
      GAS u64* ap = (GAS u64*)(ws + WS_ACK) + lane;
      for (unsigned spins = 0;; ++spins) {
        bool ok = true;
#pragma unroll
        for (int k = 0; k < 10; ++k) ok = ok & (__hip_atomic_load(ap + 64 * k, RLX_AGENT) == tag);
        if (__all(ok) || spins > SPIN_CAP) break;
        __builtin_amdgcn_s_sleep(8);
      }
      if (lane == 0) __hip_atomic_store((GAS u64*)(ws + WS_EPOCH), tag, RLX_AGENT);
    }
    return;
  }
  const size_t pidx = (size_t)d * N_ + tid;
  us r_are = Are[pidx], r_aim = Aim[pidx], r_bre = Bre[pidx], r_bim = Bim[pidx], r_cre = Cre[pidx], r_cim = Cim[pidx];
  const unsigned ldw = ((const unsigned*)logdt)[d >> 1], dsw = ((const unsigned*)Dskip)[d >> 1];
  const float dt = rbf(__expf((d & 1) ? bfhi(ldw) : bflo(ldw)));
  const float dsk = (d & 1) ? bfhi(dsw) : bflo(dsw);
  asm volatile("s_mov_b32 m0, %0" :: "s"(wave * 256) : "memory");
  float ar = dt * bf1(r_are), ai = dt * bf1(r_aim);
  f2 dB = {dt * bf1(r_bre), dt * bf1(r_bim)};
  f2 Cc = {bf1(r_cre), bf1(r_cim)};
  f2 CB = cmul(Cc, dB);
  const float LOG2E = 1.4426950408889634f;
  const float I2PH = 0.15915494309189535f;
  const float I2PL = (float)(0.15915494309189533577 - (double)0.15915494309189535f);
  float lam = ar * LOG2E;
  float rh = ai * I2PH;
  float rl = __builtin_fmaf(ai, I2PL, __builtin_fmaf(ai, I2PH, -rh));
  {
    float gg = fmaxf(1.f, __builtin_amdgcn_exp2f(lam * 2048.f));
    float bnd = row_sum((fabsf(CB.x) + fabsf(CB.y)) * gg);
    if ((lane & 15) == 0) ((float*)(kc))[wave * 4 + (lane >> 4)] = bnd;
    sy_arrive(sm, 0, lane);
  }
  f2 w1 = cexp_p(lam, rh, rl, 1.f), w16 = cexp_p(lam, rh, rl, 16.f), w256 = cexp_p(lam, rh, rl, 256.f);
  f2 w2 = cmul(w1, w1), w32 = cmul(w16, w16), w512 = cmul(w256, w256);
  f2 w4 = cmul(w2, w2), w64 = cmul(w32, w32), w1024 = cmul(w512, w512);
  f2 w8 = cmul(w4, w4), w128 = cmul(w64, w64);
#define DSW(row, val) asm volatile("ds_write_addtid_b32 %0 offset:%1" :: "v"(val), "i"((row) * TAB_PITCH * 4) : "memory")
#define PUTE(row, c) do { f2 c_ = (c); DSW((row), pk_h(c_.x, -c_.y)); } while (0)
#define PUT(row, c) do { f2 c_ = (c); DSW(16 + (row), pk_h(c_.x, c_.y)); } while (0)
  {
    f2 e[8];
    e[1] = w8; e[2] = w16; e[3] = cmul(w8, w16); e[4] = w32;
    e[5] = cmul(w32, e[1]); e[6] = cmul(w32, e[2]); e[7] = cmul(w32, e[3]);
    DSW(0, 0x00003C00u);
#pragma unroll
    for (int l = 1; l < 8; ++l) PUTE(l, e[l]);
    PUTE(8, w64);
#pragma unroll
    for (int l = 1; l < 8; ++l) PUTE(8 + l, cmul(w64, e[l]));
  }
  sy_wait(sm, 0, 3);
  STAMP(0, 1);
  const u4* gt = (const u4*)g_gather.v + 2 * tid;
  u4 ga0 = gt[0], ga1 = gt[1], gb0, gb1;
  if (wave == 0) { gb0 = gt[384]; gb1 = gt[385]; }
  float sc, unsc;
  {
    f4 q0 = *(const f4*)(kc), q1 = *(const f4*)(kc + 16), q2 = *(const f4*)(kc + 32);
    q0 += q1; q0 += q2;
    float sig = ((q0.x + q0.y) + (q0.z + q0.w)) + fabsf(dsk);
    int ex = (int)(__float_as_uint(sig) >> 23) & 255; ex = ex < 24 ? 24 : (ex > 253 ? 253 : ex);
    sc = __uint_as_float((unsigned)(267 - ex) << 23);
    unsc = __uint_as_float((unsigned)(ex - 20) << 23);
    if (tid == 0) *(unsigned*)(kc + ZIDX * 2) = 0;
    f2 c[8];
    c[0] = CB * sc;
    c[1] = cmul(c[0], w1); c[2] = cmul(c[0], w2); c[4] = cmul(c[0], w4); c[3] = cmul(c[1], w2);
    c[5] = cmul(c[1], w4); c[6] = cmul(c[2], w4); c[7] = cmul(c[3], w4);
#pragma unroll
    for (int r = 0; r < 8; ++r) {
      PUT(r, c[r]);
      f2 ch = cmul(c[r], w128); PUT(8 + r, ch);
      if ((r & 1) == 0) {
        f2 s0 = cmul(c[r], w256), s1 = cmul(ch, w256);
        PUT(16 + (r >> 1), s0); PUT(20 + (r >> 1), s1);
        if ((r & 3) == 0) {
          const int rho = r >> 2;
          f2 z0 = cmul(c[r], w512), z1 = cmul(ch, w512), z2 = cmul(s0, w512), z3 = cmul(s1, w512);
          PUT(24 + 0 + rho, z0); PUT(24 + 2 + rho, z1); PUT(24 + 4 + rho, z2); PUT(24 + 6 + rho, z3);
          if (r == 0) {
            f2 y0 = cmul(c[0], w1024);
            PUT(32, y0); PUT(33, cmul(ch, w1024)); PUT(34, cmul(s0, w1024)); PUT(35, cmul(s1, w1024));
            PUT(36, cmul(z0, w1024)); PUT(37, cmul(z1, w1024)); PUT(38, cmul(z2, w1024)); PUT(39, cmul(z3, w1024));
            PUT(40, cmul(y0, w1024));
          }
        }
      }
    }
  }
  sy_arrive(sm, 1, lane);
  sy_wait(sm, 1, 3);
  STAMP(0, 2);
  {
    const unsigned* tab = (const unsigned*)sm;
    f4 acc0 = Z, acc1 = Z;
    const unsigned* pa = tab + col * TAB_PITCH + g * 4;
    const unsigned* pb = tab + (16 + wave * 16 + col) * TAB_PITCH + g * 4;
    u4 ea[12], pb_[12];
#pragma unroll
    for (int kk = 0; kk < 12; ++kk) { ea[kk] = *(const u4*)(pa + kk * 16); pb_[kk] = *(const u4*)(pb + kk * 16); }
#pragma unroll
    for (int kk = 0; kk < 12; kk += 2) {
      acc0 = MFMA16(as_h8(ea[kk]), as_h8(pb_[kk]), acc0);
      acc1 = MFMA16(as_h8(ea[kk + 1]), as_h8(pb_[kk + 1]), acc1);
    }
    __builtin_amdgcn_sched_group_barrier(0x100, 24, 0);
    __builtin_amdgcn_sched_group_barrier(0x008, 12, 0);
    acc0 += acc1;
    acc0[0] = __builtin_fmaf(tid == 0 ? dsk : 0.f, sc, acc0[0]);
    u2 o; o.x = pk_h(acc0[0], acc0[1]); o.y = pk_h(acc0[2], acc0[3]);
    *(u2*)(kc + ((wave * 16 + col) * 16 + 4 * g) * 2) = o;
  }
  sy_arrive(sm, 2, lane);
  sy_wait(sm, 2, 3);
  *(u4*)(sm + SM_KI + tid * 16) = u4{LDP(ga0.x, ga0.y), LDP(ga0.z, ga0.w), LDP(ga1.x, ga1.y), LDP(ga1.z, ga1.w)};
  if (wave == 0) *(u4*)(sm + SM_KI + (192 + tid) * 16) = u4{LDP(gb0.x, gb0.y), LDP(gb0.z, gb0.w), LDP(gb1.x, gb1.y), LDP(gb1.z, gb1.w)};
  sy_arrive(sm, 3, lane);
  unsigned pf;
  {
    const int xcd = bid & 7;
    const int id = (bid >> 3) * 192 + tid;
    u64 pc; int o1, o2;
    asm volatile("s_getpc_b64 %0\n\ts_mov_b32 %1, k3_tail@rel32@lo+4\n\ts_mov_b32 %2, k9_end@rel32@lo+12" : "=s"(pc), "=s"(o1), "=s"(o2));
    int r3 = id / 3, q3 = id - r3 * 3;
    const char* pp0 = (const char*)((r3 < 640 ? wg + (size_t)r3 * F_ : wu + (size_t)(r3 - 640) * F_) + xcd * 192 + q3 * 64);
    int i2 = id - 3840;
    const char* pp1 = (const char*)(wd + (size_t)(i2 >> 1) * D_ + xcd * 80 + (i2 & 1) * 78);
    int i3 = (id - 6912) * 64; i3 = (i3 >= 0 && i3 < o2 - o1) ? i3 : 0;
    const char* pp2 = (const char*)(pc + (long long)o1 + i3);
    pf = *(const GAS unsigned*)(id < 3840 ? pp0 : (id < 6912 ? pp1 : pp2));
  }
  sy_wait(sm, 3, 3);
  STAMP(0, 3);
  const int i0 = 15 - col + 8 * g;
  const char* ka = sm + SM_KI + ((i0 & 1) * NR_LEN + (i0 & ~1)) * 2;
  const int n0 = 3 * wave;
  f4 a0 = Z, a1 = Z, a2 = Z;
  const unsigned fa = (unsigned)(SM_U + (256 + 16 * col + 8 * g + 256 * n0 - 528) * 2);
  h8 A[17];
#pragma unroll
  for (int k = 0; k < 17; ++k) A[k] = ld_4dw(ka + k * 64, ka + k * 64 + 8);
  __builtin_amdgcn_sched_barrier(0);
  sy_wait(sm, 4, 1);
  if (wave == 0) near_band<9, 33, true>(A, fa, a0, a1, a2);
  else if (wave == 1) near_band<0, 33, true>(A, fa, a0, a1, a2);
  else near_band<0, 25, false>(A, fa, a0, a1, a2);
  asm volatile("s_nop 7\n\ts_nop 7" : "+v"(a0), "+v"(a1), "+v"(a2));
  sy_wait(sm, 5, 1);
  {
    const char* pp = sm + SM_P + lane * 16 + (n0 - 2) * 1024;
    if (wave != 0) { a0 += *(const f4*)(pp); a1 += *(const f4*)(pp + 1024); }
    conv_epi(a0, unsc, yt + YTS * n0);
    conv_epi(a1, unsc, yt + YTS * (n0 + 1));
    if (wave != 2) { a2 += *(const f4*)(pp + 2048); conv_epi(a2, unsc, yt + YTS * (n0 + 2)); }
  }
  asm volatile("" :: "v"(pf));
  STAMP(0, 4);
}

__device__ __forceinline__ unsigned ord16(unsigned b) { return (b & 0x8000u) ? (~b & 0xffffu) : (b | 0x8000u); }
__device__ __forceinline__ void fma8(float* acc, u4 w, float xv) {
  acc[0] = __builtin_fmaf(bflo(w.x), xv, acc[0]); acc[1] = __builtin_fmaf(bfhi(w.x), xv, acc[1]);
  acc[2] = __builtin_fmaf(bflo(w.y), xv, acc[2]); acc[3] = __builtin_fmaf(bfhi(w.y), xv, acc[3]);
  acc[4] = __builtin_fmaf(bflo(w.z), xv, acc[4]); acc[5] = __builtin_fmaf(bfhi(w.z), xv, acc[5]);
  acc[6] = __builtin_fmaf(bflo(w.w), xv, acc[6]); acc[7] = __builtin_fmaf(bfhi(w.w), xv, acc[7]);
}

__device__ __forceinline__ u64 sweep_keys(char* ws, int lane, int rep, u64* lk) {
  GAS u64* kp = (GAS u64*)(ws + WS_KEYS) + rep * 256 + lane;
  u64 b;
  for (unsigned spins = 0;; ++spins) {
    u64 k0 = __hip_atomic_load(kp, RLX_AGENT), k1 = __hip_atomic_load(kp + 64, RLX_AGENT);
    u64 k2 = __hip_atomic_load(kp + 128, RLX_AGENT), k3 = __hip_atomic_load(kp + 192, RLX_AGENT);
    bool ok = (long long)(k0 & k1 & k2 & k3) < 0;
    b = k0 > k1 ? k0 : k1; u64 c = k2 > k3 ? k2 : k3; b = b > c ? b : c;
    if (__all(ok) || spins > SPIN_CAP) {
      if (lk) { lk[lane] = k0; lk[lane + 64] = k1; lk[lane + 128] = k2; lk[lane + 192] = k3; }
      break;
    }
    __builtin_amdgcn_s_sleep(1);
  }
  unsigned hk = (unsigned)(b >> 32), m = hk;
  m = max(m, (unsigned)__builtin_amdgcn_update_dpp(0, (int)m, 0x121, 0xf, 0xf, true));
  m = max(m, (unsigned)__builtin_amdgcn_update_dpp(0, (int)m, 0x122, 0xf, 0xf, true));
  m = max(m, (unsigned)__builtin_amdgcn_update_dpp(0, (int)m, 0x124, 0xf, 0xf, true));
  m = max(m, (unsigned)__builtin_amdgcn_update_dpp(0, (int)m, 0x128, 0xf, 0xf, true));
  unsigned m0 = (unsigned)__builtin_amdgcn_readlane((int)m, 0), m1 = (unsigned)__builtin_amdgcn_readlane((int)m, 16);
  unsigned m2 = (unsigned)__builtin_amdgcn_readlane((int)m, 32), m3 = (unsigned)__builtin_amdgcn_readlane((int)m, 48);
  m0 = max(max(m0, m1), max(m2, m3));
  int src = __builtin_ctzll(__ballot(hk == m0));
  unsigned lo = (unsigned)__builtin_amdgcn_readlane((int)(unsigned)b, src);
  return ((u64)m0 << 32) | lo;
}
__device__ __forceinline__ float par_sum(float v) {
  v = dpp_add<0x122>(v); v = dpp_add<0x124>(v); v = dpp_add<0x128>(v);
  return v;
}
__device__ __forceinline__ float xs_of(us xv, us yv, float wm, float wrs, float& hh) {
  hh = rbf(bf1(xv) + bf1(yv));
  return rbf(rbf(hh - wm) * wrs);
}

extern "C" __global__ __launch_bounds__(384, 2) void k3_tail(const us* __restrict__ x, const us* __restrict__ wr,
                                                  const us* __restrict__ wg, const us* __restrict__ wu,
                                                  const us* __restrict__ wd, us* __restrict__ out,
                                                  char* __restrict__ ws) {
  __shared__ __attribute__((aligned(16))) float red[5 * 32 + 16 + 8];
  __shared__ __attribute__((aligned(16))) u64 lkeys[1280];
  unsigned* redW = (unsigned*)(red + 176);
  const int tid = threadIdx.x, lane = tid & 63, wave = __builtin_amdgcn_readfirstlane(tid >> 6);
  const int bid = blockIdx.x;
  STAMP(2, 0);
  const us* ytb = (const us*)(ws + WS_YT);
  if (bid >= 256) {
    const bool f1 = bid < 352;
    const int j = f1 ? bid - 256 : bid - 352;
    const int cb = f1 ? (j & 7) * 12 + (j >> 3) : (j & 7) * 5 + (j >> 3);
    if (wave == 5) {
      __builtin_amdgcn_s_sleep(40);
      const u64 wn = sweep_keys(ws, lane, j & 7, f1 ? lkeys : nullptr);
      const unsigned wkey = (unsigned)(wn >> 32), wms = (unsigned)wn;
      const int wrow = 2047 - (int)(wkey & 2047u);
      const int c = lane & 15;
      if (f1) {
        if (lane == 0) { redW[2] = wkey; redW[3] = wms; }
        STAMPT(2, 3, 320);
        __syncthreads();
        __syncthreads();
        const float* rp = red + (c >> 3) * 16 + (c & 7);
        float gs = (rp[0] + rp[32]) + (rp[64] + rp[96]) + rp[128];
        float us_ = (rp[8] + rp[40]) + (rp[72] + rp[104]) + rp[136];
        float gb = rbf(gs);
        float sl = rbf(gb * __builtin_amdgcn_rcpf(1.f + __expf(-gb)));
        unsigned a = (unsigned)f2bf(sl * rbf(us_)) | 0x10000u;
        __hip_atomic_store((GAS unsigned*)(ws + WS_ACT) + (lane >> 4) * F_ + cb * 16 + c, a, RLX_AGENT);
        STAMPT(2, 4, 320);
      } else {
        const int col = cb * 16 + c;
        float fh;
        float fx2 = xs_of(x[(size_t)wrow * D_ + col], ytb[((size_t)(wrow >> 3) * D_ + col) * 8 + (wrow & 7)],
                          bflo(wms), __builtin_amdgcn_rcpf(bfhi(wms)), fh);
        unsigned ob = (wkey >> 11) & 0xffffu;
        unsigned rb = (ob & 0x8000u) ? (ob & 0x7fffu) : (~ob & 0xffffu);
        float sg = rbf(__builtin_amdgcn_rcpf(1.f + __expf(-bf1((us)rb))));
        __syncthreads();
        const float* rp = red + c;
        float ys = (rp[0] + rp[16]) + (rp[32] + rp[48]) + rp[64];
        float y = rbf(rbf(ys) * sg);
        if (lane < 16) out[(size_t)wrow * D_ + col] = f2bf(fh + rbf(fx2 + y));
        STAMPT(2, 5, 320);
      }
      return;
    }
    const int half = tid & 1, rb = tid >> 1;
    __builtin_amdgcn_s_sleep(16);
    if (f1) {
      const size_t o0 = (size_t)rb * F_ + cb * 16 + half * 8;
      u4 wq[8];
#pragma unroll
      for (int k = 0; k < 4; ++k) { wq[k] = *(const u4*)(wg + o0 + (size_t)k * 160 * F_); wq[4 + k] = *(const u4*)(wu + o0 + (size_t)k * 160 * F_); }
      f2 wf[32];
#pragma unroll
      for (int k = 0; k < 8; ++k) { wf[4 * k] = unpk(wq[k].x); wf[4 * k + 1] = unpk(wq[k].y); wf[4 * k + 2] = unpk(wq[k].z); wf[4 * k + 3] = unpk(wq[k].w); }
#pragma unroll
      for (int k = 0; k < 32; ++k) asm volatile("" : "+v"(wf[k]));
      __syncthreads();
      const unsigned wkey = redW[2], wms = redW[3];
      const int wrow = 2047 - (int)(wkey & 2047u);
      const float wm = bflo(wms), wrs = __builtin_amdgcn_rcpf(bfhi(wms));
      const us* xw = x + (size_t)wrow * D_ + rb;
      const us* yw = ytb + ((size_t)(wrow >> 3) * D_ + rb) * 8 + (wrow & 7);
      us xv[4], yv[4];
#pragma unroll
      for (int k = 0; k < 4; ++k) { xv[k] = xw[160 * k]; yv[k] = yw[160 * 8 * k]; }
      f2 acc[8];
#pragma unroll
      for (int i = 0; i < 8; ++i) acc[i] = f2{0.f, 0.f};
#pragma unroll
      for (int k = 0; k < 4; ++k) {
        float hh; float xs = xs_of(xv[k], yv[k], wm, wrs, hh);
        f2 x2v = {xs, xs};
#pragma unroll
        for (int i = 0; i < 4; ++i) {
          acc[i] = __builtin_elementwise_fma(wf[4 * k + i], x2v, acc[i]);
          acc[4 + i] = __builtin_elementwise_fma(wf[16 + 4 * k + i], x2v, acc[4 + i]);
        }
      }
      float w[8], o[4];
#pragma unroll
      for (int i = 0; i < 4; ++i) { w[2 * i] = swap32_add(acc[i].x, acc[4 + i].x); w[2 * i + 1] = swap32_add(acc[i].y, acc[4 + i].y); }
#pragma unroll
      for (int i = 0; i < 4; ++i) o[i] = swap16_add(w[i], w[i + 4]);
      par_sum4(o);
      const int rr = lane >> 4;
      if ((lane & 15) < 2) *(f4*)(red + wave * 32 + half * 16 + 4 * (rr & 1) + 8 * (rr >> 1)) = f4{o[0], o[1], o[2], o[3]};
      __syncthreads();
#pragma unroll
      for (int q = 0; q < 3; ++q) {
        const int cbk = j + 96 * q;
        if (cbk < 256) {
          const u64 gk = lkeys[cbk];
          const unsigned ck = (unsigned)(gk >> 32), cms = (unsigned)gk;
          if (ck != wkey) {
            const int crow = 2047 - (int)(ck & 2047u);
            const float crs = __builtin_amdgcn_rcpf(bfhi(cms)), cb_ = -bflo(cms) * crs;
            unsigned xr = ((const unsigned*)(x + (size_t)crow * D_))[tid];
            const us* yc = ytb + ((size_t)(crow >> 3) * D_ + 2 * tid) * 8 + (crow & 7);
            f2 hs = unpk(xr) + f2{bf1(yc[0]), bf1(yc[8])};
            f2 hh = unpk(pk_bf(hs.x, hs.y));
            f2 oo = hh + __builtin_elementwise_fma(hh, f2{crs, crs}, f2{cb_, cb_});
            ((unsigned*)(out + (size_t)crow * D_))[tid] = pk_bf(oo.x, oo.y);
          }
        }
      }
      STAMP(2, 6);
    } else {
      const us* wp = wd + cb * 16 + half * 8;
      u4 wq[10];
#pragma unroll
      for (int k = 0; k < 10; ++k) { int r = rb + 160 * k; r = r < F_ ? r : F_ - 1; wq[k] = *(const u4*)(wp + (size_t)r * D_); }
      f2 wf[40];
#pragma unroll
      for (int k = 0; k < 10; ++k) { wf[4 * k] = unpk(wq[k].x); wf[4 * k + 1] = unpk(wq[k].y); wf[4 * k + 2] = unpk(wq[k].z); wf[4 * k + 3] = unpk(wq[k].w); }
#pragma unroll
      for (int k = 0; k < 40; ++k) asm volatile("" : "+v"(wf[k]));
      __builtin_amdgcn_s_sleep(30);
      GAS unsigned* ap = (GAS unsigned*)(ws + WS_ACT) + (j & 3) * F_ + rb;
      const int last = rb < 96 ? 1440 : 0;
      unsigned a[10];
      for (unsigned spins = 0;; ++spins) {
#pragma unroll
        for (int k = 0; k < 9; ++k) a[k] = __hip_atomic_load(ap + 160 * k, RLX_AGENT);
        a[9] = __hip_atomic_load(ap + last, RLX_AGENT);
        unsigned all = a[0] & a[1] & a[2] & a[3] & a[4] & a[5] & a[6] & a[7] & a[8] & a[9];
        if (__all(all >> 16) || spins > SPIN_CAP) break;
        __builtin_amdgcn_s_sleep(1);
      }
      if (rb >= 96) a[9] = 0;
      f2 acc[4] = {f2{0.f, 0.f}, f2{0.f, 0.f}, f2{0.f, 0.f}, f2{0.f, 0.f}};
#pragma unroll
      for (int k = 0; k < 10; ++k) {
        float av = bflo(a[k]); f2 a2 = {av, av};
#pragma unroll
        for (int i = 0; i < 4; ++i) acc[i] = __builtin_elementwise_fma(wf[4 * k + i], a2, acc[i]);
      }
      float w0 = swap32_add(acc[0].x, acc[2].x), w1 = swap32_add(acc[0].y, acc[2].y);
      float w2 = swap32_add(acc[1].x, acc[3].x), w3 = swap32_add(acc[1].y, acc[3].y);
      float o0 = par_sum(swap16_add(w0, w2)), o1 = par_sum(swap16_add(w1, w3));
      const int rr = lane >> 4;
      if ((lane & 15) < 2) *(f2*)(red + wave * 16 + half * 8 + 2 * (rr & 1) + 4 * (rr >> 1)) = f2{o0, o1};
      __syncthreads();
    }
    return;
  }
  float* redA = red;
  float* abv = red + 160;
  const int tb = (bid & 7) * 32 + (bid >> 3);
  const int t0 = tb * 8;
  const unsigned* xb = (const unsigned*)(x + (size_t)t0 * D_);
  if (wave == 5) {
    const int r = lane & 7;
    const float piv = bf1(x[(size_t)(t0 + r) * D_]);
    const float W = *(const float*)(ws + WS_WSUM);
    __syncthreads();
    float s1 = (redA[r] + redA[24 + r]) + (redA[48 + r] + redA[72 + r]) + redA[96 + r];
    float s2 = (redA[8 + r] + redA[32 + r]) + (redA[56 + r] + redA[80 + r]) + redA[104 + r];
    float s3 = (redA[16 + r] + redA[40 + r]) + (redA[64 + r] + redA[88 + r]) + redA[112 + r];
    float mb, sb;
    ln_from_sums(s1, s2, piv, mb, sb);
    float rs = __builtin_amdgcn_rcpf(sb);
    float rv = rs * __builtin_fmaf(piv - mb, W, s3);
    unsigned key = 0x80000000u | (ord16(f2bf(rv)) << 11) | (unsigned)(2047 - (t0 + r));
    unsigned best = key;
    best = max(best, (unsigned)__builtin_amdgcn_update_dpp(0, (int)best, 0x111, 0xf, 0xf, true));
    best = max(best, (unsigned)__builtin_amdgcn_update_dpp(0, (int)best, 0x112, 0xf, 0xf, true));
    best = max(best, (unsigned)__builtin_amdgcn_update_dpp(0, (int)best, 0x114, 0xf, 0xf, true));
    best = (unsigned)__builtin_amdgcn_readlane((int)best, 7);
    if (key == best) {
      u64 gr = ((u64)best << 32) | (u64)((__float_as_uint(mb) >> 16) | (__float_as_uint(sb) & 0xffff0000u));
      __hip_atomic_store((GAS u64*)(ws + WS_KEYS) + (lane >> 3) * 256 + bid, gr, RLX_AGENT);
      redW[0] = (unsigned)r;
    }
    if (lane < 8) *(f2*)(abv + 2 * r) = f2{rs, -mb * rs};
    STAMPT(2, 2, 320);
    __syncthreads();
    return;
  }
  unsigned raw[8];
#pragma unroll
  for (int r = 0; r < 8; ++r) raw[r] = xb[r * (D_ / 2) + tid];
  const u4 y0 = *(const u4*)(ytb + ((size_t)tb * D_ + 2 * tid) * 8);
  const u4 y1 = *(const u4*)(ytb + ((size_t)tb * D_ + 2 * tid) * 8 + 8);
  const f2 wv = unpk(((const unsigned*)wr)[tid]);
  f2 h[8];
  {
    float v[24];
#pragma unroll
    for (int r = 0; r < 8; ++r) {
      const float piv = bflo(xb[r * (D_ / 2)]);
      unsigned ya = y0[r >> 1], yb = y1[r >> 1];
      f2 yy = (r & 1) ? f2{bfhi(ya), bfhi(yb)} : f2{bflo(ya), bflo(yb)};
      f2 hs = unpk(raw[r]) + yy;
      h[r] = unpk(pk_bf(hs.x, hs.y));
      f2 dd = h[r] - piv;
      v[r] = dd.x + dd.y;
      v[r + 8] = __builtin_fmaf(dd.x, dd.x, dd.y * dd.y);
      v[r + 16] = __builtin_fmaf(dd.x, wv.x, dd.y * wv.y);
    }
    float w[12], o[6];
#pragma unroll
    for (int i = 0; i < 12; ++i) w[i] = swap32_add(v[i], v[i + 12]);
#pragma unroll
    for (int i = 0; i < 6; ++i) o[i] = swap16_add(w[i], w[i + 6]);
    row_sum6(o);
    if ((lane & 15) == 0) {
      float* dst = redA + wave * 24 + (lane >> 4) * 6;
      *(f2*)dst = f2{o[0], o[1]}; *(f2*)(dst + 2) = f2{o[2], o[3]}; *(f2*)(dst + 4) = f2{o[4], o[5]};
    }
  }
  __syncthreads();
  STAMP(2, 1);
  __syncthreads();
  const int brow = (int)redW[0];
  unsigned* stg = (unsigned*)lkeys + wave * 512;
#pragma unroll
  for (int r = 0; r < 8; r += 2) {
    f4 c = *(const f4*)(abv + 2 * r);
    f2 o0 = h[r] + __builtin_elementwise_fma(h[r], f2{c.x, c.x}, f2{c.y, c.y});
    f2 o1 = h[r + 1] + __builtin_elementwise_fma(h[r + 1], f2{c.z, c.z}, f2{c.w, c.w});
    stg[r * 64 + lane] = pk_bf(o0.x, o0.y);
    stg[(r + 1) * 64 + lane] = pk_bf(o1.x, o1.y);
  }
  {
    const int rr = lane >> 4, ch = lane & 15;
    u4 q0 = *(const u4*)(stg + rr * 64 + ch * 4), q1 = *(const u4*)(stg + (rr + 4) * 64 + ch * 4);
    us* o0p = out + (size_t)(t0 + rr) * D_ + wave * 128 + ch * 8;
    if (rr != brow) asm volatile("global_store_dwordx4 %0, %1, off sc1\n\ts_nop 1" :: "v"(o0p), "v"(q0) : "memory");
    if (rr + 4 != brow) asm volatile("global_store_dwordx4 %0, %1, off sc1\n\ts_nop 1" :: "v"(o0p + 4 * D_), "v"(q1) : "memory");
  }
}

extern "C" __global__ void k9_end() {}

extern "C" void kernel_launch(void* const* d_in, const int* in_sizes, int n_in, void* d_out, int out_size,
                              void* d_ws, size_t ws_size, hipStream_t stream) {
  const us* x = (const us*)d_in[0];
  const us* Are = (const us*)d_in[1];
  const us* Aim = (const us*)d_in[2];
  const us* Bre = (const us*)d_in[3];
  const us* Bim = (const us*)d_in[4];
  const us* Cre = (const us*)d_in[5];
  const us* Cim = (const us*)d_in[6];
  const us* logdt = (const us*)d_in[7];
  const us* Dskip = (const us*)d_in[8];
  const us* wr = (const us*)d_in[10];
  const us* wg = (const us*)d_in[11];
  const us* wu = (const us*)d_in[12];
  const us* wd = (const us*)d_in[13];
  us* out = (us*)d_out;
  char* ws = (char*)d_ws;
  ka_conv<<<D_, 256, K2_LDS, stream>>>(x, Are, Aim, Bre, Bim, Cre, Cim, logdt, Dskip, wr, wg, wu, wd, out, ws);
  k3_tail<<<392, 384, 0, stream>>>(x, wr, wg, wu, wd, out, ws);
}
```

```cpp
#include <hip/hip_runtime.h>
#include <hip/hip_bf16.h>
#include <stdint.h>

typedef _Float16 h8 __attribute__((ext_vector_type(8)));
typedef _Float16 h2 __attribute__((ext_vector_type(2)));
typedef float f4 __attribute__((ext_vector_type(4)));
typedef float f2 __attribute__((ext_vector_type(2)));
typedef unsigned u4 __attribute__((ext_vector_type(4)));
typedef unsigned u2 __attribute__((ext_vector_type(2)));
typedef unsigned short us;
typedef unsigned long long u64;
#define GAS __attribute__((address_space(1)))
#define LAS __attribute__((address_space(3)))

constexpr int L_ = 2048, D_ = 640, N_ = 192, F_ = 1536;
constexpr int LP = 2048 + 64;
#define STAMPD(kid, slot) do {} while (0)
#define STAMP(kid, slot) do {} while (0)
#define STAMPT(kid, slot, thr) do {} while (0)

constexpr int NR_LEN  = 608;
constexpr int F2_OFF  = 2 * NR_LEN;
constexpr int F3_OFF  = F2_OFF + 8 * 64;
constexpr int KIMG_ELEMS = F3_OFF + 4 * 80;
constexpr int KCHUNKS = KIMG_ELEMS / 8;

constexpr size_t WS_FLAGS = 0;
constexpr size_t WS_KEYS  = WS_FLAGS;
constexpr size_t WS_ACT   = WS_FLAGS + 16384;
constexpr size_t WS_WSUM  = 40960;
constexpr size_t WS_EPOCH = 41216;
constexpr size_t WS_LNF   = 41472;
constexpr size_t WS_ACK   = WS_LNF + 32768;
constexpr size_t WS_UT    = WS_ACK + 8192;
constexpr size_t WS_YT    = WS_UT + (size_t)D_ * L_ * 2;
constexpr size_t WS_TOTAL = WS_YT + (size_t)D_ * L_ * 2;

constexpr int TAB_PITCH = 200;
constexpr int SM_KC = 57 * TAB_PITCH * 4;
constexpr int SM_U  = SM_KC + 1552;
constexpr int SM_SY = SM_U + 4608;
constexpr int K2_LDS = SM_SY + 32;
constexpr int SM_KI = 0;
constexpr int SM_P  = 4096;

constexpr int kc_of_v(int v) {
  if (v < 256)  { int h = v >> 7, r = v & 127; return ((h * 8 + (r & 7)) * 16) + (r >> 3); }
  if (v < 512)  { int w = v - 256, h = w >> 7, r = w & 127; return ((16 + h * 4 + ((r & 7) >> 1)) * 16) + (r >> 3); }
  if (v < 1024) { int w = v - 512, h = w >> 7, r = w & 127; return ((24 + h * 2 + ((r & 7) >> 2)) * 16) + (r >> 3); }
  if (v < 2048) { int w = v - 1024, h = w >> 7, r = w & 127; return ((32 + h) * 16) + (r >> 3); }
  return 40 * 16;
}
constexpr int bf16_round_int(int j) {
  if (j <= 256) return j;
  int step = j < 512 ? 2 : (j < 1024 ? 4 : 8);
  int lo = (j / step) * step, rem = j - lo;
  if (rem * 2 < step) return lo;
  if (rem * 2 > step) return lo + step;
  return ((lo / step) & 1) ? lo + step : lo;
}
constexpr int ZIDX = 768;

struct GatherTab { unsigned v[KIMG_ELEMS]; };
constexpr GatherTab make_gather() {
  GatherTab t{};
  for (int i = 0; i < KIMG_ELEMS; ++i) t.v[i] = SM_KC + ZIDX * 2;
  for (int e = 0; e < 2; ++e)
    for (int i = 0; i < NR_LEN; ++i) {
      int j = 543 - (i + e);
      if (j >= 0 && j < 515) t.v[e * NR_LEN + i] = (unsigned)(SM_KC + 2 * kc_of_v(bf16_round_int(j)));
    }
  for (int pi = 0; pi < 4; ++pi)
    for (int e = 0; e < 2; ++e)
      for (int i = 0; i < 64; ++i) {
        int p = 79 - (i + e);
        if (p >= 32 && p < 64) {
          int v = 16 * p + 8 * (pi >> 1) + ((pi & 1) ? 8 : 4);
          t.v[F2_OFF + (pi * 2 + e) * 64 + i] = (unsigned)(SM_KC + 2 * kc_of_v(v));
        }
      }
  for (int pi = 0; pi < 2; ++pi)
    for (int e = 0; e < 2; ++e)
      for (int i = 0; i < 80; ++i) {
        int qq = 127 - (i + e);
        if (qq >= 64 && qq < 128) {
          int v = 16 * qq + (pi ? 16 : 8);
          t.v[F3_OFF + (pi * 2 + e) * 80 + i] = (unsigned)(SM_KC + 2 * kc_of_v(v));
        }
      }
  return t;
}
__device__ const GatherTab g_gather = make_gather();

struct BoxTab { us v[7 * 64 * 8]; };
constexpr BoxTab make_box() {
  BoxTab t{};
  const int nn[6] = {3, 5, 3, 5, 7, 9};
  const int sh[6] = {3, 6, 11, 14, 5, 12};
  const int w0[6] = {-16, -16, -24, -24, -16, -24};
  for (int m = 0; m < 6; ++m)
    for (int lane = 0; lane < 64; ++lane)
      for (int j = 0; j < 8; ++j) {
        int b = lane & 15, k = 8 * (lane >> 4) + j;
        int idx = w0[m] + k;
        bool on = idx <= b - sh[m] && idx >= b - sh[m] - (nn[m] - 1);
        t.v[(m * 64 + lane) * 8 + j] = on ? 0x3C00 : 0;
      }
  for (int lane = 0; lane < 64; ++lane)
    for (int j = 0; j < 8; ++j) {
      int b = lane & 15, k = 8 * (lane >> 4) + j;
      t.v[(6 * 64 + lane) * 8 + j] = (k - b == 4 || k - b == 5) ? 0xFFFF : 0;
    }
  return t;
}
__device__ const BoxTab g_box = make_box();

#define RLX_AGENT __ATOMIC_RELAXED, __HIP_MEMORY_SCOPE_AGENT
constexpr unsigned SPIN_CAP = 1u << 22;
__device__ __forceinline__ float bflo(unsigned w) { return __uint_as_float(w << 16); }
__device__ __forceinline__ float bfhi(unsigned w) { return __uint_as_float(w & 0xffff0000u); }
__device__ __forceinline__ float bf1(us v) { return __uint_as_float((unsigned)v << 16); }
__device__ __forceinline__ us f2bf(float f) { __bf16 b = (__bf16)f; return __builtin_bit_cast(us, b); }
__device__ __forceinline__ float rbf(float f) { return bf1(f2bf(f)); }
typedef __bf16 b2 __attribute__((ext_vector_type(2)));
__device__ __forceinline__ unsigned pk_bf(float a, float b) {
  b2 v; v.x = (__bf16)a; v.y = (__bf16)b; return __builtin_bit_cast(unsigned, v);
}
__device__ __forceinline__ unsigned pk_h(float a, float b) {
  h2 v; v.x = (_Float16)a; v.y = (_Float16)b; return __builtin_bit_cast(unsigned, v);
}
template <int CTRL> __device__ __forceinline__ float dpp_add(float v) {
  int t = __builtin_amdgcn_update_dpp(0, __float_as_int(v), CTRL, 0xf, 0xf, true);
  return v + __int_as_float(t);
}
__device__ __forceinline__ float row_sum(float v) {
  v = dpp_add<0x121>(v); v = dpp_add<0x122>(v); v = dpp_add<0x124>(v); v = dpp_add<0x128>(v);
  return v;
}
__device__ __forceinline__ float swap32_add(float a, float b) {
  auto r = __builtin_amdgcn_permlane32_swap(__float_as_uint(a), __float_as_uint(b), false, false);
  return __uint_as_float(r[0]) + __uint_as_float(r[1]);
}
__device__ __forceinline__ float swap16_add(float a, float b) {
  auto r = __builtin_amdgcn_permlane16_swap(__float_as_uint(a), __float_as_uint(b), false, false);
  return __uint_as_float(r[0]) + __uint_as_float(r[1]);
}
__device__ __forceinline__ void wave_reduce16(const float (&v)[16], float (&out)[4]) {
  float w[8];
#pragma unroll
  for (int i = 0; i < 8; ++i) w[i] = swap32_add(v[i], v[i + 8]);
#pragma unroll
  for (int i = 0; i < 4; ++i) out[i] = row_sum(swap16_add(w[i], w[i + 4]));
}
__device__ __forceinline__ void wave_reduce8(const float (&v)[8], float (&out)[2]) {
  float w[4];
#pragma unroll
  for (int i = 0; i < 4; ++i) w[i] = swap32_add(v[i], v[i + 4]);
#pragma unroll
  for (int i = 0; i < 2; ++i) out[i] = row_sum(swap16_add(w[i], w[i + 2]));
}
__device__ __forceinline__ float wave_reduce4(const float (&v)[4]) {
  float w0 = swap32_add(v[0], v[2]), w1 = swap32_add(v[1], v[3]);
  return row_sum(swap16_add(w0, w1));
}
#define DPPA(i, st) "v_add_f32_dpp %" #i ", %" #i ", %" #i " row_ror:" #st " row_mask:0xf bank_mask:0xf\n\t"
__device__ __forceinline__ void row_sum8(float (&o)[8]) {
  asm volatile("s_nop 1\n\t"
    DPPA(0,1) DPPA(1,1) DPPA(2,1) DPPA(3,1) DPPA(4,1) DPPA(5,1) DPPA(6,1) DPPA(7,1)
    DPPA(0,2) DPPA(1,2) DPPA(2,2) DPPA(3,2) DPPA(4,2) DPPA(5,2) DPPA(6,2) DPPA(7,2)
    DPPA(0,4) DPPA(1,4) DPPA(2,4) DPPA(3,4) DPPA(4,4) DPPA(5,4) DPPA(6,4) DPPA(7,4)
    DPPA(0,8) DPPA(1,8) DPPA(2,8) DPPA(3,8) DPPA(4,8) DPPA(5,8) DPPA(6,8) DPPA(7,8)
    : "+v"(o[0]), "+v"(o[1]), "+v"(o[2]), "+v"(o[3]), "+v"(o[4]), "+v"(o[5]), "+v"(o[6]), "+v"(o[7]));
}
__device__ __forceinline__ void row_sum6(float (&o)[6]) {
  asm volatile("s_nop 1\n\t"
    DPPA(0,1) DPPA(1,1) DPPA(2,1) DPPA(3,1) DPPA(4,1) DPPA(5,1)
    DPPA(0,2) DPPA(1,2) DPPA(2,2) DPPA(3,2) DPPA(4,2) DPPA(5,2)
    DPPA(0,4) DPPA(1,4) DPPA(2,4) DPPA(3,4) DPPA(4,4) DPPA(5,4)
    DPPA(0,8) DPPA(1,8) DPPA(2,8) DPPA(3,8) DPPA(4,8) DPPA(5,8)
    : "+v"(o[0]), "+v"(o[1]), "+v"(o[2]), "+v"(o[3]), "+v"(o[4]), "+v"(o[5]));
}
__device__ __forceinline__ void row_sum4(float (&o)[4]) {
  asm volatile("s_nop 1\n\t"
    DPPA(0,1) DPPA(1,1) DPPA(2,1) DPPA(3,1)
    DPPA(0,2) DPPA(1,2) DPPA(2,2) DPPA(3,2)
    DPPA(0,4) DPPA(1,4) DPPA(2,4) DPPA(3,4)
    DPPA(0,8) DPPA(1,8) DPPA(2,8) DPPA(3,8)
    : "+v"(o[0]), "+v"(o[1]), "+v"(o[2]), "+v"(o[3]));
}
__device__ __forceinline__ void par_sum4(float (&o)[4]) {
  asm volatile("s_nop 1\n\t"
    DPPA(0,2) DPPA(1,2) DPPA(2,2) DPPA(3,2)
    DPPA(0,4) DPPA(1,4) DPPA(2,4) DPPA(3,4)
    DPPA(0,8) DPPA(1,8) DPPA(2,8) DPPA(3,8)
    : "+v"(o[0]), "+v"(o[1]), "+v"(o[2]), "+v"(o[3]));
}
__device__ __forceinline__ float rdlane(float v, int l) {
  return __int_as_float(__builtin_amdgcn_readlane(__float_as_int(v), l));
}

__device__ __forceinline__ f2 cmul(f2 a, f2 b) {
  f2 c;
  asm("v_pk_mul_f32 %0, %1, %2 op_sel:[0,0] op_sel_hi:[1,0]\n\tv_pk_fma_f32 %0, %1, %2, %0 op_sel:[1,1,0] op_sel_hi:[0,1,1] neg_lo:[1,0,0]"
      : "=&v"(c) : "v"(a), "v"(b));
  return c;
}
__device__ __forceinline__ f2 cexp_p(float lam, float rh, float rl, float p) {
  float mag = __builtin_amdgcn_exp2f(lam * p);
  float x = rh * p;
  float f = x - __builtin_rintf(x);
  f = __builtin_fmaf(rl, p, f);
  f2 w; w.x = mag * __builtin_amdgcn_cosf(f); w.y = mag * __builtin_amdgcn_sinf(f); return w;
}
#define MFMA16(a, b, c) __builtin_amdgcn_mfma_f32_16x16x32_f16((a), (b), (c), 0, 0, 0)
__device__ __forceinline__ h8 as_h8(u4 v) { return __builtin_bit_cast(h8, v); }

__device__ __forceinline__ void ln_from_sums(float s1, float s2, float piv, float& mb, float& sb) {
  float dm = s1 * (1.f / 640.f);
  float var = fmaxf(__builtin_fmaf(-dm, dm, s2 * (1.f / 640.f)), 0.f);
  mb = rbf(piv + dm);
  sb = rbf(__builtin_amdgcn_sqrtf(rbf(rbf(var) + 1e-5f)));
}
__device__ __forceinline__ f2 unpk(unsigned w) { return f2{bflo(w), bfhi(w)}; }

__device__ __forceinline__ h8 ld_h8(const char* p) { return as_h8(*(const u4*)p); }
__device__ __forceinline__ h8 ld_4dw(const char* p0, const char* p1) {
  const unsigned* a = (const unsigned*)p0; const unsigned* b = (const unsigned*)p1;
  return as_h8(u4{a[0], a[1], b[0], b[1]});
}
__device__ __forceinline__ h8 cvt_pair(f4 a, f4 b) {
  return as_h8(u4{pk_h(a[0], a[1]), pk_h(a[2], a[3]), pk_h(b[0], b[1]), pk_h(b[2], b[3])});
}
__device__ __forceinline__ void glds16(const void* g, void* l) {
  __builtin_amdgcn_global_load_lds((const GAS unsigned*)g, (LAS unsigned*)l, 16, 0, 0);
}
__device__ __forceinline__ void conv_epi(f4 acc, float sc, us* dst) {
  u2 o; o.x = pk_bf(acc[0] * sc, acc[1] * sc); o.y = pk_bf(acc[2] * sc, acc[3] * sc);
  *(u2*)dst = o;
}
#define LDU(a) ((unsigned)*(const LAS us*)(size_t)(a))
#define LDP(a, b) (LDU(a) | (LDU(b) << 16))

template <int OFF> __device__ __forceinline__ u4 lds_rd128(unsigned addr) {
  u4 v; asm volatile("ds_read_b128 %0, %1 offset:%2" : "=v"(v) : "v"(addr), "i"(OFF) : "memory"); return v;
}
template <int N> __device__ __forceinline__ void lgkm_wait() {
  asm volatile("s_waitcnt lgkmcnt(%0)" :: "i"(N) : "memory"); __builtin_amdgcn_sched_barrier(0);
}
template <int M, int M0, int M1, bool T3, int PF>
__device__ __forceinline__ void near_step(const h8 (&A)[17], u4 (&f)[PF], unsigned fa, f4& a0, f4& a1, f4& a2) {
  if constexpr (M < M1) {
    if constexpr (M >= M0 + PF) lgkm_wait<((M1 - 1 - M) < (PF - 1)) ? (M1 - 1 - M) : (PF - 1)>();
    const h8 ff = as_h8(f[(M - M0) % PF]);
    if constexpr (M <= 16) a0 = MFMA16(A[M], ff, a0);
    if constexpr (M >= 8 && M <= 24) a1 = MFMA16(A[M - 8], ff, a1);
    if constexpr (T3 && M >= 16) a2 = MFMA16(A[M - 16], ff, a2);
    __builtin_amdgcn_sched_barrier(0);
    if constexpr (M + PF < M1) f[(M - M0) % PF] = lds_rd128<(M + PF) * 64>(fa);
    near_step<M + 1, M0, M1, T3, PF>(A, f, fa, a0, a1, a2);
  }
}
template <int M, int M0, int PF> __device__ __forceinline__ void near_fill(u4 (&f)[PF], unsigned fa) {
  if constexpr (M < M0 + PF) { f[M - M0] = lds_rd128<M * 64>(fa); near_fill<M + 1, M0, PF>(f, fa); }
}
template <int M0, int M1, bool T3>
__device__ __forceinline__ void near_band(const h8 (&A)[17], unsigned fa, f4& a0, f4& a1, f4& a2) {
  constexpr int PF = 6;
  u4 f[PF];
  near_fill<M0, M0, PF>(f, fa);
  __builtin_amdgcn_s_waitcnt(0xC07F);
  __builtin_amdgcn_sched_barrier(0);
  near_step<M0, M0, M1, T3, PF>(A, f, fa, a0, a1, a2);
}

__device__ __forceinline__ void sy_arrive(char* sm, int idx, int lane) {
  asm volatile("s_waitcnt lgkmcnt(0)" ::: "memory");
  if (lane == 0) __hip_atomic_fetch_add((LAS unsigned*)(LAS char*)(sm + SM_SY) + idx, 1u, __ATOMIC_RELAXED, __HIP_MEMORY_SCOPE_WORKGROUP);
}
__device__ __forceinline__ void sy_wait(char* sm, int idx, unsigned n) {
  volatile LAS unsigned* c = (volatile LAS unsigned*)(LAS char*)(sm + SM_SY) + idx;
  while (*c < n) __builtin_amdgcn_s_sleep(0);
  asm volatile("" ::: "memory");
}

extern "C" __global__ __launch_bounds__(256, 2) void ka_conv(
    const us* __restrict__ x, const us* __restrict__ Are, const us* __restrict__ Aim, const us* __restrict__ Bre,
    const us* __restrict__ Bim, const us* __restrict__ Cre, const us* __restrict__ Cim, const us* __restrict__ logdt,
    const us* __restrict__ Dskip, const us* __restrict__ wr, const us* __restrict__ wg, const us* __restrict__ wu,
    const us* __restrict__ wd, const us* __restrict__ out, char* __restrict__ ws) {
  extern __shared__ __attribute__((aligned(16))) char sm[];
  const int tid = threadIdx.x, lane = tid & 63, wave = __builtin_amdgcn_readfirstlane(tid >> 6);
  const int bid = blockIdx.x;
  STAMP(0, 0);
  const int d = (bid & 7) * 80 + (bid >> 3);
  const int col = lane & 15, g = lane >> 4;
  char* kc = sm + SM_KC;
  const char* ub = sm + SM_U + (256 + 16 * col + 8 * g) * 2;
  us* yt = (us*)(ws + WS_YT) + ((size_t)(2 * col + (g >> 1)) * D_ + d) * 8 + (g & 1) * 4;
  constexpr int YTS = 32 * D_ * 8;
  const f4 Z = {0.f, 0.f, 0.f, 0.f};
  if (tid < 8) ((unsigned*)(sm + SM_SY))[tid] = 0;
  asm volatile("s_waitcnt lgkmcnt(0)" ::: "memory");
  u64 ep;
  us r_are, r_aim, r_bre, r_bim, r_cre, r_cim;
  const size_t pidx = (size_t)d * N_ + (tid < N_ ? tid : 0);
#define LOAD_PARAMS() do { r_are = Are[pidx]; r_aim = Aim[pidx]; r_bre = Bre[pidx]; r_bim = Bim[pidx]; r_cre = Cre[pidx]; r_cim = Cim[pidx]; \
    ep = __hip_atomic_load((GAS u64*)(ws + WS_EPOCH), RLX_AGENT); } while (0)
  unsigned selfpf = 0;
  if (bid < 256) {
    __builtin_amdgcn_s_setprio(3);
    const int tb = (bid & 7) * 32 + (bid >> 3);
    const unsigned* xa = (const unsigned*)(x + (size_t)(tb * 8 + 2 * wave) * D_);
    unsigned ra[5], rb[5];
#pragma unroll
    for (int k = 0; k < 5; ++k) { ra[k] = xa[lane + 64 * k]; rb[k] = xa[D_ / 2 + lane + 64 * k]; }
    __builtin_amdgcn_sched_barrier(0);
    LOAD_PARAMS();
    __builtin_amdgcn_sched_barrier(0);
    const float pa = bflo(xa[0]), pb = bflo(xa[D_ / 2]);
    f2 va[5], vb[5];
    f2 s1a = {0.f, 0.f}, s2a = s1a, s1b = s1a, s2b = s1a;
#pragma unroll
    for (int k = 0; k < 5; ++k) {
      va[k] = unpk(ra[k]); vb[k] = unpk(rb[k]);
      f2 da = va[k] - pa, db = vb[k] - pb;
      s1a += da; s2a = __builtin_elementwise_fma(da, da, s2a);
      s1b += db; s2b = __builtin_elementwise_fma(db, db, s2b);
    }
    float rs4 = row_sum(swap16_add(swap32_add(s1a.x + s1a.y, s2a.x + s2a.y), swap32_add(s1b.x + s1b.y, s2b.x + s2b.y)));
    const float S1 = (lane & 1) ? rdlane(rs4, 16) : rdlane(rs4, 0), S2 = (lane & 1) ? rdlane(rs4, 48) : rdlane(rs4, 32);
    float mb, sb;
    ln_from_sums(S1, S2, (lane & 1) ? pb : pa, mb, sb);
    const float aa = 128.f * __builtin_amdgcn_rcpf(sb), bb = -mb * aa;
    const float aA = rdlane(aa, 0), bA = rdlane(bb, 0), aB = rdlane(aa, 1), bB = rdlane(bb, 1);
#pragma unroll
    for (int k = 0; k < 5; ++k) {
      f2 u0 = __builtin_elementwise_fma(va[k], f2{aA, aA}, f2{bA, bA});
      f2 u1 = __builtin_elementwise_fma(vb[k], f2{aB, aB}, f2{bB, bB});
      *(u2*)(sm + wave * 2560 + (lane + 64 * k) * 8) = u2{pk_h(u0.x, u1.x), pk_h(u0.y, u1.y)};
    }
    __syncthreads();
    {
      const unsigned* tp = (const unsigned*)sm + tid;
      char* ut = ws + WS_UT + ((size_t)tb * D_ + tid) * 16;
      u4 q0 = {tp[0], tp[640], tp[1280], tp[1920]}, q1 = {tp[256], tp[896], tp[1536], tp[2176]};
      asm volatile("global_store_dwordx4 %0, %1, off sc1\n\tglobal_store_dwordx4 %2, %3, off sc1\n\ts_nop 1"
                   :: "v"(ut), "v"(q0), "v"(ut + 4096), "v"(q1) : "memory");
      if (wave < 2) {
        u4 q2 = {tp[512], tp[1152], tp[1792], tp[2432]};
        asm volatile("global_store_dwordx4 %0, %1, off sc1\n\ts_nop 1" :: "v"(ut + 8192), "v"(q2) : "memory");
      }
      asm volatile("s_waitcnt vmcnt(0)" ::: "memory");
    }
    STAMP(0, 5);
    __syncthreads();
    if (wave == 3 && lane < 16) __hip_atomic_store((GAS u64*)(ws + WS_LNF) + lane * 256 + bid, ep + 1ull, RLX_AGENT);
    __builtin_amdgcn_s_setprio(0);
  } else {
    if (wave == 3 && bid < 320) {
      u64 pc; int o1, o2;
      asm volatile("s_getpc_b64 %0\n\ts_mov_b32 %1, ka_conv@rel32@lo+4\n\ts_mov_b32 %2, k3_tail@rel32@lo+12" : "=s"(pc), "=s"(o1), "=s"(o2));
      int o = (((bid - 256) >> 3) * 64 + lane) * 64; o = o < o2 - o1 ? o : 0;
      selfpf = *(const GAS unsigned*)(pc + (long long)o1 + o);
    }
    LOAD_PARAMS();
    __builtin_amdgcn_s_barrier();
  }
  const u64 tag = ep + 1ull;
  if (wave == 3) {
    if (lane == 0) __hip_atomic_store((GAS u64*)(ws + WS_ACK) + bid, tag, RLX_AGENT);
    if (lane < 32) *(u4*)(sm + SM_U + lane * 16) = u4{0, 0, 0, 0};
    if (bid >= 256 && bid < 266) ((u4*)(ws + WS_FLAGS))[(bid - 256) * 256 + lane] = u4{0, 0, 0, 0},
        ((u4*)(ws + WS_FLAGS))[(bid - 256) * 256 + 64 + lane] = u4{0, 0, 0, 0},
        ((u4*)(ws + WS_FLAGS))[(bid - 256) * 256 + 128 + lane] = u4{0, 0, 0, 0},
        ((u4*)(ws + WS_FLAGS))[(bid - 256) * 256 + 192 + lane] = u4{0, 0, 0, 0};
    if (bid == 266) {
      const unsigned* w2 = (const unsigned*)wr + lane;
      float sw = 0.f;
#pragma unroll
      for (int k = 0; k < 5; ++k) { unsigned q = w2[64 * k]; sw += bflo(q) + bfhi(q); }
      sw = row_sum(sw);
      sw = rdlane(sw, 0) + rdlane(sw, 16) + rdlane(sw, 32) + rdlane(sw, 48);
      if (lane == 0) *(float*)(ws + WS_WSUM) = sw;
    }
    const u4* bt = (const u4*)g_box.v + lane;
    u4 bx[7];
#pragma unroll
    for (int m = 0; m < 7; ++m) bx[m] = bt[64 * m];
    {
      GAS u64* fp = (GAS u64*)(ws + WS_LNF) + (bid & 15) * 256 + lane;
      for (unsigned spins = 0;; ++spins) {
        bool ok = (__hip_atomic_load(fp, RLX_AGENT) == tag) & (__hip_atomic_load(fp + 64, RLX_AGENT) == tag) &
                  (__hip_atomic_load(fp + 128, RLX_AGENT) == tag) & (__hip_atomic_load(fp + 192, RLX_AGENT) == tag);
        if (__all(ok) || spins > SPIN_CAP) break;
        __builtin_amdgcn_s_sleep(1);
      }
    }
    STAMPT(0, 6, 192);
    {
      const char* gu = ws + WS_UT + (size_t)d * 16 + (size_t)lane * (D_ * 16);
      u4 q0, q1, q2, q3;
      asm volatile("global_load_dwordx4 %0, %4, off sc1\n\tglobal_load_dwordx4 %1, %5, off sc1\n\t"
                   "global_load_dwordx4 %2, %6, off sc1\n\tglobal_load_dwordx4 %3, %7, off sc1\n\ts_waitcnt vmcnt(0)"
                   : "=&v"(q0), "=&v"(q1), "=&v"(q2), "=&v"(q3)
                   : "v"(gu), "v"(gu + (size_t)64 * (D_ * 16)), "v"(gu + (size_t)128 * (D_ * 16)), "v"(gu + (size_t)192 * (D_ * 16)) : "memory");
      u4* dst = (u4*)(sm + SM_U + 512) + lane;
      dst[0] = q0; dst[64] = q1; dst[128] = q2; dst[192] = q3;
    }
    sy_arrive(sm, 4, lane);
    STAMPT(0, 7, 192);
    h8 sa[6], sb[6], s3[4];
#pragma unroll
    for (int tau = 0; tau < 6; ++tau) {
      h8 w1 = ld_h8(ub + (256 * tau - 16) * 2), w2 = ld_h8(ub + (256 * tau - 24) * 2);
      sa[tau] = cvt_pair(MFMA16(w1, as_h8(bx[0]), Z), MFMA16(w1, as_h8(bx[1]), Z));
      sb[tau] = cvt_pair(MFMA16(w2, as_h8(bx[2]), Z), MFMA16(w2, as_h8(bx[3]), Z));
      if (tau < 4) s3[tau] = cvt_pair(MFMA16(w1, as_h8(bx[4]), Z), MFMA16(w2, as_h8(bx[5]), Z));
    }
    u4 fs[4];
#pragma unroll
    for (int i = 0; i < 4; ++i) fs[i] = *(const u4*)(ub + (256 * (4 + i) - 1032) * 2);
#pragma unroll
    for (int i = 0; i < 6; ++i) { asm volatile("" : "+v"(sa[i]), "+v"(sb[i])); }
    sy_wait(sm, 3, 3);
    const unsigned kv = LDU(SM_KC + 32 * 16 * 2) * 0x10001u;
    f4 acc[6];
    acc[0] = Z; acc[1] = Z;
    {
      h8 As = as_h8(u4{bx[6].x & kv, bx[6].y & kv, bx[6].z & kv, bx[6].w & kv});
#pragma unroll
      for (int i = 0; i < 4; ++i) acc[2 + i] = MFMA16(As, as_h8(fs[i]), Z);
    }
    {
      const int ib = 79 - col + 4 * g;
      const char* kb = sm + SM_KI + (F2_OFF + (ib & 1) * 64 + (ib & ~1)) * 2;
#pragma unroll
      for (int dl = 2; dl <= 4; ++dl) {
        h8 tA = ld_4dw(kb - dl * 32, kb + 256 - dl * 32), tB = ld_4dw(kb + 512 - dl * 32, kb + 768 - dl * 32);
#pragma unroll
        for (int tau = 0; tau < 6; ++tau)
          if (tau + dl <= 7) { acc[tau + dl - 2] = MFMA16(sa[tau], tA, acc[tau + dl - 2]); acc[tau + dl - 2] = MFMA16(sb[tau], tB, acc[tau + dl - 2]); }
      }
    }
    {
      const int ib = 127 - col + 4 * g;
      const char* kb = sm + SM_KI + (F3_OFF + (ib & 1) * 80 + (ib & ~1)) * 2;
#pragma unroll
      for (int dl = 4; dl <= 7; ++dl) {
        h8 tt = ld_4dw(kb - dl * 32, kb + 320 - dl * 32);
#pragma unroll
        for (int tau = 0; tau < 4; ++tau)
          if (tau + dl <= 7) acc[tau + dl - 2] = MFMA16(s3[tau], tt, acc[tau + dl - 2]);
      }
    }
#pragma unroll
    for (int i = 0; i < 6; ++i) *(f4*)(sm + SM_P + i * 1024 + lane * 16) = acc[i];
    sy_arrive(sm, 5, lane);
    if (bid == 300) {
      GAS u64* ap = (GAS u64*)(ws + WS_ACK) + lane;
      for (unsigned spins = 0;; ++spins) {
        bool ok = true;
#pragma unroll
        for (int k = 0; k < 10; ++k) ok = ok & (__hip_atomic_load(ap + 64 * k, RLX_AGENT) == tag);
        if (__all(ok) || spins > SPIN_CAP) break;
        __builtin_amdgcn_s_sleep(8);
      }
      if (lane == 0) __hip_atomic_store((GAS u64*)(ws + WS_EPOCH), tag, RLX_AGENT);
    }
    asm volatile("" :: "v"(selfpf));
    return;
  }
  const unsigned ldw = ((const unsigned*)logdt)[d >> 1], dsw = ((const unsigned*)Dskip)[d >> 1];
  const float dt = rbf(__expf((d & 1) ? bfhi(ldw) : bflo(ldw)));
  const float dsk = (d & 1) ? bfhi(dsw) : bflo(dsw);
  asm volatile("s_mov_b32 m0, %0" :: "s"(wave * 256) : "memory");
  float ar = dt * bf1(r_are), ai = dt * bf1(r_aim);
  f2 dB = {dt * bf1(r_bre), dt * bf1(r_bim)};
  f2 Cc = {bf1(r_cre), bf1(r_cim)};
  f2 CB = cmul(Cc, dB);
  const float LOG2E = 1.4426950408889634f;
  const float I2PH = 0.15915494309189535f;
  const float I2PL = (float)(0.15915494309189533577 - (double)0.15915494309189535f);
  float lam = ar * LOG2E;
  float rh = ai * I2PH;
  float rl = __builtin_fmaf(ai, I2PL, __builtin_fmaf(ai, I2PH, -rh));
  {
    float gg = fmaxf(1.f, __builtin_amdgcn_exp2f(lam * 2048.f));
    float bnd = row_sum((fabsf(CB.x) + fabsf(CB.y)) * gg);
    if ((lane & 15) == 0) ((float*)(kc))[wave * 4 + (lane >> 4)] = bnd;
    sy_arrive(sm, 0, lane);
  }
  f2 w1 = cexp_p(lam, rh, rl, 1.f), w16 = cexp_p(lam, rh, rl, 16.f), w256 = cexp_p(lam, rh, rl, 256.f);
  f2 w2 = cmul(w1, w1), w32 = cmul(w16, w16), w512 = cmul(w256, w256);
  f2 w4 = cmul(w2, w2), w64 = cmul(w32, w32), w1024 = cmul(w512, w512);
  f2 w8 = cmul(w4, w4), w128 = cmul(w64, w64);
#define DSW(row, val) asm volatile("ds_write_addtid_b32 %0 offset:%1" :: "v"(val), "i"((row) * TAB_PITCH * 4) : "memory")
#define PUTE(row, c) do { f2 c_ = (c); DSW((row), pk_h(c_.x, -c_.y)); } while (0)
#define PUT(row, c) do { f2 c_ = (c); DSW(16 + (row), pk_h(c_.x, c_.y)); } while (0)
  {
    f2 e[8];
    e[1] = w8; e[2] = w16; e[3] = cmul(w8, w16); e[4] = w32;
    e[5] = cmul(w32, e[1]); e[6] = cmul(w32, e[2]); e[7] = cmul(w32, e[3]);
    DSW(0, 0x00003C00u);
#pragma unroll
    for (int l = 1; l < 8; ++l) PUTE(l, e[l]);
    PUTE(8, w64);
#pragma unroll
    for (int l = 1; l < 8; ++l) PUTE(8 + l, cmul(w64, e[l]));
  }
  sy_wait(sm, 0, 3);
  STAMP(0, 1);
  const u4* gt = (const u4*)g_gather.v + 2 * tid;
  u4 ga0 = gt[0], ga1 = gt[1], gb0, gb1;
  if (wave == 0) { gb0 = gt[384]; gb1 = gt[385]; }
  float sc, unsc;
  {
    f4 q0 = *(const f4*)(kc), q1 = *(const f4*)(kc + 16), q2 = *(const f4*)(kc + 32);
    q0 += q1; q0 += q2;
    float sig = ((q0.x + q0.y) + (q0.z + q0.w)) + fabsf(dsk);
    int ex = (int)(__float_as_uint(sig) >> 23) & 255; ex = ex < 24 ? 24 : (ex > 253 ? 253 : ex);
    sc = __uint_as_float((unsigned)(267 - ex) << 23);
    unsc = __uint_as_float((unsigned)(ex - 20) << 23);
    if (tid == 0) *(unsigned*)(kc + ZIDX * 2) = 0;
    f2 c[8];
    c[0] = CB * sc;
    c[1] = cmul(c[0], w1); c[2] = cmul(c[0], w2); c[4] = cmul(c[0], w4); c[3] = cmul(c[1], w2);
    c[5] = cmul(c[1], w4); c[6] = cmul(c[2], w4); c[7] = cmul(c[3], w4);
#pragma unroll
    for (int r = 0; r < 8; ++r) {
      PUT(r, c[r]);
      f2 ch = cmul(c[r], w128); PUT(8 + r, ch);
      if ((r & 1) == 0) {
        f2 s0 = cmul(c[r], w256), s1 = cmul(ch, w256);
        PUT(16 + (r >> 1), s0); PUT(20 + (r >> 1), s1);
        if ((r & 3) == 0) {
          const int rho = r >> 2;
          f2 z0 = cmul(c[r], w512), z1 = cmul(ch, w512), z2 = cmul(s0, w512), z3 = cmul(s1, w512);
          PUT(24 + 0 + rho, z0); PUT(24 + 2 + rho, z1); PUT(24 + 4 + rho, z2); PUT(24 + 6 + rho, z3);
          if (r == 0) {
            f2 y0 = cmul(c[0], w1024);
            PUT(32, y0); PUT(33, cmul(ch, w1024)); PUT(34, cmul(s0, w1024)); PUT(35, cmul(s1, w1024));
            PUT(36, cmul(z0, w1024)); PUT(37, cmul(z1, w1024)); PUT(38, cmul(z2, w1024)); PUT(39, cmul(z3, w1024));
            PUT(40, cmul(y0, w1024));
          }
        }
      }
    }
  }
  sy_arrive(sm, 1, lane);
  sy_wait(sm, 1, 3);
  STAMP(0, 2);
  {
    const unsigned* tab = (const unsigned*)sm;
    f4 acc0 = Z, acc1 = Z;
    const unsigned* pa = tab + col * TAB_PITCH + g * 4;
    const unsigned* pb = tab + (16 + wave * 16 + col) * TAB_PITCH + g * 4;
    u4 ea[12], pb_[12];
#pragma unroll
    for (int kk = 0; kk < 12; ++kk) { ea[kk] = *(const u4*)(pa + kk * 16); pb_[kk] = *(const u4*)(pb + kk * 16); }
#pragma unroll
    for (int kk = 0; kk < 12; kk += 2) {
      acc0 = MFMA16(as_h8(ea[kk]), as_h8(pb_[kk]), acc0);
      acc1 = MFMA16(as_h8(ea[kk + 1]), as_h8(pb_[kk + 1]), acc1);
    }
    __builtin_amdgcn_sched_group_barrier(0x100, 24, 0);
    __builtin_amdgcn_sched_group_barrier(0x008, 12, 0);
    acc0 += acc1;
    acc0[0] = __builtin_fmaf(tid == 0 ? dsk : 0.f, sc, acc0[0]);
    u2 o; o.x = pk_h(acc0[0], acc0[1]); o.y = pk_h(acc0[2], acc0[3]);
    *(u2*)(kc + ((wave * 16 + col) * 16 + 4 * g) * 2) = o;
  }
  sy_arrive(sm, 2, lane);
  sy_wait(sm, 2, 3);
  *(u4*)(sm + SM_KI + tid * 16) = u4{LDP(ga0.x, ga0.y), LDP(ga0.z, ga0.w), LDP(ga1.x, ga1.y), LDP(ga1.z, ga1.w)};
  if (wave == 0) *(u4*)(sm + SM_KI + (192 + tid) * 16) = u4{LDP(gb0.x, gb0.y), LDP(gb0.z, gb0.w), LDP(gb1.x, gb1.y), LDP(gb1.z, gb1.w)};
  sy_arrive(sm, 3, lane);
  unsigned pf;
  {
    const int xcd = bid & 7;
    const int id = (bid >> 3) * 192 + tid;
    u64 pc; int o1, o2;
    asm volatile("s_getpc_b64 %0\n\ts_mov_b32 %1, k3_tail@rel32@lo+4\n\ts_mov_b32 %2, k9_end@rel32@lo+12" : "=s"(pc), "=s"(o1), "=s"(o2));
    int r3 = id / 3, q3 = id - r3 * 3;
    const char* pp0 = (const char*)((r3 < 640 ? wg + (size_t)r3 * F_ : wu + (size_t)(r3 - 640) * F_) + xcd * 192 + q3 * 64);
    int i2 = id - 3840;
    const char* pp1 = (const char*)(wd + (size_t)(i2 >> 1) * D_ + xcd * 80 + (i2 & 1) * 78);
    int i3 = (id - 6912) * 64; i3 = (i3 >= 0 && i3 < o2 - o1) ? i3 : 0;
    const char* pp2 = (const char*)(pc + (long long)o1 + i3);
    pf = *(const GAS unsigned*)(id < 3840 ? pp0 : (id < 6912 ? pp1 : pp2));
  }
  sy_wait(sm, 3, 3);
  STAMP(0, 3);
  const int i0 = 15 - col + 8 * g;
  const char* ka = sm + SM_KI + ((i0 & 1) * NR_LEN + (i0 & ~1)) * 2;
  const int n0 = 3 * wave;
  f4 a0 = Z, a1 = Z, a2 = Z;
  const unsigned fa = (unsigned)(SM_U + (256 + 16 * col + 8 * g + 256 * n0 - 528) * 2);
  h8 A[17];
#pragma unroll
  for (int k = 0; k < 17; ++k) A[k] = ld_4dw(ka + k * 64, ka + k * 64 + 8);
  __builtin_amdgcn_sched_barrier(0);
  sy_wait(sm, 4, 1);
  if (wave == 0) near_band<9, 33, true>(A, fa, a0, a1, a2);
  else if (wave == 1) near_band<0, 33, true>(A, fa, a0, a1, a2);
  else near_band<0, 25, false>(A, fa, a0, a1, a2);
  asm volatile("s_nop 7\n\ts_nop 7" : "+v"(a0), "+v"(a1), "+v"(a2));
  sy_wait(sm, 5, 1);
  {
    const char* pp = sm + SM_P + lane * 16 + (n0 - 2) * 1024;
    if (wave != 0) { a0 += *(const f4*)(pp); a1 += *(const f4*)(pp + 1024); }
    conv_epi(a0, unsc, yt + YTS * n0);
    conv_epi(a1, unsc, yt + YTS * (n0 + 1));
    if (wave != 2) { a2 += *(const f4*)(pp + 2048); conv_epi(a2, unsc, yt + YTS * (n0 + 2)); }
  }
  asm volatile("" :: "v"(pf));
  STAMP(0, 4);
}

__device__ __forceinline__ unsigned ord16(unsigned b) { return (b & 0x8000u) ? (~b & 0xffffu) : (b | 0x8000u); }
__device__ __forceinline__ void fma8(float* acc, u4 w, float xv) {
  acc[0] = __builtin_fmaf(bflo(w.x), xv, acc[0]); acc[1] = __builtin_fmaf(bfhi(w.x), xv, acc[1]);
  acc[2] = __builtin_fmaf(bflo(w.y), xv, acc[2]); acc[3] = __builtin_fmaf(bfhi(w.y), xv, acc[3]);
  acc[4] = __builtin_fmaf(bflo(w.z), xv, acc[4]); acc[5] = __builtin_fmaf(bfhi(w.z), xv, acc[5]);
  acc[6] = __builtin_fmaf(bflo(w.w), xv, acc[6]); acc[7] = __builtin_fmaf(bfhi(w.w), xv, acc[7]);
}

__device__ __forceinline__ u64 sweep_keys(char* ws, int lane, int rep, u64* lk) {
  GAS u64* kp = (GAS u64*)(ws + WS_KEYS) + rep * 256 + lane;
  u64 b;
  for (unsigned spins = 0;; ++spins) {
    u64 k0 = __hip_atomic_load(kp, RLX_AGENT), k1 = __hip_atomic_load(kp + 64, RLX_AGENT);
    u64 k2 = __hip_atomic_load(kp + 128, RLX_AGENT), k3 = __hip_atomic_load(kp + 192, RLX_AGENT);
    bool ok = (long long)(k0 & k1 & k2 & k3) < 0;
    b = k0 > k1 ? k0 : k1; u64 c = k2 > k3 ? k2 : k3; b = b > c ? b : c;
    if (__all(ok) || spins > SPIN_CAP) {
      if (lk) { lk[lane] = k0; lk[lane + 64] = k1; lk[lane + 128] = k2; lk[lane + 192] = k3; }
      break;
    }
    __builtin_amdgcn_s_sleep(1);
  }
  unsigned hk = (unsigned)(b >> 32), m = hk;
  m = max(m, (unsigned)__builtin_amdgcn_update_dpp(0, (int)m, 0x121, 0xf, 0xf, true));
  m = max(m, (unsigned)__builtin_amdgcn_update_dpp(0, (int)m, 0x122, 0xf, 0xf, true));
  m = max(m, (unsigned)__builtin_amdgcn_update_dpp(0, (int)m, 0x124, 0xf, 0xf, true));
  m = max(m, (unsigned)__builtin_amdgcn_update_dpp(0, (int)m, 0x128, 0xf, 0xf, true));
  unsigned m0 = (unsigned)__builtin_amdgcn_readlane((int)m, 0), m1 = (unsigned)__builtin_amdgcn_readlane((int)m, 16);
  unsigned m2 = (unsigned)__builtin_amdgcn_readlane((int)m, 32), m3 = (unsigned)__builtin_amdgcn_readlane((int)m, 48);
  m0 = max(max(m0, m1), max(m2, m3));
  int src = __builtin_ctzll(__ballot(hk == m0));
  unsigned lo = (unsigned)__builtin_amdgcn_readlane((int)(unsigned)b, src);
  return ((u64)m0 << 32) | lo;
}
__device__ __forceinline__ float par_sum(float v) {
  v = dpp_add<0x122>(v); v = dpp_add<0x124>(v); v = dpp_add<0x128>(v);
  return v;
}
__device__ __forceinline__ float xs_of(us xv, us yv, float wm, float wrs, float& hh) {
  hh = rbf(bf1(xv) + bf1(yv));
  return rbf(rbf(hh - wm) * wrs);
}

extern "C" __global__ __launch_bounds__(384, 2) void k3_tail(const us* __restrict__ x, const us* __restrict__ wr,
                                                  const us* __restrict__ wg, const us* __restrict__ wu,
                                                  const us* __restrict__ wd, us* __restrict__ out,
                                                  char* __restrict__ ws) {
  __shared__ __attribute__((aligned(16))) float red[5 * 32 + 16 + 8];
  __shared__ __attribute__((aligned(16))) u64 lkeys[1280];
  unsigned* redW = (unsigned*)(red + 176);
  const int tid = threadIdx.x, lane = tid & 63, wave = __builtin_amdgcn_readfirstlane(tid >> 6);
  const int bid = blockIdx.x;
  STAMP(2, 0);
  const us* ytb = (const us*)(ws + WS_YT);
  if (bid >= 256) {
    const bool f1 = bid < 352;
    const int j = f1 ? bid - 256 : bid - 352;
    const int cb = f1 ? (j & 7) * 12 + (j >> 3) : (j & 7) * 5 + (j >> 3);
    if (wave == 5) {
      __builtin_amdgcn_s_sleep(40);
      const u64 wn = sweep_keys(ws, lane, j & 7, f1 ? lkeys : nullptr);
      const unsigned wkey = (unsigned)(wn >> 32), wms = (unsigned)wn;
      const int wrow = 2047 - (int)(wkey & 2047u);
      const int c = lane & 15;
      if (f1) {
        if (lane == 0) { redW[2] = wkey; redW[3] = wms; }
        STAMPT(2, 3, 320);
        __syncthreads();
        __syncthreads();
        const float* rp = red + (c >> 3) * 16 + (c & 7);
        float gs = (rp[0] + rp[32]) + (rp[64] + rp[96]) + rp[128];
        float us_ = (rp[8] + rp[40]) + (rp[72] + rp[104]) + rp[136];
        float gb = rbf(gs);
        float sl = rbf(gb * __builtin_amdgcn_rcpf(1.f + __expf(-gb)));
        unsigned a = (unsigned)f2bf(sl * rbf(us_)) | 0x10000u;
        __hip_atomic_store((GAS unsigned*)(ws + WS_ACT) + (lane >> 4) * F_ + cb * 16 + c, a, RLX_AGENT);
        STAMPT(2, 4, 320);
      } else {
        const int col = cb * 16 + c;
        float fh;
        float fx2 = xs_of(x[(size_t)wrow * D_ + col], ytb[((size_t)(wrow >> 3) * D_ + col) * 8 + (wrow & 7)],
                          bflo(wms), __builtin_amdgcn_rcpf(bfhi(wms)), fh);
        unsigned ob = (wkey >> 11) & 0xffffu;
        unsigned rb = (ob & 0x8000u) ? (ob & 0x7fffu) : (~ob & 0xffffu);
        float sg = rbf(__builtin_amdgcn_rcpf(1.f + __expf(-bf1((us)rb))));
        __syncthreads();
        const float* rp = red + c;
        float ys = (rp[0] + rp[16]) + (rp[32] + rp[48]) + rp[64];
        float y = rbf(rbf(ys) * sg);
        if (lane < 16) out[(size_t)wrow * D_ + col] = f2bf(fh + rbf(fx2 + y));
        STAMPT(2, 5, 320);
      }
      return;
    }
    const int half = tid & 1, rb = tid >> 1;
    __builtin_amdgcn_s_sleep(16);
    if (f1) {
      const size_t o0 = (size_t)rb * F_ + cb * 16 + half * 8;
      u4 wq[8];
#pragma unroll
      for (int k = 0; k < 4; ++k) { wq[k] = *(const u4*)(wg + o0 + (size_t)k * 160 * F_); wq[4 + k] = *(const u4*)(wu + o0 + (size_t)k * 160 * F_); }
      f2 wf[32];
#pragma unroll
      for (int k = 0; k < 8; ++k) { wf[4 * k] = unpk(wq[k].x); wf[4 * k + 1] = unpk(wq[k].y); wf[4 * k + 2] = unpk(wq[k].z); wf[4 * k + 3] = unpk(wq[k].w); }
#pragma unroll
      for (int k = 0; k < 32; ++k) asm volatile("" : "+v"(wf[k]));
      __syncthreads();
      const unsigned wkey = redW[2], wms = redW[3];
      const int wrow = 2047 - (int)(wkey & 2047u);
      const float wm = bflo(wms), wrs = __builtin_amdgcn_rcpf(bfhi(wms));
      const us* xw = x + (size_t)wrow * D_ + rb;
      const us* yw = ytb + ((size_t)(wrow >> 3) * D_ + rb) * 8 + (wrow & 7);
      us xv[4], yv[4];
#pragma unroll
      for (int k = 0; k < 4; ++k) { xv[k] = xw[160 * k]; yv[k] = yw[160 * 8 * k]; }
      f2 acc[8];
#pragma unroll
      for (int i = 0; i < 8; ++i) acc[i] = f2{0.f, 0.f};
#pragma unroll
      for (int k = 0; k < 4; ++k) {
        float hh; float xs = xs_of(xv[k], yv[k], wm, wrs, hh);
        f2 x2v = {xs, xs};
#pragma unroll
        for (int i = 0; i < 4; ++i) {
          acc[i] = __builtin_elementwise_fma(wf[4 * k + i], x2v, acc[i]);
          acc[4 + i] = __builtin_elementwise_fma(wf[16 + 4 * k + i], x2v, acc[4 + i]);
        }
      }
      float w[8], o[4];
#pragma unroll
      for (int i = 0; i < 4; ++i) { w[2 * i] = swap32_add(acc[i].x, acc[4 + i].x); w[2 * i + 1] = swap32_add(acc[i].y, acc[4 + i].y); }
#pragma unroll
      for (int i = 0; i < 4; ++i) o[i] = swap16_add(w[i], w[i + 4]);
      par_sum4(o);
      const int rr = lane >> 4;
      if ((lane & 15) < 2) *(f4*)(red + wave * 32 + half * 16 + 4 * (rr & 1) + 8 * (rr >> 1)) = f4{o[0], o[1], o[2], o[3]};
      __syncthreads();
#pragma unroll
      for (int q = 0; q < 3; ++q) {
        const int cbk = j + 96 * q;
        if (cbk < 256) {
          const u64 gk = lkeys[cbk];
          const unsigned ck = (unsigned)(gk >> 32), cms = (unsigned)gk;
          if (ck != wkey) {
            const int crow = 2047 - (int)(ck & 2047u);
            const float crs = __builtin_amdgcn_rcpf(bfhi(cms)), cb_ = -bflo(cms) * crs;
            unsigned xr = ((const unsigned*)(x + (size_t)crow * D_))[tid];
            const us* yc = ytb + ((size_t)(crow >> 3) * D_ + 2 * tid) * 8 + (crow & 7);
            f2 hs = unpk(xr) + f2{bf1(yc[0]), bf1(yc[8])};
            f2 hh = unpk(pk_bf(hs.x, hs.y));
            f2 oo = hh + __builtin_elementwise_fma(hh, f2{crs, crs}, f2{cb_, cb_});
            ((unsigned*)(out + (size_t)crow * D_))[tid] = pk_bf(oo.x, oo.y);
          }
        }
      }
      STAMP(2, 6);
    } else {
      const us* wp = wd + cb * 16 + half * 8;
      u4 wq[10];
#pragma unroll
      for (int k = 0; k < 10; ++k) { int r = rb + 160 * k; r = r < F_ ? r : F_ - 1; wq[k] = *(const u4*)(wp + (size_t)r * D_); }
      f2 wf[40];
#pragma unroll
      for (int k = 0; k < 10; ++k) { wf[4 * k] = unpk(wq[k].x); wf[4 * k + 1] = unpk(wq[k].y); wf[4 * k + 2] = unpk(wq[k].z); wf[4 * k + 3] = unpk(wq[k].w); }
#pragma unroll
      for (int k = 0; k < 40; ++k) asm volatile("" : "+v"(wf[k]));
      __builtin_amdgcn_s_sleep(30);
      GAS unsigned* ap = (GAS unsigned*)(ws + WS_ACT) + (j & 3) * F_ + rb;
      const int last = rb < 96 ? 1440 : 0;
      unsigned a[10];
      for (unsigned spins = 0;; ++spins) {
#pragma unroll
        for (int k = 0; k < 9; ++k) a[k] = __hip_atomic_load(ap + 160 * k, RLX_AGENT);
        a[9] = __hip_atomic_load(ap + last, RLX_AGENT);
        unsigned all = a[0] & a[1] & a[2] & a[3] & a[4] & a[5] & a[6] & a[7] & a[8] & a[9];
        if (__all(all >> 16) || spins > SPIN_CAP) break;
        __builtin_amdgcn_s_sleep(1);
      }
      if (rb >= 96) a[9] = 0;
      f2 acc[4] = {f2{0.f, 0.f}, f2{0.f, 0.f}, f2{0.f, 0.f}, f2{0.f, 0.f}};
#pragma unroll
      for (int k = 0; k < 10; ++k) {
        float av = bflo(a[k]); f2 a2 = {av, av};
#pragma unroll
        for (int i = 0; i < 4; ++i) acc[i] = __builtin_elementwise_fma(wf[4 * k + i], a2, acc[i]);
      }
      float w0 = swap32_add(acc[0].x, acc[2].x), w1 = swap32_add(acc[0].y, acc[2].y);
      float w2 = swap32_add(acc[1].x, acc[3].x), w3 = swap32_add(acc[1].y, acc[3].y);
      float o0 = par_sum(swap16_add(w0, w2)), o1 = par_sum(swap16_add(w1, w3));
      const int rr = lane >> 4;
      if ((lane & 15) < 2) *(f2*)(red + wave * 16 + half * 8 + 2 * (rr & 1) + 4 * (rr >> 1)) = f2{o0, o1};
      __syncthreads();
    }
    return;
  }
  float* redA = red;
  float* abv = red + 160;
  const int tb = (bid & 7) * 32 + (bid >> 3);
  const int t0 = tb * 8;
  const unsigned* xb = (const unsigned*)(x + (size_t)t0 * D_);
  if (wave == 5) {
    const int r = lane & 7;
    const float piv = bf1(x[(size_t)(t0 + r) * D_]);
    const float W = *(const float*)(ws + WS_WSUM);
    __syncthreads();
    float s1 = (redA[r] + redA[24 + r]) + (redA[48 + r] + redA[72 + r]) + redA[96 + r];
    float s2 = (redA[8 + r] + redA[32 + r]) + (redA[56 + r] + redA[80 + r]) + redA[104 + r];
    float s3 = (redA[16 + r] + redA[40 + r]) + (redA[64 + r] + redA[88 + r]) + redA[112 + r];
    float mb, sb;
    ln_from_sums(s1, s2, piv, mb, sb);
    float rs = __builtin_amdgcn_rcpf(sb);
    float rv = rs * __builtin_fmaf(piv - mb, W, s3);
    unsigned key = 0x80000000u | (ord16(f2bf(rv)) << 11) | (unsigned)(2047 - (t0 + r));
    unsigned best = key;
    best = max(best, (unsigned)__builtin_amdgcn_update_dpp(0, (int)best, 0x111, 0xf, 0xf, true));
    best = max(best, (unsigned)__builtin_amdgcn_update_dpp(0, (int)best, 0x112, 0xf, 0xf, true));
    best = max(best, (unsigned)__builtin_amdgcn_update_dpp(0, (int)best, 0x114, 0xf, 0xf, true));
    best = (unsigned)__builtin_amdgcn_readlane((int)best, 7);
    if (key == best) {
      u64 gr = ((u64)best << 32) | (u64)((__float_as_uint(mb) >> 16) | (__float_as_uint(sb) & 0xffff0000u));
      __hip_atomic_store((GAS u64*)(ws + WS_KEYS) + (lane >> 3) * 256 + bid, gr, RLX_AGENT);
      redW[0] = (unsigned)r;
    }
    if (lane < 8) *(f2*)(abv + 2 * r) = f2{rs, -mb * rs};
    STAMPT(2, 2, 320);
    __syncthreads();
    return;
  }
  unsigned raw[8];
#pragma unroll
  for (int r = 0; r < 8; ++r) raw[r] = xb[r * (D_ / 2) + tid];
  const u4 y0 = *(const u4*)(ytb + ((size_t)tb * D_ + 2 * tid) * 8);
  const u4 y1 = *(const u4*)(ytb + ((size_t)tb * D_ + 2 * tid) * 8 + 8);
  const f2 wv = unpk(((const unsigned*)wr)[tid]);
  f2 h[8];
  {
    float v[24];
#pragma unroll
    for (int r = 0; r < 8; ++r) {
      const float piv = bflo(xb[r * (D_ / 2)]);
      unsigned ya = y0[r >> 1], yb = y1[r >> 1];
      f2 yy = (r & 1) ? f2{bfhi(ya), bfhi(yb)} : f2{bflo(ya), bflo(yb)};
      f2 hs = unpk(raw[r]) + yy;
      h[r] = unpk(pk_bf(hs.x, hs.y));
      f2 dd = h[r] - piv;
      v[r] = dd.x + dd.y;
      v[r + 8] = __builtin_fmaf(dd.x, dd.x, dd.y * dd.y);
      v[r + 16] = __builtin_fmaf(dd.x, wv.x, dd.y * wv.y);
    }
    float w[12], o[6];
#pragma unroll
    for (int i = 0; i < 12; ++i) w[i] = swap32_add(v[i], v[i + 12]);
#pragma unroll
    for (int i = 0; i < 6; ++i) o[i] = swap16_add(w[i], w[i + 6]);
    row_sum6(o);
    if ((lane & 15) == 0) {
      float* dst = redA + wave * 24 + (lane >> 4) * 6;
      *(f2*)dst = f2{o[0], o[1]}; *(f2*)(dst + 2) = f2{o[2], o[3]}; *(f2*)(dst + 4) = f2{o[4], o[5]};
    }
  }
  __syncthreads();
  STAMP(2, 1);
  __syncthreads();
  const int brow = (int)redW[0];
  unsigned* stg = (unsigned*)lkeys + wave * 512;
#pragma unroll
  for (int r = 0; r < 8; r += 2) {
    f4 c = *(const f4*)(abv + 2 * r);
    f2 o0 = h[r] + __builtin_elementwise_fma(h[r], f2{c.x, c.x}, f2{c.y, c.y});
    f2 o1 = h[r + 1] + __builtin_elementwise_fma(h[r + 1], f2{c.z, c.z}, f2{c.w, c.w});
    stg[r * 64 + lane] = pk_bf(o0.x, o0.y);
    stg[(r + 1) * 64 + lane] = pk_bf(o1.x, o1.y);
  }
  {
    const int rr = lane >> 4, ch = lane & 15;
    u4 q0 = *(const u4*)(stg + rr * 64 + ch * 4), q1 = *(const u4*)(stg + (rr + 4) * 64 + ch * 4);
    us* o0p = out + (size_t)(t0 + rr) * D_ + wave * 128 + ch * 8;
    if (rr != brow) asm volatile("global_store_dwordx4 %0, %1, off sc1\n\ts_nop 1" :: "v"(o0p), "v"(q0) : "memory");
    if (rr + 4 != brow) asm volatile("global_store_dwordx4 %0, %1, off sc1\n\ts_nop 1" :: "v"(o0p + 4 * D_), "v"(q1) : "memory");
  }
}

extern "C" __global__ void k9_end() {}

extern "C" void kernel_launch(void* const* d_in, const int* in_sizes, int n_in, void* d_out, int out_size,
                              void* d_ws, size_t ws_size, hipStream_t stream) {
  const us* x = (const us*)d_in[0];
  const us* Are = (const us*)d_in[1];
  const us* Aim = (const us*)d_in[2];
  const us* Bre = (const us*)d_in[3];
  const us* Bim = (const us*)d_in[4];
  const us* Cre = (const us*)d_in[5];
  const us* Cim = (const us*)d_in[6];
  const us* logdt = (const us*)d_in[7];
  const us* Dskip = (const us*)d_in[8];
  const us* wr = (const us*)d_in[10];
  const us* wg = (const us*)d_in[11];
  const us* wu = (const us*)d_in[12];
  const us* wd = (const us*)d_in[13];
  us* out = (us*)d_out;
  char* ws = (char*)d_ws;
  ka_conv<<<D_, 256, K2_LDS, stream>>>(x, Are, Aim, Bre, Bim, Cre, Cim, logdt, Dskip, wr, wg, wu, wd, out, ws);
  k3_tail<<<392, 384, 0, stream>>>(x, wr, wg, wu, wd, out, ws);
}
```

```cpp
#include <hip/hip_runtime.h>
#include <hip/hip_bf16.h>
#include <stdint.h>

typedef _Float16 h8 __attribute__((ext_vector_type(8)));
typedef _Float16 h2 __attribute__((ext_vector_type(2)));
typedef float f4 __attribute__((ext_vector_type(4)));
typedef float f2 __attribute__((ext_vector_type(2)));
typedef unsigned u4 __attribute__((ext_vector_type(4)));
typedef unsigned u2 __attribute__((ext_vector_type(2)));
typedef unsigned short us;
typedef unsigned long long u64;
#define GAS __attribute__((address_space(1)))
#define LAS __attribute__((address_space(3)))

constexpr int L_ = 2048, D_ = 640, N_ = 192, F_ = 1536;
constexpr int LP = 2048 + 64;
#define STAMPD(kid, slot) do {} while (0)
#define STAMP(kid, slot) do {} while (0)
#define STAMPT(kid, slot, thr) do {} while (0)

constexpr int NR_LEN  = 608;
constexpr int F2_OFF  = 2 * NR_LEN;
constexpr int F3_OFF  = F2_OFF + 8 * 64;
constexpr int KIMG_ELEMS = F3_OFF + 4 * 80;
constexpr int KCHUNKS = KIMG_ELEMS / 8;

constexpr size_t WS_FLAGS = 0;
constexpr size_t WS_KEYS  = WS_FLAGS;
constexpr size_t WS_ACT   = WS_FLAGS + 16384;
constexpr size_t WS_WSUM  = 40960;
constexpr size_t WS_EPOCH = 41216;
constexpr size_t WS_LNF   = 41472;
constexpr size_t WS_ACK   = WS_LNF + 32768;
constexpr size_t WS_UT    = WS_ACK + 8192;
constexpr size_t WS_YT    = WS_UT + (size_t)D_ * L_ * 2;
constexpr size_t WS_TOTAL = WS_YT + (size_t)D_ * L_ * 2;

constexpr int TAB_PITCH = 200;
constexpr int SM_KC = 57 * TAB_PITCH * 4;
constexpr int SM_U  = SM_KC + 1552;
constexpr int SM_SY = SM_U + 4608;
constexpr int K2_LDS = SM_SY + 32;
constexpr int SM_KI = 0;
constexpr int SM_P  = 4096;

constexpr int kc_of_v(int v) {
  if (v < 256)  { int h = v >> 7, r = v & 127; return ((h * 8 + (r & 7)) * 16) + (r >> 3); }
  if (v < 512)  { int w = v - 256, h = w >> 7, r = w & 127; return ((16 + h * 4 + ((r & 7) >> 1)) * 16) + (r >> 3); }
  if (v < 1024) { int w = v - 512, h = w >> 7, r = w & 127; return ((24 + h * 2 + ((r & 7) >> 2)) * 16) + (r >> 3); }
  if (v < 2048) { int w = v - 1024, h = w >> 7, r = w & 127; return ((32 + h) * 16) + (r >> 3); }
  return 40 * 16;
}
constexpr int bf16_round_int(int j) {
  if (j <= 256) return j;
  int step = j < 512 ? 2 : (j < 1024 ? 4 : 8);
  int lo = (j / step) * step, rem = j - lo;
  if (rem * 2 < step) return lo;
  if (rem * 2 > step) return lo + step;
  return ((lo / step) & 1) ? lo + step : lo;
}
constexpr int ZIDX = 768;

struct GatherTab { unsigned v[KIMG_ELEMS]; };
constexpr GatherTab make_gather() {
  GatherTab t{};
  for (int i = 0; i < KIMG_ELEMS; ++i) t.v[i] = SM_KC + ZIDX * 2;
  for (int e = 0; e < 2; ++e)
    for (int i = 0; i < NR_LEN; ++i) {
      int j = 543 - (i + e);
      if (j >= 0 && j < 515) t.v[e * NR_LEN + i] = (unsigned)(SM_KC + 2 * kc_of_v(bf16_round_int(j)));
    }
  for (int pi = 0; pi < 4; ++pi)
    for (int e = 0; e < 2; ++e)
      for (int i = 0; i < 64; ++i) {
        int p = 79 - (i + e);
        if (p >= 32 && p < 64) {
          int v = 16 * p + 8 * (pi >> 1) + ((pi & 1) ? 8 : 4);
          t.v[F2_OFF + (pi * 2 + e) * 64 + i] = (unsigned)(SM_KC + 2 * kc_of_v(v));
        }
      }
  for (int pi = 0; pi < 2; ++pi)
    for (int e = 0; e < 2; ++e)
      for (int i = 0; i < 80; ++i) {
        int qq = 127 - (i + e);
        if (qq >= 64 && qq < 128) {
          int v = 16 * qq + (pi ? 16 : 8);
          t.v[F3_OFF + (pi * 2 + e) * 80 + i] = (unsigned)(SM_KC + 2 * kc_of_v(v));
        }
      }
  return t;
}
__device__ const GatherTab g_gather = make_gather();

struct BoxTab { us v[7 * 64 * 8]; };
constexpr BoxTab make_box() {
  BoxTab t{};
  const int nn[6] = {3, 5, 3, 5, 7, 9};
  const int sh[6] = {3, 6, 11, 14, 5, 12};
  const int w0[6] = {-16, -16, -24, -24, -16, -24};
  for (int m = 0; m < 6; ++m)
    for (int lane = 0; lane < 64; ++lane)
      for (int j = 0; j < 8; ++j) {
        int b = lane & 15, k = 8 * (lane >> 4) + j;
        int idx = w0[m] + k;
        bool on = idx <= b - sh[m] && idx >= b - sh[m] - (nn[m] - 1);
        t.v[(m * 64 + lane) * 8 + j] = on ? 0x3C00 : 0;
      }
  for (int lane = 0; lane < 64; ++lane)
    for (int j = 0; j < 8; ++j) {
      int b = lane & 15, k = 8 * (lane >> 4) + j;
      t.v[(6 * 64 + lane) * 8 + j] = (k - b == 4 || k - b == 5) ? 0xFFFF : 0;
    }
  return t;
}
__device__ const BoxTab g_box = make_box();

#define RLX_AGENT __ATOMIC_RELAXED, __HIP_MEMORY_SCOPE_AGENT
constexpr unsigned SPIN_CAP = 1u << 22;
__device__ __forceinline__ float bflo(unsigned w) { return __uint_as_float(w << 16); }
__device__ __forceinline__ float bfhi(unsigned w) { return __uint_as_float(w & 0xffff0000u); }
__device__ __forceinline__ float bf1(us v) { return __uint_as_float((unsigned)v << 16); }
__device__ __forceinline__ us f2bf(float f) { __bf16 b = (__bf16)f; return __builtin_bit_cast(us, b); }
__device__ __forceinline__ float rbf(float f) { return bf1(f2bf(f)); }
typedef __bf16 b2 __attribute__((ext_vector_type(2)));
__device__ __forceinline__ unsigned pk_bf(float a, float b) {
  b2 v; v.x = (__bf16)a; v.y = (__bf16)b; return __builtin_bit_cast(unsigned, v);
}
__device__ __forceinline__ unsigned pk_h(float a, float b) {
  h2 v; v.x = (_Float16)a; v.y = (_Float16)b; return __builtin_bit_cast(unsigned, v);
}
template <int CTRL> __device__ __forceinline__ float dpp_add(float v) {
  int t = __builtin_amdgcn_update_dpp(0, __float_as_int(v), CTRL, 0xf, 0xf, true);
  return v + __int_as_float(t);
}
__device__ __forceinline__ float row_sum(float v) {
  v = dpp_add<0x121>(v); v = dpp_add<0x122>(v); v = dpp_add<0x124>(v); v = dpp_add<0x128>(v);
  return v;
}
__device__ __forceinline__ float swap32_add(float a, float b) {
  auto r = __builtin_amdgcn_permlane32_swap(__float_as_uint(a), __float_as_uint(b), false, false);
  return __uint_as_float(r[0]) + __uint_as_float(r[1]);
}
__device__ __forceinline__ float swap16_add(float a, float b) {
  auto r = __builtin_amdgcn_permlane16_swap(__float_as_uint(a), __float_as_uint(b), false, false);
  return __uint_as_float(r[0]) + __uint_as_float(r[1]);
}
__device__ __forceinline__ void wave_reduce16(const float (&v)[16], float (&out)[4]) {
  float w[8];
#pragma unroll
  for (int i = 0; i < 8; ++i) w[i] = swap32_add(v[i], v[i + 8]);
#pragma unroll
  for (int i = 0; i < 4; ++i) out[i] = row_sum(swap16_add(w[i], w[i + 4]));
}
__device__ __forceinline__ void wave_reduce8(const float (&v)[8], float (&out)[2]) {
  float w[4];
#pragma unroll
  for (int i = 0; i < 4; ++i) w[i] = swap32_add(v[i], v[i + 4]);
#pragma unroll
  for (int i = 0; i < 2; ++i) out[i] = row_sum(swap16_add(w[i], w[i + 2]));
}
__device__ __forceinline__ float wave_reduce4(const float (&v)[4]) {
  float w0 = swap32_add(v[0], v[2]), w1 = swap32_add(v[1], v[3]);
  return row_sum(swap16_add(w0, w1));
}
#define DPPA(i, st) "v_add_f32_dpp %" #i ", %" #i ", %" #i " row_ror:" #st " row_mask:0xf bank_mask:0xf\n\t"
__device__ __forceinline__ void row_sum8(float (&o)[8]) {
  asm volatile("s_nop 1\n\t"
    DPPA(0,1) DPPA(1,1) DPPA(2,1) DPPA(3,1) DPPA(4,1) DPPA(5,1) DPPA(6,1) DPPA(7,1)
    DPPA(0,2) DPPA(1,2) DPPA(2,2) DPPA(3,2) DPPA(4,2) DPPA(5,2) DPPA(6,2) DPPA(7,2)
    DPPA(0,4) DPPA(1,4) DPPA(2,4) DPPA(3,4) DPPA(4,4) DPPA(5,4) DPPA(6,4) DPPA(7,4)
    DPPA(0,8) DPPA(1,8) DPPA(2,8) DPPA(3,8) DPPA(4,8) DPPA(5,8) DPPA(6,8) DPPA(7,8)
    : "+v"(o[0]), "+v"(o[1]), "+v"(o[2]), "+v"(o[3]), "+v"(o[4]), "+v"(o[5]), "+v"(o[6]), "+v"(o[7]));
}
__device__ __forceinline__ void row_sum6(float (&o)[6]) {
  asm volatile("s_nop 1\n\t"
    DPPA(0,1) DPPA(1,1) DPPA(2,1) DPPA(3,1) DPPA(4,1) DPPA(5,1)
    DPPA(0,2) DPPA(1,2) DPPA(2,2) DPPA(3,2) DPPA(4,2) DPPA(5,2)
    DPPA(0,4) DPPA(1,4) DPPA(2,4) DPPA(3,4) DPPA(4,4) DPPA(5,4)
    DPPA(0,8) DPPA(1,8) DPPA(2,8) DPPA(3,8) DPPA(4,8) DPPA(5,8)
    : "+v"(o[0]), "+v"(o[1]), "+v"(o[2]), "+v"(o[3]), "+v"(o[4]), "+v"(o[5]));
}
__device__ __forceinline__ void row_sum4(float (&o)[4]) {
  asm volatile("s_nop 1\n\t"
    DPPA(0,1) DPPA(1,1) DPPA(2,1) DPPA(3,1)
    DPPA(0,2) DPPA(1,2) DPPA(2,2) DPPA(3,2)
    DPPA(0,4) DPPA(1,4) DPPA(2,4) DPPA(3,4)
    DPPA(0,8) DPPA(1,8) DPPA(2,8) DPPA(3,8)
    : "+v"(o[0]), "+v"(o[1]), "+v"(o[2]), "+v"(o[3]));
}
__device__ __forceinline__ void par_sum4(float (&o)[4]) {
  asm volatile("s_nop 1\n\t"
    DPPA(0,2) DPPA(1,2) DPPA(2,2) DPPA(3,2)
    DPPA(0,4) DPPA(1,4) DPPA(2,4) DPPA(3,4)
    DPPA(0,8) DPPA(1,8) DPPA(2,8) DPPA(3,8)
    : "+v"(o[0]), "+v"(o[1]), "+v"(o[2]), "+v"(o[3]));
}
__device__ __forceinline__ float rdlane(float v, int l) {
  return __int_as_float(__builtin_amdgcn_readlane(__float_as_int(v), l));
}

__device__ __forceinline__ f2 cmul(f2 a, f2 b) {
  f2 c;
  asm("v_pk_mul_f32 %0, %1, %2 op_sel:[0,0] op_sel_hi:[1,0]\n\tv_pk_fma_f32 %0, %1, %2, %0 op_sel:[1,1,0] op_sel_hi:[0,1,1] neg_lo:[1,0,0]"
      : "=&v"(c) : "v"(a), "v"(b));
  return c;
}
__device__ __forceinline__ f2 cexp_p(float lam, float rh, float rl, float p) {
  float mag = __builtin_amdgcn_exp2f(lam * p);
  float x = rh * p;
  float f = x - __builtin_rintf(x);
  f = __builtin_fmaf(rl, p, f);
  f2 w; w.x = mag * __builtin_amdgcn_cosf(f); w.y = mag * __builtin_amdgcn_sinf(f); return w;
}
#define MFMA16(a, b, c) __builtin_amdgcn_mfma_f32_16x16x32_f16((a), (b), (c), 0, 0, 0)
__device__ __forceinline__ h8 as_h8(u4 v) { return __builtin_bit_cast(h8, v); }

__device__ __forceinline__ void ln_from_sums(float s1, float s2, float piv, float& mb, float& sb) {
  float dm = s1 * (1.f / 640.f);
  float var = fmaxf(__builtin_fmaf(-dm, dm, s2 * (1.f / 640.f)), 0.f);
  mb = rbf(piv + dm);
  sb = rbf(__builtin_amdgcn_sqrtf(rbf(rbf(var) + 1e-5f)));
}
__device__ __forceinline__ f2 unpk(unsigned w) { return f2{bflo(w), bfhi(w)}; }

__device__ __forceinline__ h8 ld_h8(const char* p) { return as_h8(*(const u4*)p); }
__device__ __forceinline__ h8 ld_4dw(const char* p0, const char* p1) {
  const unsigned* a = (const unsigned*)p0; const unsigned* b = (const unsigned*)p1;
  return as_h8(u4{a[0], a[1], b[0], b[1]});
}
__device__ __forceinline__ h8 cvt_pair(f4 a, f4 b) {
  return as_h8(u4{pk_h(a[0], a[1]), pk_h(a[2], a[3]), pk_h(b[0], b[1]), pk_h(b[2], b[3])});
}
__device__ __forceinline__ void glds16(const void* g, void* l) {
  __builtin_amdgcn_global_load_lds((const GAS unsigned*)g, (LAS unsigned*)l, 16, 0, 0);
}
__device__ __forceinline__ void conv_epi(f4 acc, float sc, us* dst) {
  u2 o; o.x = pk_bf(acc[0] * sc, acc[1] * sc); o.y = pk_bf(acc[2] * sc, acc[3] * sc);
  *(u2*)dst = o;
}
#define LDU(a) ((unsigned)*(const LAS us*)(size_t)(a))
#define LDP(a, b) (LDU(a) | (LDU(b) << 16))

template <int OFF> __device__ __forceinline__ u4 lds_rd128(unsigned addr) {
  u4 v; asm volatile("ds_read_b128 %0, %1 offset:%2" : "=v"(v) : "v"(addr), "i"(OFF) : "memory"); return v;
}
template <int N> __device__ __forceinline__ void lgkm_wait() {
  asm volatile("s_waitcnt lgkmcnt(%0)" :: "i"(N) : "memory"); __builtin_amdgcn_sched_barrier(0);
}
template <int M, int M0, int M1, bool T3, int PF>
__device__ __forceinline__ void near_step(const h8 (&A)[17], u4 (&f)[PF], unsigned fa, f4& a0, f4& a1, f4& a2) {
  if constexpr (M < M1) {
    if constexpr (M >= M0 + PF) lgkm_wait<((M1 - 1 - M) < (PF - 1)) ? (M1 - 1 - M) : (PF - 1)>();
    const h8 ff = as_h8(f[(M - M0) % PF]);
    if constexpr (M <= 16) a0 = MFMA16(A[M], ff, a0);
    if constexpr (M >= 8 && M <= 24) a1 = MFMA16(A[M - 8], ff, a1);
    if constexpr (T3 && M >= 16) a2 = MFMA16(A[M - 16], ff, a2);
    __builtin_amdgcn_sched_barrier(0);
    if constexpr (M + PF < M1) f[(M - M0) % PF] = lds_rd128<(M + PF) * 64>(fa);
    near_step<M + 1, M0, M1, T3, PF>(A, f, fa, a0, a1, a2);
  }
}
template <int M, int M0, int PF> __device__ __forceinline__ void near_fill(u4 (&f)[PF], unsigned fa) {
  if constexpr (M < M0 + PF) { f[M - M0] = lds_rd128<M * 64>(fa); near_fill<M + 1, M0, PF>(f, fa); }
}
template <int M0, int M1, bool T3>
__device__ __forceinline__ void near_band(const h8 (&A)[17], unsigned fa, f4& a0, f4& a1, f4& a2) {
  constexpr int PF = 6;
  u4 f[PF];
  near_fill<M0, M0, PF>(f, fa);
  __builtin_amdgcn_s_waitcnt(0xC07F);
  __builtin_amdgcn_sched_barrier(0);
  near_step<M0, M0, M1, T3, PF>(A, f, fa, a0, a1, a2);
}

__device__ __forceinline__ void sy_arrive(char* sm, int idx, int lane) {
  asm volatile("s_waitcnt lgkmcnt(0)" ::: "memory");
  if (lane == 0) __hip_atomic_fetch_add((LAS unsigned*)(LAS char*)(sm + SM_SY) + idx, 1u, __ATOMIC_RELAXED, __HIP_MEMORY_SCOPE_WORKGROUP);
}
__device__ __forceinline__ void sy_wait(char* sm, int idx, unsigned n) {
  volatile LAS unsigned* c = (volatile LAS unsigned*)(LAS char*)(sm + SM_SY) + idx;
  while (*c < n) __builtin_amdgcn_s_sleep(0);
  asm volatile("" ::: "memory");
}

extern "C" __global__ __launch_bounds__(256, 2) void ka_conv(
    const us* __restrict__ x, const us* __restrict__ Are, const us* __restrict__ Aim, const us* __restrict__ Bre,
    const us* __restrict__ Bim, const us* __restrict__ Cre, const us* __restrict__ Cim, const us* __restrict__ logdt,
    const us* __restrict__ Dskip, const us* __restrict__ wr, const us* __restrict__ wg, const us* __restrict__ wu,
    const us* __restrict__ wd, const us* __restrict__ out, char* __restrict__ ws) {
  extern __shared__ __attribute__((aligned(16))) char sm[];
  const int tid = threadIdx.x, lane = tid & 63, wave = __builtin_amdgcn_readfirstlane(tid >> 6);
  const int bid = blockIdx.x;
  STAMP(0, 0);
  const int d = (bid & 7) * 80 + (bid >> 3);
  const int col = lane & 15, g = lane >> 4;
  char* kc = sm + SM_KC;
  const char* ub = sm + SM_U + (256 + 16 * col + 8 * g) * 2;
  us* yt = (us*)(ws + WS_YT) + ((size_t)(2 * col + (g >> 1)) * D_ + d) * 8 + (g & 1) * 4;
  constexpr int YTS = 32 * D_ * 8;
  const f4 Z = {0.f, 0.f, 0.f, 0.f};
  if (tid < 8) ((unsigned*)(sm + SM_SY))[tid] = 0;
  asm volatile("s_waitcnt lgkmcnt(0)" ::: "memory");
  u64 ep;
  us r_are, r_aim, r_bre, r_bim, r_cre, r_cim;
  const size_t pidx = (size_t)d * N_ + (tid < N_ ? tid : 0);
#define LOAD_PARAMS() do { r_are = Are[pidx]; r_aim = Aim[pidx]; r_bre = Bre[pidx]; r_bim = Bim[pidx]; r_cre = Cre[pidx]; r_cim = Cim[pidx]; \
    ep = __hip_atomic_load((GAS u64*)(ws + WS_EPOCH), RLX_AGENT); } while (0)
  unsigned selfpf = 0;
  if (bid < 256) {
    __builtin_amdgcn_s_setprio(3);
    const int tb = (bid & 7) * 32 + (bid >> 3);
    const unsigned* xa = (const unsigned*)(x + (size_t)(tb * 8 + 2 * wave) * D_);
    unsigned ra[5], rb[5];
#pragma unroll
    for (int k = 0; k < 5; ++k) { ra[k] = xa[lane + 64 * k]; rb[k] = xa[D_ / 2 + lane + 64 * k]; }
    __builtin_amdgcn_sched_barrier(0);
    selfpf = *(const GAS unsigned*)(ws + (lane == 0 ? WS_UT + (size_t)tb * D_ * 16 + wave * 2560 : (lane == 1 ? WS_LNF + wave * 8192 : WS_YT + (size_t)(lane & 31) * 81920 + wave * 4)));
    LOAD_PARAMS();
    __builtin_amdgcn_sched_barrier(0);
    const float pa = bflo(xa[0]), pb = bflo(xa[D_ / 2]);
    f2 va[5], vb[5];
    f2 s1a = {0.f, 0.f}, s2a = s1a, s1b = s1a, s2b = s1a;
#pragma unroll
    for (int k = 0; k < 5; ++k) {
      va[k] = unpk(ra[k]); vb[k] = unpk(rb[k]);
      f2 da = va[k] - pa, db = vb[k] - pb;
      s1a += da; s2a = __builtin_elementwise_fma(da, da, s2a);
      s1b += db; s2b = __builtin_elementwise_fma(db, db, s2b);
    }
    float rs4 = row_sum(swap16_add(swap32_add(s1a.x + s1a.y, s2a.x + s2a.y), swap32_add(s1b.x + s1b.y, s2b.x + s2b.y)));
    const float S1 = (lane & 1) ? rdlane(rs4, 16) : rdlane(rs4, 0), S2 = (lane & 1) ? rdlane(rs4, 48) : rdlane(rs4, 32);
    float mb, sb;
    ln_from_sums(S1, S2, (lane & 1) ? pb : pa, mb, sb);
    const float aa = 128.f * __builtin_amdgcn_rcpf(sb), bb = -mb * aa;
    const float aA = rdlane(aa, 0), bA = rdlane(bb, 0), aB = rdlane(aa, 1), bB = rdlane(bb, 1);
#pragma unroll
    for (int k = 0; k < 5; ++k) {
      f2 u0 = __builtin_elementwise_fma(va[k], f2{aA, aA}, f2{bA, bA});
      f2 u1 = __builtin_elementwise_fma(vb[k], f2{aB, aB}, f2{bB, bB});
      *(u2*)(sm + wave * 2560 + (lane + 64 * k) * 8) = u2{pk_h(u0.x, u1.x), pk_h(u0.y, u1.y)};
    }
    __syncthreads();
    {
      const unsigned* tp = (const unsigned*)sm + tid;
      char* ut = ws + WS_UT + ((size_t)tb * D_ + tid) * 16;
      u4 q0 = {tp[0], tp[640], tp[1280], tp[1920]}, q1 = {tp[256], tp[896], tp[1536], tp[2176]};
      asm volatile("global_store_dwordx4 %0, %1, off sc1\n\tglobal_store_dwordx4 %2, %3, off sc1\n\ts_nop 1"
                   :: "v"(ut), "v"(q0), "v"(ut + 4096), "v"(q1) : "memory");
      if (wave < 2) {
        u4 q2 = {tp[512], tp[1152], tp[1792], tp[2432]};
        asm volatile("global_store_dwordx4 %0, %1, off sc1\n\ts_nop 1" :: "v"(ut + 8192), "v"(q2) : "memory");
      }
      asm volatile("s_waitcnt vmcnt(0)" ::: "memory");
    }
    STAMP(0, 5);
    __syncthreads();
    if (wave == 3 && lane < 16) __hip_atomic_store((GAS u64*)(ws + WS_LNF) + lane * 256 + bid, ep + 1ull, RLX_AGENT);
    __builtin_amdgcn_s_setprio(0);
  } else {
    if (wave == 3 && bid < 320) {
      u64 pc; int o1, o2;
      asm volatile("s_getpc_b64 %0\n\ts_mov_b32 %1, ka_conv@rel32@lo+4\n\ts_mov_b32 %2, k3_tail@rel32@lo+12" : "=s"(pc), "=s"(o1), "=s"(o2));
      int o = (((bid - 256) >> 3) * 64 + lane) * 64; o = o < o2 - o1 ? o : 0;
      selfpf = *(const GAS unsigned*)(pc + (long long)o1 + o);
    }
    LOAD_PARAMS();
    __builtin_amdgcn_s_barrier();
  }
  const u64 tag = ep + 1ull;
  if (wave == 3) {
    __builtin_amdgcn_s_setprio(3);
    if (lane == 0) __hip_atomic_store((GAS u64*)(ws + WS_ACK) + bid, tag, RLX_AGENT);
    if (lane < 32) *(u4*)(sm + SM_U + lane * 16) = u4{0, 0, 0, 0};
    if (bid >= 256 && bid < 266) ((u4*)(ws + WS_FLAGS))[(bid - 256) * 256 + lane] = u4{0, 0, 0, 0},
        ((u4*)(ws + WS_FLAGS))[(bid - 256) * 256 + 64 + lane] = u4{0, 0, 0, 0},
        ((u4*)(ws + WS_FLAGS))[(bid - 256) * 256 + 128 + lane] = u4{0, 0, 0, 0},
        ((u4*)(ws + WS_FLAGS))[(bid - 256) * 256 + 192 + lane] = u4{0, 0, 0, 0};
    if (bid == 266) {
      const unsigned* w2 = (const unsigned*)wr + lane;
      float sw = 0.f;
#pragma unroll
      for (int k = 0; k < 5; ++k) { unsigned q = w2[64 * k]; sw += bflo(q) + bfhi(q); }
      sw = row_sum(sw);
      sw = rdlane(sw, 0) + rdlane(sw, 16) + rdlane(sw, 32) + rdlane(sw, 48);
      if (lane == 0) *(float*)(ws + WS_WSUM) = sw;
    }
    const u4* bt = (const u4*)g_box.v + lane;
    u4 bx[7];
#pragma unroll
    for (int m = 0; m < 7; ++m) bx[m] = bt[64 * m];
    {
      GAS u64* fp = (GAS u64*)(ws + WS_LNF) + (bid & 15) * 256 + lane;
#define POLL0 50
#define POLLS 5
#define POLLA 8
      if (bid >= 256) __builtin_amdgcn_s_sleep(POLL0);
      for (unsigned spins = 0;; ++spins) {
        bool ok = __hip_atomic_load(fp + ((bid >> 4) & 3) * 64, RLX_AGENT) == tag;
        if (__all(ok) || spins > SPIN_CAP) break;
        __builtin_amdgcn_s_sleep(POLLA);
      }
      for (unsigned spins = 0;; ++spins) {
        bool ok = (__hip_atomic_load(fp, RLX_AGENT) == tag) & (__hip_atomic_load(fp + 64, RLX_AGENT) == tag) &
                  (__hip_atomic_load(fp + 128, RLX_AGENT) == tag) & (__hip_atomic_load(fp + 192, RLX_AGENT) == tag);
        if (__all(ok) || spins > SPIN_CAP) break;
        __builtin_amdgcn_s_sleep(POLLS);
      }
    }
    STAMPT(0, 6, 192);
    {
      const char* gu = ws + WS_UT + (size_t)d * 16 + (size_t)lane * (D_ * 16);
      u4 q0, q1, q2, q3;
      asm volatile("global_load_dwordx4 %0, %4, off sc1\n\tglobal_load_dwordx4 %1, %5, off sc1\n\t"
                   "global_load_dwordx4 %2, %6, off sc1\n\tglobal_load_dwordx4 %3, %7, off sc1\n\ts_waitcnt vmcnt(0)"
                   : "=&v"(q0), "=&v"(q1), "=&v"(q2), "=&v"(q3)
                   : "v"(gu), "v"(gu + (size_t)64 * (D_ * 16)), "v"(gu + (size_t)128 * (D_ * 16)), "v"(gu + (size_t)192 * (D_ * 16)) : "memory");
      u4* dst = (u4*)(sm + SM_U + 512) + lane;
      dst[0] = q0; dst[64] = q1; dst[128] = q2; dst[192] = q3;
    }
    sy_arrive(sm, 4, lane);
    __builtin_amdgcn_s_setprio(0);
    STAMPT(0, 7, 192);
    h8 sa[6], sb[6], s3[4];
#pragma unroll
    for (int tau = 0; tau < 6; ++tau) {
      h8 w1 = ld_h8(ub + (256 * tau - 16) * 2), w2 = ld_h8(ub + (256 * tau - 24) * 2);
      sa[tau] = cvt_pair(MFMA16(w1, as_h8(bx[0]), Z), MFMA16(w1, as_h8(bx[1]), Z));
      sb[tau] = cvt_pair(MFMA16(w2, as_h8(bx[2]), Z), MFMA16(w2, as_h8(bx[3]), Z));
      if (tau < 4) s3[tau] = cvt_pair(MFMA16(w1, as_h8(bx[4]), Z), MFMA16(w2, as_h8(bx[5]), Z));
    }
    u4 fs[4];
#pragma unroll
    for (int i = 0; i < 4; ++i) fs[i] = *(const u4*)(ub + (256 * (4 + i) - 1032) * 2);
#pragma unroll
    for (int i = 0; i < 6; ++i) { asm volatile("" : "+v"(sa[i]), "+v"(sb[i])); }
    sy_wait(sm, 3, 3);
    const unsigned kv = LDU(SM_KC + 32 * 16 * 2) * 0x10001u;
    f4 acc[6];
    acc[0] = Z; acc[1] = Z;
    {
      h8 As = as_h8(u4{bx[6].x & kv, bx[6].y & kv, bx[6].z & kv, bx[6].w & kv});
#pragma unroll
      for (int i = 0; i < 4; ++i) acc[2 + i] = MFMA16(As, as_h8(fs[i]), Z);
    }
    {
      const int ib = 79 - col + 4 * g;
      const char* kb = sm + SM_KI + (F2_OFF + (ib & 1) * 64 + (ib & ~1)) * 2;
#pragma unroll
      for (int dl = 2; dl <= 4; ++dl) {
        h8 tA = ld_4dw(kb - dl * 32, kb + 256 - dl * 32), tB = ld_4dw(kb + 512 - dl * 32, kb + 768 - dl * 32);
#pragma unroll
        for (int tau = 0; tau < 6; ++tau)
          if (tau + dl <= 7) { acc[tau + dl - 2] = MFMA16(sa[tau], tA, acc[tau + dl - 2]); acc[tau + dl - 2] = MFMA16(sb[tau], tB, acc[tau + dl - 2]); }
      }
    }
    {
      const int ib = 127 - col + 4 * g;
      const char* kb = sm + SM_KI + (F3_OFF + (ib & 1) * 80 + (ib & ~1)) * 2;
#pragma unroll
      for (int dl = 4; dl <= 7; ++dl) {
        h8 tt = ld_4dw(kb - dl * 32, kb + 320 - dl * 32);
#pragma unroll
        for (int tau = 0; tau < 4; ++tau)
          if (tau + dl <= 7) acc[tau + dl - 2] = MFMA16(s3[tau], tt, acc[tau + dl - 2]);
      }
    }
#pragma unroll
    for (int i = 0; i < 6; ++i) *(f4*)(sm + SM_P + i * 1024 + lane * 16) = acc[i];
    sy_arrive(sm, 5, lane);
    if (bid == 300) {
      GAS u64* ap = (GAS u64*)(ws + WS_ACK) + lane;
      for (unsigned spins = 0;; ++spins) {
        bool ok = true;
#pragma unroll
        for (int k = 0; k < 10; ++k) ok = ok & (__hip_atomic_load(ap + 64 * k, RLX_AGENT) == tag);
        if (__all(ok) || spins > SPIN_CAP) break;
        __builtin_amdgcn_s_sleep(8);
      }
      if (lane == 0) __hip_atomic_store((GAS u64*)(ws + WS_EPOCH), tag, RLX_AGENT);
    }
    asm volatile("" :: "v"(selfpf));
    return;
  }
  const unsigned ldw = ((const unsigned*)logdt)[d >> 1], dsw = ((const unsigned*)Dskip)[d >> 1];
  const float dt = rbf(__expf((d & 1) ? bfhi(ldw) : bflo(ldw)));
  const float dsk = (d & 1) ? bfhi(dsw) : bflo(dsw);
  asm volatile("s_mov_b32 m0, %0" :: "s"(wave * 256) : "memory");
  float ar = dt * bf1(r_are), ai = dt * bf1(r_aim);
  f2 dB = {dt * bf1(r_bre), dt * bf1(r_bim)};
  f2 Cc = {bf1(r_cre), bf1(r_cim)};
  f2 CB = cmul(Cc, dB);
  const float LOG2E = 1.4426950408889634f;
  const float I2PH = 0.15915494309189535f;
  const float I2PL = (float)(0.15915494309189533577 - (double)0.15915494309189535f);
  float lam = ar * LOG2E;
  float rh = ai * I2PH;
  float rl = __builtin_fmaf(ai, I2PL, __builtin_fmaf(ai, I2PH, -rh));
  {
    float gg = fmaxf(1.f, __builtin_amdgcn_exp2f(lam * 2048.f));
    float bnd = row_sum((fabsf(CB.x) + fabsf(CB.y)) * gg);
    if ((lane & 15) == 0) ((float*)(kc))[wave * 4 + (lane >> 4)] = bnd;
    sy_arrive(sm, 0, lane);
  }
  f2 w1 = cexp_p(lam, rh, rl, 1.f), w16 = cexp_p(lam, rh, rl, 16.f), w256 = cexp_p(lam, rh, rl, 256.f);
  f2 w2 = cmul(w1, w1), w32 = cmul(w16, w16), w512 = cmul(w256, w256);
  f2 w4 = cmul(w2, w2), w64 = cmul(w32, w32), w1024 = cmul(w512, w512);
  f2 w8 = cmul(w4, w4), w128 = cmul(w64, w64);
#define DSW(row, val) asm volatile("ds_write_addtid_b32 %0 offset:%1" :: "v"(val), "i"((row) * TAB_PITCH * 4) : "memory")
#define PUTE(row, c) do { f2 c_ = (c); DSW((row), pk_h(c_.x, -c_.y)); } while (0)
#define PUT(row, c) do { f2 c_ = (c); DSW(16 + (row), pk_h(c_.x, c_.y)); } while (0)
  {
    f2 e[8];
    e[1] = w8; e[2] = w16; e[3] = cmul(w8, w16); e[4] = w32;
    e[5] = cmul(w32, e[1]); e[6] = cmul(w32, e[2]); e[7] = cmul(w32, e[3]);
    DSW(0, 0x00003C00u);
#pragma unroll
    for (int l = 1; l < 8; ++l) PUTE(l, e[l]);
    PUTE(8, w64);
#pragma unroll
    for (int l = 1; l < 8; ++l) PUTE(8 + l, cmul(w64, e[l]));
  }
  sy_wait(sm, 0, 3);
  STAMP(0, 1);
  const u4* gt = (const u4*)g_gather.v + 2 * tid;
  u4 ga0 = gt[0], ga1 = gt[1], gb0, gb1;
  if (wave == 0) { gb0 = gt[384]; gb1 = gt[385]; }
  float sc, unsc;
  {
    f4 q0 = *(const f4*)(kc), q1 = *(const f4*)(kc + 16), q2 = *(const f4*)(kc + 32);
    q0 += q1; q0 += q2;
    float sig = ((q0.x + q0.y) + (q0.z + q0.w)) + fabsf(dsk);
    int ex = (int)(__float_as_uint(sig) >> 23) & 255; ex = ex < 24 ? 24 : (ex > 253 ? 253 : ex);
    sc = __uint_as_float((unsigned)(267 - ex) << 23);
    unsc = __uint_as_float((unsigned)(ex - 20) << 23);
    if (tid == 0) *(unsigned*)(kc + ZIDX * 2) = 0;
    f2 c[8];
    c[0] = CB * sc;
    c[1] = cmul(c[0], w1); c[2] = cmul(c[0], w2); c[4] = cmul(c[0], w4); c[3] = cmul(c[1], w2);
    c[5] = cmul(c[1], w4); c[6] = cmul(c[2], w4); c[7] = cmul(c[3], w4);
#pragma unroll
    for (int r = 0; r < 8; ++r) {
      PUT(r, c[r]);
      f2 ch = cmul(c[r], w128); PUT(8 + r, ch);
      if ((r & 1) == 0) {
        f2 s0 = cmul(c[r], w256), s1 = cmul(ch, w256);
        PUT(16 + (r >> 1), s0); PUT(20 + (r >> 1), s1);
        if ((r & 3) == 0) {
          const int rho = r >> 2;
          f2 z0 = cmul(c[r], w512), z1 = cmul(ch, w512), z2 = cmul(s0, w512), z3 = cmul(s1, w512);
          PUT(24 + 0 + rho, z0); PUT(24 + 2 + rho, z1); PUT(24 + 4 + rho, z2); PUT(24 + 6 + rho, z3);
          if (r == 0) {
            f2 y0 = cmul(c[0], w1024);
            PUT(32, y0); PUT(33, cmul(ch, w1024)); PUT(34, cmul(s0, w1024)); PUT(35, cmul(s1, w1024));
            PUT(36, cmul(z0, w1024)); PUT(37, cmul(z1, w1024)); PUT(38, cmul(z2, w1024)); PUT(39, cmul(z3, w1024));
            PUT(40, cmul(y0, w1024));
          }
        }
      }
    }
  }
  sy_arrive(sm, 1, lane);
  sy_wait(sm, 1, 3);
  STAMP(0, 2);
  {
    const unsigned* tab = (const unsigned*)sm;
    f4 acc0 = Z, acc1 = Z;
    const unsigned* pa = tab + col * TAB_PITCH + g * 4;
    const unsigned* pb = tab + (16 + wave * 16 + col) * TAB_PITCH + g * 4;
    u4 ea[12], pb_[12];
#pragma unroll
    for (int kk = 0; kk < 12; ++kk) { ea[kk] = *(const u4*)(pa + kk * 16); pb_[kk] = *(const u4*)(pb + kk * 16); }
#pragma unroll
    for (int kk = 0; kk < 12; kk += 2) {
      acc0 = MFMA16(as_h8(ea[kk]), as_h8(pb_[kk]), acc0);
      acc1 = MFMA16(as_h8(ea[kk + 1]), as_h8(pb_[kk + 1]), acc1);
    }
    __builtin_amdgcn_sched_group_barrier(0x100, 24, 0);
    __builtin_amdgcn_sched_group_barrier(0x008, 12, 0);
    acc0 += acc1;
    acc0[0] = __builtin_fmaf(tid == 0 ? dsk : 0.f, sc, acc0[0]);
    u2 o; o.x = pk_h(acc0[0], acc0[1]); o.y = pk_h(acc0[2], acc0[3]);
    *(u2*)(kc + ((wave * 16 + col) * 16 + 4 * g) * 2) = o;
  }
  sy_arrive(sm, 2, lane);
  sy_wait(sm, 2, 3);
  *(u4*)(sm + SM_KI + tid * 16) = u4{LDP(ga0.x, ga0.y), LDP(ga0.z, ga0.w), LDP(ga1.x, ga1.y), LDP(ga1.z, ga1.w)};
  if (wave == 0) *(u4*)(sm + SM_KI + (192 + tid) * 16) = u4{LDP(gb0.x, gb0.y), LDP(gb0.z, gb0.w), LDP(gb1.x, gb1.y), LDP(gb1.z, gb1.w)};
  sy_arrive(sm, 3, lane);
  unsigned pf;
  {
    const int xcd = bid & 7;
    const int id = (bid >> 3) * 192 + tid;
    u64 pc; int o1, o2;
    asm volatile("s_getpc_b64 %0\n\ts_mov_b32 %1, k3_tail@rel32@lo+4\n\ts_mov_b32 %2, k9_end@rel32@lo+12" : "=s"(pc), "=s"(o1), "=s"(o2));
    int r3 = id / 3, q3 = id - r3 * 3;
    const char* pp0 = (const char*)((r3 < 640 ? wg + (size_t)r3 * F_ : wu + (size_t)(r3 - 640) * F_) + xcd * 192 + q3 * 64);
    int i2 = id - 3840;
    const char* pp1 = (const char*)(wd + (size_t)(i2 >> 1) * D_ + xcd * 80 + (i2 & 1) * 78);
    int i3 = (id - 6912) * 64; i3 = (i3 >= 0 && i3 < o2 - o1) ? i3 : 0;
    const char* pp2 = (const char*)(pc + (long long)o1 + i3);
    pf = *(const GAS unsigned*)(id < 3840 ? pp0 : (id < 6912 ? pp1 : pp2));
  }
  sy_wait(sm, 3, 3);
  STAMP(0, 3);
  const int i0 = 15 - col + 8 * g;
  const char* ka = sm + SM_KI + ((i0 & 1) * NR_LEN + (i0 & ~1)) * 2;
  const int n0 = 3 * wave;
  f4 a0 = Z, a1 = Z, a2 = Z;
  const unsigned fa = (unsigned)(SM_U + (256 + 16 * col + 8 * g + 256 * n0 - 528) * 2);
  h8 A[17];
#pragma unroll
  for (int k = 0; k < 17; ++k) A[k] = ld_4dw(ka + k * 64, ka + k * 64 + 8);
  __builtin_amdgcn_sched_barrier(0);
  sy_wait(sm, 4, 1);
  if (wave == 0) near_band<9, 33, true>(A, fa, a0, a1, a2);
  else if (wave == 1) near_band<0, 33, true>(A, fa, a0, a1, a2);
  else near_band<0, 25, false>(A, fa, a0, a1, a2);
  asm volatile("s_nop 7\n\ts_nop 7" : "+v"(a0), "+v"(a1), "+v"(a2));
  sy_wait(sm, 5, 1);
  {
    const char* pp = sm + SM_P + lane * 16 + (n0 - 2) * 1024;
    if (wave != 0) { a0 += *(const f4*)(pp); a1 += *(const f4*)(pp + 1024); }
    conv_epi(a0, unsc, yt + YTS * n0);
    conv_epi(a1, unsc, yt + YTS * (n0 + 1));
    if (wave != 2) { a2 += *(const f4*)(pp + 2048); conv_epi(a2, unsc, yt + YTS * (n0 + 2)); }
  }
  asm volatile("" :: "v"(pf), "v"(selfpf));
  STAMP(0, 4);
}

__device__ __forceinline__ unsigned ord16(unsigned b) { return (b & 0x8000u) ? (~b & 0xffffu) : (b | 0x8000u); }
__device__ __forceinline__ void fma8(float* acc, u4 w, float xv) {
  acc[0] = __builtin_fmaf(bflo(w.x), xv, acc[0]); acc[1] = __builtin_fmaf(bfhi(w.x), xv, acc[1]);
  acc[2] = __builtin_fmaf(bflo(w.y), xv, acc[2]); acc[3] = __builtin_fmaf(bfhi(w.y), xv, acc[3]);
  acc[4] = __builtin_fmaf(bflo(w.z), xv, acc[4]); acc[5] = __builtin_fmaf(bfhi(w.z), xv, acc[5]);
  acc[6] = __builtin_fmaf(bflo(w.w), xv, acc[6]); acc[7] = __builtin_fmaf(bfhi(w.w), xv, acc[7]);
}

__device__ __forceinline__ u64 sweep_keys(char* ws, int lane, int rep, int q, u64* lk) {
  GAS u64* kp = (GAS u64*)(ws + WS_KEYS) + rep * 256 + lane;
  u64 b;
#define K3PA 3
#define K3PS 2
  for (unsigned spins = 0;; ++spins) {
    bool ok = (long long)__hip_atomic_load(kp + q * 64, RLX_AGENT) < 0;
    if (__all(ok) || spins > SPIN_CAP) break;
    __builtin_amdgcn_s_sleep(K3PA);
  }
  for (unsigned spins = 0;; ++spins) {
    u64 k0 = __hip_atomic_load(kp, RLX_AGENT), k1 = __hip_atomic_load(kp + 64, RLX_AGENT);
    u64 k2 = __hip_atomic_load(kp + 128, RLX_AGENT), k3 = __hip_atomic_load(kp + 192, RLX_AGENT);
    bool ok = (long long)(k0 & k1 & k2 & k3) < 0;
    b = k0 > k1 ? k0 : k1; u64 c = k2 > k3 ? k2 : k3; b = b > c ? b : c;
    if (__all(ok) || spins > SPIN_CAP) {
      if (lk) { lk[lane] = k0; lk[lane + 64] = k1; lk[lane + 128] = k2; lk[lane + 192] = k3; }
      break;
    }
    __builtin_amdgcn_s_sleep(K3PS);
  }
  unsigned hk = (unsigned)(b >> 32), m = hk;
  m = max(m, (unsigned)__builtin_amdgcn_update_dpp(0, (int)m, 0x121, 0xf, 0xf, true));
  m = max(m, (unsigned)__builtin_amdgcn_update_dpp(0, (int)m, 0x122, 0xf, 0xf, true));
  m = max(m, (unsigned)__builtin_amdgcn_update_dpp(0, (int)m, 0x124, 0xf, 0xf, true));
  m = max(m, (unsigned)__builtin_amdgcn_update_dpp(0, (int)m, 0x128, 0xf, 0xf, true));
  unsigned m0 = (unsigned)__builtin_amdgcn_readlane((int)m, 0), m1 = (unsigned)__builtin_amdgcn_readlane((int)m, 16);
  unsigned m2 = (unsigned)__builtin_amdgcn_readlane((int)m, 32), m3 = (unsigned)__builtin_amdgcn_readlane((int)m, 48);
  m0 = max(max(m0, m1), max(m2, m3));
  int src = __builtin_ctzll(__ballot(hk == m0));
  unsigned lo = (unsigned)__builtin_amdgcn_readlane((int)(unsigned)b, src);
  return ((u64)m0 << 32) | lo;
}
__device__ __forceinline__ float par_sum(float v) {
  v = dpp_add<0x122>(v); v = dpp_add<0x124>(v); v = dpp_add<0x128>(v);
  return v;
}
__device__ __forceinline__ float xs_of(us xv, us yv, float wm, float wrs, float& hh) {
  hh = rbf(bf1(xv) + bf1(yv));
  return rbf(rbf(hh - wm) * wrs);
}

extern "C" __global__ __launch_bounds__(384, 2) void k3_tail(const us* __restrict__ x, const us* __restrict__ wr,
                                                  const us* __restrict__ wg, const us* __restrict__ wu,
                                                  const us* __restrict__ wd, us* __restrict__ out,
                                                  char* __restrict__ ws) {
  __shared__ __attribute__((aligned(16))) float red[5 * 32 + 16 + 8];
  __shared__ __attribute__((aligned(16))) u64 lkeys[1280];
  unsigned* redW = (unsigned*)(red + 176);
  const int tid = threadIdx.x, lane = tid & 63, wave = __builtin_amdgcn_readfirstlane(tid >> 6);
  const int bid = blockIdx.x;
  STAMP(2, 0);
  const us* ytb = (const us*)(ws + WS_YT);
  if (bid >= 256) {
    const bool f1 = bid < 352;
    const int j = f1 ? bid - 256 : bid - 352;
    const int cb = f1 ? (j & 7) * 12 + (j >> 3) : (j & 7) * 5 + (j >> 3);
    if (wave == 5) {
      __builtin_amdgcn_s_sleep(40);
      const u64 wn = sweep_keys(ws, lane, j & 7, (j >> 3) & 3, f1 ? lkeys : nullptr);
      const unsigned wkey = (unsigned)(wn >> 32), wms = (unsigned)wn;
      const int wrow = 2047 - (int)(wkey & 2047u);
      const int c = lane & 15;
      if (f1) {
        if (lane == 0) { redW[2] = wkey; redW[3] = wms; }
        STAMPT(2, 3, 320);
        __syncthreads();
        __syncthreads();
        const float* rp = red + (c >> 3) * 16 + (c & 7);
        float gs = (rp[0] + rp[32]) + (rp[64] + rp[96]) + rp[128];
        float us_ = (rp[8] + rp[40]) + (rp[72] + rp[104]) + rp[136];
        float gb = rbf(gs);
        float sl = rbf(gb * __builtin_amdgcn_rcpf(1.f + __expf(-gb)));
        unsigned a = (unsigned)f2bf(sl * rbf(us_)) | 0x10000u;
        __hip_atomic_store((GAS unsigned*)(ws + WS_ACT) + (lane >> 4) * F_ + cb * 16 + c, a, RLX_AGENT);
        STAMPT(2, 4, 320);
      } else {
        __syncthreads();
        const int col = cb * 16 + c;
        float fh;
        float fx2 = xs_of(x[(size_t)wrow * D_ + col], ytb[((size_t)(wrow >> 3) * D_ + col) * 8 + (wrow & 7)],
                          bflo(wms), __builtin_amdgcn_rcpf(bfhi(wms)), fh);
        unsigned ob = (wkey >> 11) & 0xffffu;
        unsigned rb = (ob & 0x8000u) ? (ob & 0x7fffu) : (~ob & 0xffffu);
        float sg = rbf(__builtin_amdgcn_rcpf(1.f + __expf(-bf1((us)rb))));
        __syncthreads();
        const float* rp = red + c;
        float ys = (rp[0] + rp[16]) + (rp[32] + rp[48]) + rp[64];
        float y = rbf(rbf(ys) * sg);
        if (lane < 16) out[(size_t)wrow * D_ + col] = f2bf(fh + rbf(fx2 + y));
        STAMPT(2, 5, 320);
      }
      return;
    }
    const int half = tid & 1, rb = tid >> 1;
    __builtin_amdgcn_s_sleep(16);
    if (f1) {
      const size_t o0 = (size_t)rb * F_ + cb * 16 + half * 8;
      u4 wq[8];
#pragma unroll
      for (int k = 0; k < 4; ++k) { wq[k] = *(const u4*)(wg + o0 + (size_t)k * 160 * F_); wq[4 + k] = *(const u4*)(wu + o0 + (size_t)k * 160 * F_); }
      f2 wf[32];
#pragma unroll
      for (int k = 0; k < 8; ++k) { wf[4 * k] = unpk(wq[k].x); wf[4 * k + 1] = unpk(wq[k].y); wf[4 * k + 2] = unpk(wq[k].z); wf[4 * k + 3] = unpk(wq[k].w); }
#pragma unroll
      for (int k = 0; k < 32; ++k) asm volatile("" : "+v"(wf[k]));
      __syncthreads();
      const unsigned wkey = redW[2], wms = redW[3];
      const int wrow = 2047 - (int)(wkey & 2047u);
      const float wm = bflo(wms), wrs = __builtin_amdgcn_rcpf(bfhi(wms));
      const us* xw = x + (size_t)wrow * D_ + rb;
      const us* yw = ytb + ((size_t)(wrow >> 3) * D_ + rb) * 8 + (wrow & 7);
      us xv[4], yv[4];
#pragma unroll
      for (int k = 0; k < 4; ++k) { xv[k] = xw[160 * k]; yv[k] = yw[160 * 8 * k]; }
      f2 acc[8];
#pragma unroll
      for (int i = 0; i < 8; ++i) acc[i] = f2{0.f, 0.f};
#pragma unroll
      for (int k = 0; k < 4; ++k) {
        float hh; float xs = xs_of(xv[k], yv[k], wm, wrs, hh);
        f2 x2v = {xs, xs};
#pragma unroll
        for (int i = 0; i < 4; ++i) {
          acc[i] = __builtin_elementwise_fma(wf[4 * k + i], x2v, acc[i]);
          acc[4 + i] = __builtin_elementwise_fma(wf[16 + 4 * k + i], x2v, acc[4 + i]);
        }
      }
      float w[8], o[4];
#pragma unroll
      for (int i = 0; i < 4; ++i) { w[2 * i] = swap32_add(acc[i].x, acc[4 + i].x); w[2 * i + 1] = swap32_add(acc[i].y, acc[4 + i].y); }
#pragma unroll
      for (int i = 0; i < 4; ++i) o[i] = swap16_add(w[i], w[i + 4]);
      par_sum4(o);
      const int rr = lane >> 4;
      if ((lane & 15) < 2) *(f4*)(red + wave * 32 + half * 16 + 4 * (rr & 1) + 8 * (rr >> 1)) = f4{o[0], o[1], o[2], o[3]};
      __syncthreads();
#pragma unroll
      for (int q = 0; q < 3; ++q) {
        const int cbk = j + 96 * q;
        if (cbk < 256) {
          const u64 gk = lkeys[cbk];
          const unsigned ck = (unsigned)(gk >> 32), cms = (unsigned)gk;
          if (ck != wkey) {
            const int crow = 2047 - (int)(ck & 2047u);
            const float crs = __builtin_amdgcn_rcpf(bfhi(cms)), cb_ = -bflo(cms) * crs;
            unsigned xr = ((const unsigned*)(x + (size_t)crow * D_))[tid];
            const us* yc = ytb + ((size_t)(crow >> 3) * D_ + 2 * tid) * 8 + (crow & 7);
            f2 hs = unpk(xr) + f2{bf1(yc[0]), bf1(yc[8])};
            f2 hh = unpk(pk_bf(hs.x, hs.y));
            f2 oo = hh + __builtin_elementwise_fma(hh, f2{crs, crs}, f2{cb_, cb_});
            ((unsigned*)(out + (size_t)crow * D_))[tid] = pk_bf(oo.x, oo.y);
          }
        }
      }
      STAMP(2, 6);
    } else {
      const us* wp = wd + cb * 16 + half * 8;
      u4 wq[10];
#pragma unroll
      for (int k = 0; k < 10; ++k) { int r = rb + 160 * k; r = r < F_ ? r : F_ - 1; wq[k] = *(const u4*)(wp + (size_t)r * D_); }
      f2 wf[40];
#pragma unroll
      for (int k = 0; k < 10; ++k) { wf[4 * k] = unpk(wq[k].x); wf[4 * k + 1] = unpk(wq[k].y); wf[4 * k + 2] = unpk(wq[k].z); wf[4 * k + 3] = unpk(wq[k].w); }
#pragma unroll
      for (int k = 0; k < 40; ++k) asm volatile("" : "+v"(wf[k]));
      __syncthreads();
#define K3A0 20
#define K3AA 2
      __builtin_amdgcn_s_sleep(K3A0);
      GAS unsigned* ap = (GAS unsigned*)(ws + WS_ACT) + (j & 3) * F_ + rb;
      const int last = rb < 96 ? 1440 : 0;
      unsigned a[10];
      for (unsigned spins = 0;; ++spins) {
        if (__all(__hip_atomic_load(ap + 160 * wave, RLX_AGENT) >> 16) || spins > SPIN_CAP) break;
        __builtin_amdgcn_s_sleep(K3AA);
      }
      for (unsigned spins = 0;; ++spins) {
#pragma unroll
        for (int k = 0; k < 9; ++k) a[k] = __hip_atomic_load(ap + 160 * k, RLX_AGENT);
        a[9] = __hip_atomic_load(ap + last, RLX_AGENT);
        unsigned all = a[0] & a[1] & a[2] & a[3] & a[4] & a[5] & a[6] & a[7] & a[8] & a[9];
        if (__all(all >> 16) || spins > SPIN_CAP) break;
        __builtin_amdgcn_s_sleep(1);
      }
      if (rb >= 96) a[9] = 0;
      f2 acc[4] = {f2{0.f, 0.f}, f2{0.f, 0.f}, f2{0.f, 0.f}, f2{0.f, 0.f}};
#pragma unroll
      for (int k = 0; k < 10; ++k) {
        float av = bflo(a[k]); f2 a2 = {av, av};
#pragma unroll
        for (int i = 0; i < 4; ++i) acc[i] = __builtin_elementwise_fma(wf[4 * k + i], a2, acc[i]);
      }
      float w0 = swap32_add(acc[0].x, acc[2].x), w1 = swap32_add(acc[0].y, acc[2].y);
      float w2 = swap32_add(acc[1].x, acc[3].x), w3 = swap32_add(acc[1].y, acc[3].y);
      float o0 = par_sum(swap16_add(w0, w2)), o1 = par_sum(swap16_add(w1, w3));
      const int rr = lane >> 4;
      if ((lane & 15) < 2) *(f2*)(red + wave * 16 + half * 8 + 2 * (rr & 1) + 4 * (rr >> 1)) = f2{o0, o1};
      __syncthreads();
    }
    return;
  }
  float* redA = red;
  float* abv = red + 160;
  const int tb = (bid & 7) * 32 + (bid >> 3);
  const int t0 = tb * 8;
  const unsigned* xb = (const unsigned*)(x + (size_t)t0 * D_);
  if (wave == 5) {
    const int r = lane & 7;
    const float piv = bf1(x[(size_t)(t0 + r) * D_]);
    const float W = *(const float*)(ws + WS_WSUM);
    __syncthreads();
    float s1 = (redA[r] + redA[24 + r]) + (redA[48 + r] + redA[72 + r]) + redA[96 + r];
    float s2 = (redA[8 + r] + redA[32 + r]) + (redA[56 + r] + redA[80 + r]) + redA[104 + r];
    float s3 = (redA[16 + r] + redA[40 + r]) + (redA[64 + r] + redA[88 + r]) + redA[112 + r];
    float mb, sb;
    ln_from_sums(s1, s2, piv, mb, sb);
    float rs = __builtin_amdgcn_rcpf(sb);
    float rv = rs * __builtin_fmaf(piv - mb, W, s3);
    unsigned key = 0x80000000u | (ord16(f2bf(rv)) << 11) | (unsigned)(2047 - (t0 + r));
    unsigned best = key;
    best = max(best, (unsigned)__builtin_amdgcn_update_dpp(0, (int)best, 0x111, 0xf, 0xf, true));
    best = max(best, (unsigned)__builtin_amdgcn_update_dpp(0, (int)best, 0x112, 0xf, 0xf, true));
    best = max(best, (unsigned)__builtin_amdgcn_update_dpp(0, (int)best, 0x114, 0xf, 0xf, true));
    best = (unsigned)__builtin_amdgcn_readlane((int)best, 7);
    if (key == best) {
      u64 gr = ((u64)best << 32) | (u64)((__float_as_uint(mb) >> 16) | (__float_as_uint(sb) & 0xffff0000u));
      __hip_atomic_store((GAS u64*)(ws + WS_KEYS) + (lane >> 3) * 256 + bid, gr, RLX_AGENT);
      redW[0] = (unsigned)r;
    }
    if (lane < 8) *(f2*)(abv + 2 * r) = f2{rs, -mb * rs};
    STAMPT(2, 2, 320);
    __syncthreads();
    return;
  }
  unsigned raw[8];
#pragma unroll
  for (int r = 0; r < 8; ++r) raw[r] = xb[r * (D_ / 2) + tid];
  const u4 y0 = *(const u4*)(ytb + ((size_t)tb * D_ + 2 * tid) * 8);
  const u4 y1 = *(const u4*)(ytb + ((size_t)tb * D_ + 2 * tid) * 8 + 8);
  const f2 wv = unpk(((const unsigned*)wr)[tid]);
  f2 h[8];
  {
    float v[24];
#pragma unroll
    for (int r = 0; r < 8; ++r) {
      const float piv = bflo(xb[r * (D_ / 2)]);
      unsigned ya = y0[r >> 1], yb = y1[r >> 1];
      f2 yy = (r & 1) ? f2{bfhi(ya), bfhi(yb)} : f2{bflo(ya), bflo(yb)};
      f2 hs = unpk(raw[r]) + yy;
      h[r] = unpk(pk_bf(hs.x, hs.y));
      f2 dd = h[r] - piv;
      v[r] = dd.x + dd.y;
      v[r + 8] = __builtin_fmaf(dd.x, dd.x, dd.y * dd.y);
      v[r + 16] = __builtin_fmaf(dd.x, wv.x, dd.y * wv.y);
    }
    float w[12], o[6];
#pragma unroll
    for (int i = 0; i < 12; ++i) w[i] = swap32_add(v[i], v[i + 12]);
#pragma unroll
    for (int i = 0; i < 6; ++i) o[i] = swap16_add(w[i], w[i + 6]);
    row_sum6(o);
    if ((lane & 15) == 0) {
      float* dst = redA + wave * 24 + (lane >> 4) * 6;
      *(f2*)dst = f2{o[0], o[1]}; *(f2*)(dst + 2) = f2{o[2], o[3]}; *(f2*)(dst + 4) = f2{o[4], o[5]};
    }
  }
  __syncthreads();
  STAMP(2, 1);
  __syncthreads();
  const int brow = (int)redW[0];
  unsigned* stg = (unsigned*)lkeys + wave * 512;
#pragma unroll
  for (int r = 0; r < 8; r += 2) {
    f4 c = *(const f4*)(abv + 2 * r);
    f2 o0 = h[r] + __builtin_elementwise_fma(h[r], f2{c.x, c.x}, f2{c.y, c.y});
    f2 o1 = h[r + 1] + __builtin_elementwise_fma(h[r + 1], f2{c.z, c.z}, f2{c.w, c.w});
    stg[r * 64 + lane] = pk_bf(o0.x, o0.y);
    stg[(r + 1) * 64 + lane] = pk_bf(o1.x, o1.y);
  }
  {
    const int rr = lane >> 4, ch = lane & 15;
    u4 q0 = *(const u4*)(stg + rr * 64 + ch * 4), q1 = *(const u4*)(stg + (rr + 4) * 64 + ch * 4);
    us* o0p = out + (size_t)(t0 + rr) * D_ + wave * 128 + ch * 8;
    if (rr != brow) asm volatile("global_store_dwordx4 %0, %1, off sc1\n\ts_nop 1" :: "v"(o0p), "v"(q0) : "memory");
    if (rr + 4 != brow) asm volatile("global_store_dwordx4 %0, %1, off sc1\n\ts_nop 1" :: "v"(o0p + 4 * D_), "v"(q1) : "memory");
  }
}

extern "C" __global__ void k9_end() {}

extern "C" void kernel_launch(void* const* d_in, const int* in_sizes, int n_in, void* d_out, int out_size,
                              void* d_ws, size_t ws_size, hipStream_t stream) {
  const us* x = (const us*)d_in[0];
  const us* Are = (const us*)d_in[1];
  const us* Aim = (const us*)d_in[2];
  const us* Bre = (const us*)d_in[3];
  const us* Bim = (const us*)d_in[4];
  const us* Cre = (const us*)d_in[5];
  const us* Cim = (const us*)d_in[6];
  const us* logdt = (const us*)d_in[7];
  const us* Dskip = (const us*)d_in[8];
  const us* wr = (const us*)d_in[10];
  const us* wg = (const us*)d_in[11];
  const us* wu = (const us*)d_in[12];
  const us* wd = (const us*)d_in[13];
  us* out = (us*)d_out;
  char* ws = (char*)d_ws;
  ka_conv<<<D_, 256, K2_LDS, stream>>>(x, Are, Aim, Bre, Bim, Cre, Cim, logdt, Dskip, wr, wg, wu, wd, out, ws);
  k3_tail<<<392, 384, 0, stream>>>(x, wr, wg, wu, wd, out, ws);
}
```

```cpp
#include <hip/hip_runtime.h>
#include <hip/hip_bf16.h>
#include <stdint.h>

typedef _Float16 h8 __attribute__((ext_vector_type(8)));
typedef _Float16 h2 __attribute__((ext_vector_type(2)));
typedef float f4 __attribute__((ext_vector_type(4)));
typedef float f2 __attribute__((ext_vector_type(2)));
typedef unsigned u4 __attribute__((ext_vector_type(4)));
typedef unsigned u2 __attribute__((ext_vector_type(2)));
typedef unsigned short us;
typedef unsigned long long u64;
#define GAS __attribute__((address_space(1)))
#define LAS __attribute__((address_space(3)))

constexpr int L_ = 2048, D_ = 640, N_ = 192, F_ = 1536;
constexpr int LP = 2048 + 64;
#define STAMPD(kid, slot) do {} while (0)
#define STAMP(kid, slot) do {} while (0)
#define STAMPT(kid, slot, thr) do {} while (0)

constexpr int NR_LEN  = 608;
constexpr int F2_OFF  = 2 * NR_LEN;
constexpr int F3_OFF  = F2_OFF + 8 * 64;
constexpr int KIMG_ELEMS = F3_OFF + 4 * 80;
constexpr int KCHUNKS = KIMG_ELEMS / 8;

constexpr size_t WS_FLAGS = 0;
constexpr size_t WS_KEYS  = WS_FLAGS;
constexpr size_t WS_ACT   = WS_FLAGS + 16384;
constexpr size_t WS_WSUM  = 40960;
constexpr size_t WS_EPOCH = 41216;
constexpr size_t WS_LNF   = 41472;
constexpr size_t WS_ACK   = WS_LNF + 32768;
constexpr size_t WS_UT    = WS_ACK + 8192;
constexpr size_t WS_YT    = WS_UT + (size_t)D_ * L_ * 2;
constexpr size_t WS_TOTAL = WS_YT + (size_t)D_ * L_ * 2;

constexpr int TAB_PITCH = 200;
constexpr int SM_KC = 57 * TAB_PITCH * 4;
constexpr int SM_U  = SM_KC + 1552;
constexpr int SM_SY = SM_U + 4608;
constexpr int K2_LDS = SM_SY + 32;
constexpr int SM_KI = 0;
constexpr int SM_P  = 4096;

constexpr int kc_of_v(int v) {
  if (v < 256)  { int h = v >> 7, r = v & 127; return ((h * 8 + (r & 7)) * 16) + (r >> 3); }
  if (v < 512)  { int w = v - 256, h = w >> 7, r = w & 127; return ((16 + h * 4 + ((r & 7) >> 1)) * 16) + (r >> 3); }
  if (v < 1024) { int w = v - 512, h = w >> 7, r = w & 127; return ((24 + h * 2 + ((r & 7) >> 2)) * 16) + (r >> 3); }
  if (v < 2048) { int w = v - 1024, h = w >> 7, r = w & 127; return ((32 + h) * 16) + (r >> 3); }
  return 40 * 16;
}
constexpr int bf16_round_int(int j) {
  if (j <= 256) return j;
  int step = j < 512 ? 2 : (j < 1024 ? 4 : 8);
  int lo = (j / step) * step, rem = j - lo;
  if (rem * 2 < step) return lo;
  if (rem * 2 > step) return lo + step;
  return ((lo / step) & 1) ? lo + step : lo;
}
constexpr int ZIDX = 768;

struct GatherTab { unsigned v[KIMG_ELEMS]; };
constexpr GatherTab make_gather() {
  GatherTab t{};
  for (int i = 0; i < KIMG_ELEMS; ++i) t.v[i] = SM_KC + ZIDX * 2;
  for (int e = 0; e < 2; ++e)
    for (int i = 0; i < NR_LEN; ++i) {
      int j = 543 - (i + e);
      if (j >= 0 && j < 515) t.v[e * NR_LEN + i] = (unsigned)(SM_KC + 2 * kc_of_v(bf16_round_int(j)));
    }
  for (int pi = 0; pi < 4; ++pi)
    for (int e = 0; e < 2; ++e)
      for (int i = 0; i < 64; ++i) {
        int p = 79 - (i + e);
        if (p >= 32 && p < 64) {
          int v = 16 * p + 8 * (pi >> 1) + ((pi & 1) ? 8 : 4);
          t.v[F2_OFF + (pi * 2 + e) * 64 + i] = (unsigned)(SM_KC + 2 * kc_of_v(v));
        }
      }
  for (int pi = 0; pi < 2; ++pi)
    for (int e = 0; e < 2; ++e)
      for (int i = 0; i < 80; ++i) {
        int qq = 127 - (i + e);
        if (qq >= 64 && qq < 128) {
          int v = 16 * qq + (pi ? 16 : 8);
          t.v[F3_OFF + (pi * 2 + e) * 80 + i] = (unsigned)(SM_KC + 2 * kc_of_v(v));
        }
      }
  return t;
}
__device__ const GatherTab g_gather = make_gather();

struct BoxTab { us v[7 * 64 * 8]; };
constexpr BoxTab make_box() {
  BoxTab t{};
  const int nn[6] = {3, 5, 3, 5, 7, 9};
  const int sh[6] = {3, 6, 11, 14, 5, 12};
  const int w0[6] = {-16, -16, -24, -24, -16, -24};
  for (int m = 0; m < 6; ++m)
    for (int lane = 0; lane < 64; ++lane)
      for (int j = 0; j < 8; ++j) {
        int b = lane & 15, k = 8 * (lane >> 4) + j;
        int idx = w0[m] + k;
        bool on = idx <= b - sh[m] && idx >= b - sh[m] - (nn[m] - 1);
        t.v[(m * 64 + lane) * 8 + j] = on ? 0x3C00 : 0;
      }
  for (int lane = 0; lane < 64; ++lane)
    for (int j = 0; j < 8; ++j) {
      int b = lane & 15, k = 8 * (lane >> 4) + j;
      t.v[(6 * 64 + lane) * 8 + j] = (k - b == 4 || k - b == 5) ? 0xFFFF : 0;
    }
  return t;
}
__device__ const BoxTab g_box = make_box();

#define RLX_AGENT __ATOMIC_RELAXED, __HIP_MEMORY_SCOPE_AGENT
constexpr unsigned SPIN_CAP = 1u << 22;
__device__ __forceinline__ float bflo(unsigned w) { return __uint_as_float(w << 16); }
__device__ __forceinline__ float bfhi(unsigned w) { return __uint_as_float(w & 0xffff0000u); }
__device__ __forceinline__ float bf1(us v) { return __uint_as_float((unsigned)v << 16); }
__device__ __forceinline__ us f2bf(float f) { __bf16 b = (__bf16)f; return __builtin_bit_cast(us, b); }
__device__ __forceinline__ float rbf(float f) { return bf1(f2bf(f)); }
typedef __bf16 b2 __attribute__((ext_vector_type(2)));
__device__ __forceinline__ unsigned pk_bf(float a, float b) {
  b2 v; v.x = (__bf16)a; v.y = (__bf16)b; return __builtin_bit_cast(unsigned, v);
}
__device__ __forceinline__ unsigned pk_h(float a, float b) {
  h2 v; v.x = (_Float16)a; v.y = (_Float16)b; return __builtin_bit_cast(unsigned, v);
}
template <int CTRL> __device__ __forceinline__ float dpp_add(float v) {
  int t = __builtin_amdgcn_update_dpp(0, __float_as_int(v), CTRL, 0xf, 0xf, true);
  return v + __int_as_float(t);
}
__device__ __forceinline__ float row_sum(float v) {
  v = dpp_add<0x121>(v); v = dpp_add<0x122>(v); v = dpp_add<0x124>(v); v = dpp_add<0x128>(v);
  return v;
}
__device__ __forceinline__ float swap32_add(float a, float b) {
  auto r = __builtin_amdgcn_permlane32_swap(__float_as_uint(a), __float_as_uint(b), false, false);
  return __uint_as_float(r[0]) + __uint_as_float(r[1]);
}
__device__ __forceinline__ float swap16_add(float a, float b) {
  auto r = __builtin_amdgcn_permlane16_swap(__float_as_uint(a), __float_as_uint(b), false, false);
  return __uint_as_float(r[0]) + __uint_as_float(r[1]);
}
__device__ __forceinline__ void wave_reduce16(const float (&v)[16], float (&out)[4]) {
  float w[8];
#pragma unroll
  for (int i = 0; i < 8; ++i) w[i] = swap32_add(v[i], v[i + 8]);
#pragma unroll
  for (int i = 0; i < 4; ++i) out[i] = row_sum(swap16_add(w[i], w[i + 4]));
}
__device__ __forceinline__ void wave_reduce8(const float (&v)[8], float (&out)[2]) {
  float w[4];
#pragma unroll
  for (int i = 0; i < 4; ++i) w[i] = swap32_add(v[i], v[i + 4]);
#pragma unroll
  for (int i = 0; i < 2; ++i) out[i] = row_sum(swap16_add(w[i], w[i + 2]));
}
__device__ __forceinline__ float wave_reduce4(const float (&v)[4]) {
  float w0 = swap32_add(v[0], v[2]), w1 = swap32_add(v[1], v[3]);
  return row_sum(swap16_add(w0, w1));
}
#define DPPA(i, st) "v_add_f32_dpp %" #i ", %" #i ", %" #i " row_ror:" #st " row_mask:0xf bank_mask:0xf\n\t"
__device__ __forceinline__ void row_sum8(float (&o)[8]) {
  asm volatile("s_nop 1\n\t"
    DPPA(0,1) DPPA(1,1) DPPA(2,1) DPPA(3,1) DPPA(4,1) DPPA(5,1) DPPA(6,1) DPPA(7,1)
    DPPA(0,2) DPPA(1,2) DPPA(2,2) DPPA(3,2) DPPA(4,2) DPPA(5,2) DPPA(6,2) DPPA(7,2)
    DPPA(0,4) DPPA(1,4) DPPA(2,4) DPPA(3,4) DPPA(4,4) DPPA(5,4) DPPA(6,4) DPPA(7,4)
    DPPA(0,8) DPPA(1,8) DPPA(2,8) DPPA(3,8) DPPA(4,8) DPPA(5,8) DPPA(6,8) DPPA(7,8)
    : "+v"(o[0]), "+v"(o[1]), "+v"(o[2]), "+v"(o[3]), "+v"(o[4]), "+v"(o[5]), "+v"(o[6]), "+v"(o[7]));
}
__device__ __forceinline__ void row_sum6(float (&o)[6]) {
  asm volatile("s_nop 1\n\t"
    DPPA(0,1) DPPA(1,1) DPPA(2,1) DPPA(3,1) DPPA(4,1) DPPA(5,1)
    DPPA(0,2) DPPA(1,2) DPPA(2,2) DPPA(3,2) DPPA(4,2) DPPA(5,2)
    DPPA(0,4) DPPA(1,4) DPPA(2,4) DPPA(3,4) DPPA(4,4) DPPA(5,4)
    DPPA(0,8) DPPA(1,8) DPPA(2,8) DPPA(3,8) DPPA(4,8) DPPA(5,8)
    : "+v"(o[0]), "+v"(o[1]), "+v"(o[2]), "+v"(o[3]), "+v"(o[4]), "+v"(o[5]));
}
__device__ __forceinline__ void row_sum4(float (&o)[4]) {
  asm volatile("s_nop 1\n\t"
    DPPA(0,1) DPPA(1,1) DPPA(2,1) DPPA(3,1)
    DPPA(0,2) DPPA(1,2) DPPA(2,2) DPPA(3,2)
    DPPA(0,4) DPPA(1,4) DPPA(2,4) DPPA(3,4)
    DPPA(0,8) DPPA(1,8) DPPA(2,8) DPPA(3,8)
    : "+v"(o[0]), "+v"(o[1]), "+v"(o[2]), "+v"(o[3]));
}
__device__ __forceinline__ void par_sum4(float (&o)[4]) {
  asm volatile("s_nop 1\n\t"
    DPPA(0,2) DPPA(1,2) DPPA(2,2) DPPA(3,2)
    DPPA(0,4) DPPA(1,4) DPPA(2,4) DPPA(3,4)
    DPPA(0,8) DPPA(1,8) DPPA(2,8) DPPA(3,8)
    : "+v"(o[0]), "+v"(o[1]), "+v"(o[2]), "+v"(o[3]));
}
__device__ __forceinline__ float rdlane(float v, int l) {
  return __int_as_float(__builtin_amdgcn_readlane(__float_as_int(v), l));
}

__device__ __forceinline__ f2 cmul(f2 a, f2 b) {
  f2 c;
  asm("v_pk_mul_f32 %0, %1, %2 op_sel:[0,0] op_sel_hi:[1,0]\n\tv_pk_fma_f32 %0, %1, %2, %0 op_sel:[1,1,0] op_sel_hi:[0,1,1] neg_lo:[1,0,0]"
      : "=&v"(c) : "v"(a), "v"(b));
  return c;
}
__device__ __forceinline__ f2 cexp_p(float lam, float rh, float rl, float p) {
  float mag = __builtin_amdgcn_exp2f(lam * p);
  float x = rh * p;
  float f = x - __builtin_rintf(x);
  f = __builtin_fmaf(rl, p, f);
  f2 w; w.x = mag * __builtin_amdgcn_cosf(f); w.y = mag * __builtin_amdgcn_sinf(f); return w;
}
#define MFMA16(a, b, c) __builtin_amdgcn_mfma_f32_16x16x32_f16((a), (b), (c), 0, 0, 0)
__device__ __forceinline__ h8 as_h8(u4 v) { return __builtin_bit_cast(h8, v); }

__device__ __forceinline__ void ln_from_sums(float s1, float s2, float piv, float& mb, float& sb) {
  float dm = s1 * (1.f / 640.f);
  float var = fmaxf(__builtin_fmaf(-dm, dm, s2 * (1.f / 640.f)), 0.f);
  mb = rbf(piv + dm);
  sb = rbf(__builtin_amdgcn_sqrtf(rbf(rbf(var) + 1e-5f)));
}
__device__ __forceinline__ f2 unpk(unsigned w) { return f2{bflo(w), bfhi(w)}; }

__device__ __forceinline__ h8 ld_h8(const char* p) { return as_h8(*(const u4*)p); }
__device__ __forceinline__ h8 ld_4dw(const char* p0, const char* p1) {
  const unsigned* a = (const unsigned*)p0; const unsigned* b = (const unsigned*)p1;
  return as_h8(u4{a[0], a[1], b[0], b[1]});
}
__device__ __forceinline__ h8 cvt_pair(f4 a, f4 b) {
  return as_h8(u4{pk_h(a[0], a[1]), pk_h(a[2], a[3]), pk_h(b[0], b[1]), pk_h(b[2], b[3])});
}
__device__ __forceinline__ void glds16(const void* g, void* l) {
  __builtin_amdgcn_global_load_lds((const GAS unsigned*)g, (LAS unsigned*)l, 16, 0, 0);
}
__device__ __forceinline__ void conv_epi(f4 acc, float sc, us* dst) {
  u2 o; o.x = pk_bf(acc[0] * sc, acc[1] * sc); o.y = pk_bf(acc[2] * sc, acc[3] * sc);
  *(u2*)dst = o;
}
#define LDU(a) ((unsigned)*(const LAS us*)(size_t)(a))
#define LDP(a, b) (LDU(a) | (LDU(b) << 16))

template <int OFF> __device__ __forceinline__ u4 lds_rd128(unsigned addr) {
  u4 v; asm volatile("ds_read_b128 %0, %1 offset:%2" : "=v"(v) : "v"(addr), "i"(OFF) : "memory"); return v;
}
template <int N> __device__ __forceinline__ void lgkm_wait() {
  asm volatile("s_waitcnt lgkmcnt(%0)" :: "i"(N) : "memory"); __builtin_amdgcn_sched_barrier(0);
}
template <int M, int M0, int M1, bool T3, int PF>
__device__ __forceinline__ void near_step(const h8 (&A)[17], u4 (&f)[PF], unsigned fa, f4& a0, f4& a1, f4& a2) {
  if constexpr (M < M1) {
    if constexpr (M >= M0 + PF) lgkm_wait<((M1 - 1 - M) < (PF - 1)) ? (M1 - 1 - M) : (PF - 1)>();
    const h8 ff = as_h8(f[(M - M0) % PF]);
    if constexpr (M <= 16) a0 = MFMA16(A[M], ff, a0);
    if constexpr (M >= 8 && M <= 24) a1 = MFMA16(A[M - 8], ff, a1);
    if constexpr (T3 && M >= 16) a2 = MFMA16(A[M - 16], ff, a2);
    __builtin_amdgcn_sched_barrier(0);
    if constexpr (M + PF < M1) f[(M - M0) % PF] = lds_rd128<(M + PF) * 64>(fa);
    near_step<M + 1, M0, M1, T3, PF>(A, f, fa, a0, a1, a2);
  }
}
template <int M, int M0, int PF> __device__ __forceinline__ void near_fill(u4 (&f)[PF], unsigned fa) {
  if constexpr (M < M0 + PF) { f[M - M0] = lds_rd128<M * 64>(fa); near_fill<M + 1, M0, PF>(f, fa); }
}
template <int M0, int M1, bool T3>
__device__ __forceinline__ void near_band(const h8 (&A)[17], unsigned fa, f4& a0, f4& a1, f4& a2) {
  constexpr int PF = 6;
  u4 f[PF];
  near_fill<M0, M0, PF>(f, fa);
  __builtin_amdgcn_s_waitcnt(0xC07F);
  __builtin_amdgcn_sched_barrier(0);
  near_step<M0, M0, M1, T3, PF>(A, f, fa, a0, a1, a2);
}

__device__ __forceinline__ void sy_arrive(char* sm, int idx, int lane) {
  asm volatile("s_waitcnt lgkmcnt(0)" ::: "memory");
  if (lane == 0) __hip_atomic_fetch_add((LAS unsigned*)(LAS char*)(sm + SM_SY) + idx, 1u, __ATOMIC_RELAXED, __HIP_MEMORY_SCOPE_WORKGROUP);
}
__device__ __forceinline__ void sy_wait(char* sm, int idx, unsigned n) {
  volatile LAS unsigned* c = (volatile LAS unsigned*)(LAS char*)(sm + SM_SY) + idx;
  while (*c < n) __builtin_amdgcn_s_sleep(0);
  asm volatile("" ::: "memory");
}

extern "C" __global__ __launch_bounds__(256, 2) void ka_conv(
    const us* __restrict__ x, const us* __restrict__ Are, const us* __restrict__ Aim, const us* __restrict__ Bre,
    const us* __restrict__ Bim, const us* __restrict__ Cre, const us* __restrict__ Cim, const us* __restrict__ logdt,
    const us* __restrict__ Dskip, const us* __restrict__ wr, const us* __restrict__ wg, const us* __restrict__ wu,
    const us* __restrict__ wd, const us* __restrict__ out, char* __restrict__ ws) {
  extern __shared__ __attribute__((aligned(16))) char sm[];
  const int tid = threadIdx.x, lane = tid & 63, wave = __builtin_amdgcn_readfirstlane(tid >> 6);
  const int bid = blockIdx.x;
  STAMP(0, 0);
  const int d = (bid & 7) * 80 + (bid >> 3);
  const int col = lane & 15, g = lane >> 4;
  char* kc = sm + SM_KC;
  const char* ub = sm + SM_U + (256 + 16 * col + 8 * g) * 2;
  us* yt = (us*)(ws + WS_YT) + ((size_t)(2 * col + (g >> 1)) * D_ + d) * 8 + (g & 1) * 4;
  constexpr int YTS = 32 * D_ * 8;
  const f4 Z = {0.f, 0.f, 0.f, 0.f};
  if (tid < 8) ((unsigned*)(sm + SM_SY))[tid] = 0;
  asm volatile("s_waitcnt lgkmcnt(0)" ::: "memory");
  u64 ep;
  us r_are, r_aim, r_bre, r_bim, r_cre, r_cim;
  const size_t pidx = (size_t)d * N_ + (tid < N_ ? tid : 0);
#define LOAD_PARAMS() do { r_are = Are[pidx]; r_aim = Aim[pidx]; r_bre = Bre[pidx]; r_bim = Bim[pidx]; r_cre = Cre[pidx]; r_cim = Cim[pidx]; \
    ep = __hip_atomic_load((GAS u64*)(ws + WS_EPOCH), RLX_AGENT); } while (0)
  unsigned selfpf = 0;
  const bool duty = (bid & 128) && bid < 512;
  const int lnb = (bid & 127) | ((bid >> 1) & 128);
  if (duty) {
    __builtin_amdgcn_s_setprio(3);
    const int tb = (lnb & 7) * 32 + (lnb >> 3);
    const unsigned* xa = (const unsigned*)(x + (size_t)(tb * 8 + 2 * wave) * D_);
    unsigned ra[5], rb[5];
#pragma unroll
    for (int k = 0; k < 5; ++k) { ra[k] = xa[lane + 64 * k]; rb[k] = xa[D_ / 2 + lane + 64 * k]; }
    __builtin_amdgcn_sched_barrier(0);
    selfpf = *(const GAS unsigned*)(ws + (lane == 0 ? WS_UT + (size_t)tb * D_ * 16 + wave * 2560 : (lane == 1 ? WS_LNF + wave * 8192 : WS_YT + (size_t)(lane & 31) * 81920 + wave * 4)));
    LOAD_PARAMS();
    __builtin_amdgcn_sched_barrier(0);
    const float pa = bflo(xa[0]), pb = bflo(xa[D_ / 2]);
    f2 va[5], vb[5];
    f2 s1a = {0.f, 0.f}, s2a = s1a, s1b = s1a, s2b = s1a;
#pragma unroll
    for (int k = 0; k < 5; ++k) {
      va[k] = unpk(ra[k]); vb[k] = unpk(rb[k]);
      f2 da = va[k] - pa, db = vb[k] - pb;
      s1a += da; s2a = __builtin_elementwise_fma(da, da, s2a);
      s1b += db; s2b = __builtin_elementwise_fma(db, db, s2b);
    }
    float rs4 = row_sum(swap16_add(swap32_add(s1a.x + s1a.y, s2a.x + s2a.y), swap32_add(s1b.x + s1b.y, s2b.x + s2b.y)));
    const float S1 = (lane & 1) ? rdlane(rs4, 16) : rdlane(rs4, 0), S2 = (lane & 1) ? rdlane(rs4, 48) : rdlane(rs4, 32);
    float mb, sb;
    ln_from_sums(S1, S2, (lane & 1) ? pb : pa, mb, sb);
    const float aa = 128.f * __builtin_amdgcn_rcpf(sb), bb = -mb * aa;
    const float aA = rdlane(aa, 0), bA = rdlane(bb, 0), aB = rdlane(aa, 1), bB = rdlane(bb, 1);
#pragma unroll
    for (int k = 0; k < 5; ++k) {
      f2 u0 = __builtin_elementwise_fma(va[k], f2{aA, aA}, f2{bA, bA});
      f2 u1 = __builtin_elementwise_fma(vb[k], f2{aB, aB}, f2{bB, bB});
      *(u2*)(sm + wave * 2560 + (lane + 64 * k) * 8) = u2{pk_h(u0.x, u1.x), pk_h(u0.y, u1.y)};
    }
    __syncthreads();
    {
      const unsigned* tp = (const unsigned*)sm + tid;
      char* ut = ws + WS_UT + ((size_t)tb * D_ + tid) * 16;
      u4 q0 = {tp[0], tp[640], tp[1280], tp[1920]}, q1 = {tp[256], tp[896], tp[1536], tp[2176]};
      asm volatile("global_store_dwordx4 %0, %1, off sc1\n\tglobal_store_dwordx4 %2, %3, off sc1\n\ts_nop 1"
                   :: "v"(ut), "v"(q0), "v"(ut + 4096), "v"(q1) : "memory");
      if (wave < 2) {
        u4 q2 = {tp[512], tp[1152], tp[1792], tp[2432]};
        asm volatile("global_store_dwordx4 %0, %1, off sc1\n\ts_nop 1" :: "v"(ut + 8192), "v"(q2) : "memory");
      }
      asm volatile("s_waitcnt vmcnt(0)" ::: "memory");
    }
    STAMP(0, 5);
    __syncthreads();
    if (wave == 3 && lane < 16) __hip_atomic_store((GAS u64*)(ws + WS_LNF) + lane * 256 + lnb, ep + 1ull, RLX_AGENT);
    __builtin_amdgcn_s_setprio(0);
  } else {
    if (wave == 3 && bid < 64) {
      u64 pc; int o1, o2;
      asm volatile("s_getpc_b64 %0\n\ts_mov_b32 %1, ka_conv@rel32@lo+4\n\ts_mov_b32 %2, k3_tail@rel32@lo+12" : "=s"(pc), "=s"(o1), "=s"(o2));
      int o = ((bid >> 3) * 64 + lane) * 64; o = o < o2 - o1 ? o : 0;
      selfpf = *(const GAS unsigned*)(pc + (long long)o1 + o);
    }
    LOAD_PARAMS();
    __builtin_amdgcn_s_barrier();
  }
  const u64 tag = ep + 1ull;
  if (wave == 3) {
    __builtin_amdgcn_s_setprio(3);
    if (lane == 0) __hip_atomic_store((GAS u64*)(ws + WS_ACK) + bid, tag, RLX_AGENT);
    if (lane < 32) *(u4*)(sm + SM_U + lane * 16) = u4{0, 0, 0, 0};
    if (bid >= 256 && bid < 266) ((u4*)(ws + WS_FLAGS))[(bid - 256) * 256 + lane] = u4{0, 0, 0, 0},
        ((u4*)(ws + WS_FLAGS))[(bid - 256) * 256 + 64 + lane] = u4{0, 0, 0, 0},
        ((u4*)(ws + WS_FLAGS))[(bid - 256) * 256 + 128 + lane] = u4{0, 0, 0, 0},
        ((u4*)(ws + WS_FLAGS))[(bid - 256) * 256 + 192 + lane] = u4{0, 0, 0, 0};
    if (bid == 266) {
      const unsigned* w2 = (const unsigned*)wr + lane;
      float sw = 0.f;
#pragma unroll
      for (int k = 0; k < 5; ++k) { unsigned q = w2[64 * k]; sw += bflo(q) + bfhi(q); }
      sw = row_sum(sw);
      sw = rdlane(sw, 0) + rdlane(sw, 16) + rdlane(sw, 32) + rdlane(sw, 48);
      if (lane == 0) *(float*)(ws + WS_WSUM) = sw;
    }
    const u4* bt = (const u4*)g_box.v + lane;
    u4 bx[7];
#pragma unroll
    for (int m = 0; m < 7; ++m) bx[m] = bt[64 * m];
    {
      GAS u64* fp = (GAS u64*)(ws + WS_LNF) + (bid & 15) * 256 + lane;
#define POLL0 50
#define POLLS 5
#define POLLA 8
      if (!duty) __builtin_amdgcn_s_sleep(POLL0);
      for (unsigned spins = 0;; ++spins) {
        bool ok = __hip_atomic_load(fp + ((bid >> 4) & 3) * 64, RLX_AGENT) == tag;
        if (__all(ok) || spins > SPIN_CAP) break;
        __builtin_amdgcn_s_sleep(POLLA);
      }
      for (unsigned spins = 0;; ++spins) {
        bool ok = (__hip_atomic_load(fp, RLX_AGENT) == tag) & (__hip_atomic_load(fp + 64, RLX_AGENT) == tag) &
                  (__hip_atomic_load(fp + 128, RLX_AGENT) == tag) & (__hip_atomic_load(fp + 192, RLX_AGENT) == tag);
        if (__all(ok) || spins > SPIN_CAP) break;
        __builtin_amdgcn_s_sleep(POLLS);
      }
    }
    STAMPT(0, 6, 192);
    {
      const char* gu = ws + WS_UT + (size_t)d * 16 + (size_t)lane * (D_ * 16);
      u4 q0, q1, q2, q3;
      asm volatile("global_load_dwordx4 %0, %4, off sc1\n\tglobal_load_dwordx4 %1, %5, off sc1\n\t"
                   "global_load_dwordx4 %2, %6, off sc1\n\tglobal_load_dwordx4 %3, %7, off sc1\n\ts_waitcnt vmcnt(0)"
                   : "=&v"(q0), "=&v"(q1), "=&v"(q2), "=&v"(q3)
                   : "v"(gu), "v"(gu + (size_t)64 * (D_ * 16)), "v"(gu + (size_t)128 * (D_ * 16)), "v"(gu + (size_t)192 * (D_ * 16)) : "memory");
      u4* dst = (u4*)(sm + SM_U + 512) + lane;
      dst[0] = q0; dst[64] = q1; dst[128] = q2; dst[192] = q3;
    }
    sy_arrive(sm, 4, lane);
    __builtin_amdgcn_s_setprio(0);
    STAMPT(0, 7, 192);
    h8 sa[6], sb[6], s3[4];
#pragma unroll
    for (int tau = 0; tau < 6; ++tau) {
      h8 w1 = ld_h8(ub + (256 * tau - 16) * 2), w2 = ld_h8(ub + (256 * tau - 24) * 2);
      sa[tau] = cvt_pair(MFMA16(w1, as_h8(bx[0]), Z), MFMA16(w1, as_h8(bx[1]), Z));
      sb[tau] = cvt_pair(MFMA16(w2, as_h8(bx[2]), Z), MFMA16(w2, as_h8(bx[3]), Z));
      if (tau < 4) s3[tau] = cvt_pair(MFMA16(w1, as_h8(bx[4]), Z), MFMA16(w2, as_h8(bx[5]), Z));
    }
    u4 fs[4];
#pragma unroll
    for (int i = 0; i < 4; ++i) fs[i] = *(const u4*)(ub + (256 * (4 + i) - 1032) * 2);
#pragma unroll
    for (int i = 0; i < 6; ++i) { asm volatile("" : "+v"(sa[i]), "+v"(sb[i])); }
    sy_wait(sm, 3, 3);
    const unsigned kv = LDU(SM_KC + 32 * 16 * 2) * 0x10001u;
    f4 acc[6];
    acc[0] = Z; acc[1] = Z;
    {
      h8 As = as_h8(u4{bx[6].x & kv, bx[6].y & kv, bx[6].z & kv, bx[6].w & kv});
#pragma unroll
      for (int i = 0; i < 4; ++i) acc[2 + i] = MFMA16(As, as_h8(fs[i]), Z);
    }
    {
      const int ib = 79 - col + 4 * g;
      const char* kb = sm + SM_KI + (F2_OFF + (ib & 1) * 64 + (ib & ~1)) * 2;
#pragma unroll
      for (int dl = 2; dl <= 4; ++dl) {
        h8 tA = ld_4dw(kb - dl * 32, kb + 256 - dl * 32), tB = ld_4dw(kb + 512 - dl * 32, kb + 768 - dl * 32);
#pragma unroll
        for (int tau = 0; tau < 6; ++tau)
          if (tau + dl <= 7) { acc[tau + dl - 2] = MFMA16(sa[tau], tA, acc[tau + dl - 2]); acc[tau + dl - 2] = MFMA16(sb[tau], tB, acc[tau + dl - 2]); }
      }
    }
    {
      const int ib = 127 - col + 4 * g;
      const char* kb = sm + SM_KI + (F3_OFF + (ib & 1) * 80 + (ib & ~1)) * 2;
#pragma unroll
      for (int dl = 4; dl <= 7; ++dl) {
        h8 tt = ld_4dw(kb - dl * 32, kb + 320 - dl * 32);
#pragma unroll
        for (int tau = 0; tau < 4; ++tau)
          if (tau + dl <= 7) acc[tau + dl - 2] = MFMA16(s3[tau], tt, acc[tau + dl - 2]);
      }
    }
#pragma unroll
    for (int i = 0; i < 6; ++i) *(f4*)(sm + SM_P + i * 1024 + lane * 16) = acc[i];
    sy_arrive(sm, 5, lane);
    if (bid == 300) {
      GAS u64* ap = (GAS u64*)(ws + WS_ACK) + lane;
      for (unsigned spins = 0;; ++spins) {
        bool ok = true;
#pragma unroll
        for (int k = 0; k < 10; ++k) ok = ok & (__hip_atomic_load(ap + 64 * k, RLX_AGENT) == tag);
        if (__all(ok) || spins > SPIN_CAP) break;
        __builtin_amdgcn_s_sleep(8);
      }
      if (lane == 0) __hip_atomic_store((GAS u64*)(ws + WS_EPOCH), tag, RLX_AGENT);
    }
    asm volatile("" :: "v"(selfpf));
    return;
  }
  const unsigned ldw = ((const unsigned*)logdt)[d >> 1], dsw = ((const unsigned*)Dskip)[d >> 1];
  const float dt = rbf(__expf((d & 1) ? bfhi(ldw) : bflo(ldw)));
  const float dsk = (d & 1) ? bfhi(dsw) : bflo(dsw);
  asm volatile("s_mov_b32 m0, %0" :: "s"(wave * 256) : "memory");
  float ar = dt * bf1(r_are), ai = dt * bf1(r_aim);
  f2 dB = {dt * bf1(r_bre), dt * bf1(r_bim)};
  f2 Cc = {bf1(r_cre), bf1(r_cim)};
  f2 CB = cmul(Cc, dB);
  const float LOG2E = 1.4426950408889634f;
  const float I2PH = 0.15915494309189535f;
  const float I2PL = (float)(0.15915494309189533577 - (double)0.15915494309189535f);
  float lam = ar * LOG2E;
  float rh = ai * I2PH;
  float rl = __builtin_fmaf(ai, I2PL, __builtin_fmaf(ai, I2PH, -rh));
  {
    float gg = fmaxf(1.f, __builtin_amdgcn_exp2f(lam * 2048.f));
    float bnd = row_sum((fabsf(CB.x) + fabsf(CB.y)) * gg);
    if ((lane & 15) == 0) ((float*)(kc))[wave * 4 + (lane >> 4)] = bnd;
    sy_arrive(sm, 0, lane);
  }
  f2 w1 = cexp_p(lam, rh, rl, 1.f), w16 = cexp_p(lam, rh, rl, 16.f), w256 = cexp_p(lam, rh, rl, 256.f);
  f2 w2 = cmul(w1, w1), w32 = cmul(w16, w16), w512 = cmul(w256, w256);
  f2 w4 = cmul(w2, w2), w64 = cmul(w32, w32), w1024 = cmul(w512, w512);
  f2 w8 = cmul(w4, w4), w128 = cmul(w64, w64);
#define DSW(row, val) asm volatile("ds_write_addtid_b32 %0 offset:%1" :: "v"(val), "i"((row) * TAB_PITCH * 4) : "memory")
#define PUTE(row, c) do { f2 c_ = (c); DSW((row), pk_h(c_.x, -c_.y)); } while (0)
#define PUT(row, c) do { f2 c_ = (c); DSW(16 + (row), pk_h(c_.x, c_.y)); } while (0)
  {
    f2 e[8];
    e[1] = w8; e[2] = w16; e[3] = cmul(w8, w16); e[4] = w32;
    e[5] = cmul(w32, e[1]); e[6] = cmul(w32, e[2]); e[7] = cmul(w32, e[3]);
    DSW(0, 0x00003C00u);
#pragma unroll
    for (int l = 1; l < 8; ++l) PUTE(l, e[l]);
    PUTE(8, w64);
#pragma unroll
    for (int l = 1; l < 8; ++l) PUTE(8 + l, cmul(w64, e[l]));
  }
  sy_wait(sm, 0, 3);
  STAMP(0, 1);
  const u4* gt = (const u4*)g_gather.v + 2 * tid;
  u4 ga0 = gt[0], ga1 = gt[1], gb0, gb1;
  if (wave == 0) { gb0 = gt[384]; gb1 = gt[385]; }
  float sc, unsc;
  {
    f4 q0 = *(const f4*)(kc), q1 = *(const f4*)(kc + 16), q2 = *(const f4*)(kc + 32);
    q0 += q1; q0 += q2;
    float sig = ((q0.x + q0.y) + (q0.z + q0.w)) + fabsf(dsk);
    int ex = (int)(__float_as_uint(sig) >> 23) & 255; ex = ex < 24 ? 24 : (ex > 253 ? 253 : ex);
    sc = __uint_as_float((unsigned)(267 - ex) << 23);
    unsc = __uint_as_float((unsigned)(ex - 20) << 23);
    if (tid == 0) *(unsigned*)(kc + ZIDX * 2) = 0;
    f2 c[8];
    c[0] = CB * sc;
    c[1] = cmul(c[0], w1); c[2] = cmul(c[0], w2); c[4] = cmul(c[0], w4); c[3] = cmul(c[1], w2);
    c[5] = cmul(c[1], w4); c[6] = cmul(c[2], w4); c[7] = cmul(c[3], w4);
#pragma unroll
    for (int r = 0; r < 8; ++r) {
      PUT(r, c[r]);
      f2 ch = cmul(c[r], w128); PUT(8 + r, ch);
      if ((r & 1) == 0) {
        f2 s0 = cmul(c[r], w256), s1 = cmul(ch, w256);
        PUT(16 + (r >> 1), s0); PUT(20 + (r >> 1), s1);
        if ((r & 3) == 0) {
          const int rho = r >> 2;
          f2 z0 = cmul(c[r], w512), z1 = cmul(ch, w512), z2 = cmul(s0, w512), z3 = cmul(s1, w512);
          PUT(24 + 0 + rho, z0); PUT(24 + 2 + rho, z1); PUT(24 + 4 + rho, z2); PUT(24 + 6 + rho, z3);
          if (r == 0) {
            f2 y0 = cmul(c[0], w1024);
            PUT(32, y0); PUT(33, cmul(ch, w1024)); PUT(34, cmul(s0, w1024)); PUT(35, cmul(s1, w1024));
            PUT(36, cmul(z0, w1024)); PUT(37, cmul(z1, w1024)); PUT(38, cmul(z2, w1024)); PUT(39, cmul(z3, w1024));
            PUT(40, cmul(y0, w1024));
          }
        }
      }
    }
  }
  sy_arrive(sm, 1, lane);
  sy_wait(sm, 1, 3);
  STAMP(0, 2);
  {
    const unsigned* tab = (const unsigned*)sm;
    f4 acc0 = Z, acc1 = Z;
    const unsigned* pa = tab + col * TAB_PITCH + g * 4;
    const unsigned* pb = tab + (16 + wave * 16 + col) * TAB_PITCH + g * 4;
    u4 ea[12], pb_[12];
#pragma unroll
    for (int kk = 0; kk < 12; ++kk) { ea[kk] = *(const u4*)(pa + kk * 16); pb_[kk] = *(const u4*)(pb + kk * 16); }
#pragma unroll
    for (int kk = 0; kk < 12; kk += 2) {
      acc0 = MFMA16(as_h8(ea[kk]), as_h8(pb_[kk]), acc0);
      acc1 = MFMA16(as_h8(ea[kk + 1]), as_h8(pb_[kk + 1]), acc1);
    }
    __builtin_amdgcn_sched_group_barrier(0x100, 24, 0);
    __builtin_amdgcn_sched_group_barrier(0x008, 12, 0);
    acc0 += acc1;
    acc0[0] = __builtin_fmaf(tid == 0 ? dsk : 0.f, sc, acc0[0]);
    u2 o; o.x = pk_h(acc0[0], acc0[1]); o.y = pk_h(acc0[2], acc0[3]);
    *(u2*)(kc + ((wave * 16 + col) * 16 + 4 * g) * 2) = o;
  }
  sy_arrive(sm, 2, lane);
  sy_wait(sm, 2, 3);
  *(u4*)(sm + SM_KI + tid * 16) = u4{LDP(ga0.x, ga0.y), LDP(ga0.z, ga0.w), LDP(ga1.x, ga1.y), LDP(ga1.z, ga1.w)};
  if (wave == 0) *(u4*)(sm + SM_KI + (192 + tid) * 16) = u4{LDP(gb0.x, gb0.y), LDP(gb0.z, gb0.w), LDP(gb1.x, gb1.y), LDP(gb1.z, gb1.w)};
  sy_arrive(sm, 3, lane);
  unsigned pf;
  {
    const int xcd = bid & 7;
    const int id = (bid >> 3) * 192 + tid;
    u64 pc; int o1, o2;
    asm volatile("s_getpc_b64 %0\n\ts_mov_b32 %1, k3_tail@rel32@lo+4\n\ts_mov_b32 %2, k9_end@rel32@lo+12" : "=s"(pc), "=s"(o1), "=s"(o2));
    int r3 = id / 3, q3 = id - r3 * 3;
    const char* pp0 = (const char*)((r3 < 640 ? wg + (size_t)r3 * F_ : wu + (size_t)(r3 - 640) * F_) + xcd * 192 + q3 * 64);
    int i2 = id - 3840;
    const char* pp1 = (const char*)(wd + (size_t)(i2 >> 1) * D_ + xcd * 80 + (i2 & 1) * 78);
    int i3 = (id - 6912) * 64; i3 = (i3 >= 0 && i3 < o2 - o1) ? i3 : 0;
    const char* pp2 = (const char*)(pc + (long long)o1 + i3);
    pf = *(const GAS unsigned*)(id < 3840 ? pp0 : (id < 6912 ? pp1 : pp2));
  }
  sy_wait(sm, 3, 3);
  STAMP(0, 3);
  const int i0 = 15 - col + 8 * g;
  const char* ka = sm + SM_KI + ((i0 & 1) * NR_LEN + (i0 & ~1)) * 2;
  const int n0 = 3 * wave;
  f4 a0 = Z, a1 = Z, a2 = Z;
  const unsigned fa = (unsigned)(SM_U + (256 + 16 * col + 8 * g + 256 * n0 - 528) * 2);
  h8 A[17];
#pragma unroll
  for (int k = 0; k < 17; ++k) A[k] = ld_4dw(ka + k * 64, ka + k * 64 + 8);
  __builtin_amdgcn_sched_barrier(0);
  sy_wait(sm, 4, 1);
  if (wave == 0) near_band<9, 33, true>(A, fa, a0, a1, a2);
  else if (wave == 1) near_band<0, 33, true>(A, fa, a0, a1, a2);
  else near_band<0, 25, false>(A, fa, a0, a1, a2);
  asm volatile("s_nop 7\n\ts_nop 7" : "+v"(a0), "+v"(a1), "+v"(a2));
  sy_wait(sm, 5, 1);
  {
    const char* pp = sm + SM_P + lane * 16 + (n0 - 2) * 1024;
    if (wave != 0) { a0 += *(const f4*)(pp); a1 += *(const f4*)(pp + 1024); }
    conv_epi(a0, unsc, yt + YTS * n0);
    conv_epi(a1, unsc, yt + YTS * (n0 + 1));
    if (wave != 2) { a2 += *(const f4*)(pp + 2048); conv_epi(a2, unsc, yt + YTS * (n0 + 2)); }
  }
  asm volatile("" :: "v"(pf), "v"(selfpf));
  STAMP(0, 4);
}

__device__ __forceinline__ unsigned ord16(unsigned b) { return (b & 0x8000u) ? (~b & 0xffffu) : (b | 0x8000u); }
__device__ __forceinline__ void fma8(float* acc, u4 w, float xv) {
  acc[0] = __builtin_fmaf(bflo(w.x), xv, acc[0]); acc[1] = __builtin_fmaf(bfhi(w.x), xv, acc[1]);
  acc[2] = __builtin_fmaf(bflo(w.y), xv, acc[2]); acc[3] = __builtin_fmaf(bfhi(w.y), xv, acc[3]);
  acc[4] = __builtin_fmaf(bflo(w.z), xv, acc[4]); acc[5] = __builtin_fmaf(bfhi(w.z), xv, acc[5]);
  acc[6] = __builtin_fmaf(bflo(w.w), xv, acc[6]); acc[7] = __builtin_fmaf(bfhi(w.w), xv, acc[7]);
}

__device__ __forceinline__ u64 sweep_keys(char* ws, int lane, int rep, int q, u64* lk) {
  GAS u64* kp = (GAS u64*)(ws + WS_KEYS) + rep * 256 + lane;
  u64 b;
#define K3PA 3
#define K3PS 2
  for (unsigned spins = 0;; ++spins) {
    bool ok = (long long)__hip_atomic_load(kp + q * 64, RLX_AGENT) < 0;
    if (__all(ok) || spins > SPIN_CAP) break;
    __builtin_amdgcn_s_sleep(K3PA);
  }
  for (unsigned spins = 0;; ++spins) {
    u64 k0 = __hip_atomic_load(kp, RLX_AGENT), k1 = __hip_atomic_load(kp + 64, RLX_AGENT);
    u64 k2 = __hip_atomic_load(kp + 128, RLX_AGENT), k3 = __hip_atomic_load(kp + 192, RLX_AGENT);
    bool ok = (long long)(k0 & k1 & k2 & k3) < 0;
    b = k0 > k1 ? k0 : k1; u64 c = k2 > k3 ? k2 : k3; b = b > c ? b : c;
    if (__all(ok) || spins > SPIN_CAP) {
      if (lk) { lk[lane] = k0; lk[lane + 64] = k1; lk[lane + 128] = k2; lk[lane + 192] = k3; }
      break;
    }
    __builtin_amdgcn_s_sleep(K3PS);
  }
  unsigned hk = (unsigned)(b >> 32), m = hk;
  m = max(m, (unsigned)__builtin_amdgcn_update_dpp(0, (int)m, 0x121, 0xf, 0xf, true));
  m = max(m, (unsigned)__builtin_amdgcn_update_dpp(0, (int)m, 0x122, 0xf, 0xf, true));
  m = max(m, (unsigned)__builtin_amdgcn_update_dpp(0, (int)m, 0x124, 0xf, 0xf, true));
  m = max(m, (unsigned)__builtin_amdgcn_update_dpp(0, (int)m, 0x128, 0xf, 0xf, true));
  unsigned m0 = (unsigned)__builtin_amdgcn_readlane((int)m, 0), m1 = (unsigned)__builtin_amdgcn_readlane((int)m, 16);
  unsigned m2 = (unsigned)__builtin_amdgcn_readlane((int)m, 32), m3 = (unsigned)__builtin_amdgcn_readlane((int)m, 48);
  m0 = max(max(m0, m1), max(m2, m3));
  int src = __builtin_ctzll(__ballot(hk == m0));
  unsigned lo = (unsigned)__builtin_amdgcn_readlane((int)(unsigned)b, src);
  return ((u64)m0 << 32) | lo;
}
__device__ __forceinline__ float par_sum(float v) {
  v = dpp_add<0x122>(v); v = dpp_add<0x124>(v); v = dpp_add<0x128>(v);
  return v;
}
__device__ __forceinline__ float xs_of(us xv, us yv, float wm, float wrs, float& hh) {
  hh = rbf(bf1(xv) + bf1(yv));
  return rbf(rbf(hh - wm) * wrs);
}

extern "C" __global__ __launch_bounds__(384, 2) void k3_tail(const us* __restrict__ x, const us* __restrict__ wr,
                                                  const us* __restrict__ wg, const us* __restrict__ wu,
                                                  const us* __restrict__ wd, us* __restrict__ out,
                                                  char* __restrict__ ws) {
  __shared__ __attribute__((aligned(16))) float red[5 * 32 + 16 + 8];
  __shared__ __attribute__((aligned(16))) u64 lkeys[1280];
  unsigned* redW = (unsigned*)(red + 176);
  const int tid = threadIdx.x, lane = tid & 63, wave = __builtin_amdgcn_readfirstlane(tid >> 6);
  const int bid = blockIdx.x;
  STAMP(2, 0);
  const us* ytb = (const us*)(ws + WS_YT);
  if (bid >= 256) {
    const bool f1 = bid < 352;
    const int j = f1 ? bid - 256 : bid - 352;
    const int cb = f1 ? (j & 7) * 12 + (j >> 3) : (j & 7) * 5 + (j >> 3);
    if (wave == 5) {
      __builtin_amdgcn_s_sleep(40);
      const u64 wn = sweep_keys(ws, lane, j & 7, (j >> 3) & 3, f1 ? lkeys : nullptr);
      const unsigned wkey = (unsigned)(wn >> 32), wms = (unsigned)wn;
      const int wrow = 2047 - (int)(wkey & 2047u);
      const int c = lane & 15;
      if (f1) {
        if (lane == 0) { redW[2] = wkey; redW[3] = wms; }
        STAMPT(2, 3, 320);
        __syncthreads();
        __syncthreads();
        const float* rp = red + (c >> 3) * 16 + (c & 7);
        float gs = (rp[0] + rp[32]) + (rp[64] + rp[96]) + rp[128];
        float us_ = (rp[8] + rp[40]) + (rp[72] + rp[104]) + rp[136];
        float gb = rbf(gs);
        float sl = rbf(gb * __builtin_amdgcn_rcpf(1.f + __expf(-gb)));
        unsigned a = (unsigned)f2bf(sl * rbf(us_)) | 0x10000u;
        __hip_atomic_store((GAS unsigned*)(ws + WS_ACT) + (lane >> 4) * F_ + cb * 16 + c, a, RLX_AGENT);
        STAMPT(2, 4, 320);
      } else {
        __syncthreads();
        const int col = cb * 16 + c;
        float fh;
        float fx2 = xs_of(x[(size_t)wrow * D_ + col], ytb[((size_t)(wrow >> 3) * D_ + col) * 8 + (wrow & 7)],
                          bflo(wms), __builtin_amdgcn_rcpf(bfhi(wms)), fh);
        unsigned ob = (wkey >> 11) & 0xffffu;
        unsigned rb = (ob & 0x8000u) ? (ob & 0x7fffu) : (~ob & 0xffffu);
        float sg = rbf(__builtin_amdgcn_rcpf(1.f + __expf(-bf1((us)rb))));
        __syncthreads();
        const float* rp = red + c;
        float ys = (rp[0] + rp[16]) + (rp[32] + rp[48]) + rp[64];
        float y = rbf(rbf(ys) * sg);
        if (lane < 16) out[(size_t)wrow * D_ + col] = f2bf(fh + rbf(fx2 + y));
        STAMPT(2, 5, 320);
      }
      return;
    }
    const int half = tid & 1, rb = tid >> 1;
    __builtin_amdgcn_s_sleep(16);
    if (f1) {
      const size_t o0 = (size_t)rb * F_ + cb * 16 + half * 8;
      u4 wq[8];
#pragma unroll
      for (int k = 0; k < 4; ++k) { wq[k] = *(const u4*)(wg + o0 + (size_t)k * 160 * F_); wq[4 + k] = *(const u4*)(wu + o0 + (size_t)k * 160 * F_); }
      f2 wf[32];
#pragma unroll
      for (int k = 0; k < 8; ++k) { wf[4 * k] = unpk(wq[k].x); wf[4 * k + 1] = unpk(wq[k].y); wf[4 * k + 2] = unpk(wq[k].z); wf[4 * k + 3] = unpk(wq[k].w); }
#pragma unroll
      for (int k = 0; k < 32; ++k) asm volatile("" : "+v"(wf[k]));
      __syncthreads();
      const unsigned wkey = redW[2], wms = redW[3];
      const int wrow = 2047 - (int)(wkey & 2047u);
      const float wm = bflo(wms), wrs = __builtin_amdgcn_rcpf(bfhi(wms));
      const us* xw = x + (size_t)wrow * D_ + rb;
      const us* yw = ytb + ((size_t)(wrow >> 3) * D_ + rb) * 8 + (wrow & 7);
      us xv[4], yv[4];
#pragma unroll
      for (int k = 0; k < 4; ++k) { xv[k] = xw[160 * k]; yv[k] = yw[160 * 8 * k]; }
      f2 acc[8];
#pragma unroll
      for (int i = 0; i < 8; ++i) acc[i] = f2{0.f, 0.f};
#pragma unroll
      for (int k = 0; k < 4; ++k) {
        float hh; float xs = xs_of(xv[k], yv[k], wm, wrs, hh);
        f2 x2v = {xs, xs};
#pragma unroll
        for (int i = 0; i < 4; ++i) {
          acc[i] = __builtin_elementwise_fma(wf[4 * k + i], x2v, acc[i]);
          acc[4 + i] = __builtin_elementwise_fma(wf[16 + 4 * k + i], x2v, acc[4 + i]);
        }
      }
      float w[8], o[4];
#pragma unroll
      for (int i = 0; i < 4; ++i) { w[2 * i] = swap32_add(acc[i].x, acc[4 + i].x); w[2 * i + 1] = swap32_add(acc[i].y, acc[4 + i].y); }
#pragma unroll
      for (int i = 0; i < 4; ++i) o[i] = swap16_add(w[i], w[i + 4]);
      par_sum4(o);
      const int rr = lane >> 4;
      if ((lane & 15) < 2) *(f4*)(red + wave * 32 + half * 16 + 4 * (rr & 1) + 8 * (rr >> 1)) = f4{o[0], o[1], o[2], o[3]};
      __syncthreads();
#pragma unroll
      for (int q = 0; q < 3; ++q) {
        const int cbk = j + 96 * q;
        if (cbk < 256) {
          const u64 gk = lkeys[cbk];
          const unsigned ck = (unsigned)(gk >> 32), cms = (unsigned)gk;
          if (ck != wkey) {
            const int crow = 2047 - (int)(ck & 2047u);
            const float crs = __builtin_amdgcn_rcpf(bfhi(cms)), cb_ = -bflo(cms) * crs;
            unsigned xr = ((const unsigned*)(x + (size_t)crow * D_))[tid];
            const us* yc = ytb + ((size_t)(crow >> 3) * D_ + 2 * tid) * 8 + (crow & 7);
            f2 hs = unpk(xr) + f2{bf1(yc[0]), bf1(yc[8])};
            f2 hh = unpk(pk_bf(hs.x, hs.y));
            f2 oo = hh + __builtin_elementwise_fma(hh, f2{crs, crs}, f2{cb_, cb_});
            ((unsigned*)(out + (size_t)crow * D_))[tid] = pk_bf(oo.x, oo.y);
          }
        }
      }
      STAMP(2, 6);
    } else {
      const us* wp = wd + cb * 16 + half * 8;
      u4 wq[10];
#pragma unroll
      for (int k = 0; k < 10; ++k) { int r = rb + 160 * k; r = r < F_ ? r : F_ - 1; wq[k] = *(const u4*)(wp + (size_t)r * D_); }
      f2 wf[40];
#pragma unroll
      for (int k = 0; k < 10; ++k) { wf[4 * k] = unpk(wq[k].x); wf[4 * k + 1] = unpk(wq[k].y); wf[4 * k + 2] = unpk(wq[k].z); wf[4 * k + 3] = unpk(wq[k].w); }
#pragma unroll
      for (int k = 0; k < 40; ++k) asm volatile("" : "+v"(wf[k]));
      __syncthreads();
#define K3A0 20
#define K3AA 2
      __builtin_amdgcn_s_sleep(K3A0);
      GAS unsigned* ap = (GAS unsigned*)(ws + WS_ACT) + (j & 3) * F_ + rb;
      const int last = rb < 96 ? 1440 : 0;
      unsigned a[10];
      for (unsigned spins = 0;; ++spins) {
        if (__all(__hip_atomic_load(ap + 160 * wave, RLX_AGENT) >> 16) || spins > SPIN_CAP) break;
        __builtin_amdgcn_s_sleep(K3AA);
      }
      for (unsigned spins = 0;; ++spins) {
#pragma unroll
        for (int k = 0; k < 9; ++k) a[k] = __hip_atomic_load(ap + 160 * k, RLX_AGENT);
        a[9] = __hip_atomic_load(ap + last, RLX_AGENT);
        unsigned all = a[0] & a[1] & a[2] & a[3] & a[4] & a[5] & a[6] & a[7] & a[8] & a[9];
        if (__all(all >> 16) || spins > SPIN_CAP) break;
        __builtin_amdgcn_s_sleep(1);
      }
      if (rb >= 96) a[9] = 0;
      f2 acc[4] = {f2{0.f, 0.f}, f2{0.f, 0.f}, f2{0.f, 0.f}, f2{0.f, 0.f}};
#pragma unroll
      for (int k = 0; k < 10; ++k) {
        float av = bflo(a[k]); f2 a2 = {av, av};
#pragma unroll
        for (int i = 0; i < 4; ++i) acc[i] = __builtin_elementwise_fma(wf[4 * k + i], a2, acc[i]);
      }
      float w0 = swap32_add(acc[0].x, acc[2].x), w1 = swap32_add(acc[0].y, acc[2].y);
      float w2 = swap32_add(acc[1].x, acc[3].x), w3 = swap32_add(acc[1].y, acc[3].y);
      float o0 = par_sum(swap16_add(w0, w2)), o1 = par_sum(swap16_add(w1, w3));
      const int rr = lane >> 4;
      if ((lane & 15) < 2) *(f2*)(red + wave * 16 + half * 8 + 2 * (rr & 1) + 4 * (rr >> 1)) = f2{o0, o1};
      __syncthreads();
    }
    return;
  }
  float* redA = red;
  float* abv = red + 160;
  const int tb = (bid & 7) * 32 + (bid >> 3);
  const int t0 = tb * 8;
  const unsigned* xb = (const unsigned*)(x + (size_t)t0 * D_);
  if (wave == 5) {
    const int r = lane & 7;
    const float piv = bf1(x[(size_t)(t0 + r) * D_]);
    const float W = *(const float*)(ws + WS_WSUM);
    __syncthreads();
    float s1 = (redA[r] + redA[24 + r]) + (redA[48 + r] + redA[72 + r]) + redA[96 + r];
    float s2 = (redA[8 + r] + redA[32 + r]) + (redA[56 + r] + redA[80 + r]) + redA[104 + r];
    float s3 = (redA[16 + r] + redA[40 + r]) + (redA[64 + r] + redA[88 + r]) + redA[112 + r];
    float mb, sb;
    ln_from_sums(s1, s2, piv, mb, sb);
    float rs = __builtin_amdgcn_rcpf(sb);
    float rv = rs * __builtin_fmaf(piv - mb, W, s3);
    unsigned key = 0x80000000u | (ord16(f2bf(rv)) << 11) | (unsigned)(2047 - (t0 + r));
    unsigned best = key;
    best = max(best, (unsigned)__builtin_amdgcn_update_dpp(0, (int)best, 0x111, 0xf, 0xf, true));
    best = max(best, (unsigned)__builtin_amdgcn_update_dpp(0, (int)best, 0x112, 0xf, 0xf, true));
    best = max(best, (unsigned)__builtin_amdgcn_update_dpp(0, (int)best, 0x114, 0xf, 0xf, true));
    best = (unsigned)__builtin_amdgcn_readlane((int)best, 7);
    if (key == best) {
      u64 gr = ((u64)best << 32) | (u64)((__float_as_uint(mb) >> 16) | (__float_as_uint(sb) & 0xffff0000u));
      __hip_atomic_store((GAS u64*)(ws + WS_KEYS) + (lane >> 3) * 256 + bid, gr, RLX_AGENT);
      redW[0] = (unsigned)r;
    }
    if (lane < 8) *(f2*)(abv + 2 * r) = f2{rs, -mb * rs};
    STAMPT(2, 2, 320);
    __syncthreads();
    return;
  }
  unsigned raw[8];
#pragma unroll
  for (int r = 0; r < 8; ++r) raw[r] = xb[r * (D_ / 2) + tid];
  const u4 y0 = *(const u4*)(ytb + ((size_t)tb * D_ + 2 * tid) * 8);
  const u4 y1 = *(const u4*)(ytb + ((size_t)tb * D_ + 2 * tid) * 8 + 8);
  const f2 wv = unpk(((const unsigned*)wr)[tid]);
  f2 h[8];
  {
    float v[24];
#pragma unroll
    for (int r = 0; r < 8; ++r) {
      const float piv = bflo(xb[r * (D_ / 2)]);
      unsigned ya = y0[r >> 1], yb = y1[r >> 1];
      f2 yy = (r & 1) ? f2{bfhi(ya), bfhi(yb)} : f2{bflo(ya), bflo(yb)};
      f2 hs = unpk(raw[r]) + yy;
      h[r] = unpk(pk_bf(hs.x, hs.y));
      f2 dd = h[r] - piv;
      v[r] = dd.x + dd.y;
      v[r + 8] = __builtin_fmaf(dd.x, dd.x, dd.y * dd.y);
      v[r + 16] = __builtin_fmaf(dd.x, wv.x, dd.y * wv.y);
    }
    float w[12], o[6];
#pragma unroll
    for (int i = 0; i < 12; ++i) w[i] = swap32_add(v[i], v[i + 12]);
#pragma unroll
    for (int i = 0; i < 6; ++i) o[i] = swap16_add(w[i], w[i + 6]);
    row_sum6(o);
    if ((lane & 15) == 0) {
      float* dst = redA + wave * 24 + (lane >> 4) * 6;
      *(f2*)dst = f2{o[0], o[1]}; *(f2*)(dst + 2) = f2{o[2], o[3]}; *(f2*)(dst + 4) = f2{o[4], o[5]};
    }
  }
  __syncthreads();
  STAMP(2, 1);
  __syncthreads();
  const int brow = (int)redW[0];
  unsigned* stg = (unsigned*)lkeys + wave * 512;
#pragma unroll
  for (int r = 0; r < 8; r += 2) {
    f4 c = *(const f4*)(abv + 2 * r);
    f2 o0 = h[r] + __builtin_elementwise_fma(h[r], f2{c.x, c.x}, f2{c.y, c.y});
    f2 o1 = h[r + 1] + __builtin_elementwise_fma(h[r + 1], f2{c.z, c.z}, f2{c.w, c.w});
    stg[r * 64 + lane] = pk_bf(o0.x, o0.y);
    stg[(r + 1) * 64 + lane] = pk_bf(o1.x, o1.y);
  }
  {
    const int rr = lane >> 4, ch = lane & 15;
    u4 q0 = *(const u4*)(stg + rr * 64 + ch * 4), q1 = *(const u4*)(stg + (rr + 4) * 64 + ch * 4);
    us* o0p = out + (size_t)(t0 + rr) * D_ + wave * 128 + ch * 8;
    if (rr != brow) asm volatile("global_store_dwordx4 %0, %1, off sc1\n\ts_nop 1" :: "v"(o0p), "v"(q0) : "memory");
    if (rr + 4 != brow) asm volatile("global_store_dwordx4 %0, %1, off sc1\n\ts_nop 1" :: "v"(o0p + 4 * D_), "v"(q1) : "memory");
  }
}

extern "C" __global__ void k9_end() {}

extern "C" void kernel_launch(void* const* d_in, const int* in_sizes, int n_in, void* d_out, int out_size,
                              void* d_ws, size_t ws_size, hipStream_t stream) {
  const us* x = (const us*)d_in[0];
  const us* Are = (const us*)d_in[1];
  const us* Aim = (const us*)d_in[2];
  const us* Bre = (const us*)d_in[3];
  const us* Bim = (const us*)d_in[4];
  const us* Cre = (const us*)d_in[5];
  const us* Cim = (const us*)d_in[6];
  const us* logdt = (const us*)d_in[7];
  const us* Dskip = (const us*)d_in[8];
  const us* wr = (const us*)d_in[10];
  const us* wg = (const us*)d_in[11];
  const us* wu = (const us*)d_in[12];
  const us* wd = (const us*)d_in[13];
  us* out = (us*)d_out;
  char* ws = (char*)d_ws;
  ka_conv<<<D_, 256, K2_LDS, stream>>>(x, Are, Aim, Bre, Bim, Cre, Cim, logdt, Dskip, wr, wg, wu, wd, out, ws);
  k3_tail<<<392, 384, 0, stream>>>(x, wr, wg, wu, wd, out, ws);
}
```

```cpp
#include <hip/hip_runtime.h>
#include <hip/hip_bf16.h>
#include <stdint.h>

typedef _Float16 h8 __attribute__((ext_vector_type(8)));
typedef _Float16 h2 __attribute__((ext_vector_type(2)));
typedef float f4 __attribute__((ext_vector_type(4)));
typedef float f2 __attribute__((ext_vector_type(2)));
typedef unsigned u4 __attribute__((ext_vector_type(4)));
typedef unsigned u2 __attribute__((ext_vector_type(2)));
typedef unsigned short us;
typedef unsigned long long u64;
#define GAS __attribute__((address_space(1)))
#define LAS __attribute__((address_space(3)))

constexpr int L_ = 2048, D_ = 640, N_ = 192, F_ = 1536;
constexpr int LP = 2048 + 64;
#define STAMPD(kid, slot) do {} while (0)
#define STAMP(kid, slot) do {} while (0)
#define STAMPT(kid, slot, thr) do {} while (0)

constexpr int NR_LEN  = 608;
constexpr int F2_OFF  = 2 * NR_LEN;
constexpr int F3_OFF  = F2_OFF + 8 * 64;
constexpr int KIMG_ELEMS = F3_OFF + 4 * 80;
constexpr int KCHUNKS = KIMG_ELEMS / 8;

constexpr size_t WS_FLAGS = 0;
constexpr size_t WS_KEYS  = WS_FLAGS;
constexpr size_t WS_ACT   = WS_FLAGS + 16384;
constexpr size_t WS_WSUM  = 40960;
constexpr size_t WS_EPOCH = 41216;
constexpr size_t WS_LNF   = 41472;
constexpr size_t WS_ACK   = WS_LNF + 32768;
constexpr size_t WS_UT    = WS_ACK + 8192;
constexpr size_t WS_YT    = WS_UT + (size_t)D_ * L_ * 2;
constexpr size_t WS_TOTAL = WS_YT + (size_t)D_ * L_ * 2;

constexpr int TAB_PITCH = 200;
constexpr int SM_KC = 57 * TAB_PITCH * 4;
constexpr int SM_U  = SM_KC + 1552;
constexpr int SM_SY = SM_U + 4608;
constexpr int K2_LDS = SM_SY + 32;
constexpr int SM_KI = 0;
constexpr int SM_P  = 4096;

constexpr int kc_of_v(int v) {
  if (v < 256)  { int h = v >> 7, r = v & 127; return ((h * 8 + (r & 7)) * 16) + (r >> 3); }
  if (v < 512)  { int w = v - 256, h = w >> 7, r = w & 127; return ((16 + h * 4 + ((r & 7) >> 1)) * 16) + (r >> 3); }
  if (v < 1024) { int w = v - 512, h = w >> 7, r = w & 127; return ((24 + h * 2 + ((r & 7) >> 2)) * 16) + (r >> 3); }
  if (v < 2048) { int w = v - 1024, h = w >> 7, r = w & 127; return ((32 + h) * 16) + (r >> 3); }
  return 40 * 16;
}
constexpr int bf16_round_int(int j) {
  if (j <= 256) return j;
  int step = j < 512 ? 2 : (j < 1024 ? 4 : 8);
  int lo = (j / step) * step, rem = j - lo;
  if (rem * 2 < step) return lo;
  if (rem * 2 > step) return lo + step;
  return ((lo / step) & 1) ? lo + step : lo;
}
constexpr int ZIDX = 768;

struct GatherTab { unsigned v[KIMG_ELEMS]; };
constexpr GatherTab make_gather() {
  GatherTab t{};
  for (int i = 0; i < KIMG_ELEMS; ++i) t.v[i] = SM_KC + ZIDX * 2;
  for (int e = 0; e < 2; ++e)
    for (int i = 0; i < NR_LEN; ++i) {
      int j = 543 - (i + e);
      if (j >= 0 && j < 515) t.v[e * NR_LEN + i] = (unsigned)(SM_KC + 2 * kc_of_v(bf16_round_int(j)));
    }
  for (int pi = 0; pi < 4; ++pi)
    for (int e = 0; e < 2; ++e)
      for (int i = 0; i < 64; ++i) {
        int p = 79 - (i + e);
        if (p >= 32 && p < 64) {
          int v = 16 * p + 8 * (pi >> 1) + ((pi & 1) ? 8 : 4);
          t.v[F2_OFF + (pi * 2 + e) * 64 + i] = (unsigned)(SM_KC + 2 * kc_of_v(v));
        }
      }
  for (int pi = 0; pi < 2; ++pi)
    for (int e = 0; e < 2; ++e)
      for (int i = 0; i < 80; ++i) {
        int qq = 127 - (i + e);
        if (qq >= 64 && qq < 128) {
          int v = 16 * qq + (pi ? 16 : 8);
          t.v[F3_OFF + (pi * 2 + e) * 80 + i] = (unsigned)(SM_KC + 2 * kc_of_v(v));
        }
      }
  return t;
}
__device__ const GatherTab g_gather = make_gather();

struct BoxTab { us v[7 * 64 * 8]; };
constexpr BoxTab make_box() {
  BoxTab t{};
  const int nn[6] = {3, 5, 3, 5, 7, 9};
  const int sh[6] = {3, 6, 11, 14, 5, 12};
  const int w0[6] = {-16, -16, -24, -24, -16, -24};
  for (int m = 0; m < 6; ++m)
    for (int lane = 0; lane < 64; ++lane)
      for (int j = 0; j < 8; ++j) {
        int b = lane & 15, k = 8 * (lane >> 4) + j;
        int idx = w0[m] + k;
        bool on = idx <= b - sh[m] && idx >= b - sh[m] - (nn[m] - 1);
        t.v[(m * 64 + lane) * 8 + j] = on ? 0x3C00 : 0;
      }
  for (int lane = 0; lane < 64; ++lane)
    for (int j = 0; j < 8; ++j) {
      int b = lane & 15, k = 8 * (lane >> 4) + j;
      t.v[(6 * 64 + lane) * 8 + j] = (k - b == 4 || k - b == 5) ? 0xFFFF : 0;
    }
  return t;
}
__device__ const BoxTab g_box = make_box();

#define RLX_AGENT __ATOMIC_RELAXED, __HIP_MEMORY_SCOPE_AGENT
constexpr unsigned SPIN_CAP = 1u << 22;
__device__ __forceinline__ float bflo(unsigned w) { return __uint_as_float(w << 16); }
__device__ __forceinline__ float bfhi(unsigned w) { return __uint_as_float(w & 0xffff0000u); }
__device__ __forceinline__ float bf1(us v) { return __uint_as_float((unsigned)v << 16); }
__device__ __forceinline__ us f2bf(float f) { __bf16 b = (__bf16)f; return __builtin_bit_cast(us, b); }
__device__ __forceinline__ float rbf(float f) { return bf1(f2bf(f)); }
typedef __bf16 b2 __attribute__((ext_vector_type(2)));
__device__ __forceinline__ unsigned pk_bf(float a, float b) {
  b2 v; v.x = (__bf16)a; v.y = (__bf16)b; return __builtin_bit_cast(unsigned, v);
}
__device__ __forceinline__ unsigned pk_h(float a, float b) {
  h2 v; v.x = (_Float16)a; v.y = (_Float16)b; return __builtin_bit_cast(unsigned, v);
}
template <int CTRL> __device__ __forceinline__ float dpp_add(float v) {
  int t = __builtin_amdgcn_update_dpp(0, __float_as_int(v), CTRL, 0xf, 0xf, true);
  return v + __int_as_float(t);
}
__device__ __forceinline__ float row_sum(float v) {
  v = dpp_add<0x121>(v); v = dpp_add<0x122>(v); v = dpp_add<0x124>(v); v = dpp_add<0x128>(v);
  return v;
}
__device__ __forceinline__ float swap32_add(float a, float b) {
  auto r = __builtin_amdgcn_permlane32_swap(__float_as_uint(a), __float_as_uint(b), false, false);
  return __uint_as_float(r[0]) + __uint_as_float(r[1]);
}
__device__ __forceinline__ float swap16_add(float a, float b) {
  auto r = __builtin_amdgcn_permlane16_swap(__float_as_uint(a), __float_as_uint(b), false, false);
  return __uint_as_float(r[0]) + __uint_as_float(r[1]);
}
__device__ __forceinline__ void wave_reduce16(const float (&v)[16], float (&out)[4]) {
  float w[8];
#pragma unroll
  for (int i = 0; i < 8; ++i) w[i] = swap32_add(v[i], v[i + 8]);
#pragma unroll
  for (int i = 0; i < 4; ++i) out[i] = row_sum(swap16_add(w[i], w[i + 4]));
}
__device__ __forceinline__ void wave_reduce8(const float (&v)[8], float (&out)[2]) {
  float w[4];
#pragma unroll
  for (int i = 0; i < 4; ++i) w[i] = swap32_add(v[i], v[i + 4]);
#pragma unroll
  for (int i = 0; i < 2; ++i) out[i] = row_sum(swap16_add(w[i], w[i + 2]));
}
__device__ __forceinline__ float wave_reduce4(const float (&v)[4]) {
  float w0 = swap32_add(v[0], v[2]), w1 = swap32_add(v[1], v[3]);
  return row_sum(swap16_add(w0, w1));
}
#define DPPA(i, st) "v_add_f32_dpp %" #i ", %" #i ", %" #i " row_ror:" #st " row_mask:0xf bank_mask:0xf\n\t"
__device__ __forceinline__ void row_sum8(float (&o)[8]) {
  asm volatile("s_nop 1\n\t"
    DPPA(0,1) DPPA(1,1) DPPA(2,1) DPPA(3,1) DPPA(4,1) DPPA(5,1) DPPA(6,1) DPPA(7,1)
    DPPA(0,2) DPPA(1,2) DPPA(2,2) DPPA(3,2) DPPA(4,2) DPPA(5,2) DPPA(6,2) DPPA(7,2)
    DPPA(0,4) DPPA(1,4) DPPA(2,4) DPPA(3,4) DPPA(4,4) DPPA(5,4) DPPA(6,4) DPPA(7,4)
    DPPA(0,8) DPPA(1,8) DPPA(2,8) DPPA(3,8) DPPA(4,8) DPPA(5,8) DPPA(6,8) DPPA(7,8)
    : "+v"(o[0]), "+v"(o[1]), "+v"(o[2]), "+v"(o[3]), "+v"(o[4]), "+v"(o[5]), "+v"(o[6]), "+v"(o[7]));
}
__device__ __forceinline__ void row_sum6(float (&o)[6]) {
  asm volatile("s_nop 1\n\t"
    DPPA(0,1) DPPA(1,1) DPPA(2,1) DPPA(3,1) DPPA(4,1) DPPA(5,1)
    DPPA(0,2) DPPA(1,2) DPPA(2,2) DPPA(3,2) DPPA(4,2) DPPA(5,2)
    DPPA(0,4) DPPA(1,4) DPPA(2,4) DPPA(3,4) DPPA(4,4) DPPA(5,4)
    DPPA(0,8) DPPA(1,8) DPPA(2,8) DPPA(3,8) DPPA(4,8) DPPA(5,8)
    : "+v"(o[0]), "+v"(o[1]), "+v"(o[2]), "+v"(o[3]), "+v"(o[4]), "+v"(o[5]));
}
__device__ __forceinline__ void row_sum4(float (&o)[4]) {
  asm volatile("s_nop 1\n\t"
    DPPA(0,1) DPPA(1,1) DPPA(2,1) DPPA(3,1)
    DPPA(0,2) DPPA(1,2) DPPA(2,2) DPPA(3,2)
    DPPA(0,4) DPPA(1,4) DPPA(2,4) DPPA(3,4)
    DPPA(0,8) DPPA(1,8) DPPA(2,8) DPPA(3,8)
    : "+v"(o[0]), "+v"(o[1]), "+v"(o[2]), "+v"(o[3]));
}
__device__ __forceinline__ void par_sum4(float (&o)[4]) {
  asm volatile("s_nop 1\n\t"
    DPPA(0,2) DPPA(1,2) DPPA(2,2) DPPA(3,2)
    DPPA(0,4) DPPA(1,4) DPPA(2,4) DPPA(3,4)
    DPPA(0,8) DPPA(1,8) DPPA(2,8) DPPA(3,8)
    : "+v"(o[0]), "+v"(o[1]), "+v"(o[2]), "+v"(o[3]));
}
__device__ __forceinline__ float rdlane(float v, int l) {
  return __int_as_float(__builtin_amdgcn_readlane(__float_as_int(v), l));
}

__device__ __forceinline__ f2 cmul(f2 a, f2 b) {
  f2 c;
  asm("v_pk_mul_f32 %0, %1, %2 op_sel:[0,0] op_sel_hi:[1,0]\n\tv_pk_fma_f32 %0, %1, %2, %0 op_sel:[1,1,0] op_sel_hi:[0,1,1] neg_lo:[1,0,0]"
      : "=&v"(c) : "v"(a), "v"(b));
  return c;
}
__device__ __forceinline__ f2 cexp_p(float lam, float rh, float rl, float p) {
  float mag = __builtin_amdgcn_exp2f(lam * p);
  float x = rh * p;
  float f = x - __builtin_rintf(x);
  f = __builtin_fmaf(rl, p, f);
  f2 w; w.x = mag * __builtin_amdgcn_cosf(f); w.y = mag * __builtin_amdgcn_sinf(f); return w;
}
#define MFMA16(a, b, c) __builtin_amdgcn_mfma_f32_16x16x32_f16((a), (b), (c), 0, 0, 0)
__device__ __forceinline__ h8 as_h8(u4 v) { return __builtin_bit_cast(h8, v); }

__device__ __forceinline__ void ln_from_sums(float s1, float s2, float piv, float& mb, float& sb) {
  float dm = s1 * (1.f / 640.f);
  float var = fmaxf(__builtin_fmaf(-dm, dm, s2 * (1.f / 640.f)), 0.f);
  mb = rbf(piv + dm);
  sb = rbf(__builtin_amdgcn_sqrtf(rbf(rbf(var) + 1e-5f)));
}
__device__ __forceinline__ f2 unpk(unsigned w) { return f2{bflo(w), bfhi(w)}; }

__device__ __forceinline__ h8 ld_h8(const char* p) { return as_h8(*(const u4*)p); }
__device__ __forceinline__ h8 ld_4dw(const char* p0, const char* p1) {
  const unsigned* a = (const unsigned*)p0; const unsigned* b = (const unsigned*)p1;
  return as_h8(u4{a[0], a[1], b[0], b[1]});
}
__device__ __forceinline__ h8 cvt_pair(f4 a, f4 b) {
  return as_h8(u4{pk_h(a[0], a[1]), pk_h(a[2], a[3]), pk_h(b[0], b[1]), pk_h(b[2], b[3])});
}
__device__ __forceinline__ void glds16(const void* g, void* l) {
  __builtin_amdgcn_global_load_lds((const GAS unsigned*)g, (LAS unsigned*)l, 16, 0, 0);
}
__device__ __forceinline__ void conv_epi(f4 acc, float sc, us* dst) {
  u2 o; o.x = pk_bf(acc[0] * sc, acc[1] * sc); o.y = pk_bf(acc[2] * sc, acc[3] * sc);
  *(u2*)dst = o;
}
#define LDU(a) ((unsigned)*(const LAS us*)(size_t)(a))
#define LDP(a, b) (LDU(a) | (LDU(b) << 16))

template <int OFF> __device__ __forceinline__ u4 lds_rd128(unsigned addr) {
  u4 v; asm volatile("ds_read_b128 %0, %1 offset:%2" : "=v"(v) : "v"(addr), "i"(OFF) : "memory"); return v;
}
template <int N> __device__ __forceinline__ void lgkm_wait() {
  asm volatile("s_waitcnt lgkmcnt(%0)" :: "i"(N) : "memory"); __builtin_amdgcn_sched_barrier(0);
}
template <int M, int M0, int M1, bool T3, int PF>
__device__ __forceinline__ void near_step(const h8 (&A)[17], u4 (&f)[PF], unsigned fa, f4& a0, f4& a1, f4& a2) {
  if constexpr (M < M1) {
    if constexpr (M >= M0 + PF) lgkm_wait<((M1 - 1 - M) < (PF - 1)) ? (M1 - 1 - M) : (PF - 1)>();
    const h8 ff = as_h8(f[(M - M0) % PF]);
    if constexpr (M <= 16) a0 = MFMA16(A[M], ff, a0);
    if constexpr (M >= 8 && M <= 24) a1 = MFMA16(A[M - 8], ff, a1);
    if constexpr (T3 && M >= 16) a2 = MFMA16(A[M - 16], ff, a2);
    __builtin_amdgcn_sched_barrier(0);
    if constexpr (M + PF < M1) f[(M - M0) % PF] = lds_rd128<(M + PF) * 64>(fa);
    near_step<M + 1, M0, M1, T3, PF>(A, f, fa, a0, a1, a2);
  }
}
template <int M, int M0, int PF> __device__ __forceinline__ void near_fill(u4 (&f)[PF], unsigned fa) {
  if constexpr (M < M0 + PF) { f[M - M0] = lds_rd128<M * 64>(fa); near_fill<M + 1, M0, PF>(f, fa); }
}
template <int M0, int M1, bool T3>
__device__ __forceinline__ void near_band(const h8 (&A)[17], unsigned fa, f4& a0, f4& a1, f4& a2) {
  constexpr int PF = 6;
  u4 f[PF];
  near_fill<M0, M0, PF>(f, fa);
  __builtin_amdgcn_s_waitcnt(0xC07F);
  __builtin_amdgcn_sched_barrier(0);
  near_step<M0, M0, M1, T3, PF>(A, f, fa, a0, a1, a2);
}

__device__ __forceinline__ void sy_arrive(char* sm, int idx, int lane) {
  asm volatile("s_waitcnt lgkmcnt(0)" ::: "memory");
  if (lane == 0) __hip_atomic_fetch_add((LAS unsigned*)(LAS char*)(sm + SM_SY) + idx, 1u, __ATOMIC_RELAXED, __HIP_MEMORY_SCOPE_WORKGROUP);
}
__device__ __forceinline__ void sy_wait(char* sm, int idx, unsigned n) {
  volatile LAS unsigned* c = (volatile LAS unsigned*)(LAS char*)(sm + SM_SY) + idx;
#define SYSL 0
  while (*c < n) __builtin_amdgcn_s_sleep(SYSL);
  asm volatile("" ::: "memory");
}

extern "C" __global__ __launch_bounds__(256, 3) void ka_conv(
    const us* __restrict__ x, const us* __restrict__ Are, const us* __restrict__ Aim, const us* __restrict__ Bre,
    const us* __restrict__ Bim, const us* __restrict__ Cre, const us* __restrict__ Cim, const us* __restrict__ logdt,
    const us* __restrict__ Dskip, const us* __restrict__ wr, const us* __restrict__ wg, const us* __restrict__ wu,
    const us* __restrict__ wd, const us* __restrict__ out, char* __restrict__ ws) {
  extern __shared__ __attribute__((aligned(16))) char sm[];
  const int tid = threadIdx.x, lane = tid & 63, wave = __builtin_amdgcn_readfirstlane(tid >> 6);
  const int bid = blockIdx.x;
  STAMP(0, 0);
  const int d = (bid & 7) * 80 + (bid >> 3);
  const int col = lane & 15, g = lane >> 4;
  char* kc = sm + SM_KC;
  const char* ub = sm + SM_U + (256 + 16 * col + 8 * g) * 2;
  us* yt = (us*)(ws + WS_YT) + ((size_t)(2 * col + (g >> 1)) * D_ + d) * 8 + (g & 1) * 4;
  constexpr int YTS = 32 * D_ * 8;
  const f4 Z = {0.f, 0.f, 0.f, 0.f};
  if (tid < 8) ((unsigned*)(sm + SM_SY))[tid] = 0;
  asm volatile("s_waitcnt lgkmcnt(0)" ::: "memory");
  u64 ep;
  us r_are, r_aim, r_bre, r_bim, r_cre, r_cim;
  const size_t pidx = (size_t)d * N_ + (tid < N_ ? tid : 0);
#define LOAD_PARAMS() do { r_are = Are[pidx]; r_aim = Aim[pidx]; r_bre = Bre[pidx]; r_bim = Bim[pidx]; r_cre = Cre[pidx]; r_cim = Cim[pidx]; \
    ep = __hip_atomic_load((GAS u64*)(ws + WS_EPOCH), RLX_AGENT); } while (0)
  unsigned selfpf = 0;
  if (bid < 256) {
    __builtin_amdgcn_s_setprio(3);
    const int tb = (bid & 7) * 32 + (bid >> 3);
    const unsigned* xa = (const unsigned*)(x + (size_t)(tb * 8 + 2 * wave) * D_);
    unsigned ra[5], rb[5];
#pragma unroll
    for (int k = 0; k < 5; ++k) { ra[k] = xa[lane + 64 * k]; rb[k] = xa[D_ / 2 + lane + 64 * k]; }
    __builtin_amdgcn_sched_barrier(0);
    selfpf = *(const GAS unsigned*)(ws + (lane == 0 ? WS_UT + (size_t)tb * D_ * 16 + wave * 2560 : (lane == 1 ? WS_LNF + wave * 8192 : WS_YT + (size_t)(lane & 31) * 81920 + wave * 4)));
    LOAD_PARAMS();
    __builtin_amdgcn_sched_barrier(0);
    const float pa = bflo(xa[0]), pb = bflo(xa[D_ / 2]);
    f2 va[5], vb[5];
    f2 s1a = {0.f, 0.f}, s2a = s1a, s1b = s1a, s2b = s1a;
#pragma unroll
    for (int k = 0; k < 5; ++k) {
      va[k] = unpk(ra[k]); vb[k] = unpk(rb[k]);
      f2 da = va[k] - pa, db = vb[k] - pb;
      s1a += da; s2a = __builtin_elementwise_fma(da, da, s2a);
      s1b += db; s2b = __builtin_elementwise_fma(db, db, s2b);
    }
    float rs4 = row_sum(swap16_add(swap32_add(s1a.x + s1a.y, s2a.x + s2a.y), swap32_add(s1b.x + s1b.y, s2b.x + s2b.y)));
    const float S1 = (lane & 1) ? rdlane(rs4, 16) : rdlane(rs4, 0), S2 = (lane & 1) ? rdlane(rs4, 48) : rdlane(rs4, 32);
    float mb, sb;
    ln_from_sums(S1, S2, (lane & 1) ? pb : pa, mb, sb);
    const float aa = 128.f * __builtin_amdgcn_rcpf(sb), bb = -mb * aa;
    const float aA = rdlane(aa, 0), bA = rdlane(bb, 0), aB = rdlane(aa, 1), bB = rdlane(bb, 1);
#pragma unroll
    for (int k = 0; k < 5; ++k) {
      f2 u0 = __builtin_elementwise_fma(va[k], f2{aA, aA}, f2{bA, bA});
      f2 u1 = __builtin_elementwise_fma(vb[k], f2{aB, aB}, f2{bB, bB});
      *(u2*)(sm + wave * 2560 + (lane + 64 * k) * 8) = u2{pk_h(u0.x, u1.x), pk_h(u0.y, u1.y)};
    }
    __syncthreads();
    {
      const unsigned* tp = (const unsigned*)sm + tid;
      char* ut = ws + WS_UT + ((size_t)tb * D_ + tid) * 16;
      u4 q0 = {tp[0], tp[640], tp[1280], tp[1920]}, q1 = {tp[256], tp[896], tp[1536], tp[2176]};
      asm volatile("global_store_dwordx4 %0, %1, off sc1\n\tglobal_store_dwordx4 %2, %3, off sc1\n\ts_nop 1"
                   :: "v"(ut), "v"(q0), "v"(ut + 4096), "v"(q1) : "memory");
      if (wave < 2) {
        u4 q2 = {tp[512], tp[1152], tp[1792], tp[2432]};
        asm volatile("global_store_dwordx4 %0, %1, off sc1\n\ts_nop 1" :: "v"(ut + 8192), "v"(q2) : "memory");
      }
      asm volatile("s_waitcnt vmcnt(0)" ::: "memory");
    }
    STAMP(0, 5);
    __syncthreads();
    if (wave == 3 && lane < 16) __hip_atomic_store((GAS u64*)(ws + WS_LNF) + lane * 256 + bid, ep + 1ull, RLX_AGENT);
    __builtin_amdgcn_s_setprio(0);
  } else {
    if (wave == 3 && bid < 320) {
      u64 pc; int o1, o2;
      asm volatile("s_getpc_b64 %0\n\ts_mov_b32 %1, ka_conv@rel32@lo+4\n\ts_mov_b32 %2, k3_tail@rel32@lo+12" : "=s"(pc), "=s"(o1), "=s"(o2));
      int o = (((bid - 256) >> 3) * 64 + lane) * 64; o = o < o2 - o1 ? o : 0;
      selfpf = *(const GAS unsigned*)(pc + (long long)o1 + o);
    }
    LOAD_PARAMS();
    __builtin_amdgcn_s_barrier();
  }
  const u64 tag = ep + 1ull;
  if (wave == 3) {
    __builtin_amdgcn_s_setprio(3);
    if (lane == 0) __hip_atomic_store((GAS u64*)(ws + WS_ACK) + bid, tag, RLX_AGENT);
    if (lane < 32) *(u4*)(sm + SM_U + lane * 16) = u4{0, 0, 0, 0};
    if (bid >= 256 && bid < 266) ((u4*)(ws + WS_FLAGS))[(bid - 256) * 256 + lane] = u4{0, 0, 0, 0},
        ((u4*)(ws + WS_FLAGS))[(bid - 256) * 256 + 64 + lane] = u4{0, 0, 0, 0},
        ((u4*)(ws + WS_FLAGS))[(bid - 256) * 256 + 128 + lane] = u4{0, 0, 0, 0},
        ((u4*)(ws + WS_FLAGS))[(bid - 256) * 256 + 192 + lane] = u4{0, 0, 0, 0};
    if (bid == 266) {
      const unsigned* w2 = (const unsigned*)wr + lane;
      float sw = 0.f;
#pragma unroll
      for (int k = 0; k < 5; ++k) { unsigned q = w2[64 * k]; sw += bflo(q) + bfhi(q); }
      sw = row_sum(sw);
      sw = rdlane(sw, 0) + rdlane(sw, 16) + rdlane(sw, 32) + rdlane(sw, 48);
      if (lane == 0) *(float*)(ws + WS_WSUM) = sw;
    }
    const u4* bt = (const u4*)g_box.v + lane;
    u4 bx[4];
#pragma unroll
    for (int m = 0; m < 4; ++m) bx[m] = bt[64 * m];
    {
      GAS u64* fp = (GAS u64*)(ws + WS_LNF) + (bid & 15) * 256 + lane;
#define POLL0 50
#define POLLS 5
#define POLLA 8
      if (bid >= 256) __builtin_amdgcn_s_sleep(POLL0);
      for (unsigned spins = 0;; ++spins) {
        bool ok = __hip_atomic_load(fp + ((bid >> 4) & 3) * 64, RLX_AGENT) == tag;
        if (__all(ok) || spins > SPIN_CAP) break;
        __builtin_amdgcn_s_sleep(POLLA);
      }
      for (unsigned spins = 0;; ++spins) {
        bool ok = (__hip_atomic_load(fp, RLX_AGENT) == tag) & (__hip_atomic_load(fp + 64, RLX_AGENT) == tag) &
                  (__hip_atomic_load(fp + 128, RLX_AGENT) == tag) & (__hip_atomic_load(fp + 192, RLX_AGENT) == tag);
        if (__all(ok) || spins > SPIN_CAP) break;
        __builtin_amdgcn_s_sleep(POLLS);
      }
    }
    STAMPT(0, 6, 192);
    {
      const char* gu = ws + WS_UT + (size_t)d * 16 + (size_t)lane * (D_ * 16);
      u4 q0, q1, q2, q3;
      asm volatile("global_load_dwordx4 %0, %4, off sc1\n\tglobal_load_dwordx4 %1, %5, off sc1\n\t"
                   "global_load_dwordx4 %2, %6, off sc1\n\tglobal_load_dwordx4 %3, %7, off sc1\n\ts_waitcnt vmcnt(0)"
                   : "=&v"(q0), "=&v"(q1), "=&v"(q2), "=&v"(q3)
                   : "v"(gu), "v"(gu + (size_t)64 * (D_ * 16)), "v"(gu + (size_t)128 * (D_ * 16)), "v"(gu + (size_t)192 * (D_ * 16)) : "memory");
      u4* dst = (u4*)(sm + SM_U + 512) + lane;
      dst[0] = q0; dst[64] = q1; dst[128] = q2; dst[192] = q3;
    }
    sy_arrive(sm, 4, lane);
    STAMPT(0, 7, 192);
    h8 sa[6], sb[6];
#pragma unroll
    for (int tau = 0; tau < 6; ++tau) {
      h8 w1 = ld_h8(ub + (256 * tau - 16) * 2), w2 = ld_h8(ub + (256 * tau - 24) * 2);
      sa[tau] = cvt_pair(MFMA16(w1, as_h8(bx[0]), Z), MFMA16(w1, as_h8(bx[1]), Z));
      sb[tau] = cvt_pair(MFMA16(w2, as_h8(bx[2]), Z), MFMA16(w2, as_h8(bx[3]), Z));
    }
#pragma unroll
    for (int i = 0; i < 6; ++i) { asm volatile("" : "+v"(sa[i]), "+v"(sb[i])); }
    sy_wait(sm, 3, 3);
    f4 acc[6] = {Z, Z, Z, Z, Z, Z};
    {
      const int ib = 79 - col + 4 * g;
      const char* kb = sm + SM_KI + (F2_OFF + (ib & 1) * 64 + (ib & ~1)) * 2;
      h8 tA[3], tB[3];
#pragma unroll
      for (int dl = 2; dl <= 4; ++dl) {
        tA[dl - 2] = ld_4dw(kb - dl * 32, kb + 256 - dl * 32); tB[dl - 2] = ld_4dw(kb + 512 - dl * 32, kb + 768 - dl * 32);
      }
      __builtin_amdgcn_sched_barrier(0);
#pragma unroll
      for (int dl = 2; dl <= 4; ++dl)
#pragma unroll
        for (int tau = 0; tau < 6; ++tau)
          if (tau + dl <= 7) { acc[tau + dl - 2] = MFMA16(sa[tau], tA[dl - 2], acc[tau + dl - 2]); acc[tau + dl - 2] = MFMA16(sb[tau], tB[dl - 2], acc[tau + dl - 2]); }
    }
#pragma unroll
    for (int i = 0; i < 6; ++i) *(f4*)(sm + SM_P + i * 1024 + lane * 16) = acc[i];
    sy_arrive(sm, 5, lane);
    __builtin_amdgcn_s_setprio(0);
    if (bid == 300) {
      GAS u64* ap = (GAS u64*)(ws + WS_ACK) + lane;
      for (unsigned spins = 0;; ++spins) {
        bool ok = true;
#pragma unroll
        for (int k = 0; k < 10; ++k) ok = ok & (__hip_atomic_load(ap + 64 * k, RLX_AGENT) == tag);
        if (__all(ok) || spins > SPIN_CAP) break;
        __builtin_amdgcn_s_sleep(8);
      }
      if (lane == 0) __hip_atomic_store((GAS u64*)(ws + WS_EPOCH), tag, RLX_AGENT);
    }
    asm volatile("" :: "v"(selfpf));
    return;
  }
#define KGP 2
  __builtin_amdgcn_s_setprio(KGP);
  const unsigned ldw = ((const unsigned*)logdt)[d >> 1], dsw = ((const unsigned*)Dskip)[d >> 1];
  const float dt = rbf(__expf((d & 1) ? bfhi(ldw) : bflo(ldw)));
  const float dsk = (d & 1) ? bfhi(dsw) : bflo(dsw);
  asm volatile("s_mov_b32 m0, %0" :: "s"(wave * 256) : "memory");
  float ar = dt * bf1(r_are), ai = dt * bf1(r_aim);
  f2 dB = {dt * bf1(r_bre), dt * bf1(r_bim)};
  f2 Cc = {bf1(r_cre), bf1(r_cim)};
  f2 CB = cmul(Cc, dB);
  const float LOG2E = 1.4426950408889634f;
  const float I2PH = 0.15915494309189535f;
  const float I2PL = (float)(0.15915494309189533577 - (double)0.15915494309189535f);
  float lam = ar * LOG2E;
  float rh = ai * I2PH;
  float rl = __builtin_fmaf(ai, I2PL, __builtin_fmaf(ai, I2PH, -rh));
  {
    float gg = fmaxf(1.f, __builtin_amdgcn_exp2f(lam * 2048.f));
    float bnd = row_sum((fabsf(CB.x) + fabsf(CB.y)) * gg);
    if ((lane & 15) == 0) ((float*)(kc))[wave * 4 + (lane >> 4)] = bnd;
    sy_arrive(sm, 0, lane);
  }
  f2 w1 = cexp_p(lam, rh, rl, 1.f), w16 = cexp_p(lam, rh, rl, 16.f), w256 = cexp_p(lam, rh, rl, 256.f);
  f2 w2 = cmul(w1, w1), w32 = cmul(w16, w16), w512 = cmul(w256, w256);
  f2 w4 = cmul(w2, w2), w64 = cmul(w32, w32), w1024 = cmul(w512, w512);
  f2 w8 = cmul(w4, w4), w128 = cmul(w64, w64);
#define DSW(row, val) asm volatile("ds_write_addtid_b32 %0 offset:%1" :: "v"(val), "i"((row) * TAB_PITCH * 4) : "memory")
#define PUTE(row, c) do { f2 c_ = (c); DSW((row), pk_h(c_.x, -c_.y)); } while (0)
#define PUT(row, c) do { f2 c_ = (c); DSW(16 + (row), pk_h(c_.x, c_.y)); } while (0)
  {
    f2 e[8];
    e[1] = w8; e[2] = w16; e[3] = cmul(w8, w16); e[4] = w32;
    e[5] = cmul(w32, e[1]); e[6] = cmul(w32, e[2]); e[7] = cmul(w32, e[3]);
    DSW(0, 0x00003C00u);
#pragma unroll
    for (int l = 1; l < 8; ++l) PUTE(l, e[l]);
    PUTE(8, w64);
#pragma unroll
    for (int l = 1; l < 8; ++l) PUTE(8 + l, cmul(w64, e[l]));
  }
  sy_wait(sm, 0, 3);
  STAMP(0, 1);
  const u4* gt = (const u4*)g_gather.v + 2 * tid;
  u4 ga0 = gt[0], ga1 = gt[1], gb0, gb1;
  if (wave == 0) { gb0 = gt[384]; gb1 = gt[385]; }
  u4 bx4, bx5, bx6;
  if (wave == 2) { const u4* bt = (const u4*)g_box.v + lane; bx4 = bt[256]; bx5 = bt[320]; bx6 = bt[384]; }
  float sc, unsc;
  {
    f4 q0 = *(const f4*)(kc), q1 = *(const f4*)(kc + 16), q2 = *(const f4*)(kc + 32);
    q0 += q1; q0 += q2;
    float sig = ((q0.x + q0.y) + (q0.z + q0.w)) + fabsf(dsk);
    int ex = (int)(__float_as_uint(sig) >> 23) & 255; ex = ex < 24 ? 24 : (ex > 253 ? 253 : ex);
    sc = __uint_as_float((unsigned)(267 - ex) << 23);
    unsc = __uint_as_float((unsigned)(ex - 20) << 23);
    if (tid == 0) *(unsigned*)(kc + ZIDX * 2) = 0;
    f2 c[8];
    c[0] = CB * sc;
    c[1] = cmul(c[0], w1); c[2] = cmul(c[0], w2); c[4] = cmul(c[0], w4); c[3] = cmul(c[1], w2);
    c[5] = cmul(c[1], w4); c[6] = cmul(c[2], w4); c[7] = cmul(c[3], w4);
#pragma unroll
    for (int r = 0; r < 8; ++r) {
      PUT(r, c[r]);
      f2 ch = cmul(c[r], w128); PUT(8 + r, ch);
      if ((r & 1) == 0) {
        f2 s0 = cmul(c[r], w256), s1 = cmul(ch, w256);
        PUT(16 + (r >> 1), s0); PUT(20 + (r >> 1), s1);
        if ((r & 3) == 0) {
          const int rho = r >> 2;
          f2 z0 = cmul(c[r], w512), z1 = cmul(ch, w512), z2 = cmul(s0, w512), z3 = cmul(s1, w512);
          PUT(24 + 0 + rho, z0); PUT(24 + 2 + rho, z1); PUT(24 + 4 + rho, z2); PUT(24 + 6 + rho, z3);
          if (r == 0) {
            f2 y0 = cmul(c[0], w1024);
            PUT(32, y0); PUT(33, cmul(ch, w1024)); PUT(34, cmul(s0, w1024)); PUT(35, cmul(s1, w1024));
            PUT(36, cmul(z0, w1024)); PUT(37, cmul(z1, w1024)); PUT(38, cmul(z2, w1024)); PUT(39, cmul(z3, w1024));
            PUT(40, cmul(y0, w1024));
          }
        }
      }
    }
  }
  sy_arrive(sm, 1, lane);
  sy_wait(sm, 1, 3);
  STAMP(0, 2);
  {
    const unsigned* tab = (const unsigned*)sm;
    f4 acc0 = Z, acc1 = Z;
    const unsigned* pa = tab + col * TAB_PITCH + g * 4;
    const unsigned* pb = tab + (16 + wave * 16 + col) * TAB_PITCH + g * 4;
    u4 ea[12], pb_[12];
#pragma unroll
    for (int kk = 0; kk < 12; ++kk) { ea[kk] = *(const u4*)(pa + kk * 16); pb_[kk] = *(const u4*)(pb + kk * 16); }
#pragma unroll
    for (int kk = 0; kk < 12; kk += 2) {
      acc0 = MFMA16(as_h8(ea[kk]), as_h8(pb_[kk]), acc0);
      acc1 = MFMA16(as_h8(ea[kk + 1]), as_h8(pb_[kk + 1]), acc1);
    }
    __builtin_amdgcn_sched_group_barrier(0x100, 24, 0);
    __builtin_amdgcn_sched_group_barrier(0x008, 12, 0);
    acc0 += acc1;
    acc0[0] = __builtin_fmaf(tid == 0 ? dsk : 0.f, sc, acc0[0]);
    u2 o; o.x = pk_h(acc0[0], acc0[1]); o.y = pk_h(acc0[2], acc0[3]);
    *(u2*)(kc + ((wave * 16 + col) * 16 + 4 * g) * 2) = o;
  }
  sy_arrive(sm, 2, lane);
  sy_wait(sm, 2, 3);
  *(u4*)(sm + SM_KI + tid * 16) = u4{LDP(ga0.x, ga0.y), LDP(ga0.z, ga0.w), LDP(ga1.x, ga1.y), LDP(ga1.z, ga1.w)};
  if (wave == 0) *(u4*)(sm + SM_KI + (192 + tid) * 16) = u4{LDP(gb0.x, gb0.y), LDP(gb0.z, gb0.w), LDP(gb1.x, gb1.y), LDP(gb1.z, gb1.w)};
  sy_arrive(sm, 3, lane);
  unsigned pf;
  {
    const int xcd = bid & 7;
    const int id = (bid >> 3) * 192 + tid;
    u64 pc; int o1, o2;
    asm volatile("s_getpc_b64 %0\n\ts_mov_b32 %1, k3_tail@rel32@lo+4\n\ts_mov_b32 %2, k9_end@rel32@lo+12" : "=s"(pc), "=s"(o1), "=s"(o2));
    int r3 = id / 3, q3 = id - r3 * 3;
    const char* pp0 = (const char*)((r3 < 640 ? wg + (size_t)r3 * F_ : wu + (size_t)(r3 - 640) * F_) + xcd * 192 + q3 * 64);
    int i2 = id - 3840;
    const char* pp1 = (const char*)(wd + (size_t)(i2 >> 1) * D_ + xcd * 80 + (i2 & 1) * 78);
    int i3 = (id - 6912) * 64; i3 = (i3 >= 0 && i3 < o2 - o1) ? i3 : 0;
    const char* pp2 = (const char*)(pc + (long long)o1 + i3);
    pf = *(const GAS unsigned*)(id < 3840 ? pp0 : (id < 6912 ? pp1 : pp2));
  }
  sy_wait(sm, 3, 3);
  __builtin_amdgcn_s_setprio(0);
  STAMP(0, 3);
  const int i0 = 15 - col + 8 * g;
  const char* ka = sm + SM_KI + ((i0 & 1) * NR_LEN + (i0 & ~1)) * 2;
  const int n0 = 3 * wave;
  f4 a0 = Z, a1 = Z, a2 = Z;
  const unsigned fa = (unsigned)(SM_U + (256 + 16 * col + 8 * g + 256 * n0 - 528) * 2);
  if (wave == 2) {
    const int ib = 127 - col + 4 * g;
    const char* kb = sm + SM_KI + (F3_OFF + (ib & 1) * 80 + (ib & ~1)) * 2;
    h8 tt[4];
#pragma unroll
    for (int dl = 4; dl <= 7; ++dl) tt[dl - 4] = ld_4dw(kb - dl * 32, kb + 320 - dl * 32);
    const unsigned kv = LDU(SM_KC + 32 * 16 * 2) * 0x10001u;
    h8 As = as_h8(u4{bx6.x & kv, bx6.y & kv, bx6.z & kv, bx6.w & kv});
    sy_wait(sm, 4, 1);
    u4 w1[4], w2[4], fs[4];
#pragma unroll
    for (int i = 0; i < 4; ++i) {
      w1[i] = *(const u4*)(ub + (256 * i - 16) * 2); w2[i] = *(const u4*)(ub + (256 * i - 24) * 2);
      fs[i] = *(const u4*)(ub + (256 * (4 + i) - 1032) * 2);
    }
    __builtin_amdgcn_sched_barrier(0);
    f4 acc[4];
#pragma unroll
    for (int i = 0; i < 4; ++i) acc[i] = MFMA16(As, as_h8(fs[i]), Z);
#pragma unroll
    for (int tau = 0; tau < 4; ++tau) {
      h8 s3 = cvt_pair(MFMA16(as_h8(w1[tau]), as_h8(bx4), Z), MFMA16(as_h8(w2[tau]), as_h8(bx5), Z));
#pragma unroll
      for (int dl = 4; dl <= 7; ++dl)
        if (tau + dl <= 7) acc[tau + dl - 4] = MFMA16(s3, tt[dl - 4], acc[tau + dl - 4]);
    }
    *(f4*)(sm + SM_P + 6 * 1024 + lane * 16) = acc[0];
    *(f4*)(sm + SM_P + 7 * 1024 + lane * 16) = acc[1];
    a0 = acc[2]; a1 = acc[3];
    asm volatile("s_nop 7\n\ts_nop 7" : "+v"(a0), "+v"(a1));
    sy_arrive(sm, 5, lane);
  }
  h8 A[17];
#pragma unroll
  for (int k = 0; k < 17; ++k) A[k] = ld_4dw(ka + k * 64, ka + k * 64 + 8);
  __builtin_amdgcn_sched_barrier(0);
  sy_wait(sm, 4, 1);
  if (wave == 0) near_band<9, 33, true>(A, fa, a0, a1, a2);
  else if (wave == 1) near_band<0, 33, true>(A, fa, a0, a1, a2);
  else near_band<0, 25, false>(A, fa, a0, a1, a2);
  asm volatile("s_nop 7\n\ts_nop 7" : "+v"(a0), "+v"(a1), "+v"(a2));
  sy_wait(sm, 5, 2);
  {
    const char* pp = sm + SM_P + lane * 16 + (n0 - 2) * 1024;
    if (wave != 0) { a0 += *(const f4*)(pp); a1 += *(const f4*)(pp + 1024); }
    if (wave == 1) { a1 += *(const f4*)(sm + SM_P + 6 * 1024 + lane * 16); a2 += *(const f4*)(sm + SM_P + 7 * 1024 + lane * 16); }
    conv_epi(a0, unsc, yt + YTS * n0);
    conv_epi(a1, unsc, yt + YTS * (n0 + 1));
    if (wave != 2) { a2 += *(const f4*)(pp + 2048); conv_epi(a2, unsc, yt + YTS * (n0 + 2)); }
  }
  asm volatile("" :: "v"(pf), "v"(selfpf));
  STAMP(0, 4);
}

__device__ __forceinline__ unsigned ord16(unsigned b) { return (b & 0x8000u) ? (~b & 0xffffu) : (b | 0x8000u); }
__device__ __forceinline__ void fma8(float* acc, u4 w, float xv) {
  acc[0] = __builtin_fmaf(bflo(w.x), xv, acc[0]); acc[1] = __builtin_fmaf(bfhi(w.x), xv, acc[1]);
  acc[2] = __builtin_fmaf(bflo(w.y), xv, acc[2]); acc[3] = __builtin_fmaf(bfhi(w.y), xv, acc[3]);
  acc[4] = __builtin_fmaf(bflo(w.z), xv, acc[4]); acc[5] = __builtin_fmaf(bfhi(w.z), xv, acc[5]);
  acc[6] = __builtin_fmaf(bflo(w.w), xv, acc[6]); acc[7] = __builtin_fmaf(bfhi(w.w), xv, acc[7]);
}

__device__ __forceinline__ u64 sweep_keys(char* ws, int lane, int rep, int q, u64* lk) {
  GAS u64* kp = (GAS u64*)(ws + WS_KEYS) + rep * 256 + lane;
  u64 b;
#define K3PA 3
#define K3PS 2
  for (unsigned spins = 0;; ++spins) {
    bool ok = (long long)__hip_atomic_load(kp + q * 64, RLX_AGENT) < 0;
    if (__all(ok) || spins > SPIN_CAP) break;
    __builtin_amdgcn_s_sleep(K3PA);
  }
  for (unsigned spins = 0;; ++spins) {
    u64 k0 = __hip_atomic_load(kp, RLX_AGENT), k1 = __hip_atomic_load(kp + 64, RLX_AGENT);
    u64 k2 = __hip_atomic_load(kp + 128, RLX_AGENT), k3 = __hip_atomic_load(kp + 192, RLX_AGENT);
    bool ok = (long long)(k0 & k1 & k2 & k3) < 0;
    b = k0 > k1 ? k0 : k1; u64 c = k2 > k3 ? k2 : k3; b = b > c ? b : c;
    if (__all(ok) || spins > SPIN_CAP) {
      if (lk) { lk[lane] = k0; lk[lane + 64] = k1; lk[lane + 128] = k2; lk[lane + 192] = k3; }
      break;
    }
    __builtin_amdgcn_s_sleep(K3PS);
  }
  unsigned hk = (unsigned)(b >> 32), m = hk;
  m = max(m, (unsigned)__builtin_amdgcn_update_dpp(0, (int)m, 0x121, 0xf, 0xf, true));
  m = max(m, (unsigned)__builtin_amdgcn_update_dpp(0, (int)m, 0x122, 0xf, 0xf, true));
  m = max(m, (unsigned)__builtin_amdgcn_update_dpp(0, (int)m, 0x124, 0xf, 0xf, true));
  m = max(m, (unsigned)__builtin_amdgcn_update_dpp(0, (int)m, 0x128, 0xf, 0xf, true));
  unsigned m0 = (unsigned)__builtin_amdgcn_readlane((int)m, 0), m1 = (unsigned)__builtin_amdgcn_readlane((int)m, 16);
  unsigned m2 = (unsigned)__builtin_amdgcn_readlane((int)m, 32), m3 = (unsigned)__builtin_amdgcn_readlane((int)m, 48);
  m0 = max(max(m0, m1), max(m2, m3));
  int src = __builtin_ctzll(__ballot(hk == m0));
  unsigned lo = (unsigned)__builtin_amdgcn_readlane((int)(unsigned)b, src);
  return ((u64)m0 << 32) | lo;
}
__device__ __forceinline__ float par_sum(float v) {
  v = dpp_add<0x122>(v); v = dpp_add<0x124>(v); v = dpp_add<0x128>(v);
  return v;
}
__device__ __forceinline__ unsigned out_pair(f2 h, float rs, float m) {
  f2 dd = h - m;
  dd = unpk(pk_bf(dd.x, dd.y)) * rs;
  dd = h + unpk(pk_bf(dd.x, dd.y));
  return pk_bf(dd.x, dd.y);
}
__device__ __forceinline__ float xs_of(us xv, us yv, float wm, float wrs, float& hh) {
  hh = rbf(bf1(xv) + bf1(yv));
  return rbf(rbf(hh - wm) * wrs);
}

extern "C" __global__ __launch_bounds__(384, 3) void k3_tail(const us* __restrict__ x, const us* __restrict__ wr,
                                                  const us* __restrict__ wg, const us* __restrict__ wu,
                                                  const us* __restrict__ wd, us* __restrict__ out,
                                                  char* __restrict__ ws) {
  __shared__ __attribute__((aligned(16))) float red[5 * 32 + 16 + 8];
  __shared__ __attribute__((aligned(16))) u64 lkeys[1280];
  unsigned* redW = (unsigned*)(red + 176);
  const int tid = threadIdx.x, lane = tid & 63, wave = __builtin_amdgcn_readfirstlane(tid >> 6);
  const int bid = blockIdx.x;
  STAMP(2, 0);
  const us* ytb = (const us*)(ws + WS_YT);
  if (bid >= 256) {
    const bool f1 = bid < 352;
    const int j = f1 ? bid - 256 : bid - 352;
    const int cb = f1 ? (j & 7) * 12 + (j >> 3) : (j & 7) * 5 + (j >> 3);
    if (wave == 5) {
      __builtin_amdgcn_s_sleep(40);
      const u64 wn = sweep_keys(ws, lane, j & 7, (j >> 3) & 3, f1 ? lkeys : nullptr);
      const unsigned wkey = (unsigned)(wn >> 32), wms = (unsigned)wn;
      const int wrow = 2047 - (int)(wkey & 2047u);
      const int c = lane & 15;
      if (f1) {
        if (lane == 0) { redW[2] = wkey; redW[3] = wms; }
        STAMPT(2, 3, 320);
        __syncthreads();
        __syncthreads();
        const float* rp = red + (c >> 3) * 16 + (c & 7);
        float gs = (rp[0] + rp[32]) + (rp[64] + rp[96]) + rp[128];
        float us_ = (rp[8] + rp[40]) + (rp[72] + rp[104]) + rp[136];
        float gb = rbf(gs);
        float sl = rbf(gb * __builtin_amdgcn_rcpf(1.f + __expf(-gb)));
        unsigned a = (unsigned)f2bf(sl * rbf(us_)) | 0x10000u;
        __hip_atomic_store((GAS unsigned*)(ws + WS_ACT) + (lane >> 4) * F_ + cb * 16 + c, a, RLX_AGENT);
        STAMPT(2, 4, 320);
      } else {
        __syncthreads();
        const int col = cb * 16 + c;
        float fh;
        float fx2 = xs_of(x[(size_t)wrow * D_ + col], ytb[((size_t)(wrow >> 3) * D_ + col) * 8 + (wrow & 7)],
                          bflo(wms), __builtin_amdgcn_rcpf(bfhi(wms)), fh);
        unsigned ob = (wkey >> 11) & 0xffffu;
        unsigned rb = (ob & 0x8000u) ? (ob & 0x7fffu) : (~ob & 0xffffu);
        float sg = rbf(__builtin_amdgcn_rcpf(1.f + __expf(-bf1((us)rb))));
        __syncthreads();
        const float* rp = red + c;
        float ys = (rp[0] + rp[16]) + (rp[32] + rp[48]) + rp[64];
        float y = rbf(rbf(ys) * sg);
        if (lane < 16) out[(size_t)wrow * D_ + col] = f2bf(fh + rbf(fx2 + y));
        STAMPT(2, 5, 320);
      }
      return;
    }
    const int half = tid & 1, rb = tid >> 1;
    __builtin_amdgcn_s_sleep(16);
    if (f1) {
      const size_t o0 = (size_t)rb * F_ + cb * 16 + half * 8;
      u4 wq[8];
#pragma unroll
      for (int k = 0; k < 4; ++k) { wq[k] = *(const u4*)(wg + o0 + (size_t)k * 160 * F_); wq[4 + k] = *(const u4*)(wu + o0 + (size_t)k * 160 * F_); }
      f2 wf[32];
#pragma unroll
      for (int k = 0; k < 8; ++k) { wf[4 * k] = unpk(wq[k].x); wf[4 * k + 1] = unpk(wq[k].y); wf[4 * k + 2] = unpk(wq[k].z); wf[4 * k + 3] = unpk(wq[k].w); }
#pragma unroll
      for (int k = 0; k < 32; ++k) asm volatile("" : "+v"(wf[k]));
      __syncthreads();
      const unsigned wkey = redW[2], wms = redW[3];
      const int wrow = 2047 - (int)(wkey & 2047u);
      const float wm = bflo(wms), wrs = __builtin_amdgcn_rcpf(bfhi(wms));
      const us* xw = x + (size_t)wrow * D_ + rb;
      const us* yw = ytb + ((size_t)(wrow >> 3) * D_ + rb) * 8 + (wrow & 7);
      us xv[4], yv[4];
#pragma unroll
      for (int k = 0; k < 4; ++k) { xv[k] = xw[160 * k]; yv[k] = yw[160 * 8 * k]; }
      f2 acc[8];
#pragma unroll
      for (int i = 0; i < 8; ++i) acc[i] = f2{0.f, 0.f};
#pragma unroll
      for (int k = 0; k < 4; ++k) {
        float hh; float xs = xs_of(xv[k], yv[k], wm, wrs, hh);
        f2 x2v = {xs, xs};
#pragma unroll
        for (int i = 0; i < 4; ++i) {
          acc[i] = __builtin_elementwise_fma(wf[4 * k + i], x2v, acc[i]);
          acc[4 + i] = __builtin_elementwise_fma(wf[16 + 4 * k + i], x2v, acc[4 + i]);
        }
      }
      float w[8], o[4];
#pragma unroll
      for (int i = 0; i < 4; ++i) { w[2 * i] = swap32_add(acc[i].x, acc[4 + i].x); w[2 * i + 1] = swap32_add(acc[i].y, acc[4 + i].y); }
#pragma unroll
      for (int i = 0; i < 4; ++i) o[i] = swap16_add(w[i], w[i + 4]);
      par_sum4(o);
      const int rr = lane >> 4;
      if ((lane & 15) < 2) *(f4*)(red + wave * 32 + half * 16 + 4 * (rr & 1) + 8 * (rr >> 1)) = f4{o[0], o[1], o[2], o[3]};
      __syncthreads();
#pragma unroll
      for (int q = 0; q < 3; ++q) {
        const int cbk = j + 96 * q;
        if (cbk < 256) {
          const u64 gk = lkeys[cbk];
          const unsigned ck = (unsigned)(gk >> 32), cms = (unsigned)gk;
          if (ck != wkey) {
            const int crow = 2047 - (int)(ck & 2047u);
            const float crs = __builtin_amdgcn_rcpf(bfhi(cms));
            unsigned xr = ((const unsigned*)(x + (size_t)crow * D_))[tid];
            const us* yc = ytb + ((size_t)(crow >> 3) * D_ + 2 * tid) * 8 + (crow & 7);
            f2 hs = unpk(xr) + f2{bf1(yc[0]), bf1(yc[8])};
            f2 hh = unpk(pk_bf(hs.x, hs.y));
            ((unsigned*)(out + (size_t)crow * D_))[tid] = out_pair(hh, crs, bflo(cms));
          }
        }
      }
      STAMP(2, 6);
    } else {
      const us* wp = wd + cb * 16 + half * 8;
      u4 wq[10];
#pragma unroll
      for (int k = 0; k < 10; ++k) { int r = rb + 160 * k; r = r < F_ ? r : F_ - 1; wq[k] = *(const u4*)(wp + (size_t)r * D_); }
      f2 wf[40];
#pragma unroll
      for (int k = 0; k < 10; ++k) { wf[4 * k] = unpk(wq[k].x); wf[4 * k + 1] = unpk(wq[k].y); wf[4 * k + 2] = unpk(wq[k].z); wf[4 * k + 3] = unpk(wq[k].w); }
#pragma unroll
      for (int k = 0; k < 40; ++k) asm volatile("" : "+v"(wf[k]));
      __syncthreads();
#define K3A0 20
#define K3AA 2
      __builtin_amdgcn_s_sleep(K3A0);
      GAS unsigned* ap = (GAS unsigned*)(ws + WS_ACT) + (j & 3) * F_ + rb;
      const int last = rb < 96 ? 1440 : 0;
      unsigned a[10];
      for (unsigned spins = 0;; ++spins) {
        if (__all(__hip_atomic_load(ap + 160 * wave, RLX_AGENT) >> 16) || spins > SPIN_CAP) break;
        __builtin_amdgcn_s_sleep(K3AA);
      }
      for (unsigned spins = 0;; ++spins) {
#pragma unroll
        for (int k = 0; k < 9; ++k) a[k] = __hip_atomic_load(ap + 160 * k, RLX_AGENT);
        a[9] = __hip_atomic_load(ap + last, RLX_AGENT);
        unsigned all = a[0] & a[1] & a[2] & a[3] & a[4] & a[5] & a[6] & a[7] & a[8] & a[9];
        if (__all(all >> 16) || spins > SPIN_CAP) break;
        __builtin_amdgcn_s_sleep(1);
      }
      if (rb >= 96) a[9] = 0;
      f2 acc[4] = {f2{0.f, 0.f}, f2{0.f, 0.f}, f2{0.f, 0.f}, f2{0.f, 0.f}};
#pragma unroll
      for (int k = 0; k < 10; ++k) {
        float av = bflo(a[k]); f2 a2 = {av, av};
#pragma unroll
        for (int i = 0; i < 4; ++i) acc[i] = __builtin_elementwise_fma(wf[4 * k + i], a2, acc[i]);
      }
      float w0 = swap32_add(acc[0].x, acc[2].x), w1 = swap32_add(acc[0].y, acc[2].y);
      float w2 = swap32_add(acc[1].x, acc[3].x), w3 = swap32_add(acc[1].y, acc[3].y);
      float o0 = par_sum(swap16_add(w0, w2)), o1 = par_sum(swap16_add(w1, w3));
      const int rr = lane >> 4;
      if ((lane & 15) < 2) *(f2*)(red + wave * 16 + half * 8 + 2 * (rr & 1) + 4 * (rr >> 1)) = f2{o0, o1};
      __syncthreads();
    }
    return;
  }
  float* redA = red;
  float* abv = red + 160;
  const int tb = (bid & 7) * 32 + (bid >> 3);
  const int t0 = tb * 8;
  const unsigned* xb = (const unsigned*)(x + (size_t)t0 * D_);
  if (wave == 5) {
    const int r = lane & 7;
    const float piv = bf1(x[(size_t)(t0 + r) * D_]);
    const float W = *(const float*)(ws + WS_WSUM);
    __syncthreads();
    float s1 = (redA[r] + redA[24 + r]) + (redA[48 + r] + redA[72 + r]) + redA[96 + r];
    float s2 = (redA[8 + r] + redA[32 + r]) + (redA[56 + r] + redA[80 + r]) + redA[104 + r];
    float s3 = (redA[16 + r] + redA[40 + r]) + (redA[64 + r] + redA[88 + r]) + redA[112 + r];
    float mb, sb;
    ln_from_sums(s1, s2, piv, mb, sb);
    float rs = __builtin_amdgcn_rcpf(sb);
    float rv = rs * __builtin_fmaf(piv - mb, W, s3);
    unsigned key = 0x80000000u | (ord16(f2bf(rv)) << 11) | (unsigned)(2047 - (t0 + r));
    unsigned best = key;
    best = max(best, (unsigned)__builtin_amdgcn_update_dpp(0, (int)best, 0x111, 0xf, 0xf, true));
    best = max(best, (unsigned)__builtin_amdgcn_update_dpp(0, (int)best, 0x112, 0xf, 0xf, true));
    best = max(best, (unsigned)__builtin_amdgcn_update_dpp(0, (int)best, 0x114, 0xf, 0xf, true));
    best = (unsigned)__builtin_amdgcn_readlane((int)best, 7);
    if (key == best) {
      u64 gr = ((u64)best << 32) | (u64)((__float_as_uint(mb) >> 16) | (__float_as_uint(sb) & 0xffff0000u));
      __hip_atomic_store((GAS u64*)(ws + WS_KEYS) + (lane >> 3) * 256 + bid, gr, RLX_AGENT);
      redW[0] = (unsigned)r;
    }
    if (lane < 8) *(f2*)(abv + 2 * r) = f2{rs, mb};
    STAMPT(2, 2, 320);
    __syncthreads();
    return;
  }
  unsigned raw[8];
#pragma unroll
  for (int r = 0; r < 8; ++r) raw[r] = xb[r * (D_ / 2) + tid];
  const u4 y0 = *(const u4*)(ytb + ((size_t)tb * D_ + 2 * tid) * 8);
  const u4 y1 = *(const u4*)(ytb + ((size_t)tb * D_ + 2 * tid) * 8 + 8);
  const f2 wv = unpk(((const unsigned*)wr)[tid]);
  f2 h[8];
  {
    float v[24];
#pragma unroll
    for (int r = 0; r < 8; ++r) {
      const float piv = bflo(xb[r * (D_ / 2)]);
      unsigned ya = y0[r >> 1], yb = y1[r >> 1];
      f2 yy = (r & 1) ? f2{bfhi(ya), bfhi(yb)} : f2{bflo(ya), bflo(yb)};
      f2 hs = unpk(raw[r]) + yy;
      h[r] = unpk(pk_bf(hs.x, hs.y));
      f2 dd = h[r] - piv;
      v[r] = dd.x + dd.y;
      v[r + 8] = __builtin_fmaf(dd.x, dd.x, dd.y * dd.y);
      v[r + 16] = __builtin_fmaf(dd.x, wv.x, dd.y * wv.y);
    }
    float w[12], o[6];
#pragma unroll
    for (int i = 0; i < 12; ++i) w[i] = swap32_add(v[i], v[i + 12]);
#pragma unroll
    for (int i = 0; i < 6; ++i) o[i] = swap16_add(w[i], w[i + 6]);
    row_sum6(o);
    if ((lane & 15) == 0) {
      float* dst = redA + wave * 24 + (lane >> 4) * 6;
      *(f2*)dst = f2{o[0], o[1]}; *(f2*)(dst + 2) = f2{o[2], o[3]}; *(f2*)(dst + 4) = f2{o[4], o[5]};
    }
  }
  __syncthreads();
  STAMP(2, 1);
  __syncthreads();
  const int brow = (int)redW[0];
  unsigned* stg = (unsigned*)lkeys + wave * 512;
#pragma unroll
  for (int r = 0; r < 8; r += 2) {
    f4 c = *(const f4*)(abv + 2 * r);
    stg[r * 64 + lane] = out_pair(h[r], c.x, c.y);
    stg[(r + 1) * 64 + lane] = out_pair(h[r + 1], c.z, c.w);
  }
  {
    const int rr = lane >> 4, ch = lane & 15;
    u4 q0 = *(const u4*)(stg + rr * 64 + ch * 4), q1 = *(const u4*)(stg + (rr + 4) * 64 + ch * 4);
    us* o0p = out + (size_t)(t0 + rr) * D_ + wave * 128 + ch * 8;
    __builtin_amdgcn_s_sleep(60);
    if (rr != brow) asm volatile("global_store_dwordx4 %0, %1, off sc1\n\ts_nop 1" :: "v"(o0p), "v"(q0) : "memory");
    if (rr + 4 != brow) asm volatile("global_store_dwordx4 %0, %1, off sc1\n\ts_nop 1" :: "v"(o0p + 4 * D_), "v"(q1) : "memory");
  }
}

extern "C" __global__ void k9_end() {}

extern "C" void kernel_launch(void* const* d_in, const int* in_sizes, int n_in, void* d_out, int out_size,
                              void* d_ws, size_t ws_size, hipStream_t stream) {
  const us* x = (const us*)d_in[0];
  const us* Are = (const us*)d_in[1];
  const us* Aim = (const us*)d_in[2];
  const us* Bre = (const us*)d_in[3];
  const us* Bim = (const us*)d_in[4];
  const us* Cre = (const us*)d_in[5];
  const us* Cim = (const us*)d_in[6];
  const us* logdt = (const us*)d_in[7];
  const us* Dskip = (const us*)d_in[8];
  const us* wr = (const us*)d_in[10];
  const us* wg = (const us*)d_in[11];
  const us* wu = (const us*)d_in[12];
  const us* wd = (const us*)d_in[13];
  us* out = (us*)d_out;
  char* ws = (char*)d_ws;
  ka_conv<<<D_, 256, K2_LDS, stream>>>(x, Are, Aim, Bre, Bim, Cre, Cim, logdt, Dskip, wr, wg, wu, wd, out, ws);
  k3_tail<<<392, 384, 0, stream>>>(x, wr, wg, wu, wd, out, ws);
}
```

```cpp
#include <hip/hip_runtime.h>
#include <hip/hip_bf16.h>
#include <stdint.h>

typedef _Float16 h8 __attribute__((ext_vector_type(8)));
typedef _Float16 h2 __attribute__((ext_vector_type(2)));
typedef float f4 __attribute__((ext_vector_type(4)));
typedef float f2 __attribute__((ext_vector_type(2)));
typedef unsigned u4 __attribute__((ext_vector_type(4)));
typedef unsigned u2 __attribute__((ext_vector_type(2)));
typedef unsigned short us;
typedef unsigned long long u64;
#define GAS __attribute__((address_space(1)))
#define LAS __attribute__((address_space(3)))

constexpr int L_ = 2048, D_ = 640, N_ = 192, F_ = 1536;
#define STAMPD(kid, slot) do {} while (0)
#define STAMP(kid, slot) do {} while (0)
#define STAMPT(kid, slot, thr) do {} while (0)

constexpr int NR_LEN  = 608;
constexpr int F2_OFF  = 2 * NR_LEN;
constexpr int F3_OFF  = F2_OFF + 8 * 64;
constexpr int KIMG_ELEMS = F3_OFF + 4 * 80;
constexpr int KCHUNKS = KIMG_ELEMS / 8;

constexpr size_t WS_FLAGS = 0;
constexpr size_t WS_KEYS  = WS_FLAGS;
constexpr size_t WS_ACT   = WS_FLAGS + 16384;
constexpr size_t WS_WSUM  = 40960;
constexpr size_t WS_EPOCH = 41216;
constexpr size_t WS_LNF   = 41472;
constexpr size_t WS_ACK   = WS_LNF + 32768;
constexpr size_t WS_UT    = WS_ACK + 8192;
constexpr size_t WS_YT    = WS_UT + (size_t)D_ * L_ * 2;
constexpr size_t WS_TOTAL = WS_YT + (size_t)D_ * L_ * 2;

constexpr int TAB_PITCH = 200;
constexpr int SM_KC = 57 * TAB_PITCH * 4;
constexpr int SM_U  = SM_KC + 1552;
constexpr int SM_SY = SM_U + 4608;
constexpr int K2_LDS = SM_SY + 32;
constexpr int SM_KI = 0;
constexpr int SM_P  = 4096;

constexpr int kc_of_v(int v) {
  if (v < 256)  { int h = v >> 7, r = v & 127; return ((h * 8 + (r & 7)) * 16) + (r >> 3); }
  if (v < 512)  { int w = v - 256, h = w >> 7, r = w & 127; return ((16 + h * 4 + ((r & 7) >> 1)) * 16) + (r >> 3); }
  if (v < 1024) { int w = v - 512, h = w >> 7, r = w & 127; return ((24 + h * 2 + ((r & 7) >> 2)) * 16) + (r >> 3); }
  if (v < 2048) { int w = v - 1024, h = w >> 7, r = w & 127; return ((32 + h) * 16) + (r >> 3); }
  return 40 * 16;
}
constexpr int bf16_round_int(int j) {
  if (j <= 256) return j;
  int step = j < 512 ? 2 : (j < 1024 ? 4 : 8);
  int lo = (j / step) * step, rem = j - lo;
  if (rem * 2 < step) return lo;
  if (rem * 2 > step) return lo + step;
  return ((lo / step) & 1) ? lo + step : lo;
}
constexpr int ZIDX = 768;

struct GatherTab { unsigned v[KIMG_ELEMS]; };
constexpr GatherTab make_gather() {
  GatherTab t{};
  for (int i = 0; i < KIMG_ELEMS; ++i) t.v[i] = SM_KC + ZIDX * 2;
  for (int e = 0; e < 2; ++e)
    for (int i = 0; i < NR_LEN; ++i) {
      int j = 543 - (i + e);
      if (j >= 0 && j < 515) t.v[e * NR_LEN + i] = (unsigned)(SM_KC + 2 * kc_of_v(bf16_round_int(j)));
    }
  for (int pi = 0; pi < 4; ++pi)
    for (int e = 0; e < 2; ++e)
      for (int i = 0; i < 64; ++i) {
        int p = 79 - (i + e);
        if (p >= 32 && p < 64) {
          int v = 16 * p + 8 * (pi >> 1) + ((pi & 1) ? 8 : 4);
          t.v[F2_OFF + (pi * 2 + e) * 64 + i] = (unsigned)(SM_KC + 2 * kc_of_v(v));
        }
      }
  for (int pi = 0; pi < 2; ++pi)
    for (int e = 0; e < 2; ++e)
      for (int i = 0; i < 80; ++i) {
        int qq = 127 - (i + e);
        if (qq >= 64 && qq < 128) {
          int v = 16 * qq + (pi ? 16 : 8);
          t.v[F3_OFF + (pi * 2 + e) * 80 + i] = (unsigned)(SM_KC + 2 * kc_of_v(v));
        }
      }
  return t;
}
__device__ const GatherTab g_gather = make_gather();

struct BoxTab { us v[7 * 64 * 8]; };
constexpr BoxTab make_box() {
  BoxTab t{};
  const int nn[6] = {3, 5, 3, 5, 7, 9};
  const int sh[6] = {3, 6, 11, 14, 5, 12};
  const int w0[6] = {-16, -16, -24, -24, -16, -24};
  for (int m = 0; m < 6; ++m)
    for (int lane = 0; lane < 64; ++lane)
      for (int j = 0; j < 8; ++j) {
        int b = lane & 15, k = 8 * (lane >> 4) + j;
        int idx = w0[m] + k;
        bool on = idx <= b - sh[m] && idx >= b - sh[m] - (nn[m] - 1);
        t.v[(m * 64 + lane) * 8 + j] = on ? 0x3C00 : 0;
      }
  for (int lane = 0; lane < 64; ++lane)
    for (int j = 0; j < 8; ++j) {
      int b = lane & 15, k = 8 * (lane >> 4) + j;
      t.v[(6 * 64 + lane) * 8 + j] = (k - b == 4 || k - b == 5) ? 0xFFFF : 0;
    }
  return t;
}
__device__ const BoxTab g_box = make_box();

#define RLX_AGENT __ATOMIC_RELAXED, __HIP_MEMORY_SCOPE_AGENT
constexpr unsigned SPIN_CAP = 1u << 22;
__device__ __forceinline__ float bflo(unsigned w) { return __uint_as_float(w << 16); }
__device__ __forceinline__ float bfhi(unsigned w) { return __uint_as_float(w & 0xffff0000u); }
__device__ __forceinline__ float bf1(us v) { return __uint_as_float((unsigned)v << 16); }
__device__ __forceinline__ us f2bf(float f) { __bf16 b = (__bf16)f; return __builtin_bit_cast(us, b); }
__device__ __forceinline__ float rbf(float f) { return bf1(f2bf(f)); }
typedef __bf16 b2 __attribute__((ext_vector_type(2)));
__device__ __forceinline__ unsigned pk_bf(float a, float b) {
  b2 v; v.x = (__bf16)a; v.y = (__bf16)b; return __builtin_bit_cast(unsigned, v);
}
__device__ __forceinline__ unsigned pk_h(float a, float b) {
  h2 v; v.x = (_Float16)a; v.y = (_Float16)b; return __builtin_bit_cast(unsigned, v);
}
template <int CTRL> __device__ __forceinline__ float dpp_add(float v) {
  int t = __builtin_amdgcn_update_dpp(0, __float_as_int(v), CTRL, 0xf, 0xf, true);
  return v + __int_as_float(t);
}
__device__ __forceinline__ float row_sum(float v) {
  v = dpp_add<0x121>(v); v = dpp_add<0x122>(v); v = dpp_add<0x124>(v); v = dpp_add<0x128>(v);
  return v;
}
__device__ __forceinline__ float swap32_add(float a, float b) {
  auto r = __builtin_amdgcn_permlane32_swap(__float_as_uint(a), __float_as_uint(b), false, false);
  return __uint_as_float(r[0]) + __uint_as_float(r[1]);
}
__device__ __forceinline__ float swap16_add(float a, float b) {
  auto r = __builtin_amdgcn_permlane16_swap(__float_as_uint(a), __float_as_uint(b), false, false);
  return __uint_as_float(r[0]) + __uint_as_float(r[1]);
}
#define DPPA(i, st) "v_add_f32_dpp %" #i ", %" #i ", %" #i " row_ror:" #st " row_mask:0xf bank_mask:0xf\n\t"
__device__ __forceinline__ void row_sum6(float (&o)[6]) {
  asm volatile("s_nop 1\n\t"
    DPPA(0,1) DPPA(1,1) DPPA(2,1) DPPA(3,1) DPPA(4,1) DPPA(5,1)
    DPPA(0,2) DPPA(1,2) DPPA(2,2) DPPA(3,2) DPPA(4,2) DPPA(5,2)
    DPPA(0,4) DPPA(1,4) DPPA(2,4) DPPA(3,4) DPPA(4,4) DPPA(5,4)
    DPPA(0,8) DPPA(1,8) DPPA(2,8) DPPA(3,8) DPPA(4,8) DPPA(5,8)
    : "+v"(o[0]), "+v"(o[1]), "+v"(o[2]), "+v"(o[3]), "+v"(o[4]), "+v"(o[5]));
}
__device__ __forceinline__ void par_sum4(float (&o)[4]) {
  asm volatile("s_nop 1\n\t"
    DPPA(0,2) DPPA(1,2) DPPA(2,2) DPPA(3,2)
    DPPA(0,4) DPPA(1,4) DPPA(2,4) DPPA(3,4)
    DPPA(0,8) DPPA(1,8) DPPA(2,8) DPPA(3,8)
    : "+v"(o[0]), "+v"(o[1]), "+v"(o[2]), "+v"(o[3]));
}
__device__ __forceinline__ float rdlane(float v, int l) {
  return __int_as_float(__builtin_amdgcn_readlane(__float_as_int(v), l));
}

__device__ __forceinline__ f2 cmul(f2 a, f2 b) {
  f2 c;
  asm("v_pk_mul_f32 %0, %1, %2 op_sel:[0,0] op_sel_hi:[1,0]\n\tv_pk_fma_f32 %0, %1, %2, %0 op_sel:[1,1,0] op_sel_hi:[0,1,1] neg_lo:[1,0,0]"
      : "=&v"(c) : "v"(a), "v"(b));
  return c;
}
__device__ __forceinline__ f2 cexp_p(float lam, float rh, float rl, float p) {
  float mag = __builtin_amdgcn_exp2f(lam * p);
  float x = rh * p;
  float f = x - __builtin_rintf(x);
  f = __builtin_fmaf(rl, p, f);
  f2 w; w.x = mag * __builtin_amdgcn_cosf(f); w.y = mag * __builtin_amdgcn_sinf(f); return w;
}
#define MFMA16(a, b, c) __builtin_amdgcn_mfma_f32_16x16x32_f16((a), (b), (c), 0, 0, 0)
__device__ __forceinline__ h8 as_h8(u4 v) { return __builtin_bit_cast(h8, v); }

__device__ __forceinline__ void ln_from_sums(float s1, float s2, float piv, float& mb, float& sb) {
  float dm = s1 * (1.f / 640.f);
  float var = fmaxf(__builtin_fmaf(-dm, dm, s2 * (1.f / 640.f)), 0.f);
  mb = rbf(piv + dm);
  sb = rbf(__builtin_amdgcn_sqrtf(rbf(rbf(var) + 1e-5f)));
}
__device__ __forceinline__ f2 unpk(unsigned w) { return f2{bflo(w), bfhi(w)}; }

__device__ __forceinline__ h8 ld_h8(const char* p) { return as_h8(*(const u4*)p); }
__device__ __forceinline__ h8 ld_4dw(const char* p0, const char* p1) {
  const unsigned* a = (const unsigned*)p0; const unsigned* b = (const unsigned*)p1;
  return as_h8(u4{a[0], a[1], b[0], b[1]});
}
__device__ __forceinline__ h8 cvt_pair(f4 a, f4 b) {
  return as_h8(u4{pk_h(a[0], a[1]), pk_h(a[2], a[3]), pk_h(b[0], b[1]), pk_h(b[2], b[3])});
}
__device__ __forceinline__ void conv_epi(f4 acc, float sc, us* dst) {
  u2 o; o.x = pk_bf(acc[0] * sc, acc[1] * sc); o.y = pk_bf(acc[2] * sc, acc[3] * sc);
  *(u2*)dst = o;
}
#define LDU(a) ((unsigned)*(const LAS us*)(size_t)(a))
#define LDP(a, b) (LDU(a) | (LDU(b) << 16))

template <int OFF> __device__ __forceinline__ u4 lds_rd128(unsigned addr) {
  u4 v; asm volatile("ds_read_b128 %0, %1 offset:%2" : "=v"(v) : "v"(addr), "i"(OFF) : "memory"); return v;
}
template <int N> __device__ __forceinline__ void lgkm_wait() {
  asm volatile("s_waitcnt lgkmcnt(%0)" :: "i"(N) : "memory"); __builtin_amdgcn_sched_barrier(0);
}
template <int M, int M0, int M1, bool T3, int PF>
__device__ __forceinline__ void near_step(const h8 (&A)[17], u4 (&f)[PF], unsigned fa, f4& a0, f4& a1, f4& a2) {
  if constexpr (M < M1) {
    if constexpr (M >= M0 + PF) lgkm_wait<((M1 - 1 - M) < (PF - 1)) ? (M1 - 1 - M) : (PF - 1)>();
    const h8 ff = as_h8(f[(M - M0) % PF]);
    if constexpr (M <= 16) a0 = MFMA16(A[M], ff, a0);
    if constexpr (M >= 8 && M <= 24) a1 = MFMA16(A[M - 8], ff, a1);
    if constexpr (T3 && M >= 16) a2 = MFMA16(A[M - 16], ff, a2);
    __builtin_amdgcn_sched_barrier(0);
    if constexpr (M + PF < M1) f[(M - M0) % PF] = lds_rd128<(M + PF) * 64>(fa);
    near_step<M + 1, M0, M1, T3, PF>(A, f, fa, a0, a1, a2);
  }
}
template <int M, int M0, int PF> __device__ __forceinline__ void near_fill(u4 (&f)[PF], unsigned fa) {
  if constexpr (M < M0 + PF) { f[M - M0] = lds_rd128<M * 64>(fa); near_fill<M + 1, M0, PF>(f, fa); }
}
template <int M0, int M1, bool T3>
__device__ __forceinline__ void near_band(const h8 (&A)[17], unsigned fa, f4& a0, f4& a1, f4& a2) {
  constexpr int PF = 6;
  u4 f[PF];
  near_fill<M0, M0, PF>(f, fa);
  __builtin_amdgcn_s_waitcnt(0xC07F);
  __builtin_amdgcn_sched_barrier(0);
  near_step<M0, M0, M1, T3, PF>(A, f, fa, a0, a1, a2);
}

__device__ __forceinline__ void sy_arrive(char* sm, int idx, int lane) {
  asm volatile("s_waitcnt lgkmcnt(0)" ::: "memory");
  if (lane == 0) __hip_atomic_fetch_add((LAS unsigned*)(LAS char*)(sm + SM_SY) + idx, 1u, __ATOMIC_RELAXED, __HIP_MEMORY_SCOPE_WORKGROUP);
}
__device__ __forceinline__ void sy_wait(char* sm, int idx, unsigned n) {
  volatile LAS unsigned* c = (volatile LAS unsigned*)(LAS char*)(sm + SM_SY) + idx;
  while (*c < n) __builtin_amdgcn_s_sleep(0);
  asm volatile("" ::: "memory");
}

extern "C" __global__ __launch_bounds__(256, 3) void ka_conv(
    const us* __restrict__ x, const us* __restrict__ Are, const us* __restrict__ Aim, const us* __restrict__ Bre,
    const us* __restrict__ Bim, const us* __restrict__ Cre, const us* __restrict__ Cim, const us* __restrict__ logdt,
    const us* __restrict__ Dskip, const us* __restrict__ wr, const us* __restrict__ wg, const us* __restrict__ wu,
    const us* __restrict__ wd, const us* __restrict__ out, char* __restrict__ ws) {
  extern __shared__ __attribute__((aligned(16))) char sm[];
  const int tid = threadIdx.x, lane = tid & 63, wave = __builtin_amdgcn_readfirstlane(tid >> 6);
  const int bid = blockIdx.x;
  STAMP(0, 0);
  const int d = (bid & 7) * 80 + (bid >> 3);
  const int col = lane & 15, g = lane >> 4;
  char* kc = sm + SM_KC;
  const char* ub = sm + SM_U + (256 + 16 * col + 8 * g) * 2;
  us* yt = (us*)(ws + WS_YT) + ((size_t)(2 * col + (g >> 1)) * D_ + d) * 8 + (g & 1) * 4;
  constexpr int YTS = 32 * D_ * 8;
  const f4 Z = {0.f, 0.f, 0.f, 0.f};
  if (tid < 8) ((unsigned*)(sm + SM_SY))[tid] = 0;
  asm volatile("s_waitcnt lgkmcnt(0)" ::: "memory");
  u64 ep;
  us r_are, r_aim, r_bre, r_bim, r_cre, r_cim;
  const size_t pidx = (size_t)d * N_ + (tid < N_ ? tid : 0);
#define LOAD_PARAMS() do { r_are = Are[pidx]; r_aim = Aim[pidx]; r_bre = Bre[pidx]; r_bim = Bim[pidx]; r_cre = Cre[pidx]; r_cim = Cim[pidx]; \
    ep = __hip_atomic_load((GAS u64*)(ws + WS_EPOCH), RLX_AGENT); } while (0)
  unsigned selfpf = 0;
  if (bid < 256) {
    __builtin_amdgcn_s_setprio(3);
    const int tb = (bid & 7) * 32 + (bid >> 3);
    const unsigned* xa = (const unsigned*)(x + (size_t)(tb * 8 + 2 * wave) * D_);
    unsigned ra[5], rb[5];
#pragma unroll
    for (int k = 0; k < 5; ++k) { ra[k] = xa[lane + 64 * k]; rb[k] = xa[D_ / 2 + lane + 64 * k]; }
    __builtin_amdgcn_sched_barrier(0);
    selfpf = *(const GAS unsigned*)(ws + (lane == 0 ? WS_UT + (size_t)tb * D_ * 16 + wave * 2560 : (lane == 1 ? WS_LNF + wave * 8192 : WS_YT + (size_t)(lane & 31) * 81920 + wave * 4)));
    LOAD_PARAMS();
    __builtin_amdgcn_sched_barrier(0);
    const float pa = bflo(xa[0]), pb = bflo(xa[D_ / 2]);
    f2 va[5], vb[5];
    f2 s1a = {0.f, 0.f}, s2a = s1a, s1b = s1a, s2b = s1a;
#pragma unroll
    for (int k = 0; k < 5; ++k) {
      va[k] = unpk(ra[k]); vb[k] = unpk(rb[k]);
      f2 da = va[k] - pa, db = vb[k] - pb;
      s1a += da; s2a = __builtin_elementwise_fma(da, da, s2a);
      s1b += db; s2b = __builtin_elementwise_fma(db, db, s2b);
    }
    float rs4 = row_sum(swap16_add(swap32_add(s1a.x + s1a.y, s2a.x + s2a.y), swap32_add(s1b.x + s1b.y, s2b.x + s2b.y)));
    const float S1 = (lane & 1) ? rdlane(rs4, 16) : rdlane(rs4, 0), S2 = (lane & 1) ? rdlane(rs4, 48) : rdlane(rs4, 32);
    float mb, sb;
    ln_from_sums(S1, S2, (lane & 1) ? pb : pa, mb, sb);
    const float aa = 128.f * __builtin_amdgcn_rcpf(sb), bb = -mb * aa;
    const float aA = rdlane(aa, 0), bA = rdlane(bb, 0), aB = rdlane(aa, 1), bB = rdlane(bb, 1);
#pragma unroll
    for (int k = 0; k < 5; ++k) {
      f2 u0 = __builtin_elementwise_fma(va[k], f2{aA, aA}, f2{bA, bA});
      f2 u1 = __builtin_elementwise_fma(vb[k], f2{aB, aB}, f2{bB, bB});
      *(u2*)(sm + wave * 2560 + (lane + 64 * k) * 8) = u2{pk_h(u0.x, u1.x), pk_h(u0.y, u1.y)};
    }
    __syncthreads();
    {
      const unsigned* tp = (const unsigned*)sm + tid;
      char* ut = ws + WS_UT + ((size_t)tb * D_ + tid) * 16;
      u4 q0 = {tp[0], tp[640], tp[1280], tp[1920]}, q1 = {tp[256], tp[896], tp[1536], tp[2176]};
      asm volatile("global_store_dwordx4 %0, %1, off sc1\n\tglobal_store_dwordx4 %2, %3, off sc1\n\ts_nop 1"
                   :: "v"(ut), "v"(q0), "v"(ut + 4096), "v"(q1) : "memory");
      if (wave < 2) {
        u4 q2 = {tp[512], tp[1152], tp[1792], tp[2432]};
        asm volatile("global_store_dwordx4 %0, %1, off sc1\n\ts_nop 1" :: "v"(ut + 8192), "v"(q2) : "memory");
      }
      asm volatile("s_waitcnt vmcnt(0)" ::: "memory");
    }
    STAMP(0, 5);
    __syncthreads();
    if (wave == 3 && lane < 16) __hip_atomic_store((GAS u64*)(ws + WS_LNF) + lane * 256 + bid, ep + 1ull, RLX_AGENT);
    __builtin_amdgcn_s_setprio(0);
  } else {
    if (wave == 3 && bid < 320) {
      u64 pc; int o1, o2;
      asm volatile("s_getpc_b64 %0\n\ts_mov_b32 %1, ka_conv@rel32@lo+4\n\ts_mov_b32 %2, k3_tail@rel32@lo+12" : "=s"(pc), "=s"(o1), "=s"(o2));
      int o = (((bid - 256) >> 3) * 64 + lane) * 64; o = o < o2 - o1 ? o : 0;
      selfpf = *(const GAS unsigned*)(pc + (long long)o1 + o);
    }
    LOAD_PARAMS();
    __builtin_amdgcn_s_barrier();
  }
  const u64 tag = ep + 1ull;
  if (wave == 3) {
    __builtin_amdgcn_s_setprio(3);
    if (lane == 0) __hip_atomic_store((GAS u64*)(ws + WS_ACK) + bid, tag, RLX_AGENT);
    if (lane < 32) *(u4*)(sm + SM_U + lane * 16) = u4{0, 0, 0, 0};
    if (bid >= 256 && bid < 266) ((u4*)(ws + WS_FLAGS))[(bid - 256) * 256 + lane] = u4{0, 0, 0, 0},
        ((u4*)(ws + WS_FLAGS))[(bid - 256) * 256 + 64 + lane] = u4{0, 0, 0, 0},
        ((u4*)(ws + WS_FLAGS))[(bid - 256) * 256 + 128 + lane] = u4{0, 0, 0, 0},
        ((u4*)(ws + WS_FLAGS))[(bid - 256) * 256 + 192 + lane] = u4{0, 0, 0, 0};
    if (bid == 266) {
      const unsigned* w2 = (const unsigned*)wr + lane;
      float sw = 0.f;
#pragma unroll
      for (int k = 0; k < 5; ++k) { unsigned q = w2[64 * k]; sw += bflo(q) + bfhi(q); }
      sw = row_sum(sw);
      sw = rdlane(sw, 0) + rdlane(sw, 16) + rdlane(sw, 32) + rdlane(sw, 48);
      if (lane == 0) *(float*)(ws + WS_WSUM) = sw;
    }
    const u4* bt = (const u4*)g_box.v + lane;
    u4 bx[4];
#pragma unroll
    for (int m = 0; m < 4; ++m) bx[m] = bt[64 * m];
    {
      GAS u64* fp = (GAS u64*)(ws + WS_LNF) + (bid & 15) * 256 + lane;
      if (bid >= 256) __builtin_amdgcn_s_sleep(50);
      for (unsigned spins = 0;; ++spins) {
        bool ok = __hip_atomic_load(fp + ((bid >> 4) & 3) * 64, RLX_AGENT) == tag;
        if (__all(ok) || spins > SPIN_CAP) break;
        __builtin_amdgcn_s_sleep(8);
      }
      for (unsigned spins = 0;; ++spins) {
        bool ok = (__hip_atomic_load(fp, RLX_AGENT) == tag) & (__hip_atomic_load(fp + 64, RLX_AGENT) == tag) &
                  (__hip_atomic_load(fp + 128, RLX_AGENT) == tag) & (__hip_atomic_load(fp + 192, RLX_AGENT) == tag);
        if (__all(ok) || spins > SPIN_CAP) break;
        __builtin_amdgcn_s_sleep(5);
      }
    }
    STAMPT(0, 6, 192);
    {
      const char* gu = ws + WS_UT + (size_t)d * 16 + (size_t)lane * (D_ * 16);
      u4 q0, q1, q2, q3;
      asm volatile("global_load_dwordx4 %0, %4, off sc1\n\tglobal_load_dwordx4 %1, %5, off sc1\n\t"
                   "global_load_dwordx4 %2, %6, off sc1\n\tglobal_load_dwordx4 %3, %7, off sc1\n\ts_waitcnt vmcnt(0)"
                   : "=&v"(q0), "=&v"(q1), "=&v"(q2), "=&v"(q3)
                   : "v"(gu), "v"(gu + (size_t)64 * (D_ * 16)), "v"(gu + (size_t)128 * (D_ * 16)), "v"(gu + (size_t)192 * (D_ * 16)) : "memory");
      u4* dst = (u4*)(sm + SM_U + 512) + lane;
      dst[0] = q0; dst[64] = q1; dst[128] = q2; dst[192] = q3;
    }
    sy_arrive(sm, 4, lane);
    STAMPT(0, 7, 192);
    h8 sa[6], sb[6];
#pragma unroll
    for (int tau = 0; tau < 6; ++tau) {
      h8 w1 = ld_h8(ub + (256 * tau - 16) * 2), w2 = ld_h8(ub + (256 * tau - 24) * 2);
      sa[tau] = cvt_pair(MFMA16(w1, as_h8(bx[0]), Z), MFMA16(w1, as_h8(bx[1]), Z));
      sb[tau] = cvt_pair(MFMA16(w2, as_h8(bx[2]), Z), MFMA16(w2, as_h8(bx[3]), Z));
    }
#pragma unroll
    for (int i = 0; i < 6; ++i) { asm volatile("" : "+v"(sa[i]), "+v"(sb[i])); }
    sy_wait(sm, 3, 3);
    f4 acc[6] = {Z, Z, Z, Z, Z, Z};
    {
      const int ib = 79 - col + 4 * g;
      const char* kb = sm + SM_KI + (F2_OFF + (ib & 1) * 64 + (ib & ~1)) * 2;
      h8 tA[3], tB[3];
#pragma unroll
      for (int dl = 2; dl <= 4; ++dl) {
        tA[dl - 2] = ld_4dw(kb - dl * 32, kb + 256 - dl * 32); tB[dl - 2] = ld_4dw(kb + 512 - dl * 32, kb + 768 - dl * 32);
      }
      __builtin_amdgcn_sched_barrier(0);
#pragma unroll
      for (int dl = 2; dl <= 4; ++dl)
#pragma unroll
        for (int tau = 0; tau < 6; ++tau)
          if (tau + dl <= 7) { acc[tau + dl - 2] = MFMA16(sa[tau], tA[dl - 2], acc[tau + dl - 2]); acc[tau + dl - 2] = MFMA16(sb[tau], tB[dl - 2], acc[tau + dl - 2]); }
    }
#pragma unroll
    for (int i = 0; i < 6; ++i) *(f4*)(sm + SM_P + i * 1024 + lane * 16) = acc[i];
    sy_arrive(sm, 5, lane);
    __builtin_amdgcn_s_setprio(0);
    if (bid == 300) {
      GAS u64* ap = (GAS u64*)(ws + WS_ACK) + lane;
      for (unsigned spins = 0;; ++spins) {
        bool ok = true;
#pragma unroll
        for (int k = 0; k < 10; ++k) ok = ok & (__hip_atomic_load(ap + 64 * k, RLX_AGENT) == tag);
        if (__all(ok) || spins > SPIN_CAP) break;
        __builtin_amdgcn_s_sleep(8);
      }
      if (lane == 0) __hip_atomic_store((GAS u64*)(ws + WS_EPOCH), tag, RLX_AGENT);
    }
    asm volatile("" :: "v"(selfpf));
    return;
  }
  __builtin_amdgcn_s_setprio(2);
  const unsigned ldw = ((const unsigned*)logdt)[d >> 1], dsw = ((const unsigned*)Dskip)[d >> 1];
  const float dt = rbf(__expf((d & 1) ? bfhi(ldw) : bflo(ldw)));
  const float dsk = (d & 1) ? bfhi(dsw) : bflo(dsw);
  asm volatile("s_mov_b32 m0, %0" :: "s"(wave * 256) : "memory");
  float ar = dt * bf1(r_are), ai = dt * bf1(r_aim);
  f2 dB = {dt * bf1(r_bre), dt * bf1(r_bim)};
  f2 Cc = {bf1(r_cre), bf1(r_cim)};
  f2 CB = cmul(Cc, dB);
  const float LOG2E = 1.4426950408889634f;
  const float I2PH = 0.15915494309189535f;
  const float I2PL = (float)(0.15915494309189533577 - (double)0.15915494309189535f);
  float lam = ar * LOG2E;
  float rh = ai * I2PH;
  float rl = __builtin_fmaf(ai, I2PL, __builtin_fmaf(ai, I2PH, -rh));
  {
    float gg = fmaxf(1.f, __builtin_amdgcn_exp2f(lam * 2048.f));
    float bnd = row_sum((fabsf(CB.x) + fabsf(CB.y)) * gg);
    if ((lane & 15) == 0) ((float*)(kc))[wave * 4 + (lane >> 4)] = bnd;
    sy_arrive(sm, 0, lane);
  }
  f2 w1 = cexp_p(lam, rh, rl, 1.f), w16 = cexp_p(lam, rh, rl, 16.f), w256 = cexp_p(lam, rh, rl, 256.f);
  f2 w2 = cmul(w1, w1), w32 = cmul(w16, w16), w512 = cmul(w256, w256);
  f2 w4 = cmul(w2, w2), w64 = cmul(w32, w32), w1024 = cmul(w512, w512);
  f2 w8 = cmul(w4, w4), w128 = cmul(w64, w64);
#define DSW(row, val) asm volatile("ds_write_addtid_b32 %0 offset:%1" :: "v"(val), "i"((row) * TAB_PITCH * 4) : "memory")
#define PUTE(row, c) do { f2 c_ = (c); DSW((row), pk_h(c_.x, -c_.y)); } while (0)
#define PUT(row, c) do { f2 c_ = (c); DSW(16 + (row), pk_h(c_.x, c_.y)); } while (0)
  {
    f2 e[8];
    e[1] = w8; e[2] = w16; e[3] = cmul(w8, w16); e[4] = w32;
    e[5] = cmul(w32, e[1]); e[6] = cmul(w32, e[2]); e[7] = cmul(w32, e[3]);
    DSW(0, 0x00003C00u);
#pragma unroll
    for (int l = 1; l < 8; ++l) PUTE(l, e[l]);
    PUTE(8, w64);
#pragma unroll
    for (int l = 1; l < 8; ++l) PUTE(8 + l, cmul(w64, e[l]));
  }
  sy_wait(sm, 0, 3);
  STAMP(0, 1);
  const u4* gt = (const u4*)g_gather.v + 2 * tid;
  u4 ga0 = gt[0], ga1 = gt[1], gb0, gb1;
  if (wave == 0) { gb0 = gt[384]; gb1 = gt[385]; }
  u4 bx4, bx5, bx6;
  if (wave == 2) { const u4* bt = (const u4*)g_box.v + lane; bx4 = bt[256]; bx5 = bt[320]; bx6 = bt[384]; }
  float sc, unsc;
  {
    f4 q0 = *(const f4*)(kc), q1 = *(const f4*)(kc + 16), q2 = *(const f4*)(kc + 32);
    q0 += q1; q0 += q2;
    float sig = ((q0.x + q0.y) + (q0.z + q0.w)) + fabsf(dsk);
    int ex = (int)(__float_as_uint(sig) >> 23) & 255; ex = ex < 24 ? 24 : (ex > 253 ? 253 : ex);
    sc = __uint_as_float((unsigned)(267 - ex) << 23);
    unsc = __uint_as_float((unsigned)(ex - 20) << 23);
    if (tid == 0) *(unsigned*)(kc + ZIDX * 2) = 0;
    f2 c[8];
    c[0] = CB * sc;
    c[1] = cmul(c[0], w1); c[2] = cmul(c[0], w2); c[4] = cmul(c[0], w4); c[3] = cmul(c[1], w2);
    c[5] = cmul(c[1], w4); c[6] = cmul(c[2], w4); c[7] = cmul(c[3], w4);
#pragma unroll
    for (int r = 0; r < 8; ++r) {
      PUT(r, c[r]);
      f2 ch = cmul(c[r], w128); PUT(8 + r, ch);
      if ((r & 1) == 0) {
        f2 s0 = cmul(c[r], w256), s1 = cmul(ch, w256);
        PUT(16 + (r >> 1), s0); PUT(20 + (r >> 1), s1);
        if ((r & 3) == 0) {
          const int rho = r >> 2;
          f2 z0 = cmul(c[r], w512), z1 = cmul(ch, w512), z2 = cmul(s0, w512), z3 = cmul(s1, w512);
          PUT(24 + 0 + rho, z0); PUT(24 + 2 + rho, z1); PUT(24 + 4 + rho, z2); PUT(24 + 6 + rho, z3);
          if (r == 0) {
            f2 y0 = cmul(c[0], w1024);
            PUT(32, y0); PUT(33, cmul(ch, w1024)); PUT(34, cmul(s0, w1024)); PUT(35, cmul(s1, w1024));
            PUT(36, cmul(z0, w1024)); PUT(37, cmul(z1, w1024)); PUT(38, cmul(z2, w1024)); PUT(39, cmul(z3, w1024));
            PUT(40, cmul(y0, w1024));
          }
        }
      }
    }
  }
  sy_arrive(sm, 1, lane);
  sy_wait(sm, 1, 3);
  STAMP(0, 2);
  {
    const unsigned* tab = (const unsigned*)sm;
    f4 acc0 = Z, acc1 = Z;
    const unsigned* pa = tab + col * TAB_PITCH + g * 4;
    const unsigned* pb = tab + (16 + wave * 16 + col) * TAB_PITCH + g * 4;
    u4 ea[12], pb_[12];
#pragma unroll
    for (int kk = 0; kk < 12; ++kk) { ea[kk] = *(const u4*)(pa + kk * 16); pb_[kk] = *(const u4*)(pb + kk * 16); }
#pragma unroll
    for (int kk = 0; kk < 12; kk += 2) {
      acc0 = MFMA16(as_h8(ea[kk]), as_h8(pb_[kk]), acc0);
      acc1 = MFMA16(as_h8(ea[kk + 1]), as_h8(pb_[kk + 1]), acc1);
    }
    __builtin_amdgcn_sched_group_barrier(0x100, 24, 0);
    __builtin_amdgcn_sched_group_barrier(0x008, 12, 0);
    acc0 += acc1;
    acc0[0] = __builtin_fmaf(tid == 0 ? dsk : 0.f, sc, acc0[0]);
    u2 o; o.x = pk_h(acc0[0], acc0[1]); o.y = pk_h(acc0[2], acc0[3]);
    *(u2*)(kc + ((wave * 16 + col) * 16 + 4 * g) * 2) = o;
  }
  sy_arrive(sm, 2, lane);
  sy_wait(sm, 2, 3);
  *(u4*)(sm + SM_KI + tid * 16) = u4{LDP(ga0.x, ga0.y), LDP(ga0.z, ga0.w), LDP(ga1.x, ga1.y), LDP(ga1.z, ga1.w)};
  if (wave == 0) *(u4*)(sm + SM_KI + (192 + tid) * 16) = u4{LDP(gb0.x, gb0.y), LDP(gb0.z, gb0.w), LDP(gb1.x, gb1.y), LDP(gb1.z, gb1.w)};
  sy_arrive(sm, 3, lane);
  unsigned pf;
  {
    const int xcd = bid & 7;
    const int id = (bid >> 3) * 192 + tid;
    u64 pc; int o1, o2;
    asm volatile("s_getpc_b64 %0\n\ts_mov_b32 %1, k3_tail@rel32@lo+4\n\ts_mov_b32 %2, k9_end@rel32@lo+12" : "=s"(pc), "=s"(o1), "=s"(o2));
    int r3 = id / 3, q3 = id - r3 * 3;
    const char* pp0 = (const char*)((r3 < 640 ? wg + (size_t)r3 * F_ : wu + (size_t)(r3 - 640) * F_) + xcd * 192 + q3 * 64);
    int i2 = id - 3840;
    const char* pp1 = (const char*)(wd + (size_t)(i2 >> 1) * D_ + xcd * 80 + (i2 & 1) * 78);
    int i3 = (id - 6912) * 64; i3 = (i3 >= 0 && i3 < o2 - o1) ? i3 : 0;
    const char* pp2 = (const char*)(pc + (long long)o1 + i3);
    pf = *(const GAS unsigned*)(id < 3840 ? pp0 : (id < 6912 ? pp1 : pp2));
  }
  sy_wait(sm, 3, 3);
  __builtin_amdgcn_s_setprio(0);
  STAMP(0, 3);
  const int i0 = 15 - col + 8 * g;
  const char* ka = sm + SM_KI + ((i0 & 1) * NR_LEN + (i0 & ~1)) * 2;
  const int n0 = 3 * wave;
  f4 a0 = Z, a1 = Z, a2 = Z;
  const unsigned fa = (unsigned)(SM_U + (256 + 16 * col + 8 * g + 256 * n0 - 528) * 2);
  if (wave == 2) {
    const int ib = 127 - col + 4 * g;
    const char* kb = sm + SM_KI + (F3_OFF + (ib & 1) * 80 + (ib & ~1)) * 2;
    h8 tt[4];
#pragma unroll
    for (int dl = 4; dl <= 7; ++dl) tt[dl - 4] = ld_4dw(kb - dl * 32, kb + 320 - dl * 32);
    const unsigned kv = LDU(SM_KC + 32 * 16 * 2) * 0x10001u;
    h8 As = as_h8(u4{bx6.x & kv, bx6.y & kv, bx6.z & kv, bx6.w & kv});
    sy_wait(sm, 4, 1);
    u4 w1[4], w2[4], fs[4];
#pragma unroll
    for (int i = 0; i < 4; ++i) {
      w1[i] = *(const u4*)(ub + (256 * i - 16) * 2); w2[i] = *(const u4*)(ub + (256 * i - 24) * 2);
      fs[i] = *(const u4*)(ub + (256 * (4 + i) - 1032) * 2);
    }
    __builtin_amdgcn_sched_barrier(0);
    f4 acc[4];
#pragma unroll
    for (int i = 0; i < 4; ++i) acc[i] = MFMA16(As, as_h8(fs[i]), Z);
#pragma unroll
    for (int tau = 0; tau < 4; ++tau) {
      h8 s3 = cvt_pair(MFMA16(as_h8(w1[tau]), as_h8(bx4), Z), MFMA16(as_h8(w2[tau]), as_h8(bx5), Z));
#pragma unroll
      for (int dl = 4; dl <= 7; ++dl)
        if (tau + dl <= 7) acc[tau + dl - 4] = MFMA16(s3, tt[dl - 4], acc[tau + dl - 4]);
    }
    *(f4*)(sm + SM_P + 6 * 1024 + lane * 16) = acc[0];
    *(f4*)(sm + SM_P + 7 * 1024 + lane * 16) = acc[1];
    a0 = acc[2]; a1 = acc[3];
    asm volatile("s_nop 7\n\ts_nop 7" : "+v"(a0), "+v"(a1));
    sy_arrive(sm, 5, lane);
  }
  h8 A[17];
#pragma unroll
  for (int k = 0; k < 17; ++k) A[k] = ld_4dw(ka + k * 64, ka + k * 64 + 8);
  __builtin_amdgcn_sched_barrier(0);
  sy_wait(sm, 4, 1);
  if (wave == 0) near_band<9, 33, true>(A, fa, a0, a1, a2);
  else if (wave == 1) near_band<0, 33, true>(A, fa, a0, a1, a2);
  else near_band<0, 25, false>(A, fa, a0, a1, a2);
  asm volatile("s_nop 7\n\ts_nop 7" : "+v"(a0), "+v"(a1), "+v"(a2));
  sy_wait(sm, 5, 2);
  {
    const char* pp = sm + SM_P + lane * 16 + (n0 - 2) * 1024;
    if (wave != 0) { a0 += *(const f4*)(pp); a1 += *(const f4*)(pp + 1024); }
    if (wave == 1) { a1 += *(const f4*)(sm + SM_P + 6 * 1024 + lane * 16); a2 += *(const f4*)(sm + SM_P + 7 * 1024 + lane * 16); }
    conv_epi(a0, unsc, yt + YTS * n0);
    conv_epi(a1, unsc, yt + YTS * (n0 + 1));
    if (wave != 2) { a2 += *(const f4*)(pp + 2048); conv_epi(a2, unsc, yt + YTS * (n0 + 2)); }
  }
  asm volatile("" :: "v"(pf), "v"(selfpf));
  STAMP(0, 4);
}

__device__ __forceinline__ unsigned ord16(unsigned b) { return (b & 0x8000u) ? (~b & 0xffffu) : (b | 0x8000u); }

__device__ __forceinline__ u64 sweep_keys(char* ws, int lane, int rep, int q, u64* lk) {
  GAS u64* kp = (GAS u64*)(ws + WS_KEYS) + rep * 256 + lane;
  u64 b;
  for (unsigned spins = 0;; ++spins) {
    bool ok = (long long)__hip_atomic_load(kp + q * 64, RLX_AGENT) < 0;
    if (__all(ok) || spins > SPIN_CAP) break;
    __builtin_amdgcn_s_sleep(3);
  }
  for (unsigned spins = 0;; ++spins) {
    u64 k0 = __hip_atomic_load(kp, RLX_AGENT), k1 = __hip_atomic_load(kp + 64, RLX_AGENT);
    u64 k2 = __hip_atomic_load(kp + 128, RLX_AGENT), k3 = __hip_atomic_load(kp + 192, RLX_AGENT);
    bool ok = (long long)(k0 & k1 & k2 & k3) < 0;
    b = k0 > k1 ? k0 : k1; u64 c = k2 > k3 ? k2 : k3; b = b > c ? b : c;
    if (__all(ok) || spins > SPIN_CAP) {
      if (lk) { lk[lane] = k0; lk[lane + 64] = k1; lk[lane + 128] = k2; lk[lane + 192] = k3; }
      break;
    }
    __builtin_amdgcn_s_sleep(2);
  }
  unsigned hk = (unsigned)(b >> 32), m = hk;
  m = max(m, (unsigned)__builtin_amdgcn_update_dpp(0, (int)m, 0x121, 0xf, 0xf, true));
  m = max(m, (unsigned)__builtin_amdgcn_update_dpp(0, (int)m, 0x122, 0xf, 0xf, true));
  m = max(m, (unsigned)__builtin_amdgcn_update_dpp(0, (int)m, 0x124, 0xf, 0xf, true));
  m = max(m, (unsigned)__builtin_amdgcn_update_dpp(0, (int)m, 0x128, 0xf, 0xf, true));
  unsigned m0 = (unsigned)__builtin_amdgcn_readlane((int)m, 0), m1 = (unsigned)__builtin_amdgcn_readlane((int)m, 16);
  unsigned m2 = (unsigned)__builtin_amdgcn_readlane((int)m, 32), m3 = (unsigned)__builtin_amdgcn_readlane((int)m, 48);
  m0 = max(max(m0, m1), max(m2, m3));
  int src = __builtin_ctzll(__ballot(hk == m0));
  unsigned lo = (unsigned)__builtin_amdgcn_readlane((int)(unsigned)b, src);
  return ((u64)m0 << 32) | lo;
}
__device__ __forceinline__ float par_sum(float v) {
  v = dpp_add<0x122>(v); v = dpp_add<0x124>(v); v = dpp_add<0x128>(v);
  return v;
}
__device__ __forceinline__ unsigned out_pair(f2 h, float rs, float m) {
  f2 dd = h - m;
  dd = unpk(pk_bf(dd.x, dd.y)) * rs;
  dd = h + unpk(pk_bf(dd.x, dd.y));
  return pk_bf(dd.x, dd.y);
}
__device__ __forceinline__ float xs_of(us xv, us yv, float wm, float wrs, float& hh) {
  hh = rbf(bf1(xv) + bf1(yv));
  return rbf(rbf(hh - wm) * wrs);
}

extern "C" __global__ __launch_bounds__(384, 3) void k3_tail(const us* __restrict__ x, const us* __restrict__ wr,
                                                  const us* __restrict__ wg, const us* __restrict__ wu,
                                                  const us* __restrict__ wd, us* __restrict__ out,
                                                  char* __restrict__ ws) {
  __shared__ __attribute__((aligned(16))) float red[5 * 32 + 16 + 8];
  __shared__ __attribute__((aligned(16))) u64 lkeys[1280];
  unsigned* redW = (unsigned*)(red + 176);
  const int tid = threadIdx.x, lane = tid & 63, wave = __builtin_amdgcn_readfirstlane(tid >> 6);
  const int bid = blockIdx.x;
  STAMP(2, 0);
  const us* ytb = (const us*)(ws + WS_YT);
  if (bid >= 256) {
    const bool f1 = bid < 352;
    const int j = f1 ? bid - 256 : bid - 352;
    const int cb = f1 ? (j & 7) * 12 + (j >> 3) : (j & 7) * 5 + (j >> 3);
    if (wave == 5) {
      __builtin_amdgcn_s_sleep(64);
      const u64 wn = sweep_keys(ws, lane, j & 7, (j >> 3) & 3, f1 ? lkeys : nullptr);
      const unsigned wkey = (unsigned)(wn >> 32), wms = (unsigned)wn;
      const int wrow = 2047 - (int)(wkey & 2047u);
      const int c = lane & 15;
      if (f1) {
        if (lane == 0) { redW[2] = wkey; redW[3] = wms; }
        STAMPT(2, 3, 320);
        __syncthreads();
        __syncthreads();
        const float* rp = red + (c >> 3) * 16 + (c & 7);
        float gs = (rp[0] + rp[32]) + (rp[64] + rp[96]) + rp[128];
        float us_ = (rp[8] + rp[40]) + (rp[72] + rp[104]) + rp[136];
        float gb = rbf(gs);
        float sl = rbf(gb * __builtin_amdgcn_rcpf(1.f + __expf(-gb)));
        unsigned a = (unsigned)f2bf(sl * rbf(us_)) | 0x10000u;
        __hip_atomic_store((GAS unsigned*)(ws + WS_ACT) + (lane >> 4) * F_ + cb * 16 + c, a, RLX_AGENT);
        STAMPT(2, 4, 320);
      } else {
        __syncthreads();
        const int col = cb * 16 + c;
        float fh;
        float fx2 = xs_of(x[(size_t)wrow * D_ + col], ytb[((size_t)(wrow >> 3) * D_ + col) * 8 + (wrow & 7)],
                          bflo(wms), __builtin_amdgcn_rcpf(bfhi(wms)), fh);
        unsigned ob = (wkey >> 11) & 0xffffu;
        unsigned rb = (ob & 0x8000u) ? (ob & 0x7fffu) : (~ob & 0xffffu);
        float sg = rbf(__builtin_amdgcn_rcpf(1.f + __expf(-bf1((us)rb))));
        __syncthreads();
        const float* rp = red + c;
        float ys = (rp[0] + rp[16]) + (rp[32] + rp[48]) + rp[64];
        float y = rbf(rbf(ys) * sg);
        if (lane < 16) out[(size_t)wrow * D_ + col] = f2bf(fh + rbf(fx2 + y));
        STAMPT(2, 5, 320);
      }
      return;
    }
    const int half = tid & 1, rb = tid >> 1;
    __builtin_amdgcn_s_sleep(16);
    if (f1) {
      const size_t o0 = (size_t)rb * F_ + cb * 16 + half * 8;
      u4 wq[8];
#pragma unroll
      for (int k = 0; k < 4; ++k) { wq[k] = *(const u4*)(wg + o0 + (size_t)k * 160 * F_); wq[4 + k] = *(const u4*)(wu + o0 + (size_t)k * 160 * F_); }
      f2 wf[32];
#pragma unroll
      for (int k = 0; k < 8; ++k) { wf[4 * k] = unpk(wq[k].x); wf[4 * k + 1] = unpk(wq[k].y); wf[4 * k + 2] = unpk(wq[k].z); wf[4 * k + 3] = unpk(wq[k].w); }
#pragma unroll
      for (int k = 0; k < 32; ++k) asm volatile("" : "+v"(wf[k]));
      __syncthreads();
      const unsigned wkey = redW[2], wms = redW[3];
      const int wrow = 2047 - (int)(wkey & 2047u);
      const float wm = bflo(wms), wrs = __builtin_amdgcn_rcpf(bfhi(wms));
      const us* xw = x + (size_t)wrow * D_ + rb;
      const us* yw = ytb + ((size_t)(wrow >> 3) * D_ + rb) * 8 + (wrow & 7);
      us xv[4], yv[4];
#pragma unroll
      for (int k = 0; k < 4; ++k) { xv[k] = xw[160 * k]; yv[k] = yw[160 * 8 * k]; }
      f2 acc[8];
#pragma unroll
      for (int i = 0; i < 8; ++i) acc[i] = f2{0.f, 0.f};
#pragma unroll
      for (int k = 0; k < 4; ++k) {
        float hh; float xs = xs_of(xv[k], yv[k], wm, wrs, hh);
        f2 x2v = {xs, xs};
#pragma unroll
        for (int i = 0; i < 4; ++i) {
          acc[i] = __builtin_elementwise_fma(wf[4 * k + i], x2v, acc[i]);
          acc[4 + i] = __builtin_elementwise_fma(wf[16 + 4 * k + i], x2v, acc[4 + i]);
        }
      }
      float w[8], o[4];
#pragma unroll
      for (int i = 0; i < 4; ++i) { w[2 * i] = swap32_add(acc[i].x, acc[4 + i].x); w[2 * i + 1] = swap32_add(acc[i].y, acc[4 + i].y); }
#pragma unroll
      for (int i = 0; i < 4; ++i) o[i] = swap16_add(w[i], w[i + 4]);
      par_sum4(o);
      const int rr = lane >> 4;
      if ((lane & 15) < 2) *(f4*)(red + wave * 32 + half * 16 + 4 * (rr & 1) + 8 * (rr >> 1)) = f4{o[0], o[1], o[2], o[3]};
      __syncthreads();
#pragma unroll
      for (int q = 0; q < 3; ++q) {
        const int cbk = j + 96 * q;
        if (cbk < 256) {
          const u64 gk = lkeys[cbk];
          const unsigned ck = (unsigned)(gk >> 32), cms = (unsigned)gk;
          if (ck != wkey) {
            const int crow = 2047 - (int)(ck & 2047u);
            const float crs = __builtin_amdgcn_rcpf(bfhi(cms));
            unsigned xr = ((const unsigned*)(x + (size_t)crow * D_))[tid];
            const us* yc = ytb + ((size_t)(crow >> 3) * D_ + 2 * tid) * 8 + (crow & 7);
            f2 hs = unpk(xr) + f2{bf1(yc[0]), bf1(yc[8])};
            f2 hh = unpk(pk_bf(hs.x, hs.y));
            ((unsigned*)(out + (size_t)crow * D_))[tid] = out_pair(hh, crs, bflo(cms));
          }
        }
      }
      STAMP(2, 6);
    } else {
      const us* wp = wd + cb * 16 + half * 8;
      u4 wq[10];
#pragma unroll
      for (int k = 0; k < 10; ++k) { int r = rb + 160 * k; r = r < F_ ? r : F_ - 1; wq[k] = *(const u4*)(wp + (size_t)r * D_); }
      f2 wf[40];
#pragma unroll
      for (int k = 0; k < 10; ++k) { wf[4 * k] = unpk(wq[k].x); wf[4 * k + 1] = unpk(wq[k].y); wf[4 * k + 2] = unpk(wq[k].z); wf[4 * k + 3] = unpk(wq[k].w); }
#pragma unroll
      for (int k = 0; k < 40; ++k) asm volatile("" : "+v"(wf[k]));
      __syncthreads();
      __builtin_amdgcn_s_sleep(56);
      GAS unsigned* ap = (GAS unsigned*)(ws + WS_ACT) + (j & 3) * F_ + rb;
      const int last = rb < 96 ? 1440 : 0;
      unsigned a[10];
      for (unsigned spins = 0;; ++spins) {
#pragma unroll
        for (int k = 0; k < 9; ++k) a[k] = __hip_atomic_load(ap + 160 * k, RLX_AGENT);
        a[9] = __hip_atomic_load(ap + last, RLX_AGENT);
        unsigned all = a[0] & a[1] & a[2] & a[3] & a[4] & a[5] & a[6] & a[7] & a[8] & a[9];
        if (__all(all >> 16) || spins > SPIN_CAP) break;
        __builtin_amdgcn_s_sleep(1);
      }
      if (rb >= 96) a[9] = 0;
      f2 acc[4] = {f2{0.f, 0.f}, f2{0.f, 0.f}, f2{0.f, 0.f}, f2{0.f, 0.f}};
#pragma unroll
      for (int k = 0; k < 10; ++k) {
        float av = bflo(a[k]); f2 a2 = {av, av};
#pragma unroll
        for (int i = 0; i < 4; ++i) acc[i] = __builtin_elementwise_fma(wf[4 * k + i], a2, acc[i]);
      }
      float w0 = swap32_add(acc[0].x, acc[2].x), w1 = swap32_add(acc[0].y, acc[2].y);
      float w2 = swap32_add(acc[1].x, acc[3].x), w3 = swap32_add(acc[1].y, acc[3].y);
      float o0 = par_sum(swap16_add(w0, w2)), o1 = par_sum(swap16_add(w1, w3));
      const int rr = lane >> 4;
      if ((lane & 15) < 2) *(f2*)(red + wave * 16 + half * 8 + 2 * (rr & 1) + 4 * (rr >> 1)) = f2{o0, o1};
      __syncthreads();
    }
    return;
  }
  float* redA = red;
  float* abv = red + 160;
  const int tb = (bid & 7) * 32 + (bid >> 3);
  const int t0 = tb * 8;
  const unsigned* xb = (const unsigned*)(x + (size_t)t0 * D_);
  if (wave == 5) {
    const int r = lane & 7;
    const float piv = bf1(x[(size_t)(t0 + r) * D_]);
    const float W = *(const float*)(ws + WS_WSUM);
    __syncthreads();
    float s1 = (redA[r] + redA[24 + r]) + (redA[48 + r] + redA[72 + r]) + redA[96 + r];
    float s2 = (redA[8 + r] + redA[32 + r]) + (redA[56 + r] + redA[80 + r]) + redA[104 + r];
    float s3 = (redA[16 + r] + redA[40 + r]) + (redA[64 + r] + redA[88 + r]) + redA[112 + r];
    float mb, sb;
    ln_from_sums(s1, s2, piv, mb, sb);
    float rs = __builtin_amdgcn_rcpf(sb);
    float rv = rs * __builtin_fmaf(piv - mb, W, s3);
    unsigned key = 0x80000000u | (ord16(f2bf(rv)) << 11) | (unsigned)(2047 - (t0 + r));
    unsigned best = key;
    best = max(best, (unsigned)__builtin_amdgcn_update_dpp(0, (int)best, 0x111, 0xf, 0xf, true));
    best = max(best, (unsigned)__builtin_amdgcn_update_dpp(0, (int)best, 0x112, 0xf, 0xf, true));
    best = max(best, (unsigned)__builtin_amdgcn_update_dpp(0, (int)best, 0x114, 0xf, 0xf, true));
    best = (unsigned)__builtin_amdgcn_readlane((int)best, 7);
    if (key == best) {
      u64 gr = ((u64)best << 32) | (u64)((__float_as_uint(mb) >> 16) | (__float_as_uint(sb) & 0xffff0000u));
      __hip_atomic_store((GAS u64*)(ws + WS_KEYS) + (lane >> 3) * 256 + bid, gr, RLX_AGENT);
      redW[0] = (unsigned)r;
    }
    if (lane < 8) *(f2*)(abv + 2 * r) = f2{rs, mb};
    STAMPT(2, 2, 320);
    __syncthreads();
    return;
  }
  unsigned raw[8];
#pragma unroll
  for (int r = 0; r < 8; ++r) raw[r] = xb[r * (D_ / 2) + tid];
  const u4 y0 = *(const u4*)(ytb + ((size_t)tb * D_ + 2 * tid) * 8);
  const u4 y1 = *(const u4*)(ytb + ((size_t)tb * D_ + 2 * tid) * 8 + 8);
  const f2 wv = unpk(((const unsigned*)wr)[tid]);
  f2 h[8];
  {
    float v[24];
#pragma unroll
    for (int r = 0; r < 8; ++r) {
      const float piv = bflo(xb[r * (D_ / 2)]);
      unsigned ya = y0[r >> 1], yb = y1[r >> 1];
      f2 yy = (r & 1) ? f2{bfhi(ya), bfhi(yb)} : f2{bflo(ya), bflo(yb)};
      f2 hs = unpk(raw[r]) + yy;
      h[r] = unpk(pk_bf(hs.x, hs.y));
      f2 dd = h[r] - piv;
      v[r] = dd.x + dd.y;
      v[r + 8] = __builtin_fmaf(dd.x, dd.x, dd.y * dd.y);
      v[r + 16] = __builtin_fmaf(dd.x, wv.x, dd.y * wv.y);
    }
    float w[12], o[6];
#pragma unroll
    for (int i = 0; i < 12; ++i) w[i] = swap32_add(v[i], v[i + 12]);
#pragma unroll
    for (int i = 0; i < 6; ++i) o[i] = swap16_add(w[i], w[i + 6]);
    row_sum6(o);
    if ((lane & 15) == 0) {
      float* dst = redA + wave * 24 + (lane >> 4) * 6;
      *(f2*)dst = f2{o[0], o[1]}; *(f2*)(dst + 2) = f2{o[2], o[3]}; *(f2*)(dst + 4) = f2{o[4], o[5]};
    }
  }
  __syncthreads();
  STAMP(2, 1);
  __syncthreads();
  const int brow = (int)redW[0];
  unsigned* stg = (unsigned*)lkeys + wave * 512;
#pragma unroll
  for (int r = 0; r < 8; r += 2) {
    f4 c = *(const f4*)(abv + 2 * r);
    stg[r * 64 + lane] = out_pair(h[r], c.x, c.y);
    stg[(r + 1) * 64 + lane] = out_pair(h[r + 1], c.z, c.w);
  }
  {
    const int rr = lane >> 4, ch = lane & 15;
    u4 q0 = *(const u4*)(stg + rr * 64 + ch * 4), q1 = *(const u4*)(stg + (rr + 4) * 64 + ch * 4);
    us* o0p = out + (size_t)(t0 + rr) * D_ + wave * 128 + ch * 8;
    __builtin_amdgcn_s_sleep(60);
    if (rr != brow) asm volatile("global_store_dwordx4 %0, %1, off sc1\n\ts_nop 1" :: "v"(o0p), "v"(q0) : "memory");
    if (rr + 4 != brow) asm volatile("global_store_dwordx4 %0, %1, off sc1\n\ts_nop 1" :: "v"(o0p + 4 * D_), "v"(q1) : "memory");
  }
}

extern "C" __global__ void k9_end() {}

extern "C" void kernel_launch(void* const* d_in, const int* in_sizes, int n_in, void* d_out, int out_size,
                              void* d_ws, size_t ws_size, hipStream_t stream) {
  const us* x = (const us*)d_in[0];
  const us* Are = (const us*)d_in[1];
  const us* Aim = (const us*)d_in[2];
  const us* Bre = (const us*)d_in[3];
  const us* Bim = (const us*)d_in[4];
  const us* Cre = (const us*)d_in[5];
  const us* Cim = (const us*)d_in[6];
  const us* logdt = (const us*)d_in[7];
  const us* Dskip = (const us*)d_in[8];
  const us* wr = (const us*)d_in[10];
  const us* wg = (const us*)d_in[11];
  const us* wu = (const us*)d_in[12];
  const us* wd = (const us*)d_in[13];
  us* out = (us*)d_out;
  char* ws = (char*)d_ws;
  ka_conv<<<D_, 256, K2_LDS, stream>>>(x, Are, Aim, Bre, Bim, Cre, Cim, logdt, Dskip, wr, wg, wu, wd, out, ws);
  k3_tail<<<392, 384, 0, stream>>>(x, wr, wg, wu, wd, out, ws);
}
```

```cpp
#include <hip/hip_runtime.h>
#include <hip/hip_bf16.h>
#include <stdint.h>

typedef _Float16 h8 __attribute__((ext_vector_type(8)));
typedef _Float16 h2 __attribute__((ext_vector_type(2)));
typedef float f4 __attribute__((ext_vector_type(4)));
typedef float f2 __attribute__((ext_vector_type(2)));
typedef unsigned u4 __attribute__((ext_vector_type(4)));
typedef unsigned u2 __attribute__((ext_vector_type(2)));
typedef unsigned short us;
typedef unsigned long long u64;
#define GAS __attribute__((address_space(1)))
#define LAS __attribute__((address_space(3)))

constexpr int L_ = 2048, D_ = 640, N_ = 192, F_ = 1536;
#define STAMPD(kid, slot) do {} while (0)
#define STAMP(kid, slot) do {} while (0)
#define STAMPT(kid, slot, thr) do {} while (0)

constexpr int NR_LEN  = 608;
constexpr int F2_OFF  = 2 * NR_LEN;
constexpr int F3_OFF  = F2_OFF + 8 * 64;
constexpr int KIMG_ELEMS = F3_OFF + 4 * 80;
constexpr int KCHUNKS = KIMG_ELEMS / 8;

constexpr size_t WS_FLAGS = 0;
constexpr size_t WS_KEYS  = WS_FLAGS;
constexpr size_t WS_ACT   = WS_FLAGS + 16384;
constexpr size_t WS_WSUM  = 40960;
constexpr size_t WS_EPOCH = 41216;
constexpr size_t WS_LNF   = 41472;
constexpr size_t WS_ACK   = WS_LNF + 32768;
constexpr size_t WS_UT    = WS_ACK + 8192;
constexpr size_t WS_YT    = WS_UT + (size_t)D_ * L_ * 2;
constexpr size_t WS_TOTAL = WS_YT + (size_t)D_ * L_ * 2;

constexpr int TAB_PITCH = 200;
constexpr int SM_KC = 57 * TAB_PITCH * 4;
constexpr int SM_U  = SM_KC + 1552;
constexpr int SM_SY = SM_U + 4608;
constexpr int K2_LDS = SM_SY + 32;
constexpr int SM_KI = 0;
constexpr int SM_P  = 4096;

constexpr int kc_of_v(int v) {
  if (v < 256)  { int h = v >> 7, r = v & 127; return ((h * 8 + (r & 7)) * 16) + (r >> 3); }
  if (v < 512)  { int w = v - 256, h = w >> 7, r = w & 127; return ((16 + h * 4 + ((r & 7) >> 1)) * 16) + (r >> 3); }
  if (v < 1024) { int w = v - 512, h = w >> 7, r = w & 127; return ((24 + h * 2 + ((r & 7) >> 2)) * 16) + (r >> 3); }
  if (v < 2048) { int w = v - 1024, h = w >> 7, r = w & 127; return ((32 + h) * 16) + (r >> 3); }
  return 40 * 16;
}
constexpr int bf16_round_int(int j) {
  if (j <= 256) return j;
  int step = j < 512 ? 2 : (j < 1024 ? 4 : 8);
  int lo = (j / step) * step, rem = j - lo;
  if (rem * 2 < step) return lo;
  if (rem * 2 > step) return lo + step;
  return ((lo / step) & 1) ? lo + step : lo;
}
constexpr int ZIDX = 768;

struct GatherTab { unsigned v[KIMG_ELEMS]; };
constexpr GatherTab make_gather() {
  GatherTab t{};
  for (int i = 0; i < KIMG_ELEMS; ++i) t.v[i] = SM_KC + ZIDX * 2;
  for (int e = 0; e < 2; ++e)
    for (int i = 0; i < NR_LEN; ++i) {
      int j = 543 - (i + e);
      if (j >= 0 && j < 515) t.v[e * NR_LEN + i] = (unsigned)(SM_KC + 2 * kc_of_v(bf16_round_int(j)));
    }
  for (int pi = 0; pi < 4; ++pi)
    for (int e = 0; e < 2; ++e)
      for (int i = 0; i < 64; ++i) {
        int p = 79 - (i + e);
        if (p >= 32 && p < 64) {
          int v = 16 * p + 8 * (pi >> 1) + ((pi & 1) ? 8 : 4);
          t.v[F2_OFF + (pi * 2 + e) * 64 + i] = (unsigned)(SM_KC + 2 * kc_of_v(v));
        }
      }
  for (int pi = 0; pi < 2; ++pi)
    for (int e = 0; e < 2; ++e)
      for (int i = 0; i < 80; ++i) {
        int qq = 127 - (i + e);
        if (qq >= 64 && qq < 128) {
          int v = 16 * qq + (pi ? 16 : 8);
          t.v[F3_OFF + (pi * 2 + e) * 80 + i] = (unsigned)(SM_KC + 2 * kc_of_v(v));
        }
      }
  return t;
}
__device__ const GatherTab g_gather = make_gather();

struct BoxTab { us v[7 * 64 * 8]; };
constexpr BoxTab make_box() {
  BoxTab t{};
  const int nn[6] = {3, 5, 3, 5, 7, 9};
  const int sh[6] = {3, 6, 11, 14, 5, 12};
  const int w0[6] = {-16, -16, -24, -24, -16, -24};
  for (int m = 0; m < 6; ++m)
    for (int lane = 0; lane < 64; ++lane)
      for (int j = 0; j < 8; ++j) {
        int b = lane & 15, k = 8 * (lane >> 4) + j;
        int idx = w0[m] + k;
        bool on = idx <= b - sh[m] && idx >= b - sh[m] - (nn[m] - 1);
        t.v[(m * 64 + lane) * 8 + j] = on ? 0x3C00 : 0;
      }
  for (int lane = 0; lane < 64; ++lane)
    for (int j = 0; j < 8; ++j) {
      int b = lane & 15, k = 8 * (lane >> 4) + j;
      t.v[(6 * 64 + lane) * 8 + j] = (k - b == 4 || k - b == 5) ? 0xFFFF : 0;
    }
  return t;
}
__device__ const BoxTab g_box = make_box();

#define RLX_AGENT __ATOMIC_RELAXED, __HIP_MEMORY_SCOPE_AGENT
constexpr unsigned SPIN_CAP = 1u << 22;
__device__ __forceinline__ float bflo(unsigned w) { return __uint_as_float(w << 16); }
__device__ __forceinline__ float bfhi(unsigned w) { return __uint_as_float(w & 0xffff0000u); }
__device__ __forceinline__ float bf1(us v) { return __uint_as_float((unsigned)v << 16); }
__device__ __forceinline__ us f2bf(float f) { __bf16 b = (__bf16)f; return __builtin_bit_cast(us, b); }
__device__ __forceinline__ float rbf(float f) { return bf1(f2bf(f)); }
typedef __bf16 b2 __attribute__((ext_vector_type(2)));
__device__ __forceinline__ unsigned pk_bf(float a, float b) {
  b2 v; v.x = (__bf16)a; v.y = (__bf16)b; return __builtin_bit_cast(unsigned, v);
}
__device__ __forceinline__ unsigned pk_h(float a, float b) {
  h2 v; v.x = (_Float16)a; v.y = (_Float16)b; return __builtin_bit_cast(unsigned, v);
}
template <int CTRL> __device__ __forceinline__ float dpp_add(float v) {
  int t = __builtin_amdgcn_update_dpp(0, __float_as_int(v), CTRL, 0xf, 0xf, true);
  return v + __int_as_float(t);
}
__device__ __forceinline__ float row_sum(float v) {
  v = dpp_add<0x121>(v); v = dpp_add<0x122>(v); v = dpp_add<0x124>(v); v = dpp_add<0x128>(v);
  return v;
}
__device__ __forceinline__ float swap32_add(float a, float b) {
  auto r = __builtin_amdgcn_permlane32_swap(__float_as_uint(a), __float_as_uint(b), false, false);
  return __uint_as_float(r[0]) + __uint_as_float(r[1]);
}
__device__ __forceinline__ float swap16_add(float a, float b) {
  auto r = __builtin_amdgcn_permlane16_swap(__float_as_uint(a), __float_as_uint(b), false, false);
  return __uint_as_float(r[0]) + __uint_as_float(r[1]);
}
#define DPPA(i, st) "v_add_f32_dpp %" #i ", %" #i ", %" #i " row_ror:" #st " row_mask:0xf bank_mask:0xf\n\t"
__device__ __forceinline__ void row_sum6(float (&o)[6]) {
  asm volatile("s_nop 1\n\t"
    DPPA(0,1) DPPA(1,1) DPPA(2,1) DPPA(3,1) DPPA(4,1) DPPA(5,1)
    DPPA(0,2) DPPA(1,2) DPPA(2,2) DPPA(3,2) DPPA(4,2) DPPA(5,2)
    DPPA(0,4) DPPA(1,4) DPPA(2,4) DPPA(3,4) DPPA(4,4) DPPA(5,4)
    DPPA(0,8) DPPA(1,8) DPPA(2,8) DPPA(3,8) DPPA(4,8) DPPA(5,8)
    : "+v"(o[0]), "+v"(o[1]), "+v"(o[2]), "+v"(o[3]), "+v"(o[4]), "+v"(o[5]));
}
__device__ __forceinline__ void par_sum4(float (&o)[4]) {
  asm volatile("s_nop 1\n\t"
    DPPA(0,2) DPPA(1,2) DPPA(2,2) DPPA(3,2)
    DPPA(0,4) DPPA(1,4) DPPA(2,4) DPPA(3,4)
    DPPA(0,8) DPPA(1,8) DPPA(2,8) DPPA(3,8)
    : "+v"(o[0]), "+v"(o[1]), "+v"(o[2]), "+v"(o[3]));
}
__device__ __forceinline__ float rdlane(float v, int l) {
  return __int_as_float(__builtin_amdgcn_readlane(__float_as_int(v), l));
}

__device__ __forceinline__ f2 cmul(f2 a, f2 b) {
  f2 c;
  asm("v_pk_mul_f32 %0, %1, %2 op_sel:[0,0] op_sel_hi:[1,0]\n\tv_pk_fma_f32 %0, %1, %2, %0 op_sel:[1,1,0] op_sel_hi:[0,1,1] neg_lo:[1,0,0]"
      : "=&v"(c) : "v"(a), "v"(b));
  return c;
}
__device__ __forceinline__ f2 cexp_p(float lam, float rh, float rl, float p) {
  float mag = __builtin_amdgcn_exp2f(lam * p);
  float x = rh * p;
  float f = x - __builtin_rintf(x);
  f = __builtin_fmaf(rl, p, f);
  f2 w; w.x = mag * __builtin_amdgcn_cosf(f); w.y = mag * __builtin_amdgcn_sinf(f); return w;
}
#define MFMA16(a, b, c) __builtin_amdgcn_mfma_f32_16x16x32_f16((a), (b), (c), 0, 0, 0)
__device__ __forceinline__ h8 as_h8(u4 v) { return __builtin_bit_cast(h8, v); }

__device__ __forceinline__ void ln_from_sums(float s1, float s2, float piv, float& mb, float& sb) {
  float dm = s1 * (1.f / 640.f);
  float var = fmaxf(__builtin_fmaf(-dm, dm, s2 * (1.f / 640.f)), 0.f);
  mb = rbf(piv + dm);
  sb = rbf(__builtin_amdgcn_sqrtf(rbf(rbf(var) + 1e-5f)));
}
__device__ __forceinline__ f2 unpk(unsigned w) { return f2{bflo(w), bfhi(w)}; }

__device__ __forceinline__ h8 ld_h8(const char* p) { return as_h8(*(const u4*)p); }
__device__ __forceinline__ h8 ld_4dw(const char* p0, const char* p1) {
  const unsigned* a = (const unsigned*)p0; const unsigned* b = (const unsigned*)p1;
  return as_h8(u4{a[0], a[1], b[0], b[1]});
}
__device__ __forceinline__ h8 cvt_pair(f4 a, f4 b) {
  return as_h8(u4{pk_h(a[0], a[1]), pk_h(a[2], a[3]), pk_h(b[0], b[1]), pk_h(b[2], b[3])});
}
__device__ __forceinline__ void conv_epi(f4 acc, float sc, const char* up, float dq, us* dst) {
  const u2 uu = *(const u2*)up;
  const unsigned ux = uu.x, uy = uu.y;
  const h2 ua = __builtin_bit_cast(h2, ux), ub = __builtin_bit_cast(h2, uy);
  f2 z0 = f2{(float)ua.x, (float)ua.y} * dq, z1 = f2{(float)ub.x, (float)ub.y} * dq;
  f2 y0 = f2{acc[0], acc[1]} * sc, y1 = f2{acc[2], acc[3]} * sc;
  y0 = unpk(pk_bf(y0.x, y0.y)) + unpk(pk_bf(z0.x, z0.y));
  y1 = unpk(pk_bf(y1.x, y1.y)) + unpk(pk_bf(z1.x, z1.y));
  *(u2*)dst = u2{pk_bf(y0.x, y0.y), pk_bf(y1.x, y1.y)};
}
#define LDU(a) ((unsigned)*(const LAS us*)(size_t)(a))
#define LDP(a, b) (LDU(a) | (LDU(b) << 16))

template <int OFF> __device__ __forceinline__ u4 lds_rd128(unsigned addr) {
  u4 v; asm volatile("ds_read_b128 %0, %1 offset:%2" : "=v"(v) : "v"(addr), "i"(OFF) : "memory"); return v;
}
template <int N> __device__ __forceinline__ void lgkm_wait() {
  asm volatile("s_waitcnt lgkmcnt(%0)" :: "i"(N) : "memory"); __builtin_amdgcn_sched_barrier(0);
}
template <int M, int M0, int M1, bool T3, int PF>
__device__ __forceinline__ void near_step(const h8 (&A)[17], u4 (&f)[PF], unsigned fa, f4& a0, f4& a1, f4& a2) {
  if constexpr (M < M1) {
    if constexpr (M >= M0 + PF) lgkm_wait<((M1 - 1 - M) < (PF - 1)) ? (M1 - 1 - M) : (PF - 1)>();
    const h8 ff = as_h8(f[(M - M0) % PF]);
    if constexpr (M <= 16) a0 = MFMA16(A[M], ff, a0);
    if constexpr (M >= 8 && M <= 24) a1 = MFMA16(A[M - 8], ff, a1);
    if constexpr (T3 && M >= 16) a2 = MFMA16(A[M - 16], ff, a2);
    __builtin_amdgcn_sched_barrier(0);
    if constexpr (M + PF < M1) f[(M - M0) % PF] = lds_rd128<(M + PF) * 64>(fa);
    near_step<M + 1, M0, M1, T3, PF>(A, f, fa, a0, a1, a2);
  }
}
template <int M, int M0, int PF> __device__ __forceinline__ void near_fill(u4 (&f)[PF], unsigned fa) {
  if constexpr (M < M0 + PF) { f[M - M0] = lds_rd128<M * 64>(fa); near_fill<M + 1, M0, PF>(f, fa); }
}
template <int M0, int M1, bool T3>
__device__ __forceinline__ void near_band(const h8 (&A)[17], unsigned fa, f4& a0, f4& a1, f4& a2) {
  constexpr int PF = 6;
  u4 f[PF];
  near_fill<M0, M0, PF>(f, fa);
  __builtin_amdgcn_s_waitcnt(0xC07F);
  __builtin_amdgcn_sched_barrier(0);
  near_step<M0, M0, M1, T3, PF>(A, f, fa, a0, a1, a2);
}

__device__ __forceinline__ void sy_arrive(char* sm, int idx, int lane) {
  asm volatile("s_waitcnt lgkmcnt(0)" ::: "memory");
  if (lane == 0) __hip_atomic_fetch_add((LAS unsigned*)(LAS char*)(sm + SM_SY) + idx, 1u, __ATOMIC_RELAXED, __HIP_MEMORY_SCOPE_WORKGROUP);
}
__device__ __forceinline__ void sy_wait(char* sm, int idx, unsigned n) {
  volatile LAS unsigned* c = (volatile LAS unsigned*)(LAS char*)(sm + SM_SY) + idx;
  while (*c < n) __builtin_amdgcn_s_sleep(0);
  asm volatile("" ::: "memory");
}

extern "C" __global__ __launch_bounds__(256, 3) void ka_conv(
    const us* __restrict__ x, const us* __restrict__ Are, const us* __restrict__ Aim, const us* __restrict__ Bre,
    const us* __restrict__ Bim, const us* __restrict__ Cre, const us* __restrict__ Cim, const us* __restrict__ logdt,
    const us* __restrict__ Dskip, const us* __restrict__ wr, const us* __restrict__ wg, const us* __restrict__ wu,
    const us* __restrict__ wd, const us* __restrict__ out, char* __restrict__ ws) {
  extern __shared__ __attribute__((aligned(16))) char sm[];
  const int tid = threadIdx.x, lane = tid & 63, wave = __builtin_amdgcn_readfirstlane(tid >> 6);
  const int bid = blockIdx.x;
  STAMP(0, 0);
  const int d = (bid & 7) * 80 + (bid >> 3);
  const int col = lane & 15, g = lane >> 4;
  char* kc = sm + SM_KC;
  const char* ub = sm + SM_U + (256 + 16 * col + 8 * g) * 2;
  us* yt = (us*)(ws + WS_YT) + ((size_t)(2 * col + (g >> 1)) * D_ + d) * 8 + (g & 1) * 4;
  constexpr int YTS = 32 * D_ * 8;
  const f4 Z = {0.f, 0.f, 0.f, 0.f};
  if (tid < 8) ((unsigned*)(sm + SM_SY))[tid] = 0;
  asm volatile("s_waitcnt lgkmcnt(0)" ::: "memory");
  u64 ep;
  us r_are, r_aim, r_bre, r_bim, r_cre, r_cim;
  const size_t pidx = (size_t)d * N_ + (tid < N_ ? tid : 0);
#define LOAD_PARAMS() do { r_are = Are[pidx]; r_aim = Aim[pidx]; r_bre = Bre[pidx]; r_bim = Bim[pidx]; r_cre = Cre[pidx]; r_cim = Cim[pidx]; \
    ep = __hip_atomic_load((GAS u64*)(ws + WS_EPOCH), RLX_AGENT); } while (0)
  unsigned selfpf = 0;
  if (bid < 256) {
    __builtin_amdgcn_s_setprio(3);
    const int tb = (bid & 7) * 32 + (bid >> 3);
    const unsigned* xa = (const unsigned*)(x + (size_t)(tb * 8 + 2 * wave) * D_);
    unsigned ra[5], rb[5];
#pragma unroll
    for (int k = 0; k < 5; ++k) { ra[k] = xa[lane + 64 * k]; rb[k] = xa[D_ / 2 + lane + 64 * k]; }
    __builtin_amdgcn_sched_barrier(0);
    selfpf = *(const GAS unsigned*)(ws + (lane == 0 ? WS_UT + (size_t)tb * D_ * 16 + wave * 2560 : (lane == 1 ? WS_LNF + wave * 8192 : WS_YT + (size_t)(lane & 31) * 81920 + wave * 4)));
    LOAD_PARAMS();
    __builtin_amdgcn_sched_barrier(0);
    const float pa = bflo(xa[0]), pb = bflo(xa[D_ / 2]);
    f2 va[5], vb[5];
    f2 s1a = {0.f, 0.f}, s2a = s1a, s1b = s1a, s2b = s1a;
#pragma unroll
    for (int k = 0; k < 5; ++k) {
      va[k] = unpk(ra[k]); vb[k] = unpk(rb[k]);
      f2 da = va[k] - pa, db = vb[k] - pb;
      s1a += da; s2a = __builtin_elementwise_fma(da, da, s2a);
      s1b += db; s2b = __builtin_elementwise_fma(db, db, s2b);
    }
    float rs4 = row_sum(swap16_add(swap32_add(s1a.x + s1a.y, s2a.x + s2a.y), swap32_add(s1b.x + s1b.y, s2b.x + s2b.y)));
    const float S1 = (lane & 1) ? rdlane(rs4, 16) : rdlane(rs4, 0), S2 = (lane & 1) ? rdlane(rs4, 48) : rdlane(rs4, 32);
    float mb, sb;
    ln_from_sums(S1, S2, (lane & 1) ? pb : pa, mb, sb);
    const float aa = 128.f * __builtin_amdgcn_rcpf(sb);
    const float aA = rdlane(aa, 0), mA = rdlane(mb, 0), aB = rdlane(aa, 1), mB = rdlane(mb, 1);
#pragma unroll
    for (int k = 0; k < 5; ++k) {
      f2 d0 = va[k] - mA, d1 = vb[k] - mB;
      d0 = unpk(pk_bf(d0.x, d0.y)) * aA; d1 = unpk(pk_bf(d1.x, d1.y)) * aB;
      f2 u0 = unpk(pk_bf(d0.x, d0.y)), u1 = unpk(pk_bf(d1.x, d1.y));
      *(u2*)(sm + wave * 2560 + (lane + 64 * k) * 8) = u2{pk_h(u0.x, u1.x), pk_h(u0.y, u1.y)};
    }
    __syncthreads();
    {
      const unsigned* tp = (const unsigned*)sm + tid;
      char* ut = ws + WS_UT + ((size_t)tb * D_ + tid) * 16;
      u4 q0 = {tp[0], tp[640], tp[1280], tp[1920]}, q1 = {tp[256], tp[896], tp[1536], tp[2176]};
      asm volatile("global_store_dwordx4 %0, %1, off sc1\n\tglobal_store_dwordx4 %2, %3, off sc1\n\ts_nop 1"
                   :: "v"(ut), "v"(q0), "v"(ut + 4096), "v"(q1) : "memory");
      if (wave < 2) {
        u4 q2 = {tp[512], tp[1152], tp[1792], tp[2432]};
        asm volatile("global_store_dwordx4 %0, %1, off sc1\n\ts_nop 1" :: "v"(ut + 8192), "v"(q2) : "memory");
      }
      asm volatile("s_waitcnt vmcnt(0)" ::: "memory");
    }
    STAMP(0, 5);
    __syncthreads();
    if (wave == 3 && lane < 16) __hip_atomic_store((GAS u64*)(ws + WS_LNF) + lane * 256 + bid, ep + 1ull, RLX_AGENT);
    __builtin_amdgcn_s_setprio(0);
  } else {
    if (wave == 3 && bid < 320) {
      u64 pc; int o1, o2;
      asm volatile("s_getpc_b64 %0\n\ts_mov_b32 %1, ka_conv@rel32@lo+4\n\ts_mov_b32 %2, k3_tail@rel32@lo+12" : "=s"(pc), "=s"(o1), "=s"(o2));
      int o = (((bid - 256) >> 3) * 64 + lane) * 64; o = o < o2 - o1 ? o : 0;
      selfpf = *(const GAS unsigned*)(pc + (long long)o1 + o);
    }
    LOAD_PARAMS();
    __builtin_amdgcn_s_barrier();
  }
  const u64 tag = ep + 1ull;
  if (wave == 3) {
    __builtin_amdgcn_s_setprio(3);
    if (lane == 0) __hip_atomic_store((GAS u64*)(ws + WS_ACK) + bid, tag, RLX_AGENT);
    if (lane < 32) *(u4*)(sm + SM_U + lane * 16) = u4{0, 0, 0, 0};
    if (bid >= 256 && bid < 266) ((u4*)(ws + WS_FLAGS))[(bid - 256) * 256 + lane] = u4{0, 0, 0, 0},
        ((u4*)(ws + WS_FLAGS))[(bid - 256) * 256 + 64 + lane] = u4{0, 0, 0, 0},
        ((u4*)(ws + WS_FLAGS))[(bid - 256) * 256 + 128 + lane] = u4{0, 0, 0, 0},
        ((u4*)(ws + WS_FLAGS))[(bid - 256) * 256 + 192 + lane] = u4{0, 0, 0, 0};
    if (bid == 266) {
      const unsigned* w2 = (const unsigned*)wr + lane;
      float sw = 0.f;
#pragma unroll
      for (int k = 0; k < 5; ++k) { unsigned q = w2[64 * k]; sw += bflo(q) + bfhi(q); }
      sw = row_sum(sw);
      sw = rdlane(sw, 0) + rdlane(sw, 16) + rdlane(sw, 32) + rdlane(sw, 48);
      if (lane == 0) *(float*)(ws + WS_WSUM) = sw;
    }
    const u4* bt = (const u4*)g_box.v + lane;
    u4 bx[4];
#pragma unroll
    for (int m = 0; m < 4; ++m) bx[m] = bt[64 * m];
    {
      GAS u64* fp = (GAS u64*)(ws + WS_LNF) + (bid & 15) * 256 + lane;
      if (bid >= 256) __builtin_amdgcn_s_sleep(50);
      for (unsigned spins = 0;; ++spins) {
        bool ok = __hip_atomic_load(fp + ((bid >> 4) & 3) * 64, RLX_AGENT) == tag;
        if (__all(ok) || spins > SPIN_CAP) break;
        __builtin_amdgcn_s_sleep(8);
      }
      for (unsigned spins = 0;; ++spins) {
        bool ok = (__hip_atomic_load(fp, RLX_AGENT) == tag) & (__hip_atomic_load(fp + 64, RLX_AGENT) == tag) &
                  (__hip_atomic_load(fp + 128, RLX_AGENT) == tag) & (__hip_atomic_load(fp + 192, RLX_AGENT) == tag);
        if (__all(ok) || spins > SPIN_CAP) break;
        __builtin_amdgcn_s_sleep(5);
      }
    }
    STAMPT(0, 6, 192);
    {
      const char* gu = ws + WS_UT + (size_t)d * 16 + (size_t)lane * (D_ * 16);
      u4 q0, q1, q2, q3;
      asm volatile("global_load_dwordx4 %0, %4, off sc1\n\tglobal_load_dwordx4 %1, %5, off sc1\n\t"
                   "global_load_dwordx4 %2, %6, off sc1\n\tglobal_load_dwordx4 %3, %7, off sc1\n\ts_waitcnt vmcnt(0)"
                   : "=&v"(q0), "=&v"(q1), "=&v"(q2), "=&v"(q3)
                   : "v"(gu), "v"(gu + (size_t)64 * (D_ * 16)), "v"(gu + (size_t)128 * (D_ * 16)), "v"(gu + (size_t)192 * (D_ * 16)) : "memory");
      u4* dst = (u4*)(sm + SM_U + 512) + lane;
      dst[0] = q0; dst[64] = q1; dst[128] = q2; dst[192] = q3;
    }
    sy_arrive(sm, 4, lane);
    STAMPT(0, 7, 192);
    h8 sa[6], sb[6];
#pragma unroll
    for (int tau = 0; tau < 6; ++tau) {
      h8 w1 = ld_h8(ub + (256 * tau - 16) * 2), w2 = ld_h8(ub + (256 * tau - 24) * 2);
      sa[tau] = cvt_pair(MFMA16(w1, as_h8(bx[0]), Z), MFMA16(w1, as_h8(bx[1]), Z));
      sb[tau] = cvt_pair(MFMA16(w2, as_h8(bx[2]), Z), MFMA16(w2, as_h8(bx[3]), Z));
    }
#pragma unroll
    for (int i = 0; i < 6; ++i) { asm volatile("" : "+v"(sa[i]), "+v"(sb[i])); }
    sy_wait(sm, 3, 3);
    f4 acc[6] = {Z, Z, Z, Z, Z, Z};
    {
      const int ib = 79 - col + 4 * g;
      const char* kb = sm + SM_KI + (F2_OFF + (ib & 1) * 64 + (ib & ~1)) * 2;
      h8 tA[3], tB[3];
#pragma unroll
      for (int dl = 2; dl <= 4; ++dl) {
        tA[dl - 2] = ld_4dw(kb - dl * 32, kb + 256 - dl * 32); tB[dl - 2] = ld_4dw(kb + 512 - dl * 32, kb + 768 - dl * 32);
      }
      __builtin_amdgcn_sched_barrier(0);
#pragma unroll
      for (int dl = 2; dl <= 4; ++dl)
#pragma unroll
        for (int tau = 0; tau < 6; ++tau)
          if (tau + dl <= 7) { acc[tau + dl - 2] = MFMA16(sa[tau], tA[dl - 2], acc[tau + dl - 2]); acc[tau + dl - 2] = MFMA16(sb[tau], tB[dl - 2], acc[tau + dl - 2]); }
    }
#pragma unroll
    for (int i = 0; i < 6; ++i) *(f4*)(sm + SM_P + i * 1024 + lane * 16) = acc[i];
    sy_arrive(sm, 5, lane);
    __builtin_amdgcn_s_setprio(0);
    if (bid == 300) {
      GAS u64* ap = (GAS u64*)(ws + WS_ACK) + lane;
      for (unsigned spins = 0;; ++spins) {
        bool ok = true;
#pragma unroll
        for (int k = 0; k < 10; ++k) ok = ok & (__hip_atomic_load(ap + 64 * k, RLX_AGENT) == tag);
        if (__all(ok) || spins > SPIN_CAP) break;
        __builtin_amdgcn_s_sleep(8);
      }
      if (lane == 0) __hip_atomic_store((GAS u64*)(ws + WS_EPOCH), tag, RLX_AGENT);
    }
    asm volatile("" :: "v"(selfpf));
    return;
  }
  __builtin_amdgcn_s_setprio(2);
  const unsigned ldw = ((const unsigned*)logdt)[d >> 1], dsw = ((const unsigned*)Dskip)[d >> 1];
  const float dt = rbf(__expf((d & 1) ? bfhi(ldw) : bflo(ldw)));
  const float dsk = (d & 1) ? bfhi(dsw) : bflo(dsw);
  asm volatile("s_mov_b32 m0, %0" :: "s"(wave * 256) : "memory");
  float ar = dt * bf1(r_are), ai = dt * bf1(r_aim);
  f2 dB = {dt * bf1(r_bre), dt * bf1(r_bim)};
  f2 Cc = {bf1(r_cre), bf1(r_cim)};
  f2 CB = cmul(Cc, dB);
  const float LOG2E = 1.4426950408889634f;
  const float I2PH = 0.15915494309189535f;
  const float I2PL = (float)(0.15915494309189533577 - (double)0.15915494309189535f);
  float lam = ar * LOG2E;
  float rh = ai * I2PH;
  float rl = __builtin_fmaf(ai, I2PL, __builtin_fmaf(ai, I2PH, -rh));
  {
    float gg = fmaxf(1.f, __builtin_amdgcn_exp2f(lam * 2048.f));
    float bnd = row_sum((fabsf(CB.x) + fabsf(CB.y)) * gg);
    if ((lane & 15) == 0) ((float*)(kc))[wave * 4 + (lane >> 4)] = bnd;
    sy_arrive(sm, 0, lane);
  }
  f2 w1 = cexp_p(lam, rh, rl, 1.f), w16 = cexp_p(lam, rh, rl, 16.f), w256 = cexp_p(lam, rh, rl, 256.f);
  f2 w2 = cmul(w1, w1), w32 = cmul(w16, w16), w512 = cmul(w256, w256);
  f2 w4 = cmul(w2, w2), w64 = cmul(w32, w32), w1024 = cmul(w512, w512);
  f2 w8 = cmul(w4, w4), w128 = cmul(w64, w64);
#define DSW(row, val) asm volatile("ds_write_addtid_b32 %0 offset:%1" :: "v"(val), "i"((row) * TAB_PITCH * 4) : "memory")
#define PUTE(row, c) do { f2 c_ = (c); DSW((row), pk_h(c_.x, -c_.y)); } while (0)
#define PUT(row, c) do { f2 c_ = (c); DSW(16 + (row), pk_h(c_.x, c_.y)); } while (0)
  {
    f2 e[8];
    e[1] = w8; e[2] = w16; e[3] = cmul(w8, w16); e[4] = w32;
    e[5] = cmul(w32, e[1]); e[6] = cmul(w32, e[2]); e[7] = cmul(w32, e[3]);
    DSW(0, 0x00003C00u);
#pragma unroll
    for (int l = 1; l < 8; ++l) PUTE(l, e[l]);
    PUTE(8, w64);
#pragma unroll
    for (int l = 1; l < 8; ++l) PUTE(8 + l, cmul(w64, e[l]));
  }
  sy_wait(sm, 0, 3);
  STAMP(0, 1);
  const u4* gt = (const u4*)g_gather.v + 2 * tid;
  u4 ga0 = gt[0], ga1 = gt[1], gb0, gb1;
  if (wave == 0) { gb0 = gt[384]; gb1 = gt[385]; }
  u4 bx4, bx5, bx6;
  if (wave == 2) { const u4* bt = (const u4*)g_box.v + lane; bx4 = bt[256]; bx5 = bt[320]; bx6 = bt[384]; }
  float sc, unsc;
  {
    f4 q0 = *(const f4*)(kc), q1 = *(const f4*)(kc + 16), q2 = *(const f4*)(kc + 32);
    q0 += q1; q0 += q2;
    float sig = (q0.x + q0.y) + (q0.z + q0.w);
    int ex = (int)(__float_as_uint(sig) >> 23) & 255; ex = ex < 24 ? 24 : (ex > 253 ? 253 : ex);
    sc = __uint_as_float((unsigned)(267 - ex) << 23);
    unsc = __uint_as_float((unsigned)(ex - 20) << 23);
    if (tid == 0) *(unsigned*)(kc + ZIDX * 2) = 0;
    f2 c[8];
    c[0] = CB * sc;
    c[1] = cmul(c[0], w1); c[2] = cmul(c[0], w2); c[4] = cmul(c[0], w4); c[3] = cmul(c[1], w2);
    c[5] = cmul(c[1], w4); c[6] = cmul(c[2], w4); c[7] = cmul(c[3], w4);
#pragma unroll
    for (int r = 0; r < 8; ++r) {
      PUT(r, c[r]);
      f2 ch = cmul(c[r], w128); PUT(8 + r, ch);
      if ((r & 1) == 0) {
        f2 s0 = cmul(c[r], w256), s1 = cmul(ch, w256);
        PUT(16 + (r >> 1), s0); PUT(20 + (r >> 1), s1);
        if ((r & 3) == 0) {
          const int rho = r >> 2;
          f2 z0 = cmul(c[r], w512), z1 = cmul(ch, w512), z2 = cmul(s0, w512), z3 = cmul(s1, w512);
          PUT(24 + 0 + rho, z0); PUT(24 + 2 + rho, z1); PUT(24 + 4 + rho, z2); PUT(24 + 6 + rho, z3);
          if (r == 0) {
            f2 y0 = cmul(c[0], w1024);
            PUT(32, y0); PUT(33, cmul(ch, w1024)); PUT(34, cmul(s0, w1024)); PUT(35, cmul(s1, w1024));
            PUT(36, cmul(z0, w1024)); PUT(37, cmul(z1, w1024)); PUT(38, cmul(z2, w1024)); PUT(39, cmul(z3, w1024));
            PUT(40, cmul(y0, w1024));
          }
        }
      }
    }
  }
  sy_arrive(sm, 1, lane);
  sy_wait(sm, 1, 3);
  STAMP(0, 2);
  {
    const unsigned* tab = (const unsigned*)sm;
    f4 acc0 = Z, acc1 = Z;
    const unsigned* pa = tab + col * TAB_PITCH + g * 4;
    const unsigned* pb = tab + (16 + wave * 16 + col) * TAB_PITCH + g * 4;
    u4 ea[12], pb_[12];
#pragma unroll
    for (int kk = 0; kk < 12; ++kk) { ea[kk] = *(const u4*)(pa + kk * 16); pb_[kk] = *(const u4*)(pb + kk * 16); }
#pragma unroll
    for (int kk = 0; kk < 12; kk += 2) {
      acc0 = MFMA16(as_h8(ea[kk]), as_h8(pb_[kk]), acc0);
      acc1 = MFMA16(as_h8(ea[kk + 1]), as_h8(pb_[kk + 1]), acc1);
    }
    __builtin_amdgcn_sched_group_barrier(0x100, 24, 0);
    __builtin_amdgcn_sched_group_barrier(0x008, 12, 0);
    acc0 += acc1;
    u2 o; o.x = pk_h(acc0[0], acc0[1]); o.y = pk_h(acc0[2], acc0[3]);
    *(u2*)(kc + ((wave * 16 + col) * 16 + 4 * g) * 2) = o;
  }
  sy_arrive(sm, 2, lane);
  sy_wait(sm, 2, 3);
  *(u4*)(sm + SM_KI + tid * 16) = u4{LDP(ga0.x, ga0.y), LDP(ga0.z, ga0.w), LDP(ga1.x, ga1.y), LDP(ga1.z, ga1.w)};
  if (wave == 0) *(u4*)(sm + SM_KI + (192 + tid) * 16) = u4{LDP(gb0.x, gb0.y), LDP(gb0.z, gb0.w), LDP(gb1.x, gb1.y), LDP(gb1.z, gb1.w)};
  sy_arrive(sm, 3, lane);
  unsigned pf;
  {
    const int xcd = bid & 7;
    const int id = (bid >> 3) * 192 + tid;
    u64 pc; int o1, o2;
    asm volatile("s_getpc_b64 %0\n\ts_mov_b32 %1, k3_tail@rel32@lo+4\n\ts_mov_b32 %2, k9_end@rel32@lo+12" : "=s"(pc), "=s"(o1), "=s"(o2));
    int r3 = id / 3, q3 = id - r3 * 3;
    const char* pp0 = (const char*)((r3 < 640 ? wg + (size_t)r3 * F_ : wu + (size_t)(r3 - 640) * F_) + xcd * 192 + q3 * 64);
    int i2 = id - 3840;
    const char* pp1 = (const char*)(wd + (size_t)(i2 >> 1) * D_ + xcd * 80 + (i2 & 1) * 78);
    int i3 = (id - 6912) * 64; i3 = (i3 >= 0 && i3 < o2 - o1) ? i3 : 0;
    const char* pp2 = (const char*)(pc + (long long)o1 + i3);
    pf = *(const GAS unsigned*)(id < 3840 ? pp0 : (id < 6912 ? pp1 : pp2));
  }
  sy_wait(sm, 3, 3);
  __builtin_amdgcn_s_setprio(0);
  STAMP(0, 3);
  const int i0 = 15 - col + 8 * g;
  const char* ka = sm + SM_KI + ((i0 & 1) * NR_LEN + (i0 & ~1)) * 2;
  const int n0 = 3 * wave;
  f4 a0 = Z, a1 = Z, a2 = Z;
  const unsigned fa = (unsigned)(SM_U + (256 + 16 * col + 8 * g + 256 * n0 - 528) * 2);
  if (wave == 2) {
    const int ib = 127 - col + 4 * g;
    const char* kb = sm + SM_KI + (F3_OFF + (ib & 1) * 80 + (ib & ~1)) * 2;
    h8 tt[4];
#pragma unroll
    for (int dl = 4; dl <= 7; ++dl) tt[dl - 4] = ld_4dw(kb - dl * 32, kb + 320 - dl * 32);
    const unsigned kv = LDU(SM_KC + 32 * 16 * 2) * 0x10001u;
    h8 As = as_h8(u4{bx6.x & kv, bx6.y & kv, bx6.z & kv, bx6.w & kv});
    sy_wait(sm, 4, 1);
    u4 w1[4], w2[4], fs[4];
#pragma unroll
    for (int i = 0; i < 4; ++i) {
      w1[i] = *(const u4*)(ub + (256 * i - 16) * 2); w2[i] = *(const u4*)(ub + (256 * i - 24) * 2);
      fs[i] = *(const u4*)(ub + (256 * (4 + i) - 1032) * 2);
    }
    __builtin_amdgcn_sched_barrier(0);
    f4 acc[4];
#pragma unroll
    for (int i = 0; i < 4; ++i) acc[i] = MFMA16(As, as_h8(fs[i]), Z);
#pragma unroll
    for (int tau = 0; tau < 4; ++tau) {
      h8 s3 = cvt_pair(MFMA16(as_h8(w1[tau]), as_h8(bx4), Z), MFMA16(as_h8(w2[tau]), as_h8(bx5), Z));
#pragma unroll
      for (int dl = 4; dl <= 7; ++dl)
        if (tau + dl <= 7) acc[tau + dl - 4] = MFMA16(s3, tt[dl - 4], acc[tau + dl - 4]);
    }
    *(f4*)(sm + SM_P + 6 * 1024 + lane * 16) = acc[0];
    *(f4*)(sm + SM_P + 7 * 1024 + lane * 16) = acc[1];
    a0 = acc[2]; a1 = acc[3];
    asm volatile("s_nop 7\n\ts_nop 7" : "+v"(a0), "+v"(a1));
    sy_arrive(sm, 5, lane);
  }
  h8 A[17];
#pragma unroll
  for (int k = 0; k < 17; ++k) A[k] = ld_4dw(ka + k * 64, ka + k * 64 + 8);
  __builtin_amdgcn_sched_barrier(0);
  sy_wait(sm, 4, 1);
  if (wave == 0) near_band<9, 33, true>(A, fa, a0, a1, a2);
  else if (wave == 1) near_band<0, 33, true>(A, fa, a0, a1, a2);
  else near_band<0, 25, false>(A, fa, a0, a1, a2);
  asm volatile("s_nop 7\n\ts_nop 7" : "+v"(a0), "+v"(a1), "+v"(a2));
  sy_wait(sm, 5, 2);
  {
    const char* pp = sm + SM_P + lane * 16 + (n0 - 2) * 1024;
    if (wave != 0) { a0 += *(const f4*)(pp); a1 += *(const f4*)(pp + 1024); }
    if (wave == 1) { a1 += *(const f4*)(sm + SM_P + 6 * 1024 + lane * 16); a2 += *(const f4*)(sm + SM_P + 7 * 1024 + lane * 16); }
    const char* ue = sm + SM_U + (256 + 16 * col + 4 * g + 256 * n0) * 2;
    const float dq = dsk * (1.f / 128.f);
    conv_epi(a0, unsc, ue, dq, yt + YTS * n0);
    conv_epi(a1, unsc, ue + 512, dq, yt + YTS * (n0 + 1));
    if (wave != 2) { a2 += *(const f4*)(pp + 2048); conv_epi(a2, unsc, ue + 1024, dq, yt + YTS * (n0 + 2)); }
  }
  asm volatile("" :: "v"(pf), "v"(selfpf));
  STAMP(0, 4);
}

__device__ __forceinline__ unsigned ord16(unsigned b) { return (b & 0x8000u) ? (~b & 0xffffu) : (b | 0x8000u); }

__device__ __forceinline__ u64 sweep_keys(char* ws, int lane, int rep, int q, u64* lk) {
  GAS u64* kp = (GAS u64*)(ws + WS_KEYS) + rep * 256 + lane;
  u64 b;
  for (unsigned spins = 0;; ++spins) {
    bool ok = (long long)__hip_atomic_load(kp + q * 64, RLX_AGENT) < 0;
    if (__all(ok) || spins > SPIN_CAP) break;
    __builtin_amdgcn_s_sleep(3);
  }
  for (unsigned spins = 0;; ++spins) {
    u64 k0 = __hip_atomic_load(kp, RLX_AGENT), k1 = __hip_atomic_load(kp + 64, RLX_AGENT);
    u64 k2 = __hip_atomic_load(kp + 128, RLX_AGENT), k3 = __hip_atomic_load(kp + 192, RLX_AGENT);
    bool ok = (long long)(k0 & k1 & k2 & k3) < 0;
    b = k0 > k1 ? k0 : k1; u64 c = k2 > k3 ? k2 : k3; b = b > c ? b : c;
    if (__all(ok) || spins > SPIN_CAP) {
      if (lk) { lk[lane] = k0; lk[lane + 64] = k1; lk[lane + 128] = k2; lk[lane + 192] = k3; }
      break;
    }
    __builtin_amdgcn_s_sleep(2);
  }
  unsigned hk = (unsigned)(b >> 32), m = hk;
  m = max(m, (unsigned)__builtin_amdgcn_update_dpp(0, (int)m, 0x121, 0xf, 0xf, true));
  m = max(m, (unsigned)__builtin_amdgcn_update_dpp(0, (int)m, 0x122, 0xf, 0xf, true));
  m = max(m, (unsigned)__builtin_amdgcn_update_dpp(0, (int)m, 0x124, 0xf, 0xf, true));
  m = max(m, (unsigned)__builtin_amdgcn_update_dpp(0, (int)m, 0x128, 0xf, 0xf, true));
  unsigned m0 = (unsigned)__builtin_amdgcn_readlane((int)m, 0), m1 = (unsigned)__builtin_amdgcn_readlane((int)m, 16);
  unsigned m2 = (unsigned)__builtin_amdgcn_readlane((int)m, 32), m3 = (unsigned)__builtin_amdgcn_readlane((int)m, 48);
  m0 = max(max(m0, m1), max(m2, m3));
  int src = __builtin_ctzll(__ballot(hk == m0));
  unsigned lo = (unsigned)__builtin_amdgcn_readlane((int)(unsigned)b, src);
  return ((u64)m0 << 32) | lo;
}
__device__ __forceinline__ float par_sum(float v) {
  v = dpp_add<0x122>(v); v = dpp_add<0x124>(v); v = dpp_add<0x128>(v);
  return v;
}
__device__ __forceinline__ unsigned out_pair(f2 h, float rs, float m) {
  f2 dd = h - m;
  dd = unpk(pk_bf(dd.x, dd.y)) * rs;
  dd = h + unpk(pk_bf(dd.x, dd.y));
  return pk_bf(dd.x, dd.y);
}
__device__ __forceinline__ float xs_of(us xv, us yv, float wm, float wrs, float& hh) {
  hh = rbf(bf1(xv) + bf1(yv));
  return rbf(rbf(hh - wm) * wrs);
}

extern "C" __global__ __launch_bounds__(384, 3) void k3_tail(const us* __restrict__ x, const us* __restrict__ wr,
                                                  const us* __restrict__ wg, const us* __restrict__ wu,
                                                  const us* __restrict__ wd, us* __restrict__ out,
                                                  char* __restrict__ ws) {
  __shared__ __attribute__((aligned(16))) float red[5 * 32 + 16 + 8];
  __shared__ __attribute__((aligned(16))) u64 lkeys[1280];
  unsigned* redW = (unsigned*)(red + 176);
  const int tid = threadIdx.x, lane = tid & 63, wave = __builtin_amdgcn_readfirstlane(tid >> 6);
  const int bid = blockIdx.x;
  STAMP(2, 0);
  const us* ytb = (const us*)(ws + WS_YT);
  if (bid >= 256) {
    const bool f1 = bid < 352;
    const int j = f1 ? bid - 256 : bid - 352;
    const int cb = f1 ? (j & 7) * 12 + (j >> 3) : (j & 7) * 5 + (j >> 3);
    if (wave == 5) {
      __builtin_amdgcn_s_sleep(64);
      const u64 wn = sweep_keys(ws, lane, j & 7, (j >> 3) & 3, f1 ? lkeys : nullptr);
      const unsigned wkey = (unsigned)(wn >> 32), wms = (unsigned)wn;
      const int wrow = 2047 - (int)(wkey & 2047u);
      const int c = lane & 15;
      if (f1) {
        if (lane == 0) { redW[2] = wkey; redW[3] = wms; }
        STAMPT(2, 3, 320);
        __syncthreads();
        __syncthreads();
        const float* rp = red + (c >> 3) * 16 + (c & 7);
        float gs = (rp[0] + rp[32]) + (rp[64] + rp[96]) + rp[128];
        float us_ = (rp[8] + rp[40]) + (rp[72] + rp[104]) + rp[136];
        float gb = rbf(gs);
        float sl = rbf(gb * __builtin_amdgcn_rcpf(1.f + __expf(-gb)));
        unsigned a = (unsigned)f2bf(sl * rbf(us_)) | 0x10000u;
        __hip_atomic_store((GAS unsigned*)(ws + WS_ACT) + (lane >> 4) * F_ + cb * 16 + c, a, RLX_AGENT);
        STAMPT(2, 4, 320);
      } else {
        __syncthreads();
        const int col = cb * 16 + c;
        float fh;
        float fx2 = xs_of(x[(size_t)wrow * D_ + col], ytb[((size_t)(wrow >> 3) * D_ + col) * 8 + (wrow & 7)],
                          bflo(wms), __builtin_amdgcn_rcpf(bfhi(wms)), fh);
        unsigned ob = (wkey >> 11) & 0xffffu;
        unsigned rb = (ob & 0x8000u) ? (ob & 0x7fffu) : (~ob & 0xffffu);
        float sg = rbf(__builtin_amdgcn_rcpf(1.f + __expf(-bf1((us)rb))));
        __syncthreads();
        const float* rp = red + c;
        float ys = (rp[0] + rp[16]) + (rp[32] + rp[48]) + rp[64];
        float y = rbf(rbf(ys) * sg);
        if (lane < 16) out[(size_t)wrow * D_ + col] = f2bf(fh + rbf(fx2 + y));
        STAMPT(2, 5, 320);
      }
      return;
    }
    const int half = tid & 1, rb = tid >> 1;
    __builtin_amdgcn_s_sleep(16);
    if (f1) {
      const size_t o0 = (size_t)rb * F_ + cb * 16 + half * 8;
      u4 wq[8];
#pragma unroll
      for (int k = 0; k < 4; ++k) { wq[k] = *(const u4*)(wg + o0 + (size_t)k * 160 * F_); wq[4 + k] = *(const u4*)(wu + o0 + (size_t)k * 160 * F_); }
      f2 wf[32];
#pragma unroll
      for (int k = 0; k < 8; ++k) { wf[4 * k] = unpk(wq[k].x); wf[4 * k + 1] = unpk(wq[k].y); wf[4 * k + 2] = unpk(wq[k].z); wf[4 * k + 3] = unpk(wq[k].w); }
#pragma unroll
      for (int k = 0; k < 32; ++k) asm volatile("" : "+v"(wf[k]));
      __syncthreads();
      const unsigned wkey = redW[2], wms = redW[3];
      const int wrow = 2047 - (int)(wkey & 2047u);
      const float wm = bflo(wms), wrs = __builtin_amdgcn_rcpf(bfhi(wms));
      const us* xw = x + (size_t)wrow * D_ + rb;
      const us* yw = ytb + ((size_t)(wrow >> 3) * D_ + rb) * 8 + (wrow & 7);
      us xv[4], yv[4];
#pragma unroll
      for (int k = 0; k < 4; ++k) { xv[k] = xw[160 * k]; yv[k] = yw[160 * 8 * k]; }
      f2 acc[8];
#pragma unroll
      for (int i = 0; i < 8; ++i) acc[i] = f2{0.f, 0.f};
#pragma unroll
      for (int k = 0; k < 4; ++k) {
        float hh; float xs = xs_of(xv[k], yv[k], wm, wrs, hh);
        f2 x2v = {xs, xs};
#pragma unroll
        for (int i = 0; i < 4; ++i) {
          acc[i] = __builtin_elementwise_fma(wf[4 * k + i], x2v, acc[i]);
          acc[4 + i] = __builtin_elementwise_fma(wf[16 + 4 * k + i], x2v, acc[4 + i]);
        }
      }
      float w[8], o[4];
#pragma unroll
      for (int i = 0; i < 4; ++i) { w[2 * i] = swap32_add(acc[i].x, acc[4 + i].x); w[2 * i + 1] = swap32_add(acc[i].y, acc[4 + i].y); }
#pragma unroll
      for (int i = 0; i < 4; ++i) o[i] = swap16_add(w[i], w[i + 4]);
      par_sum4(o);
      const int rr = lane >> 4;
      if ((lane & 15) < 2) *(f4*)(red + wave * 32 + half * 16 + 4 * (rr & 1) + 8 * (rr >> 1)) = f4{o[0], o[1], o[2], o[3]};
      __syncthreads();
#pragma unroll
      for (int q = 0; q < 3; ++q) {
        const int cbk = j + 96 * q;
        if (cbk < 256) {
          const u64 gk = lkeys[cbk];
          const unsigned ck = (unsigned)(gk >> 32), cms = (unsigned)gk;
          if (ck != wkey) {
            const int crow = 2047 - (int)(ck & 2047u);
            const float crs = __builtin_amdgcn_rcpf(bfhi(cms));
            unsigned xr = ((const unsigned*)(x + (size_t)crow * D_))[tid];
            const us* yc = ytb + ((size_t)(crow >> 3) * D_ + 2 * tid) * 8 + (crow & 7);
            f2 hs = unpk(xr) + f2{bf1(yc[0]), bf1(yc[8])};
            f2 hh = unpk(pk_bf(hs.x, hs.y));
            ((unsigned*)(out + (size_t)crow * D_))[tid] = out_pair(hh, crs, bflo(cms));
          }
        }
      }
      STAMP(2, 6);
    } else {
      const us* wp = wd + cb * 16 + half * 8;
      u4 wq[10];
#pragma unroll
      for (int k = 0; k < 10; ++k) { int r = rb + 160 * k; r = r < F_ ? r : F_ - 1; wq[k] = *(const u4*)(wp + (size_t)r * D_); }
      f2 wf[40];
#pragma unroll
      for (int k = 0; k < 10; ++k) { wf[4 * k] = unpk(wq[k].x); wf[4 * k + 1] = unpk(wq[k].y); wf[4 * k + 2] = unpk(wq[k].z); wf[4 * k + 3] = unpk(wq[k].w); }
#pragma unroll
      for (int k = 0; k < 40; ++k) asm volatile("" : "+v"(wf[k]));
      __syncthreads();
      __builtin_amdgcn_s_sleep(56);
      GAS unsigned* ap = (GAS unsigned*)(ws + WS_ACT) + (j & 3) * F_ + rb;
      const int last = rb < 96 ? 1440 : 0;
      unsigned a[10];
      for (unsigned spins = 0;; ++spins) {
#pragma unroll
        for (int k = 0; k < 9; ++k) a[k] = __hip_atomic_load(ap + 160 * k, RLX_AGENT);
        a[9] = __hip_atomic_load(ap + last, RLX_AGENT);
        unsigned all = a[0] & a[1] & a[2] & a[3] & a[4] & a[5] & a[6] & a[7] & a[8] & a[9];
        if (__all(all >> 16) || spins > SPIN_CAP) break;
        __builtin_amdgcn_s_sleep(1);
      }
      if (rb >= 96) a[9] = 0;
      f2 acc[4] = {f2{0.f, 0.f}, f2{0.f, 0.f}, f2{0.f, 0.f}, f2{0.f, 0.f}};
#pragma unroll
      for (int k = 0; k < 10; ++k) {
        float av = bflo(a[k]); f2 a2 = {av, av};
#pragma unroll
        for (int i = 0; i < 4; ++i) acc[i] = __builtin_elementwise_fma(wf[4 * k + i], a2, acc[i]);
      }
      float w0 = swap32_add(acc[0].x, acc[2].x), w1 = swap32_add(acc[0].y, acc[2].y);
      float w2 = swap32_add(acc[1].x, acc[3].x), w3 = swap32_add(acc[1].y, acc[3].y);
      float o0 = par_sum(swap16_add(w0, w2)), o1 = par_sum(swap16_add(w1, w3));
      const int rr = lane >> 4;
      if ((lane & 15) < 2) *(f2*)(red + wave * 16 + half * 8 + 2 * (rr & 1) + 4 * (rr >> 1)) = f2{o0, o1};
      __syncthreads();
    }
    return;
  }
  float* redA = red;
  float* abv = red + 160;
  const int tb = (bid & 7) * 32 + (bid >> 3);
  const int t0 = tb * 8;
  const unsigned* xb = (const unsigned*)(x + (size_t)t0 * D_);
  if (wave == 5) {
    const int r = lane & 7;
    const float piv = bf1(x[(size_t)(t0 + r) * D_]);
    const float W = *(const float*)(ws + WS_WSUM);
    __syncthreads();
    float s1 = (redA[r] + redA[24 + r]) + (redA[48 + r] + redA[72 + r]) + redA[96 + r];
    float s2 = (redA[8 + r] + redA[32 + r]) + (redA[56 + r] + redA[80 + r]) + redA[104 + r];
    float s3 = (redA[16 + r] + redA[40 + r]) + (redA[64 + r] + redA[88 + r]) + redA[112 + r];
    float mb, sb;
    ln_from_sums(s1, s2, piv, mb, sb);
    float rs = __builtin_amdgcn_rcpf(sb);
    float rv = rs * __builtin_fmaf(piv - mb, W, s3);
    unsigned key = 0x80000000u | (ord16(f2bf(rv)) << 11) | (unsigned)(2047 - (t0 + r));
    unsigned best = key;
    best = max(best, (unsigned)__builtin_amdgcn_update_dpp(0, (int)best, 0x111, 0xf, 0xf, true));
    best = max(best, (unsigned)__builtin_amdgcn_update_dpp(0, (int)best, 0x112, 0xf, 0xf, true));
    best = max(best, (unsigned)__builtin_amdgcn_update_dpp(0, (int)best, 0x114, 0xf, 0xf, true));
    best = (unsigned)__builtin_amdgcn_readlane((int)best, 7);
    if (key == best) {
      u64 gr = ((u64)best << 32) | (u64)((__float_as_uint(mb) >> 16) | (__float_as_uint(sb) & 0xffff0000u));
      __hip_atomic_store((GAS u64*)(ws + WS_KEYS) + (lane >> 3) * 256 + bid, gr, RLX_AGENT);
      redW[0] = (unsigned)r;
    }
    if (lane < 8) *(f2*)(abv + 2 * r) = f2{rs, mb};
    STAMPT(2, 2, 320);
    __syncthreads();
    return;
  }
  unsigned raw[8];
#pragma unroll
  for (int r = 0; r < 8; ++r) raw[r] = xb[r * (D_ / 2) + tid];
  const u4 y0 = *(const u4*)(ytb + ((size_t)tb * D_ + 2 * tid) * 8);
  const u4 y1 = *(const u4*)(ytb + ((size_t)tb * D_ + 2 * tid) * 8 + 8);
  const f2 wv = unpk(((const unsigned*)wr)[tid]);
  f2 h[8];
  {
    float v[24];
#pragma unroll
    for (int r = 0; r < 8; ++r) {
      const float piv = bflo(xb[r * (D_ / 2)]);
      unsigned ya = y0[r >> 1], yb = y1[r >> 1];
      f2 yy = (r & 1) ? f2{bfhi(ya), bfhi(yb)} : f2{bflo(ya), bflo(yb)};
      f2 hs = unpk(raw[r]) + yy;
      h[r] = unpk(pk_bf(hs.x, hs.y));
      f2 dd = h[r] - piv;
      v[r] = dd.x + dd.y;
      v[r + 8] = __builtin_fmaf(dd.x, dd.x, dd.y * dd.y);
      v[r + 16] = __builtin_fmaf(dd.x, wv.x, dd.y * wv.y);
    }
    float w[12], o[6];
#pragma unroll
    for (int i = 0; i < 12; ++i) w[i] = swap32_add(v[i], v[i + 12]);
#pragma unroll
    for (int i = 0; i < 6; ++i) o[i] = swap16_add(w[i], w[i + 6]);
    row_sum6(o);
    if ((lane & 15) == 0) {
      float* dst = redA + wave * 24 + (lane >> 4) * 6;
      *(f2*)dst = f2{o[0], o[1]}; *(f2*)(dst + 2) = f2{o[2], o[3]}; *(f2*)(dst + 4) = f2{o[4], o[5]};
    }
  }
  __syncthreads();
  STAMP(2, 1);
  __syncthreads();
  const int brow = (int)redW[0];
  unsigned* stg = (unsigned*)lkeys + wave * 512;
#pragma unroll
  for (int r = 0; r < 8; r += 2) {
    f4 c = *(const f4*)(abv + 2 * r);
    stg[r * 64 + lane] = out_pair(h[r], c.x, c.y);
    stg[(r + 1) * 64 + lane] = out_pair(h[r + 1], c.z, c.w);
  }
  {
    const int rr = lane >> 4, ch = lane & 15;
    u4 q0 = *(const u4*)(stg + rr * 64 + ch * 4), q1 = *(const u4*)(stg + (rr + 4) * 64 + ch * 4);
    us* o0p = out + (size_t)(t0 + rr) * D_ + wave * 128 + ch * 8;
    __builtin_amdgcn_s_sleep(60);
    if (rr != brow) asm volatile("global_store_dwordx4 %0, %1, off sc1\n\ts_nop 1" :: "v"(o0p), "v"(q0) : "memory");
    if (rr + 4 != brow) asm volatile("global_store_dwordx4 %0, %1, off sc1\n\ts_nop 1" :: "v"(o0p + 4 * D_), "v"(q1) : "memory");
  }
}

extern "C" __global__ void k9_end() {}

extern "C" void kernel_launch(void* const* d_in, const int* in_sizes, int n_in, void* d_out, int out_size,
                              void* d_ws, size_t ws_size, hipStream_t stream) {
  const us* x = (const us*)d_in[0];
  const us* Are = (const us*)d_in[1];
  const us* Aim = (const us*)d_in[2];
  const us* Bre = (const us*)d_in[3];
  const us* Bim = (const us*)d_in[4];
  const us* Cre = (const us*)d_in[5];
  const us* Cim = (const us*)d_in[6];
  const us* logdt = (const us*)d_in[7];
  const us* Dskip = (const us*)d_in[8];
  const us* wr = (const us*)d_in[10];
  const us* wg = (const us*)d_in[11];
  const us* wu = (const us*)d_in[12];
  const us* wd = (const us*)d_in[13];
  us* out = (us*)d_out;
  char* ws = (char*)d_ws;
  ka_conv<<<D_, 256, K2_LDS, stream>>>(x, Are, Aim, Bre, Bim, Cre, Cim, logdt, Dskip, wr, wg, wu, wd, out, ws);
  k3_tail<<<392, 384, 0, stream>>>(x, wr, wg, wu, wd, out, ws);
}
```

```cpp
#include <hip/hip_runtime.h>
#include <hip/hip_bf16.h>
#include <stdint.h>

typedef _Float16 h8 __attribute__((ext_vector_type(8)));
typedef _Float16 h2 __attribute__((ext_vector_type(2)));
typedef float f4 __attribute__((ext_vector_type(4)));
typedef float f2 __attribute__((ext_vector_type(2)));
typedef unsigned u4 __attribute__((ext_vector_type(4)));
typedef unsigned u2 __attribute__((ext_vector_type(2)));
typedef unsigned short us;
typedef unsigned long long u64;
#define GAS __attribute__((address_space(1)))
#define LAS __attribute__((address_space(3)))

constexpr int L_ = 2048, D_ = 640, N_ = 192, F_ = 1536;
#define STAMPD(kid, slot) do {} while (0)
#define STAMP(kid, slot) do {} while (0)
#define STAMPT(kid, slot, thr) do {} while (0)

constexpr int NR_LEN  = 608;
constexpr int F2_OFF  = 2 * NR_LEN;
constexpr int F3_OFF  = F2_OFF + 8 * 64;
constexpr int KIMG_ELEMS = F3_OFF + 4 * 80;
constexpr int KCHUNKS = KIMG_ELEMS / 8;

constexpr size_t WS_FLAGS = 0;
constexpr size_t WS_KEYS  = WS_FLAGS;
constexpr size_t WS_ACT   = WS_FLAGS + 16384;
constexpr size_t WS_WSUM  = 40960;
constexpr size_t WS_EPOCH = 41216;
constexpr size_t WS_LNF   = 41472;
constexpr size_t WS_ACK   = WS_LNF + 32768;
constexpr size_t WS_UT    = WS_ACK + 8192;
constexpr size_t WS_YT    = WS_UT + (size_t)D_ * L_ * 2;
constexpr size_t WS_TOTAL = WS_YT + (size_t)D_ * L_ * 2;

constexpr int TAB_PITCH = 200;
constexpr int SM_KC = 57 * TAB_PITCH * 4;
constexpr int SM_U  = SM_KC + 1552;
constexpr int SM_SY = SM_U + 4608;
constexpr int K2_LDS = SM_SY + 32;
constexpr int SM_KI = 0;
constexpr int SM_P  = 4096;

constexpr int kc_of_v(int v) {
  if (v < 256)  { int h = v >> 7, r = v & 127; return ((h * 8 + (r & 7)) * 16) + (r >> 3); }
  if (v < 512)  { int w = v - 256, h = w >> 7, r = w & 127; return ((16 + h * 4 + ((r & 7) >> 1)) * 16) + (r >> 3); }
  if (v < 1024) { int w = v - 512, h = w >> 7, r = w & 127; return ((24 + h * 2 + ((r & 7) >> 2)) * 16) + (r >> 3); }
  if (v < 2048) { int w = v - 1024, h = w >> 7, r = w & 127; return ((32 + h) * 16) + (r >> 3); }
  return 40 * 16;
}
constexpr int bf16_round_int(int j) {
  if (j <= 256) return j;
  int step = j < 512 ? 2 : (j < 1024 ? 4 : 8);
  int lo = (j / step) * step, rem = j - lo;
  if (rem * 2 < step) return lo;
  if (rem * 2 > step) return lo + step;
  return ((lo / step) & 1) ? lo + step : lo;
}
constexpr int ZIDX = 768;

struct GatherTab { unsigned v[KIMG_ELEMS]; };
constexpr GatherTab make_gather() {
  GatherTab t{};
  for (int i = 0; i < KIMG_ELEMS; ++i) t.v[i] = SM_KC + ZIDX * 2;
  for (int e = 0; e < 2; ++e)
    for (int i = 0; i < NR_LEN; ++i) {
      int j = 543 - (i + e);
      if (j >= 0 && j < 515) t.v[e * NR_LEN + i] = (unsigned)(SM_KC + 2 * kc_of_v(bf16_round_int(j)));
    }
  for (int pi = 0; pi < 4; ++pi)
    for (int e = 0; e < 2; ++e)
      for (int i = 0; i < 64; ++i) {
        int p = 79 - (i + e);
        if (p >= 32 && p < 64) {
          int v = 16 * p + 8 * (pi >> 1) + ((pi & 1) ? 8 : 4);
          t.v[F2_OFF + (pi * 2 + e) * 64 + i] = (unsigned)(SM_KC + 2 * kc_of_v(v));
        }
      }
  for (int pi = 0; pi < 2; ++pi)
    for (int e = 0; e < 2; ++e)
      for (int i = 0; i < 80; ++i) {
        int qq = 127 - (i + e);
        if (qq >= 64 && qq < 128) {
          int v = 16 * qq + (pi ? 16 : 8);
          t.v[F3_OFF + (pi * 2 + e) * 80 + i] = (unsigned)(SM_KC + 2 * kc_of_v(v));
        }
      }
  return t;
}
__device__ const GatherTab g_gather = make_gather();

struct BoxTab { us v[7 * 64 * 8]; };
constexpr BoxTab make_box() {
  BoxTab t{};
  const int nn[6] = {3, 5, 3, 5, 7, 9};
  const int sh[6] = {3, 6, 11, 14, 5, 12};
  const int w0[6] = {-16, -16, -24, -24, -16, -24};
  for (int m = 0; m < 6; ++m)
    for (int lane = 0; lane < 64; ++lane)
      for (int j = 0; j < 8; ++j) {
        int b = lane & 15, k = 8 * (lane >> 4) + j;
        int idx = w0[m] + k;
        bool on = idx <= b - sh[m] && idx >= b - sh[m] - (nn[m] - 1);
        t.v[(m * 64 + lane) * 8 + j] = on ? 0x3C00 : 0;
      }
  for (int lane = 0; lane < 64; ++lane)
    for (int j = 0; j < 8; ++j) {
      int b = lane & 15, k = 8 * (lane >> 4) + j;
      t.v[(6 * 64 + lane) * 8 + j] = (k - b == 4 || k - b == 5) ? 0xFFFF : 0;
    }
  return t;
}
__device__ const BoxTab g_box = make_box();

#define RLX_AGENT __ATOMIC_RELAXED, __HIP_MEMORY_SCOPE_AGENT
constexpr unsigned SPIN_CAP = 1u << 22;
__device__ __forceinline__ float bflo(unsigned w) { return __uint_as_float(w << 16); }
__device__ __forceinline__ float bfhi(unsigned w) { return __uint_as_float(w & 0xffff0000u); }
__device__ __forceinline__ float bf1(us v) { return __uint_as_float((unsigned)v << 16); }
__device__ __forceinline__ us f2bf(float f) { __bf16 b = (__bf16)f; return __builtin_bit_cast(us, b); }
__device__ __forceinline__ float rbf(float f) { return bf1(f2bf(f)); }
typedef __bf16 b2 __attribute__((ext_vector_type(2)));
__device__ __forceinline__ unsigned pk_bf(float a, float b) {
  b2 v; v.x = (__bf16)a; v.y = (__bf16)b; return __builtin_bit_cast(unsigned, v);
}
__device__ __forceinline__ unsigned pk_h(float a, float b) {
  h2 v; v.x = (_Float16)a; v.y = (_Float16)b; return __builtin_bit_cast(unsigned, v);
}
template <int CTRL> __device__ __forceinline__ float dpp_add(float v) {
  int t = __builtin_amdgcn_update_dpp(0, __float_as_int(v), CTRL, 0xf, 0xf, true);
  return v + __int_as_float(t);
}
__device__ __forceinline__ float row_sum(float v) {
  v = dpp_add<0x121>(v); v = dpp_add<0x122>(v); v = dpp_add<0x124>(v); v = dpp_add<0x128>(v);
  return v;
}
__device__ __forceinline__ float swap32_add(float a, float b) {
  auto r = __builtin_amdgcn_permlane32_swap(__float_as_uint(a), __float_as_uint(b), false, false);
  return __uint_as_float(r[0]) + __uint_as_float(r[1]);
}
__device__ __forceinline__ float swap16_add(float a, float b) {
  auto r = __builtin_amdgcn_permlane16_swap(__float_as_uint(a), __float_as_uint(b), false, false);
  return __uint_as_float(r[0]) + __uint_as_float(r[1]);
}
#define DPPA(i, st) "v_add_f32_dpp %" #i ", %" #i ", %" #i " row_ror:" #st " row_mask:0xf bank_mask:0xf\n\t"
__device__ __forceinline__ void row_sum6(float (&o)[6]) {
  asm volatile("s_nop 1\n\t"
    DPPA(0,1) DPPA(1,1) DPPA(2,1) DPPA(3,1) DPPA(4,1) DPPA(5,1)
    DPPA(0,2) DPPA(1,2) DPPA(2,2) DPPA(3,2) DPPA(4,2) DPPA(5,2)
    DPPA(0,4) DPPA(1,4) DPPA(2,4) DPPA(3,4) DPPA(4,4) DPPA(5,4)
    DPPA(0,8) DPPA(1,8) DPPA(2,8) DPPA(3,8) DPPA(4,8) DPPA(5,8)
    : "+v"(o[0]), "+v"(o[1]), "+v"(o[2]), "+v"(o[3]), "+v"(o[4]), "+v"(o[5]));
}
__device__ __forceinline__ void par_sum4(float (&o)[4]) {
  asm volatile("s_nop 1\n\t"
    DPPA(0,2) DPPA(1,2) DPPA(2,2) DPPA(3,2)
    DPPA(0,4) DPPA(1,4) DPPA(2,4) DPPA(3,4)
    DPPA(0,8) DPPA(1,8) DPPA(2,8) DPPA(3,8)
    : "+v"(o[0]), "+v"(o[1]), "+v"(o[2]), "+v"(o[3]));
}
__device__ __forceinline__ float rdlane(float v, int l) {
  return __int_as_float(__builtin_amdgcn_readlane(__float_as_int(v), l));
}

__device__ __forceinline__ f2 cmul(f2 a, f2 b) {
  f2 c;
  asm("v_pk_mul_f32 %0, %1, %2 op_sel:[0,0] op_sel_hi:[1,0]\n\tv_pk_fma_f32 %0, %1, %2, %0 op_sel:[1,1,0] op_sel_hi:[0,1,1] neg_lo:[1,0,0]"
      : "=&v"(c) : "v"(a), "v"(b));
  return c;
}
__device__ __forceinline__ f2 cexp_p(float lam, float rh, float rl, float p) {
  float mag = __builtin_amdgcn_exp2f(lam * p);
  float x = rh * p;
  float f = x - __builtin_rintf(x);
  f = __builtin_fmaf(rl, p, f);
  f2 w; w.x = mag * __builtin_amdgcn_cosf(f); w.y = mag * __builtin_amdgcn_sinf(f); return w;
}
#define MFMA16(a, b, c) __builtin_amdgcn_mfma_f32_16x16x32_f16((a), (b), (c), 0, 0, 0)
__device__ __forceinline__ h8 as_h8(u4 v) { return __builtin_bit_cast(h8, v); }

__device__ __forceinline__ void ln_from_sums(float s1, float s2, float piv, float& mb, float& sb) {
  float dm = s1 * (1.f / 640.f);
  float var = fmaxf(__builtin_fmaf(-dm, dm, s2 * (1.f / 640.f)), 0.f);
  mb = rbf(piv + dm);
  sb = rbf(__builtin_amdgcn_sqrtf(rbf(rbf(var) + 1e-5f)));
}
__device__ __forceinline__ f2 unpk(unsigned w) { return f2{bflo(w), bfhi(w)}; }

__device__ __forceinline__ h8 ld_h8(const char* p) { return as_h8(*(const u4*)p); }
__device__ __forceinline__ h8 ld_4dw(const char* p0, const char* p1) {
  const unsigned* a = (const unsigned*)p0; const unsigned* b = (const unsigned*)p1;
  return as_h8(u4{a[0], a[1], b[0], b[1]});
}
__device__ __forceinline__ h8 cvt_pair(f4 a, f4 b) {
  return as_h8(u4{pk_h(a[0], a[1]), pk_h(a[2], a[3]), pk_h(b[0], b[1]), pk_h(b[2], b[3])});
}
__device__ __forceinline__ void conv_epi(f4 acc, float sc, const char* up, float dq, us* dst) {
  const u2 uu = *(const u2*)up;
  const unsigned ux = uu.x, uy = uu.y;
  const h2 ua = __builtin_bit_cast(h2, ux), ub = __builtin_bit_cast(h2, uy);
  f2 z0 = f2{(float)ua.x, (float)ua.y} * dq, z1 = f2{(float)ub.x, (float)ub.y} * dq;
  f2 y0 = f2{acc[0], acc[1]} * sc, y1 = f2{acc[2], acc[3]} * sc;
  y0 = unpk(pk_bf(y0.x, y0.y)) + unpk(pk_bf(z0.x, z0.y));
  y1 = unpk(pk_bf(y1.x, y1.y)) + unpk(pk_bf(z1.x, z1.y));
  *(u2*)dst = u2{pk_bf(y0.x, y0.y), pk_bf(y1.x, y1.y)};
}
#define LDU(a) ((unsigned)*(const LAS us*)(size_t)(a))
#define LDP(a, b) (LDU(a) | (LDU(b) << 16))

template <int OFF> __device__ __forceinline__ u4 lds_rd128(unsigned addr) {
  u4 v; asm volatile("ds_read_b128 %0, %1 offset:%2" : "=v"(v) : "v"(addr), "i"(OFF) : "memory"); return v;
}
template <int N> __device__ __forceinline__ void lgkm_wait() {
  asm volatile("s_waitcnt lgkmcnt(%0)" :: "i"(N) : "memory"); __builtin_amdgcn_sched_barrier(0);
}
template <int M, int M0, int M1, bool T3, int PF>
__device__ __forceinline__ void near_step(const h8 (&A)[17], u4 (&f)[PF], unsigned fa, f4& a0, f4& a1, f4& a2) {
  if constexpr (M < M1) {
    if constexpr (M >= M0 + PF) lgkm_wait<((M1 - 1 - M) < (PF - 1)) ? (M1 - 1 - M) : (PF - 1)>();
    const h8 ff = as_h8(f[(M - M0) % PF]);
    if constexpr (M <= 16) a0 = MFMA16(A[M], ff, a0);
    if constexpr (M >= 8 && M <= 24) a1 = MFMA16(A[M - 8], ff, a1);
    if constexpr (T3 && M >= 16) a2 = MFMA16(A[M - 16], ff, a2);
    __builtin_amdgcn_sched_barrier(0);
    if constexpr (M + PF < M1) f[(M - M0) % PF] = lds_rd128<(M + PF) * 64>(fa);
    near_step<M + 1, M0, M1, T3, PF>(A, f, fa, a0, a1, a2);
  }
}
template <int M, int M0, int PF> __device__ __forceinline__ void near_fill(u4 (&f)[PF], unsigned fa) {
  if constexpr (M < M0 + PF) { f[M - M0] = lds_rd128<M * 64>(fa); near_fill<M + 1, M0, PF>(f, fa); }
}
template <int M0, int M1, bool T3>
__device__ __forceinline__ void near_band(const h8 (&A)[17], unsigned fa, f4& a0, f4& a1, f4& a2) {
  constexpr int PF = 6;
  u4 f[PF];
  near_fill<M0, M0, PF>(f, fa);
  __builtin_amdgcn_s_waitcnt(0xC07F);
  __builtin_amdgcn_sched_barrier(0);
  near_step<M0, M0, M1, T3, PF>(A, f, fa, a0, a1, a2);
}

__device__ __forceinline__ void sy_arrive(char* sm, int idx, int lane) {
  asm volatile("s_waitcnt lgkmcnt(0)" ::: "memory");
  if (lane == 0) __hip_atomic_fetch_add((LAS unsigned*)(LAS char*)(sm + SM_SY) + idx, 1u, __ATOMIC_RELAXED, __HIP_MEMORY_SCOPE_WORKGROUP);
}
__device__ __forceinline__ void sy_wait(char* sm, int idx, unsigned n) {
  volatile LAS unsigned* c = (volatile LAS unsigned*)(LAS char*)(sm + SM_SY) + idx;
  while (*c < n) __builtin_amdgcn_s_sleep(0);
  asm volatile("" ::: "memory");
}

extern "C" __global__ __launch_bounds__(256, 3) void ka_conv(
    const us* __restrict__ x, const us* __restrict__ Are, const us* __restrict__ Aim, const us* __restrict__ Bre,
    const us* __restrict__ Bim, const us* __restrict__ Cre, const us* __restrict__ Cim, const us* __restrict__ logdt,
    const us* __restrict__ Dskip, const us* __restrict__ wr, const us* __restrict__ wg, const us* __restrict__ wu,
    const us* __restrict__ wd, char* __restrict__ ws) {
  extern __shared__ __attribute__((aligned(16))) char sm[];
  const int tid = threadIdx.x, lane = tid & 63, wave = __builtin_amdgcn_readfirstlane(tid >> 6);
  const int bid = blockIdx.x;
  STAMP(0, 0);
  const int d = (bid & 7) * 80 + (bid >> 3);
  const int col = lane & 15, g = lane >> 4;
  char* kc = sm + SM_KC;
  const char* ub = sm + SM_U + (256 + 16 * col + 8 * g) * 2;
  us* yt = (us*)(ws + WS_YT) + ((size_t)(2 * col + (g >> 1)) * D_ + d) * 8 + (g & 1) * 4;
  constexpr int YTS = 32 * D_ * 8;
  const f4 Z = {0.f, 0.f, 0.f, 0.f};
  if (tid < 8) ((unsigned*)(sm + SM_SY))[tid] = 0;
  asm volatile("s_waitcnt lgkmcnt(0)" ::: "memory");
  u64 ep;
  us r_are, r_aim, r_bre, r_bim, r_cre, r_cim;
  const size_t pidx = (size_t)d * N_ + (tid < N_ ? tid : 0);
#define LOAD_PARAMS() do { r_are = Are[pidx]; r_aim = Aim[pidx]; r_bre = Bre[pidx]; r_bim = Bim[pidx]; r_cre = Cre[pidx]; r_cim = Cim[pidx]; \
    ep = __hip_atomic_load((GAS u64*)(ws + WS_EPOCH), RLX_AGENT); } while (0)
  unsigned selfpf = 0;
  if (bid < 256) {
    __builtin_amdgcn_s_setprio(3);
    const int tb = (bid & 7) * 32 + (bid >> 3);
    const unsigned* xa = (const unsigned*)(x + (size_t)(tb * 8 + 2 * wave) * D_);
    unsigned ra[5], rb[5];
#pragma unroll
    for (int k = 0; k < 5; ++k) { ra[k] = xa[lane + 64 * k]; rb[k] = xa[D_ / 2 + lane + 64 * k]; }
    __builtin_amdgcn_sched_barrier(0);
    selfpf = *(const GAS unsigned*)(ws + (lane == 0 ? WS_UT + (size_t)tb * D_ * 16 + wave * 2560 : (lane == 1 ? WS_LNF + wave * 8192 : WS_YT + (size_t)(lane & 31) * 81920 + wave * 4)));
    LOAD_PARAMS();
    __builtin_amdgcn_sched_barrier(0);
    const float pa = bflo(xa[0]), pb = bflo(xa[D_ / 2]);
    f2 va[5], vb[5];
    f2 s1a = {0.f, 0.f}, s2a = s1a, s1b = s1a, s2b = s1a;
#pragma unroll
    for (int k = 0; k < 5; ++k) {
      va[k] = unpk(ra[k]); vb[k] = unpk(rb[k]);
      f2 da = va[k] - pa, db = vb[k] - pb;
      s1a += da; s2a = __builtin_elementwise_fma(da, da, s2a);
      s1b += db; s2b = __builtin_elementwise_fma(db, db, s2b);
    }
    float rs4 = row_sum(swap16_add(swap32_add(s1a.x + s1a.y, s2a.x + s2a.y), swap32_add(s1b.x + s1b.y, s2b.x + s2b.y)));
    const float S1 = (lane & 1) ? rdlane(rs4, 16) : rdlane(rs4, 0), S2 = (lane & 1) ? rdlane(rs4, 48) : rdlane(rs4, 32);
    float mb, sb;
    ln_from_sums(S1, S2, (lane & 1) ? pb : pa, mb, sb);
    const float aa = 128.f * __builtin_amdgcn_rcpf(sb);
    const float aA = rdlane(aa, 0), mA = rdlane(mb, 0), aB = rdlane(aa, 1), mB = rdlane(mb, 1);
#pragma unroll
    for (int k = 0; k < 5; ++k) {
      f2 d0 = va[k] - mA, d1 = vb[k] - mB;
      d0 = unpk(pk_bf(d0.x, d0.y)) * aA; d1 = unpk(pk_bf(d1.x, d1.y)) * aB;
      f2 u0 = unpk(pk_bf(d0.x, d0.y)), u1 = unpk(pk_bf(d1.x, d1.y));
      *(u2*)(sm + wave * 2560 + (lane + 64 * k) * 8) = u2{pk_h(u0.x, u1.x), pk_h(u0.y, u1.y)};
    }
    __syncthreads();
    {
      const unsigned* tp = (const unsigned*)sm + tid;
      char* ut = ws + WS_UT + ((size_t)tb * D_ + tid) * 16;
      u4 q0 = {tp[0], tp[640], tp[1280], tp[1920]}, q1 = {tp[256], tp[896], tp[1536], tp[2176]};
      asm volatile("global_store_dwordx4 %0, %1, off sc1\n\tglobal_store_dwordx4 %2, %3, off sc1\n\ts_nop 1"
                   :: "v"(ut), "v"(q0), "v"(ut + 4096), "v"(q1) : "memory");
      if (wave < 2) {
        u4 q2 = {tp[512], tp[1152], tp[1792], tp[2432]};
        asm volatile("global_store_dwordx4 %0, %1, off sc1\n\ts_nop 1" :: "v"(ut + 8192), "v"(q2) : "memory");
      }
      asm volatile("s_waitcnt vmcnt(0)" ::: "memory");
    }
    STAMP(0, 5);
    __syncthreads();
    if (wave == 3 && lane < 16) __hip_atomic_store((GAS u64*)(ws + WS_LNF) + lane * 256 + bid, ep + 1ull, RLX_AGENT);
    __builtin_amdgcn_s_setprio(0);
  } else {
    if (wave == 3 && bid < 320) {
      u64 pc; int o1, o2;
      asm volatile("s_getpc_b64 %0\n\ts_mov_b32 %1, ka_conv@rel32@lo+4\n\ts_mov_b32 %2, k3_tail@rel32@lo+12" : "=s"(pc), "=s"(o1), "=s"(o2));
      int o = (((bid - 256) >> 3) * 64 + lane) * 64; o = o < o2 - o1 ? o : 0;
      selfpf = *(const GAS unsigned*)(pc + (long long)o1 + o);
    }
    LOAD_PARAMS();
    __builtin_amdgcn_s_barrier();
  }
  const u64 tag = ep + 1ull;
  if (wave == 3) {
    __builtin_amdgcn_s_setprio(3);
    if (lane == 0) __hip_atomic_store((GAS u64*)(ws + WS_ACK) + bid, tag, RLX_AGENT);
    if (lane < 32) *(u4*)(sm + SM_U + lane * 16) = u4{0, 0, 0, 0};
    if (bid >= 256 && bid < 266) ((u4*)(ws + WS_FLAGS))[(bid - 256) * 256 + lane] = u4{0, 0, 0, 0},
        ((u4*)(ws + WS_FLAGS))[(bid - 256) * 256 + 64 + lane] = u4{0, 0, 0, 0},
        ((u4*)(ws + WS_FLAGS))[(bid - 256) * 256 + 128 + lane] = u4{0, 0, 0, 0},
        ((u4*)(ws + WS_FLAGS))[(bid - 256) * 256 + 192 + lane] = u4{0, 0, 0, 0};
    if (bid == 266) {
      const unsigned* w2 = (const unsigned*)wr + lane;
      float sw = 0.f;
#pragma unroll
      for (int k = 0; k < 5; ++k) { unsigned q = w2[64 * k]; sw += bflo(q) + bfhi(q); }
      sw = row_sum(sw);
      sw = rdlane(sw, 0) + rdlane(sw, 16) + rdlane(sw, 32) + rdlane(sw, 48);
      if (lane == 0) *(float*)(ws + WS_WSUM) = sw;
    }
    const u4* bt = (const u4*)g_box.v + lane;
    u4 bx[4];
#pragma unroll
    for (int m = 0; m < 4; ++m) bx[m] = bt[64 * m];
    {
      GAS u64* fp = (GAS u64*)(ws + WS_LNF) + (bid & 15) * 256 + lane;
      if (bid >= 256) __builtin_amdgcn_s_sleep(50);
      for (unsigned spins = 0;; ++spins) {
        bool ok = __hip_atomic_load(fp + ((bid >> 4) & 3) * 64, RLX_AGENT) == tag;
        if (__all(ok) || spins > SPIN_CAP) break;
        __builtin_amdgcn_s_sleep(8);
      }
      for (unsigned spins = 0;; ++spins) {
        bool ok = (__hip_atomic_load(fp, RLX_AGENT) == tag) & (__hip_atomic_load(fp + 64, RLX_AGENT) == tag) &
                  (__hip_atomic_load(fp + 128, RLX_AGENT) == tag) & (__hip_atomic_load(fp + 192, RLX_AGENT) == tag);
        if (__all(ok) || spins > SPIN_CAP) break;
        __builtin_amdgcn_s_sleep(5);
      }
    }
    STAMPT(0, 6, 192);
    {
      const char* gu = ws + WS_UT + (size_t)d * 16 + (size_t)lane * (D_ * 16);
      u4 q0, q1, q2, q3;
      asm volatile("global_load_dwordx4 %0, %4, off sc1\n\tglobal_load_dwordx4 %1, %5, off sc1\n\t"
                   "global_load_dwordx4 %2, %6, off sc1\n\tglobal_load_dwordx4 %3, %7, off sc1\n\ts_waitcnt vmcnt(0)"
                   : "=&v"(q0), "=&v"(q1), "=&v"(q2), "=&v"(q3)
                   : "v"(gu), "v"(gu + (size_t)64 * (D_ * 16)), "v"(gu + (size_t)128 * (D_ * 16)), "v"(gu + (size_t)192 * (D_ * 16)) : "memory");
      u4* dst = (u4*)(sm + SM_U + 512) + lane;
      dst[0] = q0; dst[64] = q1; dst[128] = q2; dst[192] = q3;
    }
    sy_arrive(sm, 4, lane);
    STAMPT(0, 7, 192);
    h8 sa[6], sb[6];
#pragma unroll
    for (int tau = 0; tau < 6; ++tau) {
      h8 w1 = ld_h8(ub + (256 * tau - 16) * 2), w2 = ld_h8(ub + (256 * tau - 24) * 2);
      sa[tau] = cvt_pair(MFMA16(w1, as_h8(bx[0]), Z), MFMA16(w1, as_h8(bx[1]), Z));
      sb[tau] = cvt_pair(MFMA16(w2, as_h8(bx[2]), Z), MFMA16(w2, as_h8(bx[3]), Z));
    }
#pragma unroll
    for (int i = 0; i < 6; ++i) { asm volatile("" : "+v"(sa[i]), "+v"(sb[i])); }
    sy_wait(sm, 3, 3);
    f4 acc[6] = {Z, Z, Z, Z, Z, Z};
    {
      const int ib = 79 - col + 4 * g;
      const char* kb = sm + SM_KI + (F2_OFF + (ib & 1) * 64 + (ib & ~1)) * 2;
      h8 tA[3], tB[3];
#pragma unroll
      for (int dl = 2; dl <= 4; ++dl) {
        tA[dl - 2] = ld_4dw(kb - dl * 32, kb + 256 - dl * 32); tB[dl - 2] = ld_4dw(kb + 512 - dl * 32, kb + 768 - dl * 32);
      }
      __builtin_amdgcn_sched_barrier(0);
#pragma unroll
      for (int dl = 2; dl <= 4; ++dl)
#pragma unroll
        for (int tau = 0; tau < 6; ++tau)
          if (tau + dl <= 7) { acc[tau + dl - 2] = MFMA16(sa[tau], tA[dl - 2], acc[tau + dl - 2]); acc[tau + dl - 2] = MFMA16(sb[tau], tB[dl - 2], acc[tau + dl - 2]); }
    }
#pragma unroll
    for (int i = 0; i < 6; ++i) *(f4*)(sm + SM_P + i * 1024 + lane * 16) = acc[i];
    sy_arrive(sm, 5, lane);
    __builtin_amdgcn_s_setprio(0);
    if (bid == 300) {
      GAS u64* ap = (GAS u64*)(ws + WS_ACK) + lane;
      for (unsigned spins = 0;; ++spins) {
        bool ok = true;
#pragma unroll
        for (int k = 0; k < 10; ++k) ok = ok & (__hip_atomic_load(ap + 64 * k, RLX_AGENT) == tag);
        if (__all(ok) || spins > SPIN_CAP) break;
        __builtin_amdgcn_s_sleep(8);
      }
      if (lane == 0) __hip_atomic_store((GAS u64*)(ws + WS_EPOCH), tag, RLX_AGENT);
    }
    asm volatile("" :: "v"(selfpf));
    return;
  }
  __builtin_amdgcn_s_setprio(2);
  const unsigned ldw = ((const unsigned*)logdt)[d >> 1], dsw = ((const unsigned*)Dskip)[d >> 1];
  const float dt = rbf(__expf((d & 1) ? bfhi(ldw) : bflo(ldw)));
  const float dsk = (d & 1) ? bfhi(dsw) : bflo(dsw);
  asm volatile("s_mov_b32 m0, %0" :: "s"(wave * 256) : "memory");
  float ar = dt * bf1(r_are), ai = dt * bf1(r_aim);
  f2 dB = {dt * bf1(r_bre), dt * bf1(r_bim)};
  f2 Cc = {bf1(r_cre), bf1(r_cim)};
  f2 CB = cmul(Cc, dB);
  const float LOG2E = 1.4426950408889634f;
  const float I2PH = 0.15915494309189535f;
  const float I2PL = (float)(0.15915494309189533577 - (double)0.15915494309189535f);
  float lam = ar * LOG2E;
  float rh = ai * I2PH;
  float rl = __builtin_fmaf(ai, I2PL, __builtin_fmaf(ai, I2PH, -rh));
  {
    float gg = fmaxf(1.f, __builtin_amdgcn_exp2f(lam * 2048.f));
    float bnd = row_sum((fabsf(CB.x) + fabsf(CB.y)) * gg);
    if ((lane & 15) == 0) ((float*)(kc))[wave * 4 + (lane >> 4)] = bnd;
    sy_arrive(sm, 0, lane);
  }
  f2 w1 = cexp_p(lam, rh, rl, 1.f), w16 = cexp_p(lam, rh, rl, 16.f), w256 = cexp_p(lam, rh, rl, 256.f);
  f2 w2 = cmul(w1, w1), w32 = cmul(w16, w16), w512 = cmul(w256, w256);
  f2 w4 = cmul(w2, w2), w64 = cmul(w32, w32), w1024 = cmul(w512, w512);
  f2 w8 = cmul(w4, w4), w128 = cmul(w64, w64);
#define DSW(row, val) asm volatile("ds_write_addtid_b32 %0 offset:%1" :: "v"(val), "i"((row) * TAB_PITCH * 4) : "memory")
#define PUTE(row, c) do { f2 c_ = (c); DSW((row), pk_h(c_.x, -c_.y)); } while (0)
#define PUT(row, c) do { f2 c_ = (c); DSW(16 + (row), pk_h(c_.x, c_.y)); } while (0)
  {
    f2 e[8];
    e[1] = w8; e[2] = w16; e[3] = cmul(w8, w16); e[4] = w32;
    e[5] = cmul(w32, e[1]); e[6] = cmul(w32, e[2]); e[7] = cmul(w32, e[3]);
    DSW(0, 0x00003C00u);
#pragma unroll
    for (int l = 1; l < 8; ++l) PUTE(l, e[l]);
    PUTE(8, w64);
#pragma unroll
    for (int l = 1; l < 8; ++l) PUTE(8 + l, cmul(w64, e[l]));
  }
  sy_wait(sm, 0, 3);
  STAMP(0, 1);
  const u4* gt = (const u4*)g_gather.v + 2 * tid;
  u4 ga0 = gt[0], ga1 = gt[1], gb0, gb1;
  if (wave == 0) { gb0 = gt[384]; gb1 = gt[385]; }
  u4 bx4, bx5, bx6;
  if (wave == 2) { const u4* bt = (const u4*)g_box.v + lane; bx4 = bt[256]; bx5 = bt[320]; bx6 = bt[384]; }
  float sc, unsc;
  {
    f4 q0 = *(const f4*)(kc), q1 = *(const f4*)(kc + 16), q2 = *(const f4*)(kc + 32);
    q0 += q1; q0 += q2;
    float sig = (q0.x + q0.y) + (q0.z + q0.w);
    int ex = (int)(__float_as_uint(sig) >> 23) & 255; ex = ex < 24 ? 24 : (ex > 253 ? 253 : ex);
    sc = __uint_as_float((unsigned)(267 - ex) << 23);
    unsc = __uint_as_float((unsigned)(ex - 20) << 23);
    if (tid == 0) *(unsigned*)(kc + ZIDX * 2) = 0;
    f2 c[8];
    c[0] = CB * sc;
    c[1] = cmul(c[0], w1); c[2] = cmul(c[0], w2); c[4] = cmul(c[0], w4); c[3] = cmul(c[1], w2);
    c[5] = cmul(c[1], w4); c[6] = cmul(c[2], w4); c[7] = cmul(c[3], w4);
#pragma unroll
    for (int r = 0; r < 8; ++r) {
      PUT(r, c[r]);
      f2 ch = cmul(c[r], w128); PUT(8 + r, ch);
      if ((r & 1) == 0) {
        f2 s0 = cmul(c[r], w256), s1 = cmul(ch, w256);
        PUT(16 + (r >> 1), s0); PUT(20 + (r >> 1), s1);
        if ((r & 3) == 0) {
          const int rho = r >> 2;
          f2 z0 = cmul(c[r], w512), z1 = cmul(ch, w512), z2 = cmul(s0, w512), z3 = cmul(s1, w512);
          PUT(24 + 0 + rho, z0); PUT(24 + 2 + rho, z1); PUT(24 + 4 + rho, z2); PUT(24 + 6 + rho, z3);
          if (r == 0) {
            f2 y0 = cmul(c[0], w1024);
            PUT(32, y0); PUT(33, cmul(ch, w1024)); PUT(34, cmul(s0, w1024)); PUT(35, cmul(s1, w1024));
            PUT(36, cmul(z0, w1024)); PUT(37, cmul(z1, w1024)); PUT(38, cmul(z2, w1024)); PUT(39, cmul(z3, w1024));
            PUT(40, cmul(y0, w1024));
          }
        }
      }
    }
  }
  sy_arrive(sm, 1, lane);
  sy_wait(sm, 1, 3);
  STAMP(0, 2);
  {
    const unsigned* tab = (const unsigned*)sm;
    f4 acc0 = Z, acc1 = Z;
    const unsigned* pa = tab + col * TAB_PITCH + g * 4;
    const unsigned* pb = tab + (16 + wave * 16 + col) * TAB_PITCH + g * 4;
    u4 ea[12], pb_[12];
#pragma unroll
    for (int kk = 0; kk < 12; ++kk) { ea[kk] = *(const u4*)(pa + kk * 16); pb_[kk] = *(const u4*)(pb + kk * 16); }
#pragma unroll
    for (int kk = 0; kk < 12; kk += 2) {
      acc0 = MFMA16(as_h8(ea[kk]), as_h8(pb_[kk]), acc0);
      acc1 = MFMA16(as_h8(ea[kk + 1]), as_h8(pb_[kk + 1]), acc1);
    }
    __builtin_amdgcn_sched_group_barrier(0x100, 24, 0);
    __builtin_amdgcn_sched_group_barrier(0x008, 12, 0);
    acc0 += acc1;
    u2 o; o.x = pk_h(acc0[0], acc0[1]); o.y = pk_h(acc0[2], acc0[3]);
    *(u2*)(kc + ((wave * 16 + col) * 16 + 4 * g) * 2) = o;
  }
  sy_arrive(sm, 2, lane);
  sy_wait(sm, 2, 3);
  *(u4*)(sm + SM_KI + tid * 16) = u4{LDP(ga0.x, ga0.y), LDP(ga0.z, ga0.w), LDP(ga1.x, ga1.y), LDP(ga1.z, ga1.w)};
  if (wave == 0) *(u4*)(sm + SM_KI + (192 + tid) * 16) = u4{LDP(gb0.x, gb0.y), LDP(gb0.z, gb0.w), LDP(gb1.x, gb1.y), LDP(gb1.z, gb1.w)};
  sy_arrive(sm, 3, lane);
  unsigned pf = 0;
  if (bid < 312) {
    const int xcd = bid & 7;
    const int id = (bid >> 3) * 192 + tid;
    u64 pc; int o1, o2;
    asm volatile("s_getpc_b64 %0\n\ts_mov_b32 %1, k3_tail@rel32@lo+4\n\ts_mov_b32 %2, k9_end@rel32@lo+12" : "=s"(pc), "=s"(o1), "=s"(o2));
    int r3 = id / 3, q3 = id - r3 * 3;
    const char* pp0 = (const char*)((r3 < 640 ? wg + (size_t)r3 * F_ : wu + (size_t)(r3 - 640) * F_) + xcd * 192 + q3 * 64);
    int i2 = id - 3840;
    const char* pp1 = (const char*)(wd + (size_t)(i2 >> 1) * D_ + xcd * 80 + (i2 & 1) * 78);
    int i3 = (id - 6912) * 64; i3 = (i3 >= 0 && i3 < o2 - o1) ? i3 : 0;
    const char* pp2 = id >= 7400 ? (const char*)wr + ((id - 7400) % 10) * 128 : (const char*)(pc + (long long)o1 + i3);
    pf = *(const GAS unsigned*)(id < 3840 ? pp0 : (id < 6912 ? pp1 : pp2));
  }
  sy_wait(sm, 3, 3);
  __builtin_amdgcn_s_setprio(0);
  STAMP(0, 3);
  const int i0 = 15 - col + 8 * g;
  const char* ka = sm + SM_KI + ((i0 & 1) * NR_LEN + (i0 & ~1)) * 2;
  const int n0 = 3 * wave;
  f4 a0 = Z, a1 = Z, a2 = Z;
  const unsigned fa = (unsigned)(SM_U + (256 + 16 * col + 8 * g + 256 * n0 - 528) * 2);
  if (wave == 2) {
    const int ib = 127 - col + 4 * g;
    const char* kb = sm + SM_KI + (F3_OFF + (ib & 1) * 80 + (ib & ~1)) * 2;
    h8 tt[4];
#pragma unroll
    for (int dl = 4; dl <= 7; ++dl) tt[dl - 4] = ld_4dw(kb - dl * 32, kb + 320 - dl * 32);
    const unsigned kv = LDU(SM_KC + 32 * 16 * 2) * 0x10001u;
    h8 As = as_h8(u4{bx6.x & kv, bx6.y & kv, bx6.z & kv, bx6.w & kv});
    sy_wait(sm, 4, 1);
    u4 w1[4], w2[4], fs[4];
#pragma unroll
    for (int i = 0; i < 4; ++i) {
      w1[i] = *(const u4*)(ub + (256 * i - 16) * 2); w2[i] = *(const u4*)(ub + (256 * i - 24) * 2);
      fs[i] = *(const u4*)(ub + (256 * (4 + i) - 1032) * 2);
    }
    __builtin_amdgcn_sched_barrier(0);
    f4 acc[4];
#pragma unroll
    for (int i = 0; i < 4; ++i) acc[i] = MFMA16(As, as_h8(fs[i]), Z);
#pragma unroll
    for (int tau = 0; tau < 4; ++tau) {
      h8 s3 = cvt_pair(MFMA16(as_h8(w1[tau]), as_h8(bx4), Z), MFMA16(as_h8(w2[tau]), as_h8(bx5), Z));
#pragma unroll
      for (int dl = 4; dl <= 7; ++dl)
        if (tau + dl <= 7) acc[tau + dl - 4] = MFMA16(s3, tt[dl - 4], acc[tau + dl - 4]);
    }
    *(f4*)(sm + SM_P + 6 * 1024 + lane * 16) = acc[0];
    *(f4*)(sm + SM_P + 7 * 1024 + lane * 16) = acc[1];
    a0 = acc[2]; a1 = acc[3];
    asm volatile("s_nop 7\n\ts_nop 7" : "+v"(a0), "+v"(a1));
    sy_arrive(sm, 5, lane);
  }
  h8 A[17];
#pragma unroll
  for (int k = 0; k < 17; ++k) A[k] = ld_4dw(ka + k * 64, ka + k * 64 + 8);
  __builtin_amdgcn_sched_barrier(0);
  sy_wait(sm, 4, 1);
  if (wave == 0) near_band<9, 33, true>(A, fa, a0, a1, a2);
  else if (wave == 1) near_band<0, 33, true>(A, fa, a0, a1, a2);
  else near_band<0, 25, false>(A, fa, a0, a1, a2);
  asm volatile("s_nop 7\n\ts_nop 7" : "+v"(a0), "+v"(a1), "+v"(a2));
  sy_wait(sm, 5, 2);
  {
    const char* pp = sm + SM_P + lane * 16 + (n0 - 2) * 1024;
    if (wave != 0) { a0 += *(const f4*)(pp); a1 += *(const f4*)(pp + 1024); }
    if (wave == 1) { a1 += *(const f4*)(sm + SM_P + 6 * 1024 + lane * 16); a2 += *(const f4*)(sm + SM_P + 7 * 1024 + lane * 16); }
    const char* ue = sm + SM_U + (256 + 16 * col + 4 * g + 256 * n0) * 2;
    const float dq = dsk * (1.f / 128.f);
    conv_epi(a0, unsc, ue, dq, yt + YTS * n0);
    conv_epi(a1, unsc, ue + 512, dq, yt + YTS * (n0 + 1));
    if (wave != 2) { a2 += *(const f4*)(pp + 2048); conv_epi(a2, unsc, ue + 1024, dq, yt + YTS * (n0 + 2)); }
  }
  asm volatile("" :: "v"(pf), "v"(selfpf));
  STAMP(0, 4);
}

__device__ __forceinline__ unsigned ord16(unsigned b) { return (b & 0x8000u) ? (~b & 0xffffu) : (b | 0x8000u); }

__device__ __forceinline__ u64 sweep_keys(char* ws, int lane, int rep, int q, u64* lk) {
  GAS u64* kp = (GAS u64*)(ws + WS_KEYS) + rep * 256 + lane;
  u64 b;
  for (unsigned spins = 0;; ++spins) {
    bool ok = (long long)__hip_atomic_load(kp + q * 64, RLX_AGENT) < 0;
    if (__all(ok) || spins > SPIN_CAP) break;
    __builtin_amdgcn_s_sleep(3);
  }
  for (unsigned spins = 0;; ++spins) {
    u64 k0 = __hip_atomic_load(kp, RLX_AGENT), k1 = __hip_atomic_load(kp + 64, RLX_AGENT);
    u64 k2 = __hip_atomic_load(kp + 128, RLX_AGENT), k3 = __hip_atomic_load(kp + 192, RLX_AGENT);
    bool ok = (long long)(k0 & k1 & k2 & k3) < 0;
    b = k0 > k1 ? k0 : k1; u64 c = k2 > k3 ? k2 : k3; b = b > c ? b : c;
    if (__all(ok) || spins > SPIN_CAP) {
      if (lk) { lk[lane] = k0; lk[lane + 64] = k1; lk[lane + 128] = k2; lk[lane + 192] = k3; }
      break;
    }
    __builtin_amdgcn_s_sleep(2);
  }
  unsigned hk = (unsigned)(b >> 32), m = hk;
  m = max(m, (unsigned)__builtin_amdgcn_update_dpp(0, (int)m, 0x121, 0xf, 0xf, true));
  m = max(m, (unsigned)__builtin_amdgcn_update_dpp(0, (int)m, 0x122, 0xf, 0xf, true));
  m = max(m, (unsigned)__builtin_amdgcn_update_dpp(0, (int)m, 0x124, 0xf, 0xf, true));
  m = max(m, (unsigned)__builtin_amdgcn_update_dpp(0, (int)m, 0x128, 0xf, 0xf, true));
  unsigned m0 = (unsigned)__builtin_amdgcn_readlane((int)m, 0), m1 = (unsigned)__builtin_amdgcn_readlane((int)m, 16);
  unsigned m2 = (unsigned)__builtin_amdgcn_readlane((int)m, 32), m3 = (unsigned)__builtin_amdgcn_readlane((int)m, 48);
  m0 = max(max(m0, m1), max(m2, m3));
  int src = __builtin_ctzll(__ballot(hk == m0));
  unsigned lo = (unsigned)__builtin_amdgcn_readlane((int)(unsigned)b, src);
  return ((u64)m0 << 32) | lo;
}
__device__ __forceinline__ float par_sum(float v) {
  v = dpp_add<0x122>(v); v = dpp_add<0x124>(v); v = dpp_add<0x128>(v);
  return v;
}
__device__ __forceinline__ unsigned out_pair(f2 h, float rs, float m) {
  f2 dd = h - m;
  dd = unpk(pk_bf(dd.x, dd.y)) * rs;
  dd = h + unpk(pk_bf(dd.x, dd.y));
  return pk_bf(dd.x, dd.y);
}
__device__ __forceinline__ float xs_of(us xv, us yv, float wm, float wrs, float& hh) {
  hh = rbf(bf1(xv) + bf1(yv));
  return rbf(rbf(hh - wm) * wrs);
}

extern "C" __global__ __launch_bounds__(384, 3) void k3_tail(const us* __restrict__ x, const us* __restrict__ wr,
                                                  const us* __restrict__ wg, const us* __restrict__ wu,
                                                  const us* __restrict__ wd, us* __restrict__ out,
                                                  char* __restrict__ ws) {
  __shared__ __attribute__((aligned(16))) float red[5 * 32 + 16 + 8];
  __shared__ __attribute__((aligned(16))) u64 lkeys[1280];
  unsigned* redW = (unsigned*)(red + 176);
  const int tid = threadIdx.x, lane = tid & 63, wave = __builtin_amdgcn_readfirstlane(tid >> 6);
  const int bid = blockIdx.x;
  STAMP(2, 0);
  const us* ytb = (const us*)(ws + WS_YT);
  if (bid >= 256) {
    const bool f1 = bid < 352;
    const int j = f1 ? bid - 256 : bid - 352;
    const int cb = f1 ? (j & 7) * 12 + (j >> 3) : (j & 7) * 5 + (j >> 3);
    if (wave == 5) {
      __builtin_amdgcn_s_sleep(64);
      const u64 wn = sweep_keys(ws, lane, j & 7, (j >> 3) & 3, f1 ? lkeys : nullptr);
      const unsigned wkey = (unsigned)(wn >> 32), wms = (unsigned)wn;
      const int wrow = 2047 - (int)(wkey & 2047u);
      const int c = lane & 15;
      if (f1) {
        if (lane == 0) { redW[2] = wkey; redW[3] = wms; }
        STAMPT(2, 3, 320);
        __syncthreads();
        __syncthreads();
        const float* rp = red + (c >> 3) * 16 + (c & 7);
        float gs = (rp[0] + rp[32]) + (rp[64] + rp[96]) + rp[128];
        float us_ = (rp[8] + rp[40]) + (rp[72] + rp[104]) + rp[136];
        float gb = rbf(gs);
        float sl = rbf(gb * __builtin_amdgcn_rcpf(1.f + __expf(-gb)));
        unsigned a = (unsigned)f2bf(sl * rbf(us_)) | 0x10000u;
        __hip_atomic_store((GAS unsigned*)(ws + WS_ACT) + (lane >> 4) * F_ + cb * 16 + c, a, RLX_AGENT);
        STAMPT(2, 4, 320);
      } else {
        __syncthreads();
        const int col = cb * 16 + c;
        float fh;
        float fx2 = xs_of(x[(size_t)wrow * D_ + col], ytb[((size_t)(wrow >> 3) * D_ + col) * 8 + (wrow & 7)],
                          bflo(wms), __builtin_amdgcn_rcpf(bfhi(wms)), fh);
        unsigned ob = (wkey >> 11) & 0xffffu;
        unsigned rb = (ob & 0x8000u) ? (ob & 0x7fffu) : (~ob & 0xffffu);
        float sg = rbf(__builtin_amdgcn_rcpf(1.f + __expf(-bf1((us)rb))));
        __syncthreads();
        const float* rp = red + c;
        float ys = (rp[0] + rp[16]) + (rp[32] + rp[48]) + rp[64];
        float y = rbf(rbf(ys) * sg);
        if (lane < 16) out[(size_t)wrow * D_ + col] = f2bf(fh + rbf(fx2 + y));
        STAMPT(2, 5, 320);
      }
      return;
    }
    const int half = tid & 1, rb = tid >> 1;
    __builtin_amdgcn_s_sleep(16);
    if (f1) {
      const size_t o0 = (size_t)rb * F_ + cb * 16 + half * 8;
      u4 wq[8];
#pragma unroll
      for (int k = 0; k < 4; ++k) { wq[k] = *(const u4*)(wg + o0 + (size_t)k * 160 * F_); wq[4 + k] = *(const u4*)(wu + o0 + (size_t)k * 160 * F_); }
      f2 wf[32];
#pragma unroll
      for (int k = 0; k < 8; ++k) { wf[4 * k] = unpk(wq[k].x); wf[4 * k + 1] = unpk(wq[k].y); wf[4 * k + 2] = unpk(wq[k].z); wf[4 * k + 3] = unpk(wq[k].w); }
#pragma unroll
      for (int k = 0; k < 32; ++k) asm volatile("" : "+v"(wf[k]));
      __syncthreads();
      const unsigned wkey = redW[2], wms = redW[3];
      const int wrow = 2047 - (int)(wkey & 2047u);
      const float wm = bflo(wms), wrs = __builtin_amdgcn_rcpf(bfhi(wms));
      const us* xw = x + (size_t)wrow * D_ + rb;
      const us* yw = ytb + ((size_t)(wrow >> 3) * D_ + rb) * 8 + (wrow & 7);
      us xv[4], yv[4];
#pragma unroll
      for (int k = 0; k < 4; ++k) { xv[k] = xw[160 * k]; yv[k] = yw[160 * 8 * k]; }
      f2 acc[8];
#pragma unroll
      for (int i = 0; i < 8; ++i) acc[i] = f2{0.f, 0.f};
#pragma unroll
      for (int k = 0; k < 4; ++k) {
        float hh; float xs = xs_of(xv[k], yv[k], wm, wrs, hh);
        f2 x2v = {xs, xs};
#pragma unroll
        for (int i = 0; i < 4; ++i) {
          acc[i] = __builtin_elementwise_fma(wf[4 * k + i], x2v, acc[i]);
          acc[4 + i] = __builtin_elementwise_fma(wf[16 + 4 * k + i], x2v, acc[4 + i]);
        }
      }
      float w[8], o[4];
#pragma unroll
      for (int i = 0; i < 4; ++i) { w[2 * i] = swap32_add(acc[i].x, acc[4 + i].x); w[2 * i + 1] = swap32_add(acc[i].y, acc[4 + i].y); }
#pragma unroll
      for (int i = 0; i < 4; ++i) o[i] = swap16_add(w[i], w[i + 4]);
      par_sum4(o);
      const int rr = lane >> 4;
      if ((lane & 15) < 2) *(f4*)(red + wave * 32 + half * 16 + 4 * (rr & 1) + 8 * (rr >> 1)) = f4{o[0], o[1], o[2], o[3]};
      __syncthreads();
#pragma unroll
      for (int q = 0; q < 3; ++q) {
        const int cbk = j + 96 * q;
        if (cbk < 256) {
          const u64 gk = lkeys[cbk];
          const unsigned ck = (unsigned)(gk >> 32), cms = (unsigned)gk;
          if (ck != wkey) {
            const int crow = 2047 - (int)(ck & 2047u);
            const float crs = __builtin_amdgcn_rcpf(bfhi(cms));
            unsigned xr = ((const unsigned*)(x + (size_t)crow * D_))[tid];
            const us* yc = ytb + ((size_t)(crow >> 3) * D_ + 2 * tid) * 8 + (crow & 7);
            f2 hs = unpk(xr) + f2{bf1(yc[0]), bf1(yc[8])};
            f2 hh = unpk(pk_bf(hs.x, hs.y));
            ((unsigned*)(out + (size_t)crow * D_))[tid] = out_pair(hh, crs, bflo(cms));
          }
        }
      }
      STAMP(2, 6);
    } else {
      const us* wp = wd + cb * 16 + half * 8;
      u4 wq[10];
#pragma unroll
      for (int k = 0; k < 10; ++k) { int r = rb + 160 * k; r = r < F_ ? r : F_ - 1; wq[k] = *(const u4*)(wp + (size_t)r * D_); }
      f2 wf[40];
#pragma unroll
      for (int k = 0; k < 10; ++k) { wf[4 * k] = unpk(wq[k].x); wf[4 * k + 1] = unpk(wq[k].y); wf[4 * k + 2] = unpk(wq[k].z); wf[4 * k + 3] = unpk(wq[k].w); }
#pragma unroll
      for (int k = 0; k < 40; ++k) asm volatile("" : "+v"(wf[k]));
      __syncthreads();
      __builtin_amdgcn_s_sleep(56);
      GAS unsigned* ap = (GAS unsigned*)(ws + WS_ACT) + (j & 3) * F_ + rb;
      const int last = rb < 96 ? 1440 : 0;
      unsigned a[10];
      for (unsigned spins = 0;; ++spins) {
#pragma unroll
        for (int k = 0; k < 9; ++k) a[k] = __hip_atomic_load(ap + 160 * k, RLX_AGENT);
        a[9] = __hip_atomic_load(ap + last, RLX_AGENT);
        unsigned all = a[0] & a[1] & a[2] & a[3] & a[4] & a[5] & a[6] & a[7] & a[8] & a[9];
        if (__all(all >> 16) || spins > SPIN_CAP) break;
        __builtin_amdgcn_s_sleep(1);
      }
      if (rb >= 96) a[9] = 0;
      f2 acc[4] = {f2{0.f, 0.f}, f2{0.f, 0.f}, f2{0.f, 0.f}, f2{0.f, 0.f}};
#pragma unroll
      for (int k = 0; k < 10; ++k) {
        float av = bflo(a[k]); f2 a2 = {av, av};
#pragma unroll
        for (int i = 0; i < 4; ++i) acc[i] = __builtin_elementwise_fma(wf[4 * k + i], a2, acc[i]);
      }
      float w0 = swap32_add(acc[0].x, acc[2].x), w1 = swap32_add(acc[0].y, acc[2].y);
      float w2 = swap32_add(acc[1].x, acc[3].x), w3 = swap32_add(acc[1].y, acc[3].y);
      float o0 = par_sum(swap16_add(w0, w2)), o1 = par_sum(swap16_add(w1, w3));
      const int rr = lane >> 4;
      if ((lane & 15) < 2) *(f2*)(red + wave * 16 + half * 8 + 2 * (rr & 1) + 4 * (rr >> 1)) = f2{o0, o1};
      __syncthreads();
    }
    return;
  }
  float* redA = red;
  float* abv = red + 160;
  const int tb = (bid & 7) * 32 + (bid >> 3);
  const int t0 = tb * 8;
  const unsigned* xb = (const unsigned*)(x + (size_t)t0 * D_);
  if (wave == 5) {
    const int r = lane & 7;
    const float piv = bf1(x[(size_t)(t0 + r) * D_]);
    const float W = *(const float*)(ws + WS_WSUM);
    __syncthreads();
    float s1 = (redA[r] + redA[24 + r]) + (redA[48 + r] + redA[72 + r]) + redA[96 + r];
    float s2 = (redA[8 + r] + redA[32 + r]) + (redA[56 + r] + redA[80 + r]) + redA[104 + r];
    float s3 = (redA[16 + r] + redA[40 + r]) + (redA[64 + r] + redA[88 + r]) + redA[112 + r];
    float mb, sb;
    ln_from_sums(s1, s2, piv, mb, sb);
    float rs = __builtin_amdgcn_rcpf(sb);
    float rv = rs * __builtin_fmaf(piv - mb, W, s3);
    unsigned key = 0x80000000u | (ord16(f2bf(rv)) << 11) | (unsigned)(2047 - (t0 + r));
    unsigned best = key;
    best = max(best, (unsigned)__builtin_amdgcn_update_dpp(0, (int)best, 0x111, 0xf, 0xf, true));
    best = max(best, (unsigned)__builtin_amdgcn_update_dpp(0, (int)best, 0x112, 0xf, 0xf, true));
    best = max(best, (unsigned)__builtin_amdgcn_update_dpp(0, (int)best, 0x114, 0xf, 0xf, true));
    best = (unsigned)__builtin_amdgcn_readlane((int)best, 7);
    if (key == best) {
      u64 gr = ((u64)best << 32) | (u64)((__float_as_uint(mb) >> 16) | (__float_as_uint(sb) & 0xffff0000u));
      __hip_atomic_store((GAS u64*)(ws + WS_KEYS) + (lane >> 3) * 256 + bid, gr, RLX_AGENT);
      redW[0] = (unsigned)r;
    }
    if (lane < 8) *(f2*)(abv + 2 * r) = f2{rs, mb};
    STAMPT(2, 2, 320);
    __syncthreads();
    return;
  }
  unsigned raw[8];
#pragma unroll
  for (int r = 0; r < 8; ++r) raw[r] = xb[r * (D_ / 2) + tid];
  const u4 y0 = *(const u4*)(ytb + ((size_t)tb * D_ + 2 * tid) * 8);
  const u4 y1 = *(const u4*)(ytb + ((size_t)tb * D_ + 2 * tid) * 8 + 8);
  const f2 wv = unpk(((const unsigned*)wr)[tid]);
  f2 h[8];
  {
    float v[24];
#pragma unroll
    for (int r = 0; r < 8; ++r) {
      const float piv = bflo(xb[r * (D_ / 2)]);
      unsigned ya = y0[r >> 1], yb = y1[r >> 1];
      f2 yy = (r & 1) ? f2{bfhi(ya), bfhi(yb)} : f2{bflo(ya), bflo(yb)};
      f2 hs = unpk(raw[r]) + yy;
      h[r] = unpk(pk_bf(hs.x, hs.y));
      f2 dd = h[r] - piv;
      v[r] = dd.x + dd.y;
      v[r + 8] = __builtin_fmaf(dd.x, dd.x, dd.y * dd.y);
      v[r + 16] = __builtin_fmaf(dd.x, wv.x, dd.y * wv.y);
    }
    float w[12], o[6];
#pragma unroll
    for (int i = 0; i < 12; ++i) w[i] = swap32_add(v[i], v[i + 12]);
#pragma unroll
    for (int i = 0; i < 6; ++i) o[i] = swap16_add(w[i], w[i + 6]);
    row_sum6(o);
    if ((lane & 15) == 0) {
      float* dst = redA + wave * 24 + (lane >> 4) * 6;
      *(f2*)dst = f2{o[0], o[1]}; *(f2*)(dst + 2) = f2{o[2], o[3]}; *(f2*)(dst + 4) = f2{o[4], o[5]};
    }
  }
  __syncthreads();
  STAMP(2, 1);
  __syncthreads();
  const int brow = (int)redW[0];
  unsigned* stg = (unsigned*)lkeys + wave * 512;
#pragma unroll
  for (int r = 0; r < 8; r += 2) {
    f4 c = *(const f4*)(abv + 2 * r);
    stg[r * 64 + lane] = out_pair(h[r], c.x, c.y);
    stg[(r + 1) * 64 + lane] = out_pair(h[r + 1], c.z, c.w);
  }
  {
    const int rr = lane >> 4, ch = lane & 15;
    u4 q0 = *(const u4*)(stg + rr * 64 + ch * 4), q1 = *(const u4*)(stg + (rr + 4) * 64 + ch * 4);
    us* o0p = out + (size_t)(t0 + rr) * D_ + wave * 128 + ch * 8;
    __builtin_amdgcn_s_sleep(60);
    if (rr != brow) asm volatile("global_store_dwordx4 %0, %1, off sc1\n\ts_nop 1" :: "v"(o0p), "v"(q0) : "memory");
    if (rr + 4 != brow) asm volatile("global_store_dwordx4 %0, %1, off sc1\n\ts_nop 1" :: "v"(o0p + 4 * D_), "v"(q1) : "memory");
  }
}

extern "C" __global__ void k9_end() {}

extern "C" void kernel_launch(void* const* d_in, const int* in_sizes, int n_in, void* d_out, int out_size,
                              void* d_ws, size_t ws_size, hipStream_t stream) {
  const us* x = (const us*)d_in[0];
  const us* Are = (const us*)d_in[1];
  const us* Aim = (const us*)d_in[2];
  const us* Bre = (const us*)d_in[3];
  const us* Bim = (const us*)d_in[4];
  const us* Cre = (const us*)d_in[5];
  const us* Cim = (const us*)d_in[6];
  const us* logdt = (const us*)d_in[7];
  const us* Dskip = (const us*)d_in[8];
  const us* wr = (const us*)d_in[10];
  const us* wg = (const us*)d_in[11];
  const us* wu = (const us*)d_in[12];
  const us* wd = (const us*)d_in[13];
  us* out = (us*)d_out;
  char* ws = (char*)d_ws;
  ka_conv<<<D_, 256, K2_LDS, stream>>>(x, Are, Aim, Bre, Bim, Cre, Cim, logdt, Dskip, wr, wg, wu, wd, ws);
  k3_tail<<<392, 384, 0, stream>>>(x, wr, wg, wu, wd, out, ws);
}
```
